```python
import torch

def _softmax(x, axis=-1):
    return torch.softmax(x, dim=axis)
def _gelu(x):
    return torch.nn.functional.gelu(x, approximate="tanh")
def _silu(x):
    return torch.nn.functional.silu(x)
def _sigmoid(x):
    return torch.sigmoid(x)
def _layernorm(x, axis=-1, eps=1e-5):
    m = x.mean(dim=axis, keepdim=True)
    v = x.var(dim=axis, keepdim=True, unbiased=False)
    return (x - m) / torch.sqrt(v + eps)
def _scatter_add(tgt, idx, src, axis=0):
    return tgt.clone().index_add_(axis, idx, src)
def _segment_sum(src, seg, n):
    out = torch.zeros((n,) + src.shape[1:], dtype=src.dtype)
    return out.index_add_(0, seg, src)
def _pad(x, pw):
    flat = tuple(v for pair in reversed(pw) for v in pair)
    return torch.nn.functional.pad(x, flat)

kernel_name = 'recurrent_scan__linear_kernel_hedgehog__moe_arch_mod__prompt'

def setup_inputs():

    _w = (lambda a: (lambda t: t.to(torch.bfloat16) if t.is_floating_point() else t)(torch.as_tensor(a)))
    def _A_setup():
        B, S, D, H = 1, 2048, 1024, 32
        Dh = D // H
        return {
            "x": _w((lambda *s: torch.randn(*s, dtype=torch.bfloat16) * 0.05)(B, S, D)),
            "wqkv": _w((lambda *s: torch.randn(*s, dtype=torch.bfloat16) * 0.2)(D, 3 * D)),
            "w_phi": _w((lambda *s: torch.randn(*s, dtype=torch.bfloat16) * 0.2)(H, Dh, Dh) * (Dh ** -0.5)),
            "wo": _w((lambda *s: torch.randn(*s, dtype=torch.bfloat16) * 0.05)(D, D)),
        }
    _A_in = _A_setup()

    def _B_setup():
        B, S, D, F, C = 1, 2048, 1024, 3584, 1
        return {
            "x": _w((lambda *s: torch.randn(*s, dtype=torch.bfloat16) * 0.05)(B, S, D)),
            "w_r": _w((lambda *s: torch.randn(*s, dtype=torch.bfloat16) * 0.05)(D)),
            "w_gate": _w((lambda *s: torch.randn(*s, dtype=torch.bfloat16) * 0.05)(D, F)),
            "w_up": _w((lambda *s: torch.randn(*s, dtype=torch.bfloat16) * 0.05)(D, F)),
            "w_down": _w((lambda *s: torch.randn(*s, dtype=torch.bfloat16) * 0.05)(F, D)),
        }
    _B_in = _B_setup()

    return {
        "A_x": _A_in["x"],
        "A_wqkv": _A_in["wqkv"],
        "A_w_phi": _A_in["w_phi"],
        "A_wo": _A_in["wo"],
        "B_x": _B_in["x"],
        "B_w_r": _B_in["w_r"],
        "B_w_gate": _B_in["w_gate"],
        "B_w_up": _B_in["w_up"],
        "B_w_down": _B_in["w_down"],
    }

def reference(A_x, A_wqkv, A_w_phi, A_wo, B_x, B_w_r, B_w_gate, B_w_up, B_w_down):
    def _A_ref(x, wqkv, w_phi, wo):
        B, S, D = x.shape
        H = 32; Dh = D // H
        qkv = (lambda x, *d: x.permute(*d))((x @ wqkv).reshape(B, S, 3, H, Dh), 2, 0, 3, 1, 4)
        q, k, v = qkv[0], qkv[1], qkv[2]
        pq = torch.einsum("bhsd,hdf->bhsf", q, w_phi)
        pk = torch.einsum("bhsd,hdf->bhsf", k, w_phi)
        phi_q = torch.concatenate([_softmax(pq, axis=-1), _softmax(-pq, axis=-1)], axis=-1)
        phi_k = torch.concatenate([_softmax(pk, axis=-1), _softmax(-pk, axis=-1)], axis=-1)
        kv = torch.einsum("bhsf,bhsd->bhfd", phi_k, v)
        z  = torch.einsum("bhsf->bhf", phi_k)
        num = torch.einsum("bhqf,bhfd->bhqd", phi_q, kv)
        den = torch.einsum("bhqf,bhf->bhq", phi_q, z)[..., None] + 1e-6
        o = num / den
        return (lambda x, *d: x.permute(*d))(o, 0, 2, 1, 3).reshape(B, S, D) @ wo

    def _B_ref(x, w_r, w_gate, w_up, w_down):
        B, S, D = x.shape
        C = 1
        r = x @ w_r
        idx = torch.argsort(-r, axis=-1)[:, :C]
        idx_e = (lambda x, n, axis: torch.repeat_interleave(x, n, dim=axis))(idx.reshape(B, C, 1), D, axis=2)
        x_sel = (lambda x, i, axis: torch.take_along_dim(x, i, dim=axis))(x, idx_e, axis=1)
        g = _silu(x_sel @ w_gate)
        u = x_sel @ w_up
        y = (g * u) @ w_down
        r_sel = _sigmoid((lambda x, i, axis: torch.take_along_dim(x, i, dim=axis))(r, idx, axis=1))
        y = y * r_sel.reshape(B, C, 1)
        b_off = (torch.arange(B) * S).reshape(B, 1)
        flat_idx = (idx + b_off).reshape(B * C)
        zero = (x * 0.0).reshape(B * S, D)
        upd = _scatter_add(zero, flat_idx, y.reshape(B * C, D), axis=0)
        return x + upd.reshape(B, S, D)

    x = A_x
    xn = _layernorm(x, axis=-1)
    s = _A_ref(xn, A_wqkv, A_w_phi, A_wo)
    g = _silu(_B_ref(xn, B_w_r, B_w_gate, B_w_up, B_w_down))
    return x + s * g
```

```cpp
#define I1 10
#define I2 10
#define I3 10
#define I4 10
#define I5 10
#define I6 10
#define I7 10
#define WOK 2
#define S2A 10
#define KP 9
#define KC 10
#define F1_ST 26
#define PF2_KT 8
#define H1_DELAY 8
#define PACE 1
constexpr int PACE_T[16] = {0, 108, 72, 60, 0, 0, 0, 0, 0, 0, 0, 0, 0, 0, 0, 0};
constexpr int PACE_3[4] = {0, 60,50,50};
constexpr int DVN[20] = {0,2,2,2,0,0,1,0,1};
constexpr int dv_first(int k) { int n = 0; for (int i = -2; i < k; ++i) n += DVN[i + 2]; return n; }
constexpr bool CSS(int S) { return S < 2 || S == 6 || S == 7; }
#define WH0 6
#define WPOST 4
#define YK -1
#define YP_ 11
#define PRIO_T 2
#define PRIO_H 1
#include <hip/hip_runtime.h>
#include <hip/hip_bf16.h>
#include <stdint.h>
#include <utility>
#include <type_traits>

typedef __attribute__((ext_vector_type(8))) short bf16x8;
typedef __attribute__((ext_vector_type(4))) short s16x4;
typedef __attribute__((ext_vector_type(16))) float f32x16;
typedef __attribute__((ext_vector_type(4))) float f32x4;
typedef __attribute__((ext_vector_type(2))) float f32x2;
typedef __attribute__((ext_vector_type(4))) unsigned u32x4;
typedef __attribute__((ext_vector_type(2))) unsigned u32x2;
typedef __attribute__((ext_vector_type(2))) __bf16 bfx2;
typedef __attribute__((ext_vector_type(4))) short v4i16_t;
typedef unsigned long long u64;
typedef unsigned short u16;

#define DI __device__ __forceinline__
#define MFMA(a, b, c) __builtin_amdgcn_mfma_f32_32x32x16_bf16((a), (b), (c), 0, 0, 0)
#define MFMA16H(a, b, c) __builtin_amdgcn_mfma_f32_16x16x16bf16_1k((a), (b), (c), 0, 0, 0)
#define MFMA16(a, b, c) __builtin_amdgcn_mfma_f32_16x16x32_bf16((a), (b), (c), 0, 0, 0)

DI unsigned pk2(float a, float b) { f32x2 v = {a, b}; return __builtin_bit_cast(unsigned, __builtin_convertvector(v, bfx2)); }
DI float bflo(unsigned u) { return __uint_as_float(u << 16); }
DI float bfhi(unsigned u) { return __uint_as_float(u & 0xffff0000u); }
DI float rbf(float x) { return (float)(__bf16)x; }
DI s16x4 trread(const char* p) {
  return __builtin_bit_cast(s16x4, __builtin_amdgcn_ds_read_tr16_b64_v4i16((__attribute__((address_space(3))) v4i16_t*)p));
}
DI bf16x8 cat8(s16x4 lo, s16x4 hi) { return __builtin_shufflevector(lo, hi, 0, 1, 2, 3, 4, 5, 6, 7); }
#define WAIT_V(n) asm volatile("s_waitcnt vmcnt(%0)" ::"n"(n) : "memory")
#define RAW_BARRIER() do { asm volatile("s_waitcnt lgkmcnt(0)" ::: "memory"); __builtin_amdgcn_s_barrier(); asm volatile("" ::: "memory"); } while (0)
#define DS_B128(dst, addr, off) asm volatile("ds_read_b128 %0, %1 offset:%2" : "=v"(dst) : "v"(addr), "n"(off))
#define DS_TR(dst, addr, off) asm volatile("ds_read_b64_tr_b16 %0, %1 offset:%2" : "=v"(dst) : "v"(addr), "n"(off))
#define LDS_OFF(p) ((unsigned)(uintptr_t)(__attribute__((address_space(3))) const char*)(p))
template <class F, int... I> DI void static_for_impl(F&& f, std::integer_sequence<int, I...>) { (f(std::integral_constant<int, I>{}), ...); }
template <int N, class F> DI void static_for(F&& f) { static_for_impl(f, std::make_integer_sequence<int, N>{}); }
DI float divc(float a, float b, float rinv) { const float q = a * rinv; return __builtin_fmaf(__builtin_fmaf(-q, b, a), rinv, q); }
DI float rcp_nr(float d) { const float r = __builtin_amdgcn_rcpf(d); return __builtin_fmaf(__builtin_fmaf(-d, r, 1.0f), r, r); }
DI float own(float v) { asm volatile("" : "+v"(v)); return v; }
DI float silu_r(float x) { return x * __builtin_amdgcn_rcpf(1.0f + __expf(-x)); }
#define SB0() __builtin_amdgcn_sched_barrier(0)
#define GLDS_S(vo, sp, ld) asm volatile("s_mov_b32 m0, %2\n\ts_nop 0\n\tglobal_load_lds_dwordx4 %0, %1" :: "v"(vo), "s"(sp), "s"(ld) : "m0", "memory")
template <int IMM> DI void gl_m0(unsigned vo, const void* sp, unsigned ls) {
  asm volatile("s_add_u32 m0, %2, %3\n\ts_nop 0\n\tglobal_load_lds_dwordx4 %0, %1" :: "v"(vo), "s"(sp), "s"(ls), "n"(IMM) : "m0", "memory", "scc");
}
template <int IMM> DI void gl_nt(unsigned vo, const void* sp, unsigned ls) {
  asm volatile("s_add_u32 m0, %2, %3\n\ts_nop 0\n\tglobal_load_lds_dwordx4 %0, %1 nt" :: "v"(vo), "s"(sp), "s"(ls), "n"(IMM) : "m0", "memory", "scc");
}
template <int N> DI void gl_run(unsigned vo, const void* sp, unsigned ls) {
  unsigned lo = (unsigned)(uintptr_t)sp, hi = (unsigned)((uintptr_t)sp >> 32);
  static_for<N>([&](auto ic) {
    constexpr int i = decltype(ic)::value;
    gl_m0<i * 1024>(vo, (const void*)(((uintptr_t)hi << 32) | lo), ls);
    if constexpr (i + 1 < N) asm volatile("s_add_u32 %0, %0, 0x4000\n\ts_addc_u32 %1, %1, 0" : "+s"(lo), "+s"(hi) :: "scc");
  });
}
#define GLDS_SC1(vo, sp, ld) asm volatile("s_mov_b32 m0, %2\n\ts_nop 0\n\tglobal_load_lds_dwordx4 %0, %1 sc1" :: "v"(vo), "s"(sp), "s"(ld) : "m0", "memory")
DI float x32_add(float v) { auto r = __builtin_amdgcn_permlane32_swap(__float_as_uint(v), __float_as_uint(v), false, false); return __uint_as_float(r[0]) + __uint_as_float(r[1]); }
DI float x32_max(float v) { auto r = __builtin_amdgcn_permlane32_swap(__float_as_uint(v), __float_as_uint(v), false, false); float o; asm("v_max_f32 %0, %1, %2" : "=v"(o) : "v"(r[0]), "v"(r[1])); return o; }
DI float x32_min(float v) { auto r = __builtin_amdgcn_permlane32_swap(__float_as_uint(v), __float_as_uint(v), false, false); float o; asm("v_min_f32 %0, %1, %2" : "=v"(o) : "v"(r[0]), "v"(r[1])); return o; }
DI float x16_add(float v) { auto r = __builtin_amdgcn_permlane16_swap(__float_as_uint(v), __float_as_uint(v), false, false); return __uint_as_float(r[0]) + __uint_as_float(r[1]); }
template <int CTRL, int RMASK> DI float dpp_add(float v) {
  return v + __builtin_bit_cast(float, __builtin_amdgcn_update_dpp(0, __builtin_bit_cast(int, v), CTRL, RMASK, 0xf, true));
}

DI unsigned ln_pair(unsigned u, float m, float den, float rinv) {
  const f32x2 mm = {m, m}, dd = {den, den}, rr = {rinv, rinv};
  const f32x2 d0 = f32x2{bflo(u), bfhi(u)} - mm;
  const unsigned du = pk2(d0[0], d0[1]);
  const f32x2 d = {bflo(du), bfhi(du)};
  const f32x2 q = d * rr;
  const f32x2 r = __builtin_elementwise_fma(__builtin_elementwise_fma(-q, dd, d), rr, q);
  return pk2(r[0], r[1]);
}
DI float dot2(unsigned a, unsigned b, float c) { return __builtin_amdgcn_fdot2_f32_bf16(__builtin_bit_cast(bfx2, a), __builtin_bit_cast(bfx2, b), c, false); }
DI float half_total(float v) {
  v = dpp_add<0x121, 0xf>(v); v = dpp_add<0x122, 0xf>(v); v = dpp_add<0x124, 0xf>(v); v = dpp_add<0x128, 0xf>(v);
  return x16_add(v);
}
DI void ln_rows(const u32x4 (&xr)[4], const u32x4 (&wr)[4], int h, float& m, float& den, float& rinv, float& r) {
  const unsigned c0 = __builtin_amdgcn_readlane(xr[0][0], 0) << 16, c1 = __builtin_amdgcn_readlane(xr[0][0], 32) << 16;
  const float c = __uint_as_float(h ? c1 : c0);
  const f32x2 cc = {c, c};
  float s1[2] = {0.f, 0.f}, sx[2] = {0.f, 0.f}, sw[2] = {0.f, 0.f};
  f32x2 q[2] = {{0.f, 0.f}, {0.f, 0.f}};
#pragma unroll
  for (int i = 0; i < 4; ++i)
#pragma unroll
    for (int e = 0; e < 4; ++e) {
      const unsigned u = xr[i][e];
      s1[e & 1] = dot2(u, 0x3F803F80u, s1[e & 1]); sx[e & 1] = dot2(u, wr[i][e], sx[e & 1]); sw[e & 1] = dot2(wr[i][e], 0x3F803F80u, sw[e & 1]);
      const f32x2 d = f32x2{bflo(u), bfhi(u)} - cc;
      q[e & 1] = __builtin_elementwise_fma(d, d, q[e & 1]);
    }
  const f32x2 q2 = q[0] + q[1];
  const float mean = half_total(s1[0] + s1[1]) * (1.0f / 1024.0f), m2 = half_total(q2[0] + own(q2[1])) * (1.0f / 1024.0f);
  const float tx = half_total(sx[0] + sx[1]), tw = half_total(sw[0] + sw[1]);
  const float dm = mean - c;
  const float var = fmaxf(__builtin_fmaf(-dm, dm, m2), 0.f);
  m = rbf(mean);
  den = rbf(sqrtf(rbf(rbf(var) + 1e-5f)));
  rinv = rcp_nr(den);
  r = rbf(divc(__builtin_fmaf(-m, tw, tx), den, rinv));
}

template <bool RND, class HK> DI void phi_fn(const bf16x8 (&wp)[2], const bf16x8 f0, const bf16x8 f1, f32x2 (&ep)[8], f32x2 (&en)[8], HK&& hk) {
  f32x16 p;
#pragma unroll
  for (int i = 0; i < 16; ++i) p[i] = 0.f;
  p = MFMA(wp[0], f0, p);
  p = MFMA(wp[1], f1, p);
  hk(std::integral_constant<int, 0>{});
  float mx = -3.0e38f, mn = 3.0e38f;
  f32x2 p2[8];
  static_for<8>([&](auto ic) {
    constexpr int i = decltype(ic)::value;
    if constexpr (RND) { const unsigned u = pk2(p[2 * i], p[2 * i + 1]); p2[i] = f32x2{bflo(u), bfhi(u)}; }
    else p2[i] = f32x2{p[2 * i], p[2 * i + 1]};
    mx = fmaxf(fmaxf(mx, p2[i][0]), p2[i][1]); mn = fminf(fminf(mn, p2[i][0]), p2[i][1]);
    if constexpr (i & 1) hk(std::integral_constant<int, 1 + (i >> 1)>{});
  });
  mx = x32_max(mx); mn = x32_min(mn);
  hk(std::integral_constant<int, 5>{});
  const float L2E = 1.44269504088896340736f, mxl = -mx * L2E, mnl = mn * L2E;
  const f32x2 cp = {L2E, L2E}, cn = {-L2E, -L2E}, bp = {mxl, mxl}, bn = {mnl, mnl};
  f32x2 sp2, sn2;
  static_for<8>([&](auto ic) {
    constexpr int i = decltype(ic)::value;
    const f32x2 tp2 = __builtin_elementwise_fma(p2[i], cp, bp), tn2 = __builtin_elementwise_fma(p2[i], cn, bn);
    ep[i] = f32x2{__builtin_amdgcn_exp2f(tp2[0]), __builtin_amdgcn_exp2f(tp2[1])};
    en[i] = f32x2{__builtin_amdgcn_exp2f(tn2[0]), __builtin_amdgcn_exp2f(tn2[1])};
    if constexpr (i == 0) { sp2 = ep[0]; sn2 = en[0]; } else { sp2 += ep[i]; sn2 += en[i]; }
    hk(std::integral_constant<int, 6 + i>{});
  });
  const float sp = x32_add(sp2[0] + own(sp2[1])), sn = x32_add(sn2[0] + own(sn2[1]));
  hk(std::integral_constant<int, 14>{});
  const float rp = __builtin_amdgcn_rcpf(sp), rn = __builtin_amdgcn_rcpf(sn);
  const f32x2 rp2 = {rp, rp}, rn2 = {rn, rn};
  hk(std::integral_constant<int, 15>{});
#pragma unroll
  for (int i = 0; i < 8; ++i) { ep[i] = ep[i] * rp2; en[i] = en[i] * rn2; }
  hk(std::integral_constant<int, 16>{});
}

constexpr int DM = 1024, FF = 3584;
constexpr unsigned SENT = 0xFFFFFFFFu;
constexpr unsigned SPIN_CAP = 1u << 17;

constexpr unsigned WS_EPOCH = 0;
constexpr unsigned WS_F1 = 4096;
constexpr unsigned WS_F4 = 896u << 10;
constexpr unsigned WS_STAT = 20480;
constexpr unsigned WS_CS = 36864;
constexpr unsigned WS_GUP = WS_CS + 16 * 16 * 768;
constexpr unsigned WS_YP = WS_GUP + 256 * 512;
constexpr unsigned WS_KVF = WS_YP + 32 * 4096;
constexpr unsigned WS_ZP = WS_KVF + 256 * 640;
constexpr unsigned WS_XA = 800u << 10;
constexpr unsigned WS_RI = 840u << 10;
static_assert(WS_ZP + 131072 <= WS_XA && WS_XA + 32768 <= WS_RI, "ws map");
constexpr unsigned WS_KVP = 1u << 20;
constexpr unsigned WS_O = 8u << 20;
constexpr unsigned WS_END = 16u << 20;

constexpr int ABYTES = 128 * 128, WSLAB = 64 * 128, STAGE1 = ABYTES + 3 * WSLAB, NST1 = 3;
constexpr int XB = NST1 * STAGE1;
constexpr int X_WO = XB;
constexpr int X_CSP = XB + 8192;
constexpr int X_P = 131072 + 4096;
constexpr int X_XW = X_P;
constexpr int X_XA = X_XW + 6144;
constexpr int X_ST = X_XA + 2048;
constexpr int X_MD = X_ST + 1024;
constexpr int X_AM = X_MD + 1024;
constexpr int X_XS = X_AM + 64;
constexpr int X_YR = X_XS + 512;
constexpr int X_YS = X_YR + 2048;
constexpr int X_WP = X_YS + 2048;
constexpr int X_KB = X_WP + 4096;
constexpr int LDS_TOTAL = 163840;
static_assert(X_KB + 2 * 4224 <= LDS_TOTAL && 131072 + 6 * 2048 <= X_MD && X_YS + 64 + 4096 <= X_KB && X_YS % 128 == 64, "lds");
static_assert(LDS_TOTAL <= 163840, "lds");
constexpr int gu_at(int ks) { return STAGE1 + ks * 8192; }
constexpr int HANDOFF = 106496;

#define LD128(off) __builtin_amdgcn_raw_buffer_load_b128(rs, (int)(off), 0, 16)
#define ST128(v, off) __builtin_amdgcn_raw_buffer_store_b128((v), rs, (int)(off), 0, 16)
#define ST64(v, off) __builtin_amdgcn_raw_buffer_store_b64((v), rs, (int)(off), 0, 16)
DI u32x4 f2u_tag(f32x4 v, unsigned t8) { u32x4 u = __builtin_bit_cast(u32x4, v); return (u & 0xFFFFFF00u) | t8; }
DI unsigned or4(u32x4 v) { return (v[0] | v[1]) | (v[2] | v[3]); }
#define SPIN_NEXT(sp) { if (++(sp) > SPIN_CAP) break; __builtin_amdgcn_s_sleep(1); }
#define TAG_OF(en) ((en) * 0x9E3779B97F4A7C15ull)

__global__ __launch_bounds__(512, 2) void kf_all(const u16* __restrict__ x, const u16* __restrict__ wqkv, const u16* __restrict__ wphi,
                                              const u16* __restrict__ wo, const u16* __restrict__ w_r, const u16* __restrict__ w_gate,
                                              const u16* __restrict__ w_up, const u16* __restrict__ w_down, char* ws, u16* __restrict__ out) {
  extern __shared__ __attribute__((aligned(16))) char smem[];
  asm volatile(".Lkf_pf_start:");
  const int b = blockIdx.x;
  const int xcd = b & 7, jj = b >> 3;
  const int panel = (xcd >> 2) * 8 + (jj & 7), hp = (xcd & 3) * 4 + (jj >> 3);
  const int id = panel * 16 + hp;
  const int tid = threadIdx.x, l = tid & 63, w = __builtin_amdgcn_readfirstlane(tid >> 6);
  unsigned pfd;
  {
    u64 pc; unsigned len, back;
    asm volatile("s_getpc_b64 %0\n.Lkf_pf_here:\n\ts_mov_b32 %1, .Lkf_pf_end+1152-.Lkf_pf_start\n\ts_mov_b32 %2, .Lkf_pf_here-.Lkf_pf_start" : "=s"(pc), "=s"(len), "=s"(back));
    unsigned o = (unsigned)(jj * 8 + w) * 256u;
    o = o + 256u <= len ? o : len - 256u;
    const u64 pa = (pc - back + o) & ~63ull;
    asm volatile("s_load_dword %0, %1, 0x0\n\ts_load_dword %0, %1, 0x40\n\ts_load_dword %0, %1, 0x80\n\ts_load_dword %0, %1, 0xc0" : "=&s"(pfd) : "s"(pa) : "memory");
  }
  asm volatile("" : "+s"(x), "+s"(wqkv), "+s"(wphi), "+s"(wo), "+s"(w_r), "+s"(w_gate), "+s"(w_up), "+s"(w_down), "+s"(ws), "+s"(out));
  SB0();
#define GLD(dst, p) asm volatile("global_load_dwordx4 %0, %1, off" : "=v"(dst) : "v"(p) : "memory")
  u32x4 xr4[4], wr4[4];
  if (w < 4) {
    const u16* xr = x + (size_t)(panel * 128 + hp * 8 + (tid >> 5)) * DM + (l & 31) * 8;
    const u16* wr = w_r + (l & 31) * 8;
    GLD(xr4[0], xr); GLD(xr4[1], xr + 256); GLD(xr4[2], xr + 512); GLD(xr4[3], xr + 768);
    GLD(wr4[0], wr); GLD(wr4[1], wr + 256); GLD(wr4[2], wr + 512); GLD(wr4[3], wr + 768);
  }
  SB0();
  const int cw = w & 3, wm = cw & 1, wn = cw >> 1;
  const int h = l >> 5, r31 = l & 31, blk = (l >> 4) & 1, tq = (l & 15) >> 2, tp = l & 3;
  const int hd = hp * 2 + wn;
  const unsigned lbase = LDS_OFF(smem);
  const __amdgpu_buffer_rsrc_t rs = __builtin_amdgcn_make_buffer_rsrc((void*)ws, 0, (int)WS_END, 0x00020000);
  bf16x8 fr[4];
  bf16x8 wp[2];
  f32x4 qa[4][2]; float ri[4];
  const int kh = w >> 2;
  const int wt = wm * 2 + kh;
  char* img = smem + (wn * 4 + wt) * 4608 + r31 * 144 + h * 8;
  auto wpload = [&]() {
    const char* p = smem + X_WP + wn * 2048 + (8 * h + tq) * 64 + (16 * blk + 4 * tp) * 2;
    wp[0] = cat8(trread(p), trread(p + 256)); wp[1] = cat8(trread(p + 1024), trread(p + 1280));
  };
  auto imgw = [&](int g, const f32x2 (&ep)[8], const f32x2 (&en)[8]) {
    const u32x2 vp = {pk2(ep[2 * g][0], ep[2 * g][1]), pk2(ep[2 * g + 1][0], ep[2 * g + 1][1])};
    const u32x2 vn = {pk2(en[2 * g][0], en[2 * g][1]), pk2(en[2 * g + 1][0], en[2 * g + 1][1])};
    *(u32x2*)(img + g * 16) = vp;
    *(u32x2*)(img + 64 + g * 16) = vn;
  };
  auto pack_t = [&](const f32x4 x0, const f32x4 x1, float r0, float r1) {
    const f32x2 a0 = f32x2{x0[0], x0[1]} * f32x2{r0, r0}, a1 = f32x2{x0[2], x0[3]} * f32x2{r0, r0};
    const f32x2 b0 = f32x2{x1[0], x1[1]} * f32x2{r1, r1}, b1 = f32x2{x1[2], x1[3]} * f32x2{r1, r1};
    const auto s0 = __builtin_amdgcn_permlane16_swap(pk2(a0[0], a0[1]), pk2(b0[0], b0[1]), false, false);
    const auto s1 = __builtin_amdgcn_permlane16_swap(pk2(a1[0], a1[1]), pk2(b1[0], b1[1]), false, false);
    return u32x4{s0[0], s1[0], s0[1], s1[1]};
  };
  const int hr = hp ^ 8;
  const int p3 = ((xcd >> 1) * 4 + (jj & 3)) ^ 8, nb = (int)(((xcd & 1) ? 0xF7EDCA98u : 0xB3654210u) >> (jj & 28)) & 15;
  const int flip = jj & 1;
  const unsigned voO = (unsigned)((l >> 3) * DM + (((l & 7) ^ (((l >> 4) & 1) | ((l >> 5) << 2))) * 8)) * 2;
  const bool has_gu = (hp & 7) != 3;
  const int gr = !has_gu ? 224 + (jj - 24) * 4 + (xcd >> 1) : jj >= 28 ? 8 * (4 * (xcd >> 1) + jj - 16) + 6 : (xcd == 6 && jj >= 12) ? 8 * (24 + (jj & 3)) + 2 * ((jj >> 2) - 3) : b;
  const int gkq = (gr / 56) & 3;
  u32x4 dv[8]; unsigned ogq;
  u32x4 wr_[16];
    const int srow = l >> 3, sch = l & 7;
    unsigned voA[4], voW[6];
#pragma unroll
    for (int i = 0; i < 4; ++i) voA[i] = (unsigned)((panel * 128 + (4 * cw + i) * 8 + srow) * DM + ((sch ^ (4 * (i & 1) + (srow >> 1))) * 8)) * 2;
#pragma unroll
    for (int i = 0; i < 6; ++i) voW[i] = (unsigned)(((2 * cw + (i & 1)) * 8 + srow) * 3072 + (i >> 1) * 1024 + hp * 64 + ((sch ^ ((((srow >> 1) & 1) << 2) | ((i & 1) << 1))) * 8)) * 2;
    const unsigned lwa = lbase + cw * 4096, lww = lbase + ABYTES + cw * 2048;
    const int rot = 4 * ((panel & 7) >> 1) + 2 * (panel >> 3) + 4 * (xcd & 3);
    const u64 kord = !has_gu ? (0xFB73EA6D95C21840ull ^ ((panel & 1) ? 0x1111111111111111ull : 0ull)) : (panel & 8) ? ((panel & 1) ? 0xD95FB73EA62C1840ull : 0xD95FB73EA62C1840ull) : (panel & 1) ? 0xFB73EA62D95C1840ull : 0xFB73EA62D95C1840ull;
    auto stagep = [&](int it, auto bufc, auto loc, auto hic) {
      constexpr int B = decltype(bufc)::value * STAGE1, lo = decltype(loc)::value, hi = decltype(hic)::value;
      const int kt = ((int)(kord >> (4 * it)) + rot) & 15;
      const u16* gx = x + kt * 64; const u16* gq_ = wqkv + (size_t)kt * 64 * 3072;
      static_for<hi - lo>([&](auto ic) {
        constexpr int i = lo + decltype(ic)::value;
        if constexpr (i < 4) gl_m0<B + i * 1024>(voA[i], gx, lwa);
        else gl_m0<B + ((i - 4) >> 1) * WSLAB + (i & 1) * 1024>(voW[i - 4], gq_, lww);
      });
    };
    auto stage = [&](int it, auto bufc) { stagep(it, bufc, std::integral_constant<int, 0>{}, std::integral_constant<int, 10>{}); };
  unsigned pa0, pvh, dst, dz, pgw, ogu, oh1, oh1z;
  auto kvsetup = [&]() {
    const int g = l >> 4, c15 = l & 15;
    pa0 = lbase + kh * 18432 + (4 * g + tq) * 144 + (4 * cw + tp) * 8;
    pvh = lbase + HANDOFF + kh * 12288 + l * 16;
    dst = WS_KVP + (unsigned)((hp * 16 + panel) * 16 + w * 2 + (g & 1)) * 1024 + c15 * 32 + (g >> 1) * 16;
    dz = WS_ZP + (unsigned)((hp * 16 + w * 2 + (g & 1)) * 16 + panel) * 32 + (g >> 1) * 16;
    pgw = lbase + gu_at(0) + kh * 4096 + (8 * g + tq) * 128 + (((2 * cw + (tp >> 1)) ^ ((((tq >> 1) & 1) << 2) | ((g & 1) << 1))) * 16) + 8 * (tp & 1);
    ogu = WS_GUP + (unsigned)gr * 512 + w * 64 + g * 16;
    oh1 = WS_KVP + (unsigned)((hr * 16 + 4 * g) * 16 + panel) * 1024 + cw * 256 + c15 * 16;
    oh1z = WS_ZP + (unsigned)((hr * 16 + panel) * 16) * 32 + g * 128 + (l & 7) * 16;
    asm volatile("" : "+v"(pa0), "+v"(pvh), "+v"(dst), "+v"(dz), "+v"(pgw), "+v"(ogu), "+v"(oh1), "+v"(oh1z));
  };
#define IC(n) std::integral_constant<int, (n)>{}
#define CODE_PF(tagn, FROM, d) do { u64 pc_; unsigned len_; int off_; \
    asm volatile("s_getpc_b64 %0\n.Lkf_pf_h" #tagn ":\n\ts_mov_b32 %1, .Lkf_pf_end+1152-" FROM "\n\ts_mov_b32 %2, " FROM "-.Lkf_pf_h" #tagn : "=s"(pc_), "=s"(len_), "=s"(off_)); \
    unsigned o_ = (unsigned)(jj * 4 + cw) * 256u; o_ = o_ + 256u <= len_ ? o_ : len_ - 256u; \
    const u64 pa_ = (pc_ + (long long)off_ + o_) & ~63ull; \
    asm volatile("s_load_dword %0, %1, 0x0\n\ts_load_dword %0, %1, 0x40\n\ts_load_dword %0, %1, 0x80\n\ts_load_dword %0, %1, 0xc0" : "=&s"(d) : "s"(pa_) : "memory"); } while (0)
#define PF_DONE(d) asm volatile("s_waitcnt lgkmcnt(0)" : "+s"(d) :: "memory")
  if (w < 4) {
    unsigned oc2;
    {
      const unsigned r0 = (unsigned)(panel * 128 + hp * 8);
      oc2 = l < 33 ? WS_KVF + (unsigned)(hr * 16 + panel) * 640 + l * 16 : l == 33 ? WS_F1 + (unsigned)id * 32 : l == 34 ? WS_STAT + r0 * 8 : l == 35 ? WS_RI + r0 * 4
          : l < 40 ? WS_F4 + (unsigned)(((panel * 4 + (l - 36)) * 16 + hp) * 2) * 32 : l < 44 ? WS_GUP + (unsigned)gr * 512 + (l - 40) * 128
          : WS_ZP + (unsigned)((hp * 16 + (l & 15)) * 16 + panel) * 32;
    }
    u32x4 tm, k0, k1; u32x2 tk;
    const unsigned tmofs = lbase + X_CSP + (l < 46 ? l : 45) * 16, kofs = lbase + X_CSP + 768, tgofs = lbase + X_AM + 48;
    unsigned opay;
    { const unsigned r0 = (unsigned)(panel * 128 + hp * 8);
      opay = l < 32 ? WS_CS + (unsigned)(hp * 8 + (panel >> 1)) * 768 + (panel & 1) * 512 + l * 16 : l < 40 ? WS_XA + r0 * 16 + (l - 32) * 16 : l < 44 ? WS_STAT + r0 * 8 + (l - 40) * 16 : WS_RI + r0 * 4 + (l - 44) * 16; }
    const unsigned oflag = WS_F1 + (unsigned)id * 32;
    unsigned mz; asm volatile("s_cmp_eq_u32 %1, 0\n\ts_cselect_b32 %0, -1, 0" : "=s"(mz) : "s"(cw) : "scc");
    const u64 mk_all = ((u64)mz << 32) | mz, mk_pay = ((u64)(mz & 0x3FFFu) << 32) | (mz & ((panel & 1) ? 0xFFFFu : 0xFFFFFFFFu)), mk_one = mz & 1u;
    asm volatile("" : "+v"(oc2), "+v"(opay));
#define MST(op, data, off, mask) asm volatile("s_mov_b64 exec, %3\n\t" op " %0, %1, %2, 0 offen sc1\n\ts_mov_b64 exec, -1" :: "v"(data), "v"(off), "s"(rs), "s"(mask) : "memory")
    f32x4 csa[2] = {{0.f, 0.f, 0.f, 0.f}, {0.f, 0.f, 0.f, 0.f}};
    const bf16x8 ones = {0x3F80, 0x3F80, 0x3F80, 0x3F80, 0x3F80, 0x3F80, 0x3F80, 0x3F80};
    s16x4 cl[2][2], ch[2][2];
    const unsigned csw = lbase + X_CSP + (wm * 64 + wn * 32 + r31) * 4;
    f32x4 acc[4][6];
#pragma unroll
    for (int t = 0; t < 4; ++t)
#pragma unroll
      for (int a = 0; a < 6; ++a) acc[t][a] = f32x4{0.f, 0.f, 0.f, 0.f};
    const int g4 = l >> 4, c15 = l & 15;
    unsigned asw[2][2], wofs[2][2], cofs[2];
#pragma unroll
    for (int e = 0; e < 2; ++e) {
      asw[0][e] = lbase + (wm * 64 + c15) * 128 + (((4 * e + g4) ^ ((c15 >> 1) & 7)) * 16);
      wofs[0][e] = lbase + ABYTES + (8 * g4 + tq) * 128 + (((wn * 4 + 2 * e + (tp >> 1)) ^ ((((tq >> 1) & 1) << 2) | ((g4 & 1) << 1))) * 16) + 8 * (tp & 1);
      asw[1][e] = asw[0][e] + 2 * STAGE1; wofs[1][e] = wofs[0][e] + 2 * STAGE1;
      cofs[e] = wofs[0][e] + ((panel & 1) ? 2 : wm) * WSLAB;
    }
    asm volatile("" : "+v"(asw[0][0]), "+v"(asw[0][1]), "+v"(asw[1][0]), "+v"(asw[1][1]), "+v"(wofs[0][0]), "+v"(wofs[0][1]), "+v"(wofs[1][0]), "+v"(wofs[1][1]), "+v"(cofs[0]), "+v"(cofs[1]));
    bf16x8 af[2][4]; s16x4 wl[2][6], wh_[2][6];
    s16x4 ea[4], ew[6]; f32x4 rv[4];
    const unsigned eaofs = lbase + X_XA + (wm * 64 + c15) * 16;
    const unsigned ewofs = lbase + X_XW + g4 * 1536 + (wn * 32 + c15) * 4;
    const unsigned riofs = lbase + X_ST + (wm * 64 + c15) * 4, rvofs = lbase + X_ST + (wm * 64 + 4 * g4) * 4;
    auto rd = [&](auto ic, auto stc) {
      constexpr int i = decltype(ic)::value, S = decltype(stc)::value, s = S & 1, e = S & 1, buf = (S >> 1) % NST1;
      constexpr int b2 = buf == 2, bo = buf == 1 ? STAGE1 : 0;
      (void)&cl; (void)&ch; (void)&cofs; (void)&af; (void)&wl; (void)&wh_; (void)&asw; (void)&wofs;
      if constexpr (i == 0) DS_B128(af[s][0], asw[b2][e], bo);
      else if constexpr (i == 1) DS_TR(wl[s][0], wofs[b2][0], bo + e * 4096);
      else if constexpr (i == 2) DS_TR(wh_[s][0], wofs[b2][0], bo + e * 4096 + 512);
      else if constexpr (i < 6) DS_B128(af[s][i - 2], asw[b2][e], bo + (i - 2) * 2048);
      else if constexpr (i < 16) {
        constexpr int j = (i - 4) >> 1, off = bo + e * 4096 + (j >> 1) * WSLAB;
        if constexpr (i & 1) DS_TR(wh_[s][j], wofs[b2][j & 1], off + 512); else DS_TR(wl[s][j], wofs[b2][j & 1], off);
      } else {
        constexpr int c = (i - 16) >> 1;
        if constexpr (i & 1) DS_TR(ch[s][c], cofs[c], bo + e * 4096 + 512); else DS_TR(cl[s][c], cofs[c], bo + e * 4096);
      }
    };
    {
      kvsetup();
      SB0();
      asm volatile("s_waitcnt vmcnt(0) lgkmcnt(0)" : "+s"(pfd), "+v"(xr4[0]), "+v"(xr4[1]), "+v"(xr4[2]), "+v"(xr4[3]), "+v"(wr4[0]), "+v"(wr4[1]), "+v"(wr4[2]), "+v"(wr4[3]) :: "memory");
      SB0();
      float m, den, rinv, r;
      ln_rows(xr4, wr4, h, m, den, rinv, r);
      const int rw = tid >> 5;
      const unsigned rb = __float_as_uint(r) >> 16, ord = rb ^ ((rb & 0x8000u) ? 0xFFFFu : 0x8000u), nm = pk2(-m, -m);
      if (r31 == 0) { *(u32x4*)(smem + X_CSP + 512 + rw * 16) = u32x4{nm, nm, nm, nm};
        *(f32x2*)(smem + X_CSP + 640 + rw * 8) = f32x2{m, den}; *(float*)(smem + X_CSP + 704 + rw * 4) = rinv;
        *(unsigned*)(smem + X_CSP + 768 + rw * 4) = (ord << 11) | (unsigned)(2047 - (id * 8 + rw)); }
      SB0();
    }
    __builtin_amdgcn_s_barrier();
    static_for<20>([&](auto ic) { rd(ic, IC(0)); });
    static_for<31>([&](auto stc) {
      constexpr int S = decltype(stc)::value, s = S & 1;
      constexpr bool bnd = (S & 1) == 1, more = S + 1 < 32;
      static_for<24>([&](auto mc) {
        constexpr int m = decltype(mc)::value, j = m >> 2, t = m & 3;
        (void)&tm; (void)&k0; (void)&k1; (void)&tk; (void)&ea; (void)&ew; (void)&ri; (void)&rv; (void)&oc2; (void)&opay; (void)&oflag; (void)&mk_all; (void)&mk_pay; (void)&mk_one; (void)&rs;
        (void)&tmofs; (void)&kofs; (void)&tgofs; (void)&csw; (void)&csa; (void)&eaofs; (void)&ewofs; (void)&riofs; (void)&rvofs;
        constexpr int issued = more ? (m < 2 ? 0 : m - 2 > 16 ? 16 : m - 2) : 0;
        if constexpr (m == 0) {
          if constexpr (bnd) asm volatile("s_waitcnt lgkmcnt(0)" : "+v"(af[s][0]), "+v"(af[s][1]), "+v"(af[s][2]), "+v"(af[s][3]), "+v"(wl[s][0]), "+v"(wh_[s][0]), "+v"(wl[s][1]), "+v"(wh_[s][1]),
                                          "+v"(wl[s][2]), "+v"(wh_[s][2]), "+v"(wl[s][3]), "+v"(wh_[s][3]), "+v"(wl[s][4]), "+v"(wh_[s][4]), "+v"(wl[s][5]), "+v"(wh_[s][5]));
          else asm volatile("s_waitcnt lgkmcnt(10)" : "+v"(af[s][0]), "+v"(af[s][1]), "+v"(af[s][2]), "+v"(af[s][3]), "+v"(wl[s][0]), "+v"(wh_[s][0]));
        } else if constexpr (!bnd && t == 0) {
          constexpr int al = 10 - 2 * j + issued;
          asm volatile("s_waitcnt lgkmcnt(%2)" : "+v"(wl[s][j]), "+v"(wh_[s][j]) : "n"(al > 15 ? 15 : al));
        }
        SB0();
        const bf16x8 wf = cat8(wl[s][j], wh_[s][j]);
        if constexpr (j < 4) acc[t][j] = MFMA16(wf, af[s][t], acc[t][j]);
        else acc[t][j] = MFMA16(af[s][t], wf, acc[t][j]);
        SB0();
        if constexpr (m == 1) {
          if constexpr (S == 1) { const u32x4 sv = {SENT, SENT, SENT, SENT}; MST("buffer_store_dwordx4", sv, oc2, mk_all); }
          if constexpr (S == 11) { asm volatile("" : "+v"(tm), "+v"(k0), "+v"(k1), "+v"(tk)); MST("buffer_store_dwordx4", tm, opay, mk_pay); }
          if constexpr (S == F1_ST / 2) { WAIT_V(0); MST("buffer_store_dwordx2", tk, oflag, mk_one); }
          if constexpr (bnd && more) __builtin_amdgcn_s_barrier();
        }
        if constexpr (more && m >= 1 && m - 1 < (CSS(S + 1) ? 20 : 16)) rd(IC(m - 1), IC(S + 1));
        if constexpr (S == 8 && m == 18) {
          asm volatile("" : "+v"(csa[0]), "+v"(csa[1]));
          const float c = blk ? csa[1][0] : csa[0][0];
          const unsigned u = pk2(c, c - rbf(c));
          asm volatile("ds_write_b32 %0, %1" :: "v"(csw), "v"(u) : "memory");
        }
        if constexpr (S == 9 && m >= 18 && m < 22) {
          if constexpr (m == 18) DS_B128(tm, tmofs, 0);
          if constexpr (m == 19) DS_B128(k0, kofs, 0);
          if constexpr (m == 20) DS_B128(k1, kofs, 16);
          if constexpr (m == 21) asm volatile("ds_read_b64 %0, %1" : "=v"(tk) : "v"(tgofs));
        }
        if constexpr (S == 11 && m == 18) { k0 = __builtin_elementwise_max(k0, k1); asm volatile("" : "+v"(k0)); }
        if constexpr (S == 11 && m == 20) { tk[0] = (tk[0] & ~0x7FFFFFFu) | max(max(k0[0], k0[1]), max(k0[2], k0[3])); asm volatile("" : "+v"(tk)); }
#define DS_B64(dst, addr, off) asm volatile("ds_read_b64 %0, %1 offset:%2" : "=v"(dst) : "v"(addr), "n"(off))
#define DS_B32(dst, addr, off) asm volatile("ds_read_b32 %0, %1 offset:%2" : "=v"(dst) : "v"(addr), "n"(off))
#define DS_2(dst, addr, o0, o1) asm volatile("ds_read2_b32 %0, %1 offset0:%2 offset1:%3" : "=v"(dst) : "v"(addr), "n"(o0), "n"(o1))
        if constexpr (S == 30 && m >= 17) {
          if constexpr (m < 21) DS_B64(ea[m - 17], eaofs, (m - 17) * 256);
          else if constexpr (m < 23) { const unsigned a_ = ewofs + 256; DS_2(ew[m - 19], a_, (m - 21) * 16, (m - 21) * 16 + 192); }
        }
        SB0();
      });
      if constexpr (CSS(S)) {
        asm volatile("s_waitcnt lgkmcnt(15)" : "+v"(cl[s][0]), "+v"(ch[s][0]), "+v"(cl[s][1]), "+v"(ch[s][1]));
        SB0();
        csa[0] = MFMA16(ones, cat8(cl[s][0], ch[s][0]), csa[0]); csa[1] = MFMA16(ones, cat8(cl[s][1], ch[s][1]), csa[1]);
        SB0();
      }
    });
    {
      __builtin_amdgcn_s_setprio(PRIO_T);
      asm volatile("s_waitcnt lgkmcnt(0)" : "+v"(af[1][0]), "+v"(af[1][1]), "+v"(af[1][2]), "+v"(af[1][3]), "+v"(wl[1][0]), "+v"(wh_[1][0]), "+v"(wl[1][1]), "+v"(wh_[1][1]),
                   "+v"(wl[1][2]), "+v"(wh_[1][2]), "+v"(wl[1][3]), "+v"(wh_[1][3]), "+v"(wl[1][4]), "+v"(wh_[1][4]), "+v"(wl[1][5]), "+v"(wh_[1][5]));
      asm volatile("" : "+v"(ea[0]), "+v"(ea[1]), "+v"(ea[2]), "+v"(ea[3]), "+v"(ew[2]), "+v"(ew[3]));
      SB0();
      auto mm = [&](int t, int j) { const bf16x8 wf = cat8(wl[1][j], wh_[1][j]); if (j < 4) acc[t][j] = MFMA16(wf, af[1][t], acc[t][j]); else acc[t][j] = MFMA16(af[1][t], wf, acc[t][j]); };
      auto ff = [&](int t, int j) { if (j < 4) acc[t][j] = MFMA16H(ew[j], ea[t], acc[t][j]); else acc[t][j] = MFMA16H(ea[t], ew[j], acc[t][j]); };
      auto dm = [&](int n) {
        SB0();
        const int t = n & 3, j = (n < 16 ? 4 : 0) + ((n >> 2) & 1);
        if (n & 8) ff(t, j); else mm(t, j);
        SB0();
      };
      const unsigned ew0 = ewofs, ew2 = ewofs + 512;
      mm(2, 2); SB0(); DS_B32(ri[2], riofs, 128); DS_B32(ri[3], riofs, 192); SB0();
      mm(3, 2); SB0(); DS_B32(ri[0], riofs, 0); DS_B32(ri[1], riofs, 64); SB0();
      mm(2, 3); SB0(); DS_2(ew[4], ew2, 0, 192); DS_2(ew[5], ew2, 16, 208); SB0();
      mm(3, 3); SB0(); DS_2(ew[0], ew0, 0, 192); DS_2(ew[1], ew0, 16, 208); SB0();
      ff(2, 2); ff(3, 2); SB0(); DS_B128(rv[0], rvofs, 0); DS_B128(rv[1], rvofs, 64); SB0();
      ff(2, 3); ff(3, 3); SB0(); DS_B128(rv[2], rvofs, 128); DS_B128(rv[3], rvofs, 192); SB0();
      mm(0, 2); mm(1, 2);
      SB0();
      asm volatile("s_waitcnt lgkmcnt(0)" : "+v"(ri[0]), "+v"(ri[1]), "+v"(ri[2]), "+v"(ri[3]), "+v"(ew[0]), "+v"(ew[1]), "+v"(ew[4]), "+v"(ew[5]), "+v"(rv[0]), "+v"(rv[1]), "+v"(rv[2]), "+v"(rv[3]));
      SB0();
      char* ho = smem + HANDOFF + cw * 6144 + l * 16;
      *(u32x4*)(ho + 2048) = pack_t(acc[2][2], acc[3][2], ri[2], ri[3]);
      SB0(); mm(0, 3); mm(1, 3); SB0();
      *(u32x4*)(ho + 3072) = pack_t(acc[2][3], acc[3][3], ri[2], ri[3]);
      SB0(); ff(0, 2); ff(1, 2); ff(0, 3); ff(1, 3); SB0();
      RAW_BARRIER();
      wpload();
      dm(0); dm(1);
      fr[2] = __builtin_bit_cast(bf16x8, pack_t(acc[0][2], acc[1][2], ri[0], ri[1]));
      dm(2); dm(3);
      fr[3] = __builtin_bit_cast(bf16x8, pack_t(acc[0][3], acc[1][3], ri[0], ri[1]));
      dm(4);
      f32x2 ep[8], en[8];
      phi_fn<false>(wp, fr[2], fr[3], ep, en, [&](auto kc) { constexpr int k = decltype(kc)::value; if constexpr (k == 0) { dm(5); dm(6); dm(7); } else if constexpr (7 + k < 16) dm(7 + k); });
      __builtin_amdgcn_s_setprio(0);
#pragma unroll
      for (int g = 0; g < 4; ++g) imgw(g, ep, en);
#pragma unroll
      for (int u = 0; u < 4; ++u) {
        const int t2 = u >> 1, db = u & 1;
        const f32x4 x0 = acc[2 * t2][4 + db] * rv[2 * t2], x1 = acc[2 * t2 + 1][4 + db] * rv[2 * t2 + 1];
        char* pv = t2 ? ho + 4096 : ho;
        *(u32x4*)(pv + db * 1024) = u32x4{pk2(x0[0], x0[1]), pk2(x0[2], x0[3]), pk2(x1[0], x1[1]), pk2(x1[2], x1[3])};
      }
#pragma unroll
      for (int t = 0; t < 4; ++t) { qa[t][0] = acc[t][0]; qa[t][1] = acc[t][1]; wr_[t] = __builtin_bit_cast(u32x4, af[1][t]); }
      wr_[4] = __builtin_bit_cast(u32x4, cat8(wl[1][0], wh_[1][0])); wr_[5] = __builtin_bit_cast(u32x4, cat8(wl[1][1], wh_[1][1]));
      wr_[6] = __builtin_bit_cast(u32x4, cat8(ea[0], ea[1])); wr_[7] = __builtin_bit_cast(u32x4, cat8(ea[2], ea[3])); wr_[8] = __builtin_bit_cast(u32x4, cat8(ew[0], ew[1]));
    }
  } else {
    const int t = tid - 256;
    __builtin_amdgcn_s_setprio(PRIO_H);
    u64 epoch;
    asm volatile("s_load_dwordx2 %0, %1, 0x0" : "=&s"(epoch) : "s"(ws) : "memory");
    stagep(0, IC(0), IC(0), IC(10));
    asm volatile("s_waitcnt lgkmcnt(0)" : "+s"(pfd), "+s"(epoch) :: "memory");
    const u64 enx = epoch + 1 + (((unsigned)epoch & 0xFFu) == 0xFEu);
    const u64 tag = TAG_OF(enx);
    if (t == 0) { *(u32x2*)(smem + X_AM) = u32x2{0u, 0u};
      *(u32x4*)(smem + X_AM + 48) = u32x4{(unsigned)tag, (unsigned)(tag >> 32), (unsigned)enx, (unsigned)(enx >> 32)}; }
    u32x2 fl, si; unsigned pfd2, tpace = 0, xs;
    unsigned ofl = WS_F1 + (unsigned)t * 32, orA = WS_CS + (unsigned)(hp * 6144 + cw * 1024 + l * 16);
    unsigned orB = (cw < 2 ? WS_CS + (unsigned)(hp * 6144 + 4096 + cw * 1024) : WS_XA + (unsigned)(panel * 2048 + (cw - 2) * 1024)) + l * 16;
    unsigned orC = ((cw & 1) ? WS_STAT + (unsigned)p3 * 1024 : WS_RI + (unsigned)panel * 512) + l * 16;
    unsigned oxs = (unsigned)(gkq * 256 + cw * 64 + r31 * 2) * 2, akey = lbase + X_AM + 4, vol = (unsigned)l * 16;
    unsigned voG = (unsigned)((8 * cw + srow) * FF + ((sch ^ ((((srow >> 1) & 1) << 2) | ((cw & 1) << 1))) * 8)) * 2;
    const u16* gg_ = w_gate + (size_t)gkq * 256 * FF + ((gr + 1) % 56) * 64;
    const u16* gu_ = w_up + (size_t)gkq * 256 * FF + ((gr + 3) % 56) * 64;
    const u16* gwo = wo + (size_t)(((xcd >> 1) * 4 + (jj >> 3)) * 64 + 2 * cw * 8) * DM + ((xcd & 1) * 8 + (jj & 7)) * 64;
    const u16* gwp = wphi + hp * 2048 + cw * 512;
    const u16* gdw = w_down + (size_t)(((gr >> 3) & 31) * 128 + 32 * cw) * DM + ((gr + 1) & 7) * 128;
    unsigned odw = (unsigned)((l >> 4) * DM + (l & 15) * 8) * 2;
    const u16* gw2a = wo + (size_t)(((cw * 10) ^ (flip * 64)) * 8) * DM + nb * 64;
    const u16* gw2b = wo + (size_t)(((104 + cw * 6) ^ (flip * 64)) * 8) * DM + nb * 64;
    asm volatile("" : "+s"(gw2a), "+s"(gw2b));
    asm volatile("" : "+s"(gg_), "+s"(gu_), "+s"(gwo), "+s"(gwp), "+s"(gdw));
    { const int f = (gr >> 3) * 128 + 32 * cw + 4 * (l & 7), fs = (f >> 6) - ((l & 8) ? 3 : 1);
      ogq = WS_GUP + (unsigned)((l >> 4) * 56 + (fs < 0 ? fs + 56 : fs)) * 512 + (f & 63) * 4 + (l & 8) * 32; }
    asm volatile("" : "+v"(voG), "+v"(odw), "+v"(ogq));
    kvsetup();
    asm volatile("s_waitcnt lgkmcnt(0)" : "+v"(ofl), "+v"(orA), "+v"(orB), "+v"(orC), "+v"(oxs), "+v"(akey), "+v"(vol) :: "memory");
    stagep(0, IC(0), IC(10), IC(10));
    stagep(1, IC(1), IC(0), IC(10));
    WAIT_V(10);
    __builtin_amdgcn_s_barrier();
    stagep(1, IC(1), IC(10), IC(10));
    auto wopf = [&]() {
      GLDS_S(voO, gwo, lbase + X_WO + 2 * cw * 1024); GLDS_S(voO, gwo + 8 * DM, lbase + X_WO + (2 * cw + 1) * 1024);
      GLDS_S(vol, gwp, lbase + X_WP + cw * 1024);
    };
    auto dvl = [&](auto kc) {
      constexpr int k = decltype(kc)::value;
      static_for<DVN[k + 2]>([&](auto ic) {
        constexpr int i = dv_first(k) + decltype(ic)::value;
        u32x4& d_ = dv[i]; const unsigned o_ = odw; const u16* p_ = gdw + (size_t)(4 * i) * DM;
        asm volatile("global_load_dwordx4 %0, %1, %2 nt" : "=v"(d_) : "v"(o_), "s"(p_) : "memory");
      });
    };
    if constexpr (DVN[0] != 0) { if (has_gu) dvl(IC(-2)); }
    stagep(2, IC(2), IC(0), IC(S2A));
    if constexpr (WOK == -1) wopf();
    if constexpr (DVN[1] != 0) { if (has_gu) dvl(IC(-1)); }
    const unsigned lgu = lbase + cw * 1024;
    auto gul = [&](auto ksc) {
      constexpr int ks = decltype(ksc)::value;
      gl_nt<gu_at(ks)>(voG, gg_ + (size_t)ks * 32 * FF, lgu); gl_nt<gu_at(ks) + 4096>(voG, gu_ + (size_t)ks * 32 * FF, lgu);
    };
#define BLD64(dst, off) asm volatile("buffer_load_dwordx2 %0, %1, %2, 0 offen sc1" : "=v"(dst) : "v"(off), "s"(rs) : "memory")
    static_for<15>([&](auto ktc) {
      constexpr int kt = decltype(ktc)::value;
      constexpr auto exw = [](int k) { return DVN[k + 2]; };
      constexpr auto exo = [](int k) { return k == KP ? 1 : k == KC + 1 ? 3 : k == KC + 2 ? 2 : 0; };
      constexpr int exa = ((kt - 2 == WOK || kt - 1 == WOK) ? 3 : 0) - (kt == 0 ? 10 - S2A : 0) + (kt - 2 > KC ? exo(kt - 2) : 0);
      if constexpr (exw(kt - 2) + exw(kt - 1) != 0) { if (has_gu) WAIT_V(10 + exa + exw(kt - 2) + exw(kt - 1) + exo(kt - 1)); else WAIT_V(10 + exa + exo(kt - 1)); }
      else if constexpr (kt < 14) WAIT_V(10 + exa + exo(kt - 1));
      else { if (has_gu) WAIT_V(10); else WAIT_V(0); }
      if constexpr (PACE_T[kt] != 0) { if (cw == 0) { while ((unsigned)__builtin_amdgcn_s_memrealtime() - tpace < (unsigned)(has_gu ? PACE_T[kt] : PACE_3[kt])) {} } }
      __builtin_amdgcn_s_barrier();
      if constexpr (PACE_T[kt + 1] != 0) { if (cw == 0) tpace = (unsigned)__builtin_amdgcn_s_memrealtime(); SB0(); }
      if constexpr (kt == 0) stagep(2, IC(2), IC(S2A), IC(10));
#define UMAX_DPP(C) { const unsigned o = (unsigned)__builtin_amdgcn_update_dpp(0, (int)key, C, 0xf, 0xf, true); key = o > key ? o : key; asm volatile("" : "+v"(key)); }
#define SP(lo, hi) if constexpr (kt + NST1 < 16) { SB0(); stagep(kt + NST1, IC(kt % NST1), IC(lo), IC(hi)); SB0(); }
      SP(0, 10)
      if constexpr (kt == WOK) wopf();
      if constexpr (kt == PF2_KT) CODE_PF(2, ".Lkf_pf_mid", pfd2);
      if constexpr (kt == PF2_KT + 2) PF_DONE(pfd2);
      if constexpr (kt == KP) BLD64(fl, ofl);
      if constexpr (kt == KC) {
        static_assert(DVN[KP + 2] == 0, "count");
        if constexpr (KC == KP + 1) asm volatile("s_waitcnt vmcnt(10)" : "+v"(fl) :: "memory"); else asm volatile("" : "+v"(fl) :: "memory");
#define FL_OK() __all((((fl[0] ^ (unsigned)tag) >> 27) | (fl[1] ^ (unsigned)(tag >> 32))) == 0u)
        if (__builtin_expect(!FL_OK(), 0)) {
          for (unsigned sp = 0;;) {
            asm volatile("buffer_load_dwordx2 %0, %1, %2, 0 offen sc1\n\ts_waitcnt vmcnt(0)" : "=v"(fl) : "v"(ofl), "s"(rs) : "memory");
            if (FL_OK()) break;
            SPIN_NEXT(sp);
          }
        }
        unsigned key = fl[0];
        UMAX_DPP(0x121) UMAX_DPP(0x122) UMAX_DPP(0x124) UMAX_DPP(0x128)
        asm volatile("s_mov_b64 exec, %2\n\tds_max_u32 %0, %1\n\ts_mov_b64 exec, -1\n\ts_waitcnt lgkmcnt(0)" :: "v"(akey), "v"(key), "s"(0x0001000100010001ull) : "memory");
      }
      if constexpr (kt == KC + 1) { GLDS_SC1(orA, ws, lbase + X_XW + cw * 1024); GLDS_SC1(orB, ws, lbase + X_XW + 4096 + cw * 1024); GLDS_SC1(orC, ws, lbase + X_ST + (cw & 1) * 1024); }
      if constexpr (kt == KC + 2) {
        const int bi = 2047 - (int)(__builtin_amdgcn_readfirstlane(*(const unsigned*)(smem + X_AM + 4)) & 2047u);
        asm volatile("global_load_dword %0, %1, %2" : "=v"(xs) : "v"(oxs), "s"(x + (size_t)bi * DM) : "memory");
        asm volatile("buffer_load_dwordx2 %0, off, %1, %2 sc1" : "=v"(si) : "s"(rs), "s"(WS_STAT + (unsigned)bi * 8) : "memory");
      }
      if constexpr (DVN[kt + 2] != 0) { if (has_gu) dvl(ktc); }
      if constexpr (kt == 13) { if (has_gu) { gul(IC(0)); gul(IC(1)); gul(IC(2)); gul(IC(3)); gul(IC(4)); } }
      if constexpr (kt == 14) asm volatile("" : "+v"(xs), "+v"(si) :: "memory");
      if constexpr (kt == 14) { if (has_gu) { gul(IC(5)); gul(IC(6)); gul(IC(7)); } }
    });
    auto wld = [&](auto ic) {
      constexpr int i = decltype(ic)::value;
      u32x4& d_ = wr_[i]; const unsigned o_ = voO; const u16* p_ = (i < 10 ? gw2a : gw2b) + (size_t)((i < 10 ? i : i - 10) * 8) * DM;
      asm volatile("global_load_dwordx4 %0, %1, %2" : "=v"(d_) : "v"(o_), "s"(p_) : "memory");
    };
    wpload();
    {
      const float m = __uint_as_float(si[0]), den = own(__uint_as_float(si[1])), rinv = rcp_nr(den);
      *(unsigned*)(smem + X_XS + cw * 128 + r31 * 4) = ln_pair(xs, m, den, rinv);
    }
    RAW_BARRIER();
    const char* ho = smem + HANDOFF + cw * 6144 + l * 16;
    fr[2] = *(const bf16x8*)(ho + 2048); fr[3] = *(const bf16x8*)(ho + 3072);
    f32x2 ep[8], en[8];
    phi_fn<false>(wp, fr[2], fr[3], ep, en, [&](auto kc) {
      constexpr int k = decltype(kc)::value;
      static_for<16 - WPOST>([&](auto ic) {
        constexpr int i = decltype(ic)::value;
        if constexpr (k == WH0 + (i * (17 - WH0)) / (16 - WPOST)) { SB0(); wld(ic); SB0(); }
      });
    });
#pragma unroll
    for (int g = 0; g < 4; ++g) imgw(g, ep, en);
    SB0();
    static_for<WPOST>([&](auto ic) { wld(IC(16 - WPOST + decltype(ic)::value)); });
  }
  asm volatile(".Lkf_pf_mid:");
  f32x16 zero;
#pragma unroll
  for (int i = 0; i < 16; ++i) zero[i] = 0.f;
  u32x2 tg; unsigned t8, rkey;
  {
    __builtin_amdgcn_s_setprio(2);
    RAW_BARRIER();
    u32x4 am; unsigned rk;
    { const unsigned a_ = lbase + X_AM; DS_B128(am, a_, 48); asm volatile("ds_read_b32 %0, %1 offset:4" : "=v"(rk) : "v"(a_)); }
    s16x4 al[4], ah[4]; bf16x8 v0[4], v1[4];
    DS_TR(al[0], pa0, 0); DS_TR(ah[0], pa0, 2304); DS_B128(v0[0], pvh, 0); DS_B128(v1[0], pvh, 1024);
    DS_TR(al[1], pa0, 4608); DS_TR(ah[1], pa0, 4608 + 2304); DS_B128(v0[1], pvh, 4096); DS_B128(v1[1], pvh, 4096 + 1024);
    DS_TR(al[2], pa0, 9216); DS_TR(ah[2], pa0, 9216 + 2304); DS_B128(v0[2], pvh, 6144); DS_B128(v1[2], pvh, 6144 + 1024);
    DS_TR(al[3], pa0, 13824); DS_TR(ah[3], pa0, 13824 + 2304); DS_B128(v0[3], pvh, 6144 + 4096); DS_B128(v1[3], pvh, 6144 + 4096 + 1024);
    const bf16x8 ones = {0x3F80, 0x3F80, 0x3F80, 0x3F80, 0x3F80, 0x3F80, 0x3F80, 0x3F80};
    f32x4 k0 = {0.f, 0.f, 0.f, 0.f}, k1 = k0, zz = k0;
    static_for<4>([&](auto tbc) {
      constexpr int tb = decltype(tbc)::value;
      asm volatile("s_waitcnt lgkmcnt(%4)" : "+v"(al[tb]), "+v"(ah[tb]), "+v"(v0[tb]), "+v"(v1[tb]) : "n"(12 - 4 * tb));
      const bf16x8 af = cat8(al[tb], ah[tb]);
      k0 = MFMA16(af, v0[tb], k0); k1 = MFMA16(af, v1[tb], k1); zz = MFMA16(af, ones, zz);
      SB0();
    });
    asm volatile("" : "+v"(am), "+v"(rk));
    auto tagv = [&](f32x4 v) { const u32x4 u = __builtin_bit_cast(u32x4, v); return (u & 0xFFFFFF00u) | (am[2] & 0xFFu); };
    ST128(tagv(k0), dst); ST128(tagv(k1), dst + 512);
    const u32x4 zt = tagv(zz);
    asm volatile("s_mov_b64 exec, %3\n\tbuffer_store_dwordx4 %0, %1, %2, 0 offen sc1\n\ts_mov_b64 exec, -1" :: "v"(zt), "v"(dz), "s"(rs), "s"(0x0001000100010001ull) : "memory");
    SB0();
    tg = u32x2{am[0], am[1]}; t8 = __builtin_amdgcn_readfirstlane(am[2]) & 0xFFu; rkey = __builtin_amdgcn_readfirstlane(rk);
  }
  {
    bf16x8 xf[8];
    const unsigned px = lbase + X_XS + (l >> 4) * 16, hq = lbase + X_XW + cw * 2048 + l * 16;
    static_for<8>([&](auto kc) { constexpr int ks = decltype(kc)::value; (void)&xf; (void)&px; DS_B128(xf[ks], px, ks * 64); });
    if (w >= 4) {
      __builtin_amdgcn_s_setprio(0);
      WAIT_V(19);
    } else {
      auto lo4 = [](u32x4 v) { return __builtin_bit_cast(s16x4, u32x2{v[0], v[1]}); };
      auto hi4 = [](u32x4 v) { return __builtin_bit_cast(s16x4, u32x2{v[2], v[3]}); };
#pragma unroll
      for (int tt = 0; tt < 4; ++tt) {
        const int t = tt ^ 2;
#pragma unroll
        for (int j = 0; j < 2; ++j) qa[t][j] = MFMA16(__builtin_bit_cast(bf16x8, wr_[4 + j]), __builtin_bit_cast(bf16x8, wr_[t]), qa[t][j]);
        if (tt & 1) {
#pragma unroll
          for (int u = t - 1; u <= t; ++u)
#pragma unroll
            for (int j = 0; j < 2; ++j) qa[u][j] = MFMA16H(j ? hi4(wr_[8]) : lo4(wr_[8]), (u & 1) ? hi4(wr_[6 + (u >> 1)]) : lo4(wr_[6 + (u >> 1)]), qa[u][j]);
        }
      }
      const u32x4 q0 = pack_t(qa[2][0], qa[3][0], ri[2], ri[3]), q1 = pack_t(qa[2][1], qa[3][1], ri[2], ri[3]);
      asm volatile("ds_write_b128 %0, %1\n\tds_write_b128 %0, %2 offset:1024" :: "v"(hq), "v"(q0), "v"(q1) : "memory");
    }
    RAW_BARRIER();
    asm volatile("" : "+v"(xf[0]), "+v"(xf[1]), "+v"(xf[2]), "+v"(xf[3]), "+v"(xf[4]), "+v"(xf[5]), "+v"(xf[6]), "+v"(xf[7]));
    if (has_gu) {
      s16x4 gl[8], gh[8];
      static_for<8>([&](auto kc) { constexpr int ks = decltype(kc)::value; (void)&gl; (void)&gh; (void)&pgw;
        DS_TR(gl[ks], pgw, ks * 8192); DS_TR(gh[ks], pgw, ks * 8192 + 512); });
      f32x4 c0 = {0.f, 0.f, 0.f, 0.f};
      static_for<8>([&](auto kc) { constexpr int ks = decltype(kc)::value;
        asm volatile("s_waitcnt lgkmcnt(%2)" : "+v"(gl[ks]), "+v"(gh[ks]) : "n"(14 - 2 * ks));
        c0 = MFMA16(cat8(gl[ks], gh[ks]), xf[ks], c0);
        SB0(); });
      const u32x4 gt_ = f2u_tag(c0, t8);
      asm volatile("s_mov_b64 exec, %3\n\tbuffer_store_dwordx4 %0, %1, %2, 0 offen sc1\n\ts_mov_b64 exec, -1" :: "v"(gt_), "v"(ogu), "s"(rs), "s"(0x0001000100010001ull) : "memory");
    }
  }
  const int idx = 2047 - (int)(rkey & 2047u);
  const int fb = kh ^ ((idx >> 4) & 1);
  const int erow = cw * 32 + fb * 16 + (l & 15), ech = l >> 4;
  u32x4 xv[2];
  const bool sw = ech & 1;
#define FSWAP(v) v = u32x4{sw ? v[2] : v[0], sw ? v[3] : v[1], sw ? v[0] : v[2], sw ? v[1] : v[3]}
  const bool has_idx = w < 4 && (idx >> 5) == p3 * 4 + w;
  unsigned xrt = 0;
  unsigned of4, op4, oo0, oo1, kba, kbz, odo, o2, oz2, wa_, wz_, oxg;
  auto later = [&]() {
    SB0();
    of4 = WS_F4 + (unsigned)(((panel * 4 + wt) * 16 + hp) * 2 + wn) * 32; op4 = WS_F4 + (unsigned)(((p3 * 4 + cw) * 16 + 8 * (kh ^ flip)) * 2 + (l & 15)) * 32;
    oo0 = WS_O + 512 + (unsigned)((((p3 * 4 + cw) * 2 + fb) * 32 + (kh ^ flip) * 16) * 128 + h * 64 + r31) * 16; oo1 = oo0 ^ 65536u;
    kba = lbase + X_KB + wn * 4224 + l * 16; kbz = lbase + X_KB + wn * 4224 + 4096 + h * 64;
    odo = WS_O + 512 + (unsigned)((((panel * 4 + wt) * 2 + blk) * 32 + hd) * 128 + h * 16 + (l & 15)) * 16;
    oxg = (unsigned)((p3 * 128 + cw * 32 + fb * 16 + (l >> 3)) * DM + nb * 64 + ((l & 7) ^ (l >> 3)) * 8) * 2;
    asm volatile("" : "+v"(oxg), "+v"(kba), "+v"(kbz), "+v"(odo), "+v"(of4), "+v"(op4), "+v"(oo0), "+v"(oo1));
    SB0();
  };
  if (w < 4) {
    u32x4 v[4], z; unsigned pfd3, od; u64 mkp;
    {
      u32x4 pa[5];
      pa[4] = u32x4{t8, t8, t8, t8};
#define H1LD(p) asm volatile("buffer_load_dwordx4 %0, %5, %7, 0 offen sc1\n\tbuffer_load_dwordx4 %1, %5, %7, %8 offen sc1\n\tbuffer_load_dwordx4 %2, %5, %7, %9 offen sc1\n\t" \
                   "buffer_load_dwordx4 %3, %5, %7, %10 offen sc1\n\ts_cmp_lg_u32 %11, 3\n\ts_cbranch_scc1 1f\n\tbuffer_load_dwordx4 %4, %6, %7, 0 offen sc1\n1:" \
                   : "=&v"(p[0]), "=&v"(p[1]), "=&v"(p[2]), "=&v"(p[3]), "+v"(p[4]) : "v"(oh1), "v"(oh1z), "s"(rs), "s"(16384), "s"(32768), "s"(49152), "s"(cw) : "memory", "scc")
      __builtin_amdgcn_s_sleep(H1_DELAY);
      H1LD(pa);
      SB0();
      CODE_PF(3, ".Lkf_pf_mid", pfd3);
      od = WS_KVF + (unsigned)(hr * 16 + panel) * 640 + (blk ? 512 : cw * 128) + (l & 15) * 8 + h * 4;
      mkp = cw == 3 ? 0x0003FFFF0003FFFFull : 0x0000FFFF0000FFFFull;
      asm volatile("" : "+s"(mkp));
      fr[0] = __builtin_bit_cast(bf16x8, pack_t(qa[0][0], qa[1][0], ri[0], ri[1])); fr[1] = __builtin_bit_cast(bf16x8, pack_t(qa[0][1], qa[1][1], ri[0], ri[1]));
      asm volatile("" : "+v"(od), "+v"(fr[0]), "+v"(fr[1]));
      asm volatile("global_load_ushort %0, %1, %2" : "=v"(xrt) : "v"((unsigned)(idx * DM + nb * 64 + 4 * (l & 15) + ech) * 2), "s"(x) : "memory");
      later();
      o2 = WS_KVF + (unsigned)(hp * 16 + wn * 8 + wm * 4 + h) * 640 + r31 * 16;
      oz2 = WS_KVF + (unsigned)(hp * 16 + wn * 8 + wm * 4 + (l & 3)) * 640 + 512;
      wa_ = kba + wm * 2048; wz_ = lbase + X_KB + wn * 4224 + 4096 + (l & 1) * 64 + (wm * 2 + ((l >> 1) & 1)) * 16;
      asm volatile("" : "+v"(o2), "+v"(oz2), "+v"(wa_), "+v"(wz_));
      asm volatile("s_waitcnt vmcnt(0)" : "+v"(pa[0]), "+v"(pa[1]), "+v"(pa[2]), "+v"(pa[3]), "+v"(pa[4]), "+v"(xrt) :: "memory");
      u64 b0, b1, b2, b3, b4;
      auto miss = [&]() __attribute__((always_inline)) {
        v[0] = pa[0] ^ t8; v[1] = pa[1] ^ t8; v[2] = pa[2] ^ t8; v[3] = pa[3] ^ t8; z = pa[4] ^ t8;
        b0 = __ballot((or4(v[0]) & 0xFFu) != 0u); b1 = __ballot((or4(v[1]) & 0xFFu) != 0u); b2 = __ballot((or4(v[2]) & 0xFFu) != 0u); b3 = __ballot((or4(v[3]) & 0xFFu) != 0u); b4 = __ballot((or4(z) & 0xFFu) != 0u);
        return ((b0 | b1) | (b2 | b3) | b4) != 0;
      };
      if (miss()) {
        unsigned sp = 0;
        do {
        asm volatile("s_wqm_b64 exec, %11\n\ts_cbranch_execz 1f\n\tbuffer_load_dwordx4 %0, %5, %7, 0 offen sc1\n1:\n\ts_wqm_b64 exec, %12\n\ts_cbranch_execz 2f\n\tbuffer_load_dwordx4 %1, %5, %7, %8 offen sc1\n2:\n\t"
                     "s_wqm_b64 exec, %13\n\ts_cbranch_execz 3f\n\tbuffer_load_dwordx4 %2, %5, %7, %9 offen sc1\n3:\n\ts_wqm_b64 exec, %14\n\ts_cbranch_execz 4f\n\tbuffer_load_dwordx4 %3, %5, %7, %10 offen sc1\n4:\n\t"
                     "s_wqm_b64 exec, %15\n\ts_cbranch_execz 5f\n\tbuffer_load_dwordx4 %4, %6, %7, 0 offen sc1\n5:\n\ts_mov_b64 exec, -1\n\ts_waitcnt vmcnt(0)"
                     : "+v"(pa[0]), "+v"(pa[1]), "+v"(pa[2]), "+v"(pa[3]), "+v"(pa[4]) : "v"(oh1), "v"(oh1z), "s"(rs), "s"(16384), "s"(32768), "s"(49152), "s"(b0), "s"(b1), "s"(b2), "s"(b3), "s"(b4) : "memory", "scc");
        } while (miss() && ++sp < SPIN_CAP);
      }
    f32x4 s = (__builtin_bit_cast(f32x4, v[0]) + __builtin_bit_cast(f32x4, v[1])) + (__builtin_bit_cast(f32x4, v[2]) + __builtin_bit_cast(f32x4, v[3]));
    f32x4 zz = __builtin_bit_cast(f32x4, z);
#pragma unroll
    for (int e = 0; e < 4; ++e) { zz[e] = dpp_add<0x122, 0xf>(zz[e]); zz[e] = dpp_add<0x124, 0xf>(zz[e]); }
    auto sw32 = [](float a, float c) { const auto r = __builtin_amdgcn_permlane32_swap(__float_as_uint(a), __float_as_uint(c), false, false); return __uint_as_float(r[0]) + __uint_as_float(r[1]); };
    auto sw16 = [](float a, float c) { const auto r = __builtin_amdgcn_permlane16_swap(__float_as_uint(a), __float_as_uint(c), false, false); return __uint_as_float(r[0]) + __uint_as_float(r[1]); };
    const float e0 = sw16(sw32(s[0], s[2]), sw32(zz[0], zz[2])), e1 = sw16(sw32(own(s[1]), own(s[3])), sw32(zz[1], zz[3]));
    const unsigned kvb = min(pk2(e0, e1), SENT - 1u);
    asm volatile("s_mov_b64 exec, %3\n\tbuffer_store_dword %0, %1, %2, 0 offen sc1\n\ts_mov_b64 exec, -1" :: "v"(kvb), "v"(od), "s"(rs), "s"(mkp) : "memory");
    PF_DONE(pfd3);
    }
  } else {
    const char* hq = smem + X_XW + cw * 2048 + l * 16;
    fr[0] = *(const bf16x8*)hq; fr[1] = *(const bf16x8*)(hq + 1024);
  }
  bf16x8 pf[4];
  {
    f32x2 ep[8], en[8];
    phi_fn<true>(wp, fr[0], fr[1], ep, en, [](auto) {});
#pragma unroll
    for (int s = 0; s < 2; ++s) {
      pf[s] = __builtin_bit_cast(bf16x8, u32x4{pk2(ep[4 * s][0], ep[4 * s][1]), pk2(ep[4 * s + 1][0], ep[4 * s + 1][1]), pk2(ep[4 * s + 2][0], ep[4 * s + 2][1]), pk2(ep[4 * s + 3][0], ep[4 * s + 3][1])});
      pf[2 + s] = __builtin_bit_cast(bf16x8, u32x4{pk2(en[4 * s][0], en[4 * s][1]), pk2(en[4 * s + 1][0], en[4 * s + 1][1]), pk2(en[4 * s + 2][0], en[4 * s + 2][1]), pk2(en[4 * s + 3][0], en[4 * s + 3][1])});
    }
    asm volatile("" : "+v"(pf[0]), "+v"(pf[1]), "+v"(pf[2]), "+v"(pf[3]));
  }
  SB0();
  if (b == 0 && tid == 64) ST64(*(const u32x2*)(smem + X_AM + 56), WS_EPOCH);
  u32x4 gq, gq2; f32x2 df[8][4]; unsigned ayr;
#define GQLD(tail) asm volatile("buffer_load_dwordx4 %0, %1, %2, 0 offen sc1" tail : "=v"(gq) : "v"(ogq), "s"(rs) : "memory")
  if (w >= 4) {
    {
      WAIT_V(3);
      SB0();
      char* pa_ = smem + cw * 10 * 1024 + l * 16; char* pb_ = smem + (104 + cw * 6) * 1024 + l * 16;
#pragma unroll
      for (int i = 0; i < 10; ++i) *(u32x4*)(pa_ + i * 1024) = wr_[i];
#pragma unroll
      for (int i = 0; i < 6; ++i) *(u32x4*)(pb_ + i * 1024) = wr_[10 + i];
      SB0();
    }
    asm volatile("" : "+v"(dv[0]), "+v"(dv[1]), "+v"(dv[2]), "+v"(dv[3]), "+v"(dv[4]), "+v"(dv[5]), "+v"(dv[6]), "+v"(dv[7]) :: "memory");
    SB0();
#pragma unroll
    for (int i = 0; i < 8; ++i)
#pragma unroll
      for (int e = 0; e < 4; ++e) { df[i][e] = f32x2{bflo(dv[i][e]), bfhi(dv[i][e])}; asm volatile("" : "+v"(df[i][e])); }
    SB0();
    later();
    ayr = lbase + X_YR + (cw * 128 + (l & 15) * 8 + (l >> 4)) * 4;
    asm volatile("" : "+v"(ayr));
    if (has_gu) GQLD("");
    SB0();
  }
  auto downp = [&]() __attribute__((always_inline)) {
            auto red4 = [](float s0, float s1, float s2, float s3) {
              const auto r01 = __builtin_amdgcn_permlane16_swap(__float_as_uint(s0), __float_as_uint(s1), false, false);
              const auto r23 = __builtin_amdgcn_permlane16_swap(__float_as_uint(s2), __float_as_uint(s3), false, false);
              const float t01 = __uint_as_float(r01[0]) + __uint_as_float(r01[1]), t23 = __uint_as_float(r23[0]) + __uint_as_float(r23[1]);
              const auto r = __builtin_amdgcn_permlane32_swap(__float_as_uint(t01), __float_as_uint(t23), false, false);
              return __uint_as_float(r[0]) + __uint_as_float(r[1]);
            };
            const float gg = rbf(red4(__uint_as_float(gq[0]), __uint_as_float(gq[1]), __uint_as_float(gq[2]), __uint_as_float(gq[3])));
            const float uu = __builtin_bit_cast(float, __builtin_amdgcn_update_dpp(0, __builtin_bit_cast(int, gg), 0x128, 0xf, 0xf, true));
            const float hv0 = rbf(rbf(silu_r(gg)) * uu);
            f32x2 ya[4] = {{0.f, 0.f}, {0.f, 0.f}, {0.f, 0.f}, {0.f, 0.f}};
            static_for<8>([&](auto ic) {
              constexpr int i = decltype(ic)::value;
              const float hv = __builtin_bit_cast(float, __builtin_amdgcn_update_dpp(0, __builtin_bit_cast(int, hv0), 0x150 + i, 0xf, 0xf, true));
              const f32x2 h2 = {hv, hv};
#pragma unroll
              for (int e = 0; e < 4; ++e) ya[e] = __builtin_elementwise_fma(h2, df[i][e], ya[e]);
            });
            const float y0 = red4(ya[0][0], own(ya[0][1]), ya[1][0], own(ya[1][1])), y1 = red4(ya[2][0], own(ya[2][1]), ya[3][0], own(ya[3][1]));
            asm volatile("ds_write2_b32 %0, %1, %2 offset1:4" :: "v"(ayr), "v"(y0), "v"(y1) : "memory");
  };
  const unsigned lxv = lbase + (w < 6 ? 131072 + w * 2048 : X_YS + 64 + (w - 6) * 2048);
  {
    u32x4 kf[4], zf[4];
    if (w < 4) {
      u32x4 a, c, z; u64 m0 = ~0ull, m1 = ~0ull, m2 = ~0ull;
      unsigned sp = 0;
      do {
        asm volatile("s_mov_b64 exec, %8\n\ts_cbranch_execz 1f\n\tbuffer_load_dwordx4 %0, %3, %5, 0 offen sc1\n1:\n\ts_mov_b64 exec, %9\n\ts_cbranch_execz 2f\n\tbuffer_load_dwordx4 %1, %3, %5, 0 offen offset:1280 sc1\n2:\n\t"
                     "s_mov_b64 exec, %10\n\ts_cbranch_execz 3f\n\tbuffer_load_dwordx4 %2, %4, %5, 0 offen sc1\n3:\n\ts_mov_b64 exec, -1\n\ts_waitcnt vmcnt(0)\n\t"
                     "ds_write_b128 %6, %0\n\tds_write_b128 %6, %1 offset:1024\n\tds_write_b128 %7, %2"
                     : "+v"(a), "+v"(c), "+v"(z) : "v"(o2), "v"(oz2), "s"(rs), "v"(wa_), "v"(wz_), "s"(m0), "s"(m1), "s"(m2) : "memory");
        m0 = __ballot(max(max(max(a[0], a[1]), a[2]), a[3]) == SENT); m1 = __ballot(max(max(max(c[0], c[1]), c[2]), c[3]) == SENT); m2 = __ballot(max(max(max(z[0], z[1]), z[2]), z[3]) == SENT);
      } while ((m0 | m1 | m2) != 0 && ++sp < SPIN_CAP);
    }
    RAW_BARRIER();
    DS_B128(kf[0], kba, 0); DS_B128(zf[0], kbz, 0); DS_B128(kf[1], kba, 1024); DS_B128(zf[1], kbz, 16);
    DS_B128(kf[2], kba, 2048); DS_B128(zf[2], kbz, 32); DS_B128(kf[3], kba, 3072); DS_B128(zf[3], kbz, 48);
    if (w >= 4 && has_gu) asm volatile("buffer_load_dwordx4 %0, %1, %2, 0 offen sc1" : "=v"(gq2) : "v"(ogq), "s"(rs) : "memory");
    gl_m0<0>(oxg, x, lxv); gl_m0<1024>(oxg, x + 8 * DM, lxv);
    __builtin_amdgcn_s_setprio(0);
    f32x16 num = zero;
    float d0 = 0.f, d1 = 0.f;
    static_for<4>([&](auto cc) {
      constexpr int c = decltype(cc)::value;
      asm volatile("s_waitcnt lgkmcnt(%2)" : "+v"(kf[c]), "+v"(zf[c]) : "n"(6 - 2 * c));
      num = MFMA(__builtin_bit_cast(bf16x8, kf[c]), pf[c], num);
      const u32x4 pc = __builtin_bit_cast(u32x4, pf[c]);
      d0 = dot2(zf[c][0], pc[0], d0); d1 = dot2(zf[c][1], pc[1], d1); d0 = dot2(zf[c][2], pc[2], d0); d1 = dot2(zf[c][3], pc[3], d1);
      SB0();
    });
    float den = x32_add(d0 + d1);
    den = rbf(rbf(den) + 1e-6f);
    const float rinv = rcp_nr(den);
    const f32x2 rr = {rinv, rinv};
#pragma unroll
    for (int q = 0; q < 2; ++q) {
      u32x4 u;
#pragma unroll
      for (int e = 0; e < 4; ++e) {
        const f32x2 r = f32x2{num[8 * q + 2 * e], num[8 * q + 2 * e + 1]} * rr;
        u[e] = pk2(r[0], r[1]);
      }
      ST128(u, odo + q * 1024);
      SB0();
    }
  }
  const int p0 = 8 * w + 40;
  const u16* gw2 = wo + (size_t)((p0 ^ (flip * 64)) * 8) * DM + nb * 64; unsigned lw2 = lbase + p0 * 1024;
  asm volatile("" : "+s"(gw2), "+s"(lw2));
  bool glate = false;
  if (w >= 4 && has_gu) {
    asm volatile("s_waitcnt vmcnt(5)" : "+v"(gq) :: "memory");
    gq ^= t8;
    glate = !__all((or4(gq) & 0xFFu) == 0u);
    if (glate) {
      asm volatile("s_waitcnt vmcnt(4)\n\tv_xor_b32 %0, %8, %4\n\tv_xor_b32 %1, %8, %5\n\tv_xor_b32 %2, %8, %6\n\tv_xor_b32 %3, %8, %7"
                   : "=&v"(gq[0]), "=&v"(gq[1]), "=&v"(gq[2]), "=&v"(gq[3]) : "v"(gq2[0]), "v"(gq2[1]), "v"(gq2[2]), "v"(gq2[3]), "s"(t8) : "memory");
      glate = !__all((or4(gq) & 0xFFu) == 0u);
    }
    if (!glate) downp();
    asm volatile("s_waitcnt vmcnt(4)" :: "v"(gq2) : "memory");
  }
  asm volatile("s_waitcnt vmcnt(0)\n\ts_mov_b64 exec, 1\n\tbuffer_store_dwordx2 %0, %1, %2, 0 offen sc1\n\ts_mov_b64 exec, -1" :: "v"(tg), "v"(of4), "s"(rs) : "memory");
  if (__builtin_expect(glate, 0)) {
    for (unsigned sp = 0;;) {
      GQLD("\n\ts_waitcnt vmcnt(0)");
      gq ^= t8;
      if (__all((or4(gq) & 0xFFu) == 0u)) break;
      SPIN_NEXT(sp);
    }
    downp();
  }
  RAW_BARRIER();
  float rxn = 0.f, rsg = 0.f;
  u32x4 yq[7];
  const unsigned oy = WS_YP + (unsigned)(l >> 4) * 4096 + nb * 256 + (l & 15) * 16;
#define YLD(tail) asm volatile("buffer_load_dwordx4 %0, %7, %8, 0 offen sc1\n\tbuffer_load_dwordx4 %1, %7, %8, %9 offen sc1\n\tbuffer_load_dwordx4 %2, %7, %8, %10 offen sc1\n\t" \
    "buffer_load_dwordx4 %3, %7, %8, %11 offen sc1\n\tbuffer_load_dwordx4 %4, %7, %8, %12 offen sc1\n\tbuffer_load_dwordx4 %5, %7, %8, %13 offen sc1\n\t" \
    "buffer_load_dwordx4 %6, %7, %8, %14 offen sc1" tail \
    : "=&v"(yq[0]), "=&v"(yq[1]), "=&v"(yq[2]), "=&v"(yq[3]), "=&v"(yq[4]), "=&v"(yq[5]), "=&v"(yq[6]) \
    : "v"(oy), "s"(rs), "s"(16384), "s"(2 * 16384), "s"(3 * 16384), "s"(4 * 16384), "s"(5 * 16384), "s"(6 * 16384) : "memory")
  f32x2 xf[2][4], gf[2][4];
  __attribute__((address_space(3))) f32x4* pw = (__attribute__((address_space(3))) f32x4*)(smem + 131072 + w * 4096) + l;
  unsigned prd = lbase + 131072 + (w ^ 4) * 4096 + l * 16;
  u16* po = out + (size_t)(p3 * 128 + cw * 32 + fb * 16 + (l >> 3)) * DM + nb * 64 + (l & 7) * 8; u16* po2 = po + 8 * DM;
  unsigned tw0 = lbase + 131072 + (w ^ 4) * 4096 + (l & 15) * 128 + ((ech ^ (l & 7)) * 16), tw1 = tw0 ^ 64, trd = lbase + 131072 + (w ^ 4) * 4096 + (l >> 3) * 128 + (((l & 7) ^ (l >> 3)) * 16);
    unsigned bw[4];
#pragma unroll
    for (int n = 0; n < 4; ++n) bw[n] = lbase + kh * 65536 + (16 * h + 4 * blk + tq) * 128 + (((4 * (n >> 1) + tp) ^ ((tq >> 1) | (blk << 2))) * 16) + 8 * ((n ^ tp) & 1);
  {
  if (w == 1 && has_gu && l < 32) {
    const f32x4* y = (const f32x4*)(smem + X_YR) + l;
    ST128(f2u_tag((y[0] + y[32]) + (y[64] + y[96]), t8), WS_YP + (unsigned)(gr >> 3) * 4096 + ((gr + 1) & 7) * 512 + l * 16);
  }
    SB0();
    gl_run<8>(voO, gw2, lw2);
    u32x2 f0;
    asm volatile("buffer_load_dwordx2 %0, %1, %2, 0 offen sc1" : "=v"(f0) : "v"(op4), "s"(rs) : "memory");
    SB0();
    {
      const f32x2 md = *(const f32x2*)(smem + X_MD + erow * 8);
      { const unsigned ax = lxv + (l & 15) * 128 + ((ech ^ (l & 7)) * 16), a0 = ax + (sw ? 8 : 0), a1 = ax + (sw ? 0 : 8);
        u32x2 q0, q1, q2, q3;
        asm volatile("ds_read_b64 %0, %4\n\tds_read_b64 %1, %5\n\tds_read_b64 %2, %6\n\tds_read_b64 %3, %7\n\ts_waitcnt lgkmcnt(0)" : "=&v"(q0), "=&v"(q1), "=&v"(q2), "=&v"(q3) : "v"(a0), "v"(a1), "v"(a0 ^ 64), "v"(a1 ^ 64) : "memory");
        xv[0] = u32x4{q0[0], q0[1], q1[0], q1[1]}; xv[1] = u32x4{q2[0], q2[1], q3[0], q3[1]}; }
      const float rinv = rcp_nr(own(md[1])), mr = -md[0] * rinv, L2E = 1.44269504088896340736f, rl = -L2E * rinv, ml = -L2E * mr;
#pragma unroll
      for (int i = 0; i < 2; ++i)
#pragma unroll
        for (int e = 0; e < 4; ++e) {
          xf[i][e] = f32x2{bflo(xv[i][e]), bfhi(xv[i][e])};
          const f32x2 xn = __builtin_elementwise_fma(xf[i][e], f32x2{rinv, rinv}, f32x2{mr, mr}), tn = __builtin_elementwise_fma(xf[i][e], f32x2{rl, rl}, f32x2{ml, ml});
          const f32x2 dd = f32x2{__builtin_amdgcn_exp2f(tn[0]), __builtin_amdgcn_exp2f(tn[1])} + f32x2{1.0f, 1.0f};
          gf[i][e] = xn * f32x2{__builtin_amdgcn_rcpf(dd[0]), __builtin_amdgcn_rcpf(dd[1])};
        }
#pragma unroll
      for (int i = 0; i < 2; ++i) asm volatile("" : "+v"(xf[i][0]), "+v"(xf[i][1]), "+v"(xf[i][2]), "+v"(xf[i][3]), "+v"(gf[i][0]), "+v"(gf[i][1]), "+v"(gf[i][2]), "+v"(gf[i][3]));
      asm volatile("" : "+v"(pw), "+v"(prd), "+v"(po2), "+v"(tw0), "+v"(tw1), "+v"(trd));
      asm volatile("" : "+v"(po), "+v"(bw[0]), "+v"(bw[1]), "+v"(bw[2]), "+v"(bw[3]));
    }
    if (has_idx) {
      const unsigned ord = (rkey >> 11) & 0xFFFFu;
      rsg = rbf(1.0f / (1.0f + __expf(-__uint_as_float((ord ^ ((ord & 0x8000u) ? 0x8000u : 0xFFFFu)) << 16))));
      const f32x2 md = *(const f32x2*)(smem + X_MD + (idx & 127) * 8);
      const float dn = own(md[1]), rinv = rcp_nr(dn);
      rxn = bflo(ln_pair(xrt, md[0], dn, rinv));
      asm volatile("" : "+v"(rxn), "+v"(rsg));
      { u64 pc_; unsigned d_, o_;
        asm volatile("s_getpc_b64 %0\n.Lkf_own1:\n\ts_mov_b32 %1, .Lkf_own0-.Lkf_own1" : "=s"(pc_), "=s"(o_));
        pc_ += o_;
        asm volatile("s_load_dword %0, %1, 0x0\n\ts_load_dword %0, %1, 0x80\n\ts_load_dword %0, %1, 0x100\n\ts_load_dword %0, %1, 0x180\n\ts_load_dword %0, %1, 0x200\n\ts_load_dword %0, %1, 0x280\n\ts_waitcnt lgkmcnt(0)" : "=&s"(d_) : "s"(pc_) : "memory"); }
    }
    u64 ne;
    SB0();
    asm volatile("s_waitcnt vmcnt(0)\n\tv_cmp_ne_u64 %0, %1, %2" : "=s"(ne) : "v"(f0), "v"(tg) : "memory");
    for (unsigned sp = 0; ne != 0 && sp < SPIN_CAP; ++sp) {
      u32x2 f;
      asm volatile("buffer_load_dwordx2 %1, %2, %3, 0 offen sc1\n\ts_waitcnt vmcnt(0)\n\tv_cmp_ne_u64 %0, %1, %4" : "=s"(ne), "=&v"(f) : "v"(op4), "s"(rs), "v"(tg) : "memory");
    }
  }
  constexpr int OWIN = 4;
  u32x4 of[16][2];
  {
    SB0();
#pragma unroll
    for (int i = 0; i < OWIN; ++i) { of[i][0] = LD128(oo0 + i * 2048); of[i][1] = LD128(oo1 + i * 2048); }
    SB0();
    if (has_idx) YLD("");
    SB0();
  }
    auto owner = [&](auto nc) __attribute__((always_inline)) {
      asm volatile(".Lkf_own0:\n\ts_waitcnt vmcnt(%7)" : "+v"(yq[0]), "+v"(yq[1]), "+v"(yq[2]), "+v"(yq[3]), "+v"(yq[4]), "+v"(yq[5]), "+v"(yq[6]) : "n"(decltype(nc)::value) : "memory");
      for (unsigned sp = 0;; ++sp) {
        unsigned bad = 0;
#pragma unroll
        for (int i = 0; i < 7; ++i) { yq[i] ^= t8; bad |= or4(yq[i]); }
        if (__builtin_expect(__all((bad & 0xFFu) == 0u) || sp > SPIN_CAP, 1)) break;
        YLD("\n\ts_waitcnt vmcnt(0)");
      }
#define YF(i) __builtin_bit_cast(f32x4, yq[i])
      const f32x4 y4 = ((YF(0) + YF(1)) + (YF(2) + YF(3))) + ((YF(4) + YF(5)) + YF(6));
      const auto r01 = __builtin_amdgcn_permlane16_swap(__float_as_uint(y4[0]), __float_as_uint(own(y4[1])), false, false);
      const auto r23 = __builtin_amdgcn_permlane16_swap(__float_as_uint(y4[2]), __float_as_uint(own(y4[3])), false, false);
      const float t01 = __uint_as_float(r01[0]) + __uint_as_float(r01[1]), t23 = __uint_as_float(r23[0]) + __uint_as_float(r23[1]);
      const auto r = __builtin_amdgcn_permlane32_swap(__float_as_uint(t01), __float_as_uint(t23), false, false);
      const float y = __uint_as_float(r[0]) + __uint_as_float(r[1]);
      const float g1 = rbf(silu_r(rbf(rxn + rbf(rbf(y) * rsg))));
      (void)&gf; (void)&oy; (void)&rs; const u64 mk_ = 0x0001000100010001ull << (idx & 15);
      const unsigned gb = lbase + (131072 + w * 4096), aw_ = gb + (l & 15) * 16 + ech * 4, a0 = gb + ech * 32 + (ech & 1) * 16, a1 = a0 ^ 16;
      asm volatile("ds_write_b32 %8, %9\n\ts_mov_b64 exec, %12\n\ts_waitcnt lgkmcnt(0)\n\t"
                   "ds_read_b64 %0, %10\n\tds_read_b64 %1, %10 offset:8\n\tds_read_b64 %2, %11\n\tds_read_b64 %3, %11 offset:8\n\t"
                   "ds_read_b64 %4, %10 offset:128\n\tds_read_b64 %5, %10 offset:136\n\tds_read_b64 %6, %11 offset:128\n\tds_read_b64 %7, %11 offset:136\n\t"
                   "s_mov_b64 exec, -1\n\ts_waitcnt lgkmcnt(0)"
                   : "+v"(gf[0][0]), "+v"(gf[0][1]), "+v"(gf[0][2]), "+v"(gf[0][3]), "+v"(gf[1][0]), "+v"(gf[1][1]), "+v"(gf[1][2]), "+v"(gf[1][3])
                   : "v"(aw_), "v"(g1), "v"(a0), "v"(a1), "s"(mk_) : "memory");
    };
  f32x4 acc3[2][4];
#pragma unroll
  for (int t = 0; t < 2; ++t)
#pragma unroll
    for (int n = 0; n < 4; ++n) acc3[t][n] = f32x4{0.f, 0.f, 0.f, 0.f};
  {
    s16x4 wa[2][8];
    auto rd3 = [&](auto kc, auto nc) {
      constexpr int k = decltype(kc)::value, n = decltype(nc)::value;
      (void)&wa; (void)&bw;
      DS_TR(wa[k & 1][2 * n], bw[n], k * 4096); DS_TR(wa[k & 1][2 * n + 1], bw[n], k * 4096 + 1024);
    };
    RAW_BARRIER();
    static_for<4>([&](auto nc) { rd3(IC(0), nc); });
    static_for<16>([&](auto kc) {
      constexpr int k = decltype(kc)::value, sx = k & 1;
      static_for<4>([&](auto nc) {
        constexpr int n = decltype(nc)::value;
        asm volatile("s_waitcnt lgkmcnt(%2)" : "+v"(wa[sx][2 * n]), "+v"(wa[sx][2 * n + 1]) : "n"(k < 15 ? 6 : n < 2 ? 6 - 2 * n : 5 - n));
        SB0();
        const bf16x8 wf = cat8(wa[sx][2 * n], wa[sx][2 * n + 1]);
        acc3[0][n] = MFMA16(wf, __builtin_bit_cast(bf16x8, of[k][0]), acc3[0][n]);
        SB0();
        if constexpr (n == 0 && (k & 3) == 0) __builtin_amdgcn_s_setprio(3 - (k >> 2));
        if constexpr (k + 1 < 16) rd3(IC(k + 1), nc);
        if constexpr (k + OWIN < 16 && n == 3) { of[k + OWIN][0] = LD128(oo0 + (k + OWIN) * 2048); }
        SB0();
        acc3[1][n] = MFMA16(wf, __builtin_bit_cast(bf16x8, of[k][1]), acc3[1][n]);
        SB0();
        if constexpr (k + OWIN < 16 && n == 3) { of[k + OWIN][1] = LD128(oo1 + (k + OWIN) * 2048); SB0(); }
        if constexpr (k == YK && n == 3) { (void)&yq; (void)&oy; (void)&rs; if (has_idx) YLD(""); SB0(); }
        if constexpr (k == YP_ && n == 3) { constexpr int e_ = YP_ + OWIN < 16 ? YP_ : 15 - OWIN; if (has_idx) owner(IC(2 * (e_ - YK))); SB0(); }
        if constexpr (k == 15 && n > 0) { pw[(n - 1) * 64] = acc3[1][n - 1]; SB0(); }
      });
    });
  }
  {
    pw[192] = acc3[1][3];
    SB0();
    SB0();
#pragma unroll
    for (int i = 0; i < 2; ++i)
#pragma unroll
      for (int e = 0; e < 4; ++e) {
        const f32x4 a = acc3[0][2 * i + (e >> 1)];
        xf[i][e] = __builtin_elementwise_fma(f32x2{a[2 * (e & 1)], a[2 * (e & 1) + 1]}, gf[i][e], xf[i][e]);
        asm volatile("" : "+v"(xf[i][e]));
      }
    RAW_BARRIER();
    f32x4 pr[4];
    DS_B128(pr[0], prd, 0); DS_B128(pr[1], prd, 1024); DS_B128(pr[2], prd, 2048); DS_B128(pr[3], prd, 3072);
    SB0();
    u32x4 res[2];
#pragma unroll
    for (int i = 0; i < 2; ++i) {
      if (i == 0) asm volatile("s_waitcnt lgkmcnt(2)" : "+v"(pr[0]), "+v"(pr[1])); else asm volatile("s_waitcnt lgkmcnt(1)" : "+v"(pr[2]), "+v"(pr[3]));
      const f32x4 s0 = pr[2 * i], s1 = pr[2 * i + 1];
      const f32x2 o0 = __builtin_elementwise_fma(f32x2{s0[0], s0[1]}, gf[i][0], xf[i][0]), o1 = __builtin_elementwise_fma(f32x2{s0[2], s0[3]}, gf[i][1], xf[i][1]);
      const f32x2 o2 = __builtin_elementwise_fma(f32x2{s1[0], s1[1]}, gf[i][2], xf[i][2]), o3 = __builtin_elementwise_fma(f32x2{s1[2], s1[3]}, gf[i][3], xf[i][3]);
      res[i] = u32x4{pk2(o0[0], o0[1]), pk2(o1[0], o1[1]), pk2(o2[0], o2[1]), pk2(o3[0], o3[1])};
      FSWAP(res[i]);
      asm volatile("ds_write_b128 %0, %1" :: "v"(i ? tw1 : tw0), "v"(res[i]) : "memory");
    }
    asm volatile("s_waitcnt lgkmcnt(0)\n\tds_read_b128 %0, %2\n\tds_read_b128 %1, %2 offset:1024\n\ts_waitcnt lgkmcnt(1)\n\t"
                 "global_store_dwordx4 %3, %0, off sc1\n\ts_waitcnt lgkmcnt(0)\n\tglobal_store_dwordx4 %4, %1, off sc1\n\ts_nop 1"
                 : "=&v"(res[0]), "=&v"(res[1]) : "v"(trd), "v"(po), "v"(po2) : "memory");
  }
  asm volatile(".Lkf_pf_end:" ::: "memory");
}

extern "C" void kernel_launch(void* const* d_in, const int* in_sizes, int n_in,
                              void* d_out, int out_size, void* d_ws, size_t ws_size,
                              hipStream_t stream) {
  (void)hipFuncSetAttribute((const void*)kf_all, hipFuncAttributeMaxDynamicSharedMemorySize, LDS_TOTAL);
  kf_all<<<256, 512, LDS_TOTAL, stream>>>((const u16*)d_in[0], (const u16*)d_in[1], (const u16*)d_in[2], (const u16*)d_in[3],
                                          (const u16*)d_in[5], (const u16*)d_in[6], (const u16*)d_in[7], (const u16*)d_in[8],
                                          (char*)d_ws, (u16*)d_out);
}
```

```cpp
#define I1 10
#define I2 10
#define I3 10
#define I4 10
#define I5 10
#define I6 10
#define I7 10
#define WOK 2
#define S2A 10
#define KP 9
#define KC 10
#define F1_ST 26
#define PF2_KT 8
#define H1_DELAY 8
#define PACE 1
constexpr int PACE_T[16] = {0, 108, 72, 60, 0, 0, 0, 0, 0, 0, 0, 0, 0, 0, 0, 0};
constexpr int PACE_3[4] = {0, 60,50,50};
constexpr int DVN[20] = {0,2,2,2,0,0,1,0,1};
constexpr int dv_first(int k) { int n = 0; for (int i = -2; i < k; ++i) n += DVN[i + 2]; return n; }
constexpr bool CSS(int S) { return S < 2 || S == 6 || S == 7; }
#define NREG 12
#define WH0 10
#define WPOST 4
#define YK -1
#define YP_ 11
#define PRIO_T 2
#define PRIO_H 1
#include <hip/hip_runtime.h>
#include <hip/hip_bf16.h>
#include <stdint.h>
#include <utility>
#include <type_traits>

typedef __attribute__((ext_vector_type(8))) short bf16x8;
typedef __attribute__((ext_vector_type(4))) short s16x4;
typedef __attribute__((ext_vector_type(16))) float f32x16;
typedef __attribute__((ext_vector_type(4))) float f32x4;
typedef __attribute__((ext_vector_type(2))) float f32x2;
typedef __attribute__((ext_vector_type(4))) unsigned u32x4;
typedef __attribute__((ext_vector_type(2))) unsigned u32x2;
typedef __attribute__((ext_vector_type(2))) __bf16 bfx2;
typedef __attribute__((ext_vector_type(4))) short v4i16_t;
typedef unsigned long long u64;
typedef unsigned short u16;

#define DI __device__ __forceinline__
#define MFMA(a, b, c) __builtin_amdgcn_mfma_f32_32x32x16_bf16((a), (b), (c), 0, 0, 0)
#define MFMA16H(a, b, c) __builtin_amdgcn_mfma_f32_16x16x16bf16_1k((a), (b), (c), 0, 0, 0)
#define MFMA16(a, b, c) __builtin_amdgcn_mfma_f32_16x16x32_bf16((a), (b), (c), 0, 0, 0)

DI unsigned pk2(float a, float b) { f32x2 v = {a, b}; return __builtin_bit_cast(unsigned, __builtin_convertvector(v, bfx2)); }
DI float bflo(unsigned u) { return __uint_as_float(u << 16); }
DI float bfhi(unsigned u) { return __uint_as_float(u & 0xffff0000u); }
DI float rbf(float x) { return (float)(__bf16)x; }
DI s16x4 trread(const char* p) {
  return __builtin_bit_cast(s16x4, __builtin_amdgcn_ds_read_tr16_b64_v4i16((__attribute__((address_space(3))) v4i16_t*)p));
}
DI bf16x8 cat8(s16x4 lo, s16x4 hi) { return __builtin_shufflevector(lo, hi, 0, 1, 2, 3, 4, 5, 6, 7); }
#define WAIT_V(n) asm volatile("s_waitcnt vmcnt(%0)" ::"n"(n) : "memory")
#define RAW_BARRIER() do { asm volatile("s_waitcnt lgkmcnt(0)" ::: "memory"); __builtin_amdgcn_s_barrier(); asm volatile("" ::: "memory"); } while (0)
#define DS_B128(dst, addr, off) asm volatile("ds_read_b128 %0, %1 offset:%2" : "=v"(dst) : "v"(addr), "n"(off))
#define DS_TR(dst, addr, off) asm volatile("ds_read_b64_tr_b16 %0, %1 offset:%2" : "=v"(dst) : "v"(addr), "n"(off))
#define LDS_OFF(p) ((unsigned)(uintptr_t)(__attribute__((address_space(3))) const char*)(p))
template <class F, int... I> DI void static_for_impl(F&& f, std::integer_sequence<int, I...>) { (f(std::integral_constant<int, I>{}), ...); }
template <int N, class F> DI void static_for(F&& f) { static_for_impl(f, std::make_integer_sequence<int, N>{}); }
DI float divc(float a, float b, float rinv) { const float q = a * rinv; return __builtin_fmaf(__builtin_fmaf(-q, b, a), rinv, q); }
DI float rcp_nr(float d) { const float r = __builtin_amdgcn_rcpf(d); return __builtin_fmaf(__builtin_fmaf(-d, r, 1.0f), r, r); }
DI float own(float v) { asm volatile("" : "+v"(v)); return v; }
DI float silu_r(float x) { return x * __builtin_amdgcn_rcpf(1.0f + __expf(-x)); }
#define SB0() __builtin_amdgcn_sched_barrier(0)
#define GLDS_S(vo, sp, ld) asm volatile("s_mov_b32 m0, %2\n\ts_nop 0\n\tglobal_load_lds_dwordx4 %0, %1" :: "v"(vo), "s"(sp), "s"(ld) : "m0", "memory")
template <int IMM> DI void gl_m0(unsigned vo, const void* sp, unsigned ls) {
  asm volatile("s_add_u32 m0, %2, %3\n\ts_nop 0\n\tglobal_load_lds_dwordx4 %0, %1" :: "v"(vo), "s"(sp), "s"(ls), "n"(IMM) : "m0", "memory", "scc");
}
template <int IMM> DI void gl_nt(unsigned vo, const void* sp, unsigned ls) {
  asm volatile("s_add_u32 m0, %2, %3\n\ts_nop 0\n\tglobal_load_lds_dwordx4 %0, %1 nt" :: "v"(vo), "s"(sp), "s"(ls), "n"(IMM) : "m0", "memory", "scc");
}
template <int N> DI void gl_run(unsigned vo, const void* sp, unsigned ls) {
  unsigned lo = (unsigned)(uintptr_t)sp, hi = (unsigned)((uintptr_t)sp >> 32);
  static_for<N>([&](auto ic) {
    constexpr int i = decltype(ic)::value;
    gl_m0<i * 1024>(vo, (const void*)(((uintptr_t)hi << 32) | lo), ls);
    if constexpr (i + 1 < N) asm volatile("s_add_u32 %0, %0, 0x4000\n\ts_addc_u32 %1, %1, 0" : "+s"(lo), "+s"(hi) :: "scc");
  });
}
#define GLDS_SC1(vo, sp, ld) asm volatile("s_mov_b32 m0, %2\n\ts_nop 0\n\tglobal_load_lds_dwordx4 %0, %1 sc1" :: "v"(vo), "s"(sp), "s"(ld) : "m0", "memory")
DI float x32_add(float v) { auto r = __builtin_amdgcn_permlane32_swap(__float_as_uint(v), __float_as_uint(v), false, false); return __uint_as_float(r[0]) + __uint_as_float(r[1]); }
DI float x32_max(float v) { auto r = __builtin_amdgcn_permlane32_swap(__float_as_uint(v), __float_as_uint(v), false, false); float o; asm("v_max_f32 %0, %1, %2" : "=v"(o) : "v"(r[0]), "v"(r[1])); return o; }
DI float x32_min(float v) { auto r = __builtin_amdgcn_permlane32_swap(__float_as_uint(v), __float_as_uint(v), false, false); float o; asm("v_min_f32 %0, %1, %2" : "=v"(o) : "v"(r[0]), "v"(r[1])); return o; }
DI float x16_add(float v) { auto r = __builtin_amdgcn_permlane16_swap(__float_as_uint(v), __float_as_uint(v), false, false); return __uint_as_float(r[0]) + __uint_as_float(r[1]); }
template <int CTRL, int RMASK> DI float dpp_add(float v) {
  return v + __builtin_bit_cast(float, __builtin_amdgcn_update_dpp(0, __builtin_bit_cast(int, v), CTRL, RMASK, 0xf, true));
}

DI unsigned ln_pair(unsigned u, float m, float den, float rinv) {
  const f32x2 mm = {m, m}, dd = {den, den}, rr = {rinv, rinv};
  const f32x2 d0 = f32x2{bflo(u), bfhi(u)} - mm;
  const unsigned du = pk2(d0[0], d0[1]);
  const f32x2 d = {bflo(du), bfhi(du)};
  const f32x2 q = d * rr;
  const f32x2 r = __builtin_elementwise_fma(__builtin_elementwise_fma(-q, dd, d), rr, q);
  return pk2(r[0], r[1]);
}
DI float dot2(unsigned a, unsigned b, float c) { return __builtin_amdgcn_fdot2_f32_bf16(__builtin_bit_cast(bfx2, a), __builtin_bit_cast(bfx2, b), c, false); }
DI float half_total(float v) {
  v = dpp_add<0x121, 0xf>(v); v = dpp_add<0x122, 0xf>(v); v = dpp_add<0x124, 0xf>(v); v = dpp_add<0x128, 0xf>(v);
  return x16_add(v);
}
DI void ln_rows(const u32x4 (&xr)[4], const u32x4 (&wr)[4], int h, float& m, float& den, float& rinv, float& r) {
  const unsigned c0 = __builtin_amdgcn_readlane(xr[0][0], 0) << 16, c1 = __builtin_amdgcn_readlane(xr[0][0], 32) << 16;
  const float c = __uint_as_float(h ? c1 : c0);
  const f32x2 cc = {c, c};
  float s1[2] = {0.f, 0.f}, sx[2] = {0.f, 0.f}, sw[2] = {0.f, 0.f};
  f32x2 q[2] = {{0.f, 0.f}, {0.f, 0.f}};
#pragma unroll
  for (int i = 0; i < 4; ++i)
#pragma unroll
    for (int e = 0; e < 4; ++e) {
      const unsigned u = xr[i][e];
      s1[e & 1] = dot2(u, 0x3F803F80u, s1[e & 1]); sx[e & 1] = dot2(u, wr[i][e], sx[e & 1]); sw[e & 1] = dot2(wr[i][e], 0x3F803F80u, sw[e & 1]);
      const f32x2 d = f32x2{bflo(u), bfhi(u)} - cc;
      q[e & 1] = __builtin_elementwise_fma(d, d, q[e & 1]);
    }
  const f32x2 q2 = q[0] + q[1];
  const float mean = half_total(s1[0] + s1[1]) * (1.0f / 1024.0f), m2 = half_total(q2[0] + own(q2[1])) * (1.0f / 1024.0f);
  const float tx = half_total(sx[0] + sx[1]), tw = half_total(sw[0] + sw[1]);
  const float dm = mean - c;
  const float var = fmaxf(__builtin_fmaf(-dm, dm, m2), 0.f);
  m = rbf(mean);
  den = rbf(sqrtf(rbf(rbf(var) + 1e-5f)));
  rinv = rcp_nr(den);
  r = rbf(divc(__builtin_fmaf(-m, tw, tx), den, rinv));
}

template <bool RND, class HK> DI void phi_fn(const bf16x8 (&wp)[2], const bf16x8 f0, const bf16x8 f1, f32x2 (&ep)[8], f32x2 (&en)[8], HK&& hk) {
  f32x16 p;
#pragma unroll
  for (int i = 0; i < 16; ++i) p[i] = 0.f;
  p = MFMA(wp[0], f0, p);
  p = MFMA(wp[1], f1, p);
  hk(std::integral_constant<int, 0>{});
  float mx = -3.0e38f, mn = 3.0e38f;
  f32x2 p2[8];
  static_for<8>([&](auto ic) {
    constexpr int i = decltype(ic)::value;
    if constexpr (RND) { const unsigned u = pk2(p[2 * i], p[2 * i + 1]); p2[i] = f32x2{bflo(u), bfhi(u)}; }
    else p2[i] = f32x2{p[2 * i], p[2 * i + 1]};
    mx = fmaxf(fmaxf(mx, p2[i][0]), p2[i][1]); mn = fminf(fminf(mn, p2[i][0]), p2[i][1]);
    if constexpr (i & 1) hk(std::integral_constant<int, 1 + (i >> 1)>{});
  });
  mx = x32_max(mx); mn = x32_min(mn);
  hk(std::integral_constant<int, 5>{});
  const float L2E = 1.44269504088896340736f, mxl = -mx * L2E, mnl = mn * L2E;
  const f32x2 cp = {L2E, L2E}, cn = {-L2E, -L2E}, bp = {mxl, mxl}, bn = {mnl, mnl};
  f32x2 sp2, sn2;
  static_for<8>([&](auto ic) {
    constexpr int i = decltype(ic)::value;
    const f32x2 tp2 = __builtin_elementwise_fma(p2[i], cp, bp), tn2 = __builtin_elementwise_fma(p2[i], cn, bn);
    ep[i] = f32x2{__builtin_amdgcn_exp2f(tp2[0]), __builtin_amdgcn_exp2f(tp2[1])};
    en[i] = f32x2{__builtin_amdgcn_exp2f(tn2[0]), __builtin_amdgcn_exp2f(tn2[1])};
    if constexpr (i == 0) { sp2 = ep[0]; sn2 = en[0]; } else { sp2 += ep[i]; sn2 += en[i]; }
    hk(std::integral_constant<int, 6 + i>{});
  });
  const float sp = x32_add(sp2[0] + own(sp2[1])), sn = x32_add(sn2[0] + own(sn2[1]));
  hk(std::integral_constant<int, 14>{});
  const float rp = __builtin_amdgcn_rcpf(sp), rn = __builtin_amdgcn_rcpf(sn);
  const f32x2 rp2 = {rp, rp}, rn2 = {rn, rn};
  hk(std::integral_constant<int, 15>{});
#pragma unroll
  for (int i = 0; i < 8; ++i) { ep[i] = ep[i] * rp2; en[i] = en[i] * rn2; }
  hk(std::integral_constant<int, 16>{});
}

constexpr int DM = 1024, FF = 3584;
constexpr unsigned SENT = 0xFFFFFFFFu;
constexpr unsigned SPIN_CAP = 1u << 17;

constexpr unsigned WS_EPOCH = 0;
constexpr unsigned WS_F1 = 4096;
constexpr unsigned WS_F4 = 896u << 10;
constexpr unsigned WS_STAT = 20480;
constexpr unsigned WS_CS = 36864;
constexpr unsigned WS_GUP = WS_CS + 16 * 16 * 768;
constexpr unsigned WS_YP = WS_GUP + 256 * 512;
constexpr unsigned WS_KVF = WS_YP + 32 * 4096;
constexpr unsigned WS_ZP = WS_KVF + 256 * 640;
constexpr unsigned WS_XA = 800u << 10;
constexpr unsigned WS_RI = 840u << 10;
static_assert(WS_ZP + 131072 <= WS_XA && WS_XA + 32768 <= WS_RI, "ws map");
constexpr unsigned WS_KVP = 1u << 20;
constexpr unsigned WS_O = 8u << 20;
constexpr unsigned WS_END = 16u << 20;

constexpr int ABYTES = 128 * 128, WSLAB = 64 * 128, STAGE1 = ABYTES + 3 * WSLAB, NST1 = 3;
constexpr int XB = NST1 * STAGE1;
constexpr int X_WO = XB;
constexpr int X_CSP = XB + 8192;
constexpr int X_P = 131072 + 4096;
constexpr int X_XW = X_P;
constexpr int X_XA = X_XW + 6144;
constexpr int X_ST = X_XA + 2048;
constexpr int X_MD = X_ST + 1024;
constexpr int X_AM = X_MD + 1024;
constexpr int X_XS = X_AM + 64;
constexpr int X_YR = X_XS + 512;
constexpr int X_YS = X_YR + 2048;
constexpr int X_WP = X_YS + 2048;
constexpr int X_KB = X_WP + 4096;
constexpr int LDS_TOTAL = 163840;
static_assert(X_KB + 2 * 4224 <= LDS_TOTAL && 131072 + 6 * 2048 <= X_MD && X_YS + 64 + 4096 <= X_KB && X_YS % 128 == 64, "lds");
static_assert(LDS_TOTAL <= 163840, "lds");
constexpr int gu_at(int ks) { return STAGE1 + ks * 8192; }
constexpr int HANDOFF = 106496;

#define LD128(off) __builtin_amdgcn_raw_buffer_load_b128(rs, (int)(off), 0, 16)
#define ST128(v, off) __builtin_amdgcn_raw_buffer_store_b128((v), rs, (int)(off), 0, 16)
#define ST64(v, off) __builtin_amdgcn_raw_buffer_store_b64((v), rs, (int)(off), 0, 16)
DI u32x4 f2u_tag(f32x4 v, unsigned t8) { u32x4 u = __builtin_bit_cast(u32x4, v); return (u & 0xFFFFFF00u) | t8; }
DI unsigned or4(u32x4 v) { return (v[0] | v[1]) | (v[2] | v[3]); }
#define SPIN_NEXT(sp) { if (++(sp) > SPIN_CAP) break; __builtin_amdgcn_s_sleep(1); }
#define TAG_OF(en) ((en) * 0x9E3779B97F4A7C15ull)

__global__ __launch_bounds__(512, 2) void kf_all(const u16* __restrict__ x, const u16* __restrict__ wqkv, const u16* __restrict__ wphi,
                                              const u16* __restrict__ wo, const u16* __restrict__ w_r, const u16* __restrict__ w_gate,
                                              const u16* __restrict__ w_up, const u16* __restrict__ w_down, char* ws, u16* __restrict__ out) {
  extern __shared__ __attribute__((aligned(16))) char smem[];
  asm volatile(".Lkf_pf_start:");
  const int b = blockIdx.x;
  const int xcd = b & 7, jj = b >> 3;
  const int panel = (xcd >> 2) * 8 + (jj & 7), hp = (xcd & 3) * 4 + (jj >> 3);
  const int id = panel * 16 + hp;
  const int tid = threadIdx.x, l = tid & 63, w = __builtin_amdgcn_readfirstlane(tid >> 6);
  unsigned pfd;
  {
    u64 pc; unsigned len, back;
    asm volatile("s_getpc_b64 %0\n.Lkf_pf_here:\n\ts_mov_b32 %1, .Lkf_pf_end+1152-.Lkf_pf_start\n\ts_mov_b32 %2, .Lkf_pf_here-.Lkf_pf_start" : "=s"(pc), "=s"(len), "=s"(back));
    unsigned o = (unsigned)(jj * 8 + w) * 256u;
    o = o + 256u <= len ? o : len - 256u;
    const u64 pa = (pc - back + o) & ~63ull;
    asm volatile("s_load_dword %0, %1, 0x0\n\ts_load_dword %0, %1, 0x40\n\ts_load_dword %0, %1, 0x80\n\ts_load_dword %0, %1, 0xc0" : "=&s"(pfd) : "s"(pa) : "memory");
  }
  asm volatile("" : "+s"(x), "+s"(wqkv), "+s"(wphi), "+s"(wo), "+s"(w_r), "+s"(w_gate), "+s"(w_up), "+s"(w_down), "+s"(ws), "+s"(out));
  SB0();
#define GLD(dst, p) asm volatile("global_load_dwordx4 %0, %1, off" : "=v"(dst) : "v"(p) : "memory")
  u32x4 xr4[4], wr4[4];
  if (w < 4) {
    const u16* xr = x + (size_t)(panel * 128 + hp * 8 + (tid >> 5)) * DM + (l & 31) * 8;
    const u16* wr = w_r + (l & 31) * 8;
    GLD(xr4[0], xr); GLD(xr4[1], xr + 256); GLD(xr4[2], xr + 512); GLD(xr4[3], xr + 768);
    GLD(wr4[0], wr); GLD(wr4[1], wr + 256); GLD(wr4[2], wr + 512); GLD(wr4[3], wr + 768);
  }
  SB0();
  const int cw = w & 3, wm = cw & 1, wn = cw >> 1;
  const int h = l >> 5, r31 = l & 31, blk = (l >> 4) & 1, tq = (l & 15) >> 2, tp = l & 3;
  const int hd = hp * 2 + wn;
  const unsigned lbase = LDS_OFF(smem);
  const __amdgpu_buffer_rsrc_t rs = __builtin_amdgcn_make_buffer_rsrc((void*)ws, 0, (int)WS_END, 0x00020000);
  bf16x8 fr[4];
  bf16x8 wp[2];
  f32x4 qa[4][2]; float ri[4];
  const int kh = w >> 2;
  const int wt = wm * 2 + kh;
  char* img = smem + (wn * 4 + wt) * 4608 + r31 * 144 + h * 8;
  auto wpload = [&]() {
    const char* p = smem + X_WP + wn * 2048 + (8 * h + tq) * 64 + (16 * blk + 4 * tp) * 2;
    wp[0] = cat8(trread(p), trread(p + 256)); wp[1] = cat8(trread(p + 1024), trread(p + 1280));
  };
  auto imgw = [&](int g, const f32x2 (&ep)[8], const f32x2 (&en)[8]) {
    const u32x2 vp = {pk2(ep[2 * g][0], ep[2 * g][1]), pk2(ep[2 * g + 1][0], ep[2 * g + 1][1])};
    const u32x2 vn = {pk2(en[2 * g][0], en[2 * g][1]), pk2(en[2 * g + 1][0], en[2 * g + 1][1])};
    *(u32x2*)(img + g * 16) = vp;
    *(u32x2*)(img + 64 + g * 16) = vn;
  };
  auto pack_t = [&](const f32x4 x0, const f32x4 x1, float r0, float r1) {
    const f32x2 a0 = f32x2{x0[0], x0[1]} * f32x2{r0, r0}, a1 = f32x2{x0[2], x0[3]} * f32x2{r0, r0};
    const f32x2 b0 = f32x2{x1[0], x1[1]} * f32x2{r1, r1}, b1 = f32x2{x1[2], x1[3]} * f32x2{r1, r1};
    const auto s0 = __builtin_amdgcn_permlane16_swap(pk2(a0[0], a0[1]), pk2(b0[0], b0[1]), false, false);
    const auto s1 = __builtin_amdgcn_permlane16_swap(pk2(a1[0], a1[1]), pk2(b1[0], b1[1]), false, false);
    return u32x4{s0[0], s1[0], s0[1], s1[1]};
  };
  const int hr = hp ^ 8;
  const int p3 = ((xcd >> 1) * 4 + (jj & 3)) ^ 8, nb = (int)(((xcd & 1) ? 0xF7EDCA98u : 0xB3654210u) >> (jj & 28)) & 15;
  const int flip = jj & 1;
  const unsigned voO = (unsigned)((l >> 3) * DM + (((l & 7) ^ (((l >> 4) & 1) | ((l >> 5) << 2))) * 8)) * 2;
  const bool has_gu = (hp & 7) != 3;
  const int gr = !has_gu ? 224 + (jj - 24) * 4 + (xcd >> 1) : jj >= 28 ? 8 * (4 * (xcd >> 1) + jj - 16) + 6 : (xcd == 6 && jj >= 12) ? 8 * (24 + (jj & 3)) + 2 * ((jj >> 2) - 3) : b;
  const int gkq = (gr / 56) & 3;
  u32x4 dv[8]; unsigned ogq;
  u32x4 wr_[16];
    const int srow = l >> 3, sch = l & 7;
    unsigned voA[4], voW[6];
#pragma unroll
    for (int i = 0; i < 4; ++i) voA[i] = (unsigned)((panel * 128 + (4 * cw + i) * 8 + srow) * DM + ((sch ^ (4 * (i & 1) + (srow >> 1))) * 8)) * 2;
#pragma unroll
    for (int i = 0; i < 6; ++i) voW[i] = (unsigned)(((2 * cw + (i & 1)) * 8 + srow) * 3072 + (i >> 1) * 1024 + hp * 64 + ((sch ^ ((((srow >> 1) & 1) << 2) | ((i & 1) << 1))) * 8)) * 2;
    const unsigned lwa = lbase + cw * 4096, lww = lbase + ABYTES + cw * 2048;
    const int rot = 4 * ((panel & 7) >> 1) + 2 * (panel >> 3) + 4 * (xcd & 3);
    const u64 kord = !has_gu ? (0xFB73EA6D95C21840ull ^ ((panel & 1) ? 0x1111111111111111ull : 0ull)) : (panel & 8) ? ((panel & 1) ? 0xD95FB73EA62C1840ull : 0xD95FB73EA62C1840ull) : (panel & 1) ? 0xFB73EA62D95C1840ull : 0xFB73EA62D95C1840ull;
    auto stagep = [&](int it, auto bufc, auto loc, auto hic) {
      constexpr int B = decltype(bufc)::value * STAGE1, lo = decltype(loc)::value, hi = decltype(hic)::value;
      const int kt = ((int)(kord >> (4 * it)) + rot) & 15;
      const u16* gx = x + kt * 64; const u16* gq_ = wqkv + (size_t)kt * 64 * 3072;
      static_for<hi - lo>([&](auto ic) {
        constexpr int i = lo + decltype(ic)::value;
        if constexpr (i < 4) gl_m0<B + i * 1024>(voA[i], gx, lwa);
        else gl_m0<B + ((i - 4) >> 1) * WSLAB + (i & 1) * 1024>(voW[i - 4], gq_, lww);
      });
    };
    auto stage = [&](int it, auto bufc) { stagep(it, bufc, std::integral_constant<int, 0>{}, std::integral_constant<int, 10>{}); };
  unsigned pa0, pvh, dst, dz, pgw, ogu, oh1, oh1z;
  auto kvsetup = [&]() {
    const int g = l >> 4, c15 = l & 15;
    pa0 = lbase + kh * 18432 + (4 * g + tq) * 144 + (4 * cw + tp) * 8;
    pvh = lbase + HANDOFF + kh * 12288 + l * 16;
    dst = WS_KVP + (unsigned)((hp * 16 + panel) * 16 + w * 2 + (g & 1)) * 512 + c15 * 32 + (g >> 1) * 16;
    dz = WS_ZP + (unsigned)((hp * 16 + w * 2 + (g & 1)) * 16 + panel) * 32 + (g >> 1) * 16;
    pgw = lbase + gu_at(0) + kh * 4096 + (8 * g + tq) * 128 + (((2 * cw + (tp >> 1)) ^ ((((tq >> 1) & 1) << 2) | ((g & 1) << 1))) * 16) + 8 * (tp & 1);
    ogu = WS_GUP + (unsigned)gr * 512 + w * 64 + g * 16;
    oh1 = WS_KVP + (unsigned)((hr * 16 + (l >> 3)) * 16 + panel) * 512 + cw * 128 + (l & 7) * 16;
    oh1z = WS_ZP + (unsigned)((hr * 16 + panel) * 16) * 32 + l * 4;
    asm volatile("" : "+v"(pa0), "+v"(pvh), "+v"(dst), "+v"(dz), "+v"(pgw), "+v"(ogu), "+v"(oh1), "+v"(oh1z));
  };
#define IC(n) std::integral_constant<int, (n)>{}
#define CODE_PF(tagn, FROM, d) do { u64 pc_; unsigned len_; int off_; \
    asm volatile("s_getpc_b64 %0\n.Lkf_pf_h" #tagn ":\n\ts_mov_b32 %1, .Lkf_pf_end+1152-" FROM "\n\ts_mov_b32 %2, " FROM "-.Lkf_pf_h" #tagn : "=s"(pc_), "=s"(len_), "=s"(off_)); \
    unsigned o_ = (unsigned)(jj * 4 + cw) * 256u; o_ = o_ + 256u <= len_ ? o_ : len_ - 256u; \
    const u64 pa_ = (pc_ + (long long)off_ + o_) & ~63ull; \
    asm volatile("s_load_dword %0, %1, 0x0\n\ts_load_dword %0, %1, 0x40\n\ts_load_dword %0, %1, 0x80\n\ts_load_dword %0, %1, 0xc0" : "=&s"(d) : "s"(pa_) : "memory"); } while (0)
#define PF_DONE(d) asm volatile("s_waitcnt lgkmcnt(0)" : "+s"(d) :: "memory")
  if (w < 4) {
    unsigned oc2;
    {
      const unsigned r0 = (unsigned)(panel * 128 + hp * 8);
      oc2 = l < 33 ? WS_KVF + (unsigned)(hr * 16 + panel) * 640 + l * 16 : l == 33 ? WS_F1 + (unsigned)id * 32 : l == 34 ? WS_STAT + r0 * 8 : l == 35 ? WS_RI + r0 * 4
          : l < 40 ? WS_F4 + (unsigned)(((panel * 4 + (l - 36)) * 16 + hp) * 2) * 32 : l < 44 ? WS_GUP + (unsigned)gr * 512 + (l - 40) * 128
          : WS_ZP + (unsigned)((hp * 16 + (l & 15)) * 16 + panel) * 32;
    }
    u32x4 tm, k0, k1; u32x2 tk;
    const unsigned tmofs = lbase + X_CSP + (l < 46 ? l : 45) * 16, kofs = lbase + X_CSP + 768, tgofs = lbase + X_AM + 48;
    unsigned opay;
    { const unsigned r0 = (unsigned)(panel * 128 + hp * 8);
      opay = l < 32 ? WS_CS + (unsigned)(hp * 8 + (panel >> 1)) * 768 + (panel & 1) * 512 + l * 16 : l < 40 ? WS_XA + r0 * 16 + (l - 32) * 16 : l < 44 ? WS_STAT + r0 * 8 + (l - 40) * 16 : WS_RI + r0 * 4 + (l - 44) * 16; }
    const unsigned oflag = WS_F1 + (unsigned)id * 32;
    unsigned mz; asm volatile("s_cmp_eq_u32 %1, 0\n\ts_cselect_b32 %0, -1, 0" : "=s"(mz) : "s"(cw) : "scc");
    const u64 mk_all = ((u64)mz << 32) | mz, mk_pay = ((u64)(mz & 0x3FFFu) << 32) | (mz & ((panel & 1) ? 0xFFFFu : 0xFFFFFFFFu)), mk_one = mz & 1u;
    asm volatile("" : "+v"(oc2), "+v"(opay));
#define MST(op, data, off, mask) asm volatile("s_mov_b64 exec, %3\n\t" op " %0, %1, %2, 0 offen sc1\n\ts_mov_b64 exec, -1" :: "v"(data), "v"(off), "s"(rs), "s"(mask) : "memory")
    f32x4 csa[2] = {{0.f, 0.f, 0.f, 0.f}, {0.f, 0.f, 0.f, 0.f}};
    const bf16x8 ones = {0x3F80, 0x3F80, 0x3F80, 0x3F80, 0x3F80, 0x3F80, 0x3F80, 0x3F80};
    s16x4 cl[2][2], ch[2][2];
    const unsigned csw = lbase + X_CSP + (wm * 64 + wn * 32 + r31) * 4;
    f32x4 acc[4][6];
#pragma unroll
    for (int t = 0; t < 4; ++t)
#pragma unroll
      for (int a = 0; a < 6; ++a) acc[t][a] = f32x4{0.f, 0.f, 0.f, 0.f};
    const int g4 = l >> 4, c15 = l & 15;
    unsigned asw[2][2], wofs[2][2], cofs[2];
#pragma unroll
    for (int e = 0; e < 2; ++e) {
      asw[0][e] = lbase + (wm * 64 + c15) * 128 + (((4 * e + g4) ^ ((c15 >> 1) & 7)) * 16);
      wofs[0][e] = lbase + ABYTES + (8 * g4 + tq) * 128 + (((wn * 4 + 2 * e + (tp >> 1)) ^ ((((tq >> 1) & 1) << 2) | ((g4 & 1) << 1))) * 16) + 8 * (tp & 1);
      asw[1][e] = asw[0][e] + 2 * STAGE1; wofs[1][e] = wofs[0][e] + 2 * STAGE1;
      cofs[e] = wofs[0][e] + ((panel & 1) ? 2 : wm) * WSLAB;
    }
    asm volatile("" : "+v"(asw[0][0]), "+v"(asw[0][1]), "+v"(asw[1][0]), "+v"(asw[1][1]), "+v"(wofs[0][0]), "+v"(wofs[0][1]), "+v"(wofs[1][0]), "+v"(wofs[1][1]), "+v"(cofs[0]), "+v"(cofs[1]));
    bf16x8 af[2][4]; s16x4 wl[2][6], wh_[2][6];
    s16x4 ea[4], ew[6]; f32x4 rv[4];
    const unsigned eaofs = lbase + X_XA + (wm * 64 + c15) * 16;
    const unsigned ewofs = lbase + X_XW + g4 * 1536 + (wn * 32 + c15) * 4;
    const unsigned riofs = lbase + X_ST + (wm * 64 + c15) * 4, rvofs = lbase + X_ST + (wm * 64 + 4 * g4) * 4;
    auto rd = [&](auto ic, auto stc) {
      constexpr int i = decltype(ic)::value, S = decltype(stc)::value, s = S & 1, e = S & 1, buf = (S >> 1) % NST1;
      constexpr int b2 = buf == 2, bo = buf == 1 ? STAGE1 : 0;
      (void)&cl; (void)&ch; (void)&cofs; (void)&af; (void)&wl; (void)&wh_; (void)&asw; (void)&wofs;
      if constexpr (i == 0) DS_B128(af[s][0], asw[b2][e], bo);
      else if constexpr (i == 1) DS_TR(wl[s][0], wofs[b2][0], bo + e * 4096);
      else if constexpr (i == 2) DS_TR(wh_[s][0], wofs[b2][0], bo + e * 4096 + 512);
      else if constexpr (i < 6) DS_B128(af[s][i - 2], asw[b2][e], bo + (i - 2) * 2048);
      else if constexpr (i < 16) {
        constexpr int j = (i - 4) >> 1, off = bo + e * 4096 + (j >> 1) * WSLAB;
        if constexpr (i & 1) DS_TR(wh_[s][j], wofs[b2][j & 1], off + 512); else DS_TR(wl[s][j], wofs[b2][j & 1], off);
      } else {
        constexpr int c = (i - 16) >> 1;
        if constexpr (i & 1) DS_TR(ch[s][c], cofs[c], bo + e * 4096 + 512); else DS_TR(cl[s][c], cofs[c], bo + e * 4096);
      }
    };
    {
      kvsetup();
      SB0();
      asm volatile("s_waitcnt vmcnt(0) lgkmcnt(0)" : "+s"(pfd), "+v"(xr4[0]), "+v"(xr4[1]), "+v"(xr4[2]), "+v"(xr4[3]), "+v"(wr4[0]), "+v"(wr4[1]), "+v"(wr4[2]), "+v"(wr4[3]) :: "memory");
      SB0();
      float m, den, rinv, r;
      ln_rows(xr4, wr4, h, m, den, rinv, r);
      const int rw = tid >> 5;
      const unsigned rb = __float_as_uint(r) >> 16, ord = rb ^ ((rb & 0x8000u) ? 0xFFFFu : 0x8000u), nm = pk2(-m, -m);
      if (r31 == 0) { *(u32x4*)(smem + X_CSP + 512 + rw * 16) = u32x4{nm, nm, nm, nm};
        *(f32x2*)(smem + X_CSP + 640 + rw * 8) = f32x2{m, den}; *(float*)(smem + X_CSP + 704 + rw * 4) = rinv;
        *(unsigned*)(smem + X_CSP + 768 + rw * 4) = (ord << 11) | (unsigned)(2047 - (id * 8 + rw)); }
      SB0();
    }
    __builtin_amdgcn_s_barrier();
    static_for<20>([&](auto ic) { rd(ic, IC(0)); });
    static_for<31>([&](auto stc) {
      constexpr int S = decltype(stc)::value, s = S & 1;
      constexpr bool bnd = (S & 1) == 1, more = S + 1 < 32;
      static_for<24>([&](auto mc) {
        constexpr int m = decltype(mc)::value, j = m >> 2, t = m & 3;
        (void)&tm; (void)&k0; (void)&k1; (void)&tk; (void)&ea; (void)&ew; (void)&ri; (void)&rv; (void)&oc2; (void)&opay; (void)&oflag; (void)&mk_all; (void)&mk_pay; (void)&mk_one; (void)&rs;
        (void)&tmofs; (void)&kofs; (void)&tgofs; (void)&csw; (void)&csa; (void)&eaofs; (void)&ewofs; (void)&riofs; (void)&rvofs;
        constexpr int issued = more ? (m < 2 ? 0 : m - 2 > 16 ? 16 : m - 2) : 0;
        if constexpr (m == 0) {
          if constexpr (bnd) asm volatile("s_waitcnt lgkmcnt(0)" : "+v"(af[s][0]), "+v"(af[s][1]), "+v"(af[s][2]), "+v"(af[s][3]), "+v"(wl[s][0]), "+v"(wh_[s][0]), "+v"(wl[s][1]), "+v"(wh_[s][1]),
                                          "+v"(wl[s][2]), "+v"(wh_[s][2]), "+v"(wl[s][3]), "+v"(wh_[s][3]), "+v"(wl[s][4]), "+v"(wh_[s][4]), "+v"(wl[s][5]), "+v"(wh_[s][5]));
          else asm volatile("s_waitcnt lgkmcnt(10)" : "+v"(af[s][0]), "+v"(af[s][1]), "+v"(af[s][2]), "+v"(af[s][3]), "+v"(wl[s][0]), "+v"(wh_[s][0]));
        } else if constexpr (!bnd && t == 0) {
          constexpr int al = 10 - 2 * j + issued;
          asm volatile("s_waitcnt lgkmcnt(%2)" : "+v"(wl[s][j]), "+v"(wh_[s][j]) : "n"(al > 15 ? 15 : al));
        }
        SB0();
        const bf16x8 wf = cat8(wl[s][j], wh_[s][j]);
        if constexpr (j < 4) acc[t][j] = MFMA16(wf, af[s][t], acc[t][j]);
        else acc[t][j] = MFMA16(af[s][t], wf, acc[t][j]);
        SB0();
        if constexpr (m == 1) {
          if constexpr (S == 1) { const u32x4 sv = {SENT, SENT, SENT, SENT}; MST("buffer_store_dwordx4", sv, oc2, mk_all); }
          if constexpr (S == 11) { asm volatile("" : "+v"(tm), "+v"(k0), "+v"(k1), "+v"(tk)); MST("buffer_store_dwordx4", tm, opay, mk_pay); }
          if constexpr (S == F1_ST / 2) { WAIT_V(0); MST("buffer_store_dwordx2", tk, oflag, mk_one); }
          if constexpr (bnd && more) __builtin_amdgcn_s_barrier();
        }
        if constexpr (more && m >= 1 && m - 1 < (CSS(S + 1) ? 20 : 16)) rd(IC(m - 1), IC(S + 1));
        if constexpr (S == 8 && m == 18) {
          asm volatile("" : "+v"(csa[0]), "+v"(csa[1]));
          const float c = blk ? csa[1][0] : csa[0][0];
          const unsigned u = pk2(c, c - rbf(c));
          asm volatile("ds_write_b32 %0, %1" :: "v"(csw), "v"(u) : "memory");
        }
        if constexpr (S == 9 && m >= 18 && m < 22) {
          if constexpr (m == 18) DS_B128(tm, tmofs, 0);
          if constexpr (m == 19) DS_B128(k0, kofs, 0);
          if constexpr (m == 20) DS_B128(k1, kofs, 16);
          if constexpr (m == 21) asm volatile("ds_read_b64 %0, %1" : "=v"(tk) : "v"(tgofs));
        }
        if constexpr (S == 11 && m == 18) { k0 = __builtin_elementwise_max(k0, k1); asm volatile("" : "+v"(k0)); }
        if constexpr (S == 11 && m == 20) { tk[0] = (tk[0] & ~0x7FFFFFFu) | max(max(k0[0], k0[1]), max(k0[2], k0[3])); asm volatile("" : "+v"(tk)); }
#define DS_B64(dst, addr, off) asm volatile("ds_read_b64 %0, %1 offset:%2" : "=v"(dst) : "v"(addr), "n"(off))
#define DS_B32(dst, addr, off) asm volatile("ds_read_b32 %0, %1 offset:%2" : "=v"(dst) : "v"(addr), "n"(off))
#define DS_2(dst, addr, o0, o1) asm volatile("ds_read2_b32 %0, %1 offset0:%2 offset1:%3" : "=v"(dst) : "v"(addr), "n"(o0), "n"(o1))
        if constexpr (S == 30 && m >= 17) {
          if constexpr (m < 21) DS_B64(ea[m - 17], eaofs, (m - 17) * 256);
          else if constexpr (m < 23) { const unsigned a_ = ewofs + 256; DS_2(ew[m - 19], a_, (m - 21) * 16, (m - 21) * 16 + 192); }
        }
        SB0();
      });
      if constexpr (CSS(S)) {
        asm volatile("s_waitcnt lgkmcnt(15)" : "+v"(cl[s][0]), "+v"(ch[s][0]), "+v"(cl[s][1]), "+v"(ch[s][1]));
        SB0();
        csa[0] = MFMA16(ones, cat8(cl[s][0], ch[s][0]), csa[0]); csa[1] = MFMA16(ones, cat8(cl[s][1], ch[s][1]), csa[1]);
        SB0();
      }
    });
    {
      __builtin_amdgcn_s_setprio(PRIO_T);
      asm volatile("s_waitcnt lgkmcnt(0)" : "+v"(af[1][0]), "+v"(af[1][1]), "+v"(af[1][2]), "+v"(af[1][3]), "+v"(wl[1][0]), "+v"(wh_[1][0]), "+v"(wl[1][1]), "+v"(wh_[1][1]),
                   "+v"(wl[1][2]), "+v"(wh_[1][2]), "+v"(wl[1][3]), "+v"(wh_[1][3]), "+v"(wl[1][4]), "+v"(wh_[1][4]), "+v"(wl[1][5]), "+v"(wh_[1][5]));
      asm volatile("" : "+v"(ea[0]), "+v"(ea[1]), "+v"(ea[2]), "+v"(ea[3]), "+v"(ew[2]), "+v"(ew[3]));
      SB0();
      auto mm = [&](int t, int j) { const bf16x8 wf = cat8(wl[1][j], wh_[1][j]); if (j < 4) acc[t][j] = MFMA16(wf, af[1][t], acc[t][j]); else acc[t][j] = MFMA16(af[1][t], wf, acc[t][j]); };
      auto ff = [&](int t, int j) { if (j < 4) acc[t][j] = MFMA16H(ew[j], ea[t], acc[t][j]); else acc[t][j] = MFMA16H(ea[t], ew[j], acc[t][j]); };
      auto dm = [&](int n) {
        SB0();
        const int t = n & 3, j = (n < 16 ? 4 : 0) + ((n >> 2) & 1);
        if (n & 8) ff(t, j); else mm(t, j);
        SB0();
      };
      const unsigned ew0 = ewofs, ew2 = ewofs + 512;
      mm(2, 2); SB0(); DS_B32(ri[2], riofs, 128); DS_B32(ri[3], riofs, 192); SB0();
      mm(3, 2); SB0(); DS_B32(ri[0], riofs, 0); DS_B32(ri[1], riofs, 64); SB0();
      mm(2, 3); SB0(); DS_2(ew[4], ew2, 0, 192); DS_2(ew[5], ew2, 16, 208); SB0();
      mm(3, 3); SB0(); DS_2(ew[0], ew0, 0, 192); DS_2(ew[1], ew0, 16, 208); SB0();
      ff(2, 2); ff(3, 2); SB0(); DS_B128(rv[0], rvofs, 0); DS_B128(rv[1], rvofs, 64); SB0();
      ff(2, 3); ff(3, 3); SB0(); DS_B128(rv[2], rvofs, 128); DS_B128(rv[3], rvofs, 192); SB0();
      mm(0, 2); mm(1, 2);
      SB0();
      asm volatile("s_waitcnt lgkmcnt(0)" : "+v"(ri[0]), "+v"(ri[1]), "+v"(ri[2]), "+v"(ri[3]), "+v"(ew[0]), "+v"(ew[1]), "+v"(ew[4]), "+v"(ew[5]), "+v"(rv[0]), "+v"(rv[1]), "+v"(rv[2]), "+v"(rv[3]));
      SB0();
      char* ho = smem + HANDOFF + cw * 6144 + l * 16;
      *(u32x4*)(ho + 2048) = pack_t(acc[2][2], acc[3][2], ri[2], ri[3]);
      SB0(); mm(0, 3); mm(1, 3); SB0();
      *(u32x4*)(ho + 3072) = pack_t(acc[2][3], acc[3][3], ri[2], ri[3]);
      SB0(); ff(0, 2); ff(1, 2); ff(0, 3); ff(1, 3); SB0();
      RAW_BARRIER();
      wpload();
      dm(0); dm(1);
      fr[2] = __builtin_bit_cast(bf16x8, pack_t(acc[0][2], acc[1][2], ri[0], ri[1]));
      dm(2); dm(3);
      fr[3] = __builtin_bit_cast(bf16x8, pack_t(acc[0][3], acc[1][3], ri[0], ri[1]));
      dm(4);
      f32x2 ep[8], en[8];
      phi_fn<false>(wp, fr[2], fr[3], ep, en, [&](auto kc) { constexpr int k = decltype(kc)::value; if constexpr (k == 0) { dm(5); dm(6); dm(7); } else if constexpr (7 + k < 16) dm(7 + k); });
      __builtin_amdgcn_s_setprio(0);
#pragma unroll
      for (int g = 0; g < 4; ++g) imgw(g, ep, en);
#pragma unroll
      for (int u = 0; u < 4; ++u) {
        const int t2 = u >> 1, db = u & 1;
        const f32x4 x0 = acc[2 * t2][4 + db] * rv[2 * t2], x1 = acc[2 * t2 + 1][4 + db] * rv[2 * t2 + 1];
        char* pv = t2 ? ho + 4096 : ho;
        *(u32x4*)(pv + db * 1024) = u32x4{pk2(x0[0], x0[1]), pk2(x0[2], x0[3]), pk2(x1[0], x1[1]), pk2(x1[2], x1[3])};
      }
#pragma unroll
      for (int t = 0; t < 4; ++t) { qa[t][0] = acc[t][0]; qa[t][1] = acc[t][1]; wr_[t] = __builtin_bit_cast(u32x4, af[1][t]); }
      wr_[4] = __builtin_bit_cast(u32x4, cat8(wl[1][0], wh_[1][0])); wr_[5] = __builtin_bit_cast(u32x4, cat8(wl[1][1], wh_[1][1]));
      wr_[6] = __builtin_bit_cast(u32x4, cat8(ea[0], ea[1])); wr_[7] = __builtin_bit_cast(u32x4, cat8(ea[2], ea[3])); wr_[8] = __builtin_bit_cast(u32x4, cat8(ew[0], ew[1]));
    }
  } else {
    const int t = tid - 256;
    __builtin_amdgcn_s_setprio(PRIO_H);
    u64 epoch;
    asm volatile("s_load_dwordx2 %0, %1, 0x0" : "=&s"(epoch) : "s"(ws) : "memory");
    stagep(0, IC(0), IC(0), IC(10));
    asm volatile("s_waitcnt lgkmcnt(0)" : "+s"(pfd), "+s"(epoch) :: "memory");
    const u64 enx = epoch + (((unsigned)epoch & 0xFEu) == 0xFEu ? 3u - ((unsigned)epoch & 1u) : 1u);
    const u64 tag = TAG_OF(enx);
    if (t == 0) { *(u32x2*)(smem + X_AM) = u32x2{0u, 0u};
      const unsigned e2 = ((unsigned)enx & 0x55u) | (((unsigned)enx & 0xAAu) << 15);
      *(u32x4*)(smem + X_AM + 32) = u32x4{e2, e2 >> 2, e2 >> 4, e2 >> 6} & 0x00010001u;
      *(u32x4*)(smem + X_AM + 48) = u32x4{(unsigned)tag, (unsigned)(tag >> 32), (unsigned)enx, (unsigned)(enx >> 32)}; }
    u32x2 fl, si; unsigned pfd2, tpace = 0, xs;
    unsigned ofl = WS_F1 + (unsigned)t * 32, orA = WS_CS + (unsigned)(hp * 6144 + cw * 1024 + l * 16);
    unsigned orB = (cw < 2 ? WS_CS + (unsigned)(hp * 6144 + 4096 + cw * 1024) : WS_XA + (unsigned)(panel * 2048 + (cw - 2) * 1024)) + l * 16;
    unsigned orC = ((cw & 1) ? WS_STAT + (unsigned)p3 * 1024 : WS_RI + (unsigned)panel * 512) + l * 16;
    unsigned oxs = (unsigned)(gkq * 256 + cw * 64 + r31 * 2) * 2, akey = lbase + X_AM + 4, vol = (unsigned)l * 16;
    unsigned voG = (unsigned)((8 * cw + srow) * FF + ((sch ^ ((((srow >> 1) & 1) << 2) | ((cw & 1) << 1))) * 8)) * 2;
    const u16* gg_ = w_gate + (size_t)gkq * 256 * FF + ((gr + 1) % 56) * 64;
    const u16* gu_ = w_up + (size_t)gkq * 256 * FF + ((gr + 3) % 56) * 64;
    const u16* gwo = wo + (size_t)(((xcd >> 1) * 4 + (jj >> 3)) * 64 + 2 * cw * 8) * DM + ((xcd & 1) * 8 + (jj & 7)) * 64;
    const u16* gwp = wphi + hp * 2048 + cw * 512;
    const u16* gdw = w_down + (size_t)(((gr >> 3) & 31) * 128 + 32 * cw) * DM + ((gr + 1) & 7) * 128;
    unsigned odw = (unsigned)((l >> 4) * DM + (l & 15) * 8) * 2;
    const u16* gw2a = wo + (size_t)(((cw * 10) ^ (flip * 64)) * 8) * DM + nb * 64;
    const u16* gw2b = wo + (size_t)(((104 + cw * 6) ^ (flip * 64)) * 8) * DM + nb * 64;
    asm volatile("" : "+s"(gw2a), "+s"(gw2b));
    asm volatile("" : "+s"(gg_), "+s"(gu_), "+s"(gwo), "+s"(gwp), "+s"(gdw));
    { const int f = (gr >> 3) * 128 + 32 * cw + 4 * (l & 7), fs = (f >> 6) - ((l & 8) ? 3 : 1);
      ogq = WS_GUP + (unsigned)((l >> 4) * 56 + (fs < 0 ? fs + 56 : fs)) * 512 + (f & 63) * 4 + (l & 8) * 32; }
    asm volatile("" : "+v"(voG), "+v"(odw), "+v"(ogq));
    kvsetup();
    asm volatile("s_waitcnt lgkmcnt(0)" : "+v"(ofl), "+v"(orA), "+v"(orB), "+v"(orC), "+v"(oxs), "+v"(akey), "+v"(vol) :: "memory");
    stagep(0, IC(0), IC(10), IC(10));
    stagep(1, IC(1), IC(0), IC(10));
    WAIT_V(10);
    __builtin_amdgcn_s_barrier();
    stagep(1, IC(1), IC(10), IC(10));
    auto wopf = [&]() {
      GLDS_S(voO, gwo, lbase + X_WO + 2 * cw * 1024); GLDS_S(voO, gwo + 8 * DM, lbase + X_WO + (2 * cw + 1) * 1024);
      GLDS_S(vol, gwp, lbase + X_WP + cw * 1024);
    };
    auto dvl = [&](auto kc) {
      constexpr int k = decltype(kc)::value;
      static_for<DVN[k + 2]>([&](auto ic) {
        constexpr int i = dv_first(k) + decltype(ic)::value;
        u32x4& d_ = dv[i]; const unsigned o_ = odw; const u16* p_ = gdw + (size_t)(4 * i) * DM;
        asm volatile("global_load_dwordx4 %0, %1, %2 nt" : "=v"(d_) : "v"(o_), "s"(p_) : "memory");
      });
    };
    if constexpr (DVN[0] != 0) { if (has_gu) dvl(IC(-2)); }
    stagep(2, IC(2), IC(0), IC(S2A));
    if constexpr (WOK == -1) wopf();
    if constexpr (DVN[1] != 0) { if (has_gu) dvl(IC(-1)); }
    const unsigned lgu = lbase + cw * 1024;
    auto gul = [&](auto ksc) {
      constexpr int ks = decltype(ksc)::value;
      gl_nt<gu_at(ks)>(voG, gg_ + (size_t)ks * 32 * FF, lgu); gl_nt<gu_at(ks) + 4096>(voG, gu_ + (size_t)ks * 32 * FF, lgu);
    };
#define BLD64(dst, off) asm volatile("buffer_load_dwordx2 %0, %1, %2, 0 offen sc1" : "=v"(dst) : "v"(off), "s"(rs) : "memory")
    static_for<15>([&](auto ktc) {
      constexpr int kt = decltype(ktc)::value;
      constexpr auto exw = [](int k) { return DVN[k + 2]; };
      constexpr auto exo = [](int k) { return k == KP ? 1 : k == KC + 1 ? 3 : k == KC + 2 ? 2 : 0; };
      constexpr int exa = ((kt - 2 == WOK || kt - 1 == WOK) ? 3 : 0) - (kt == 0 ? 10 - S2A : 0) + (kt - 2 > KC ? exo(kt - 2) : 0);
      if constexpr (exw(kt - 2) + exw(kt - 1) != 0) { if (has_gu) WAIT_V(10 + exa + exw(kt - 2) + exw(kt - 1) + exo(kt - 1)); else WAIT_V(10 + exa + exo(kt - 1)); }
      else if constexpr (kt < 14) WAIT_V(10 + exa + exo(kt - 1));
      else { if (has_gu) WAIT_V(10); else WAIT_V(0); }
      if constexpr (PACE_T[kt] != 0) { if (cw == 0) { while ((unsigned)__builtin_amdgcn_s_memrealtime() - tpace < (unsigned)(has_gu ? PACE_T[kt] : PACE_3[kt])) {} } }
      __builtin_amdgcn_s_barrier();
      if constexpr (PACE_T[kt + 1] != 0) { if (cw == 0) tpace = (unsigned)__builtin_amdgcn_s_memrealtime(); SB0(); }
      if constexpr (kt == 0) stagep(2, IC(2), IC(S2A), IC(10));
#define UMAX_DPP(C) { const unsigned o = (unsigned)__builtin_amdgcn_update_dpp(0, (int)key, C, 0xf, 0xf, true); key = o > key ? o : key; asm volatile("" : "+v"(key)); }
#define SP(lo, hi) if constexpr (kt + NST1 < 16) { SB0(); stagep(kt + NST1, IC(kt % NST1), IC(lo), IC(hi)); SB0(); }
      SP(0, 10)
      if constexpr (kt == WOK) wopf();
      if constexpr (kt == PF2_KT) CODE_PF(2, ".Lkf_pf_mid", pfd2);
      if constexpr (kt == PF2_KT + 2) PF_DONE(pfd2);
      if constexpr (kt == KP) BLD64(fl, ofl);
      if constexpr (kt == KC) {
        static_assert(DVN[KP + 2] == 0, "count");
        if constexpr (KC == KP + 1) asm volatile("s_waitcnt vmcnt(10)" : "+v"(fl) :: "memory"); else asm volatile("" : "+v"(fl) :: "memory");
#define FL_OK() __all((((fl[0] ^ (unsigned)tag) >> 27) | (fl[1] ^ (unsigned)(tag >> 32))) == 0u)
        if (__builtin_expect(!FL_OK(), 0)) {
          for (unsigned sp = 0;;) {
            asm volatile("buffer_load_dwordx2 %0, %1, %2, 0 offen sc1\n\ts_waitcnt vmcnt(0)" : "=v"(fl) : "v"(ofl), "s"(rs) : "memory");
            if (FL_OK()) break;
            SPIN_NEXT(sp);
          }
        }
        unsigned key = fl[0];
        UMAX_DPP(0x121) UMAX_DPP(0x122) UMAX_DPP(0x124) UMAX_DPP(0x128)
        asm volatile("s_mov_b64 exec, %2\n\tds_max_u32 %0, %1\n\ts_mov_b64 exec, -1\n\ts_waitcnt lgkmcnt(0)" :: "v"(akey), "v"(key), "s"(0x0001000100010001ull) : "memory");
      }
      if constexpr (kt == KC + 1) { GLDS_SC1(orA, ws, lbase + X_XW + cw * 1024); GLDS_SC1(orB, ws, lbase + X_XW + 4096 + cw * 1024); GLDS_SC1(orC, ws, lbase + X_ST + (cw & 1) * 1024); }
      if constexpr (kt == KC + 2) {
        const int bi = 2047 - (int)(__builtin_amdgcn_readfirstlane(*(const unsigned*)(smem + X_AM + 4)) & 2047u);
        asm volatile("global_load_dword %0, %1, %2" : "=v"(xs) : "v"(oxs), "s"(x + (size_t)bi * DM) : "memory");
        asm volatile("buffer_load_dwordx2 %0, off, %1, %2 sc1" : "=v"(si) : "s"(rs), "s"(WS_STAT + (unsigned)bi * 8) : "memory");
      }
      if constexpr (DVN[kt + 2] != 0) { if (has_gu) dvl(ktc); }
      if constexpr (kt == 13) { if (has_gu) { gul(IC(0)); gul(IC(1)); gul(IC(2)); gul(IC(3)); gul(IC(4)); } }
      if constexpr (kt == 14) asm volatile("" : "+v"(xs), "+v"(si) :: "memory");
      if constexpr (kt == 14) { if (has_gu) { gul(IC(5)); gul(IC(6)); gul(IC(7)); } }
    });
    auto wld = [&](auto ic) {
      constexpr int i = decltype(ic)::value;
      u32x4& d_ = wr_[i]; const unsigned o_ = voO; const u16* p_ = (i < 10 ? gw2a : gw2b) + (size_t)((i < 10 ? i : i - 10) * 8) * DM;
      asm volatile("global_load_dwordx4 %0, %1, %2" : "=v"(d_) : "v"(o_), "s"(p_) : "memory");
    };
    wpload();
    {
      const float m = __uint_as_float(si[0]), den = own(__uint_as_float(si[1])), rinv = rcp_nr(den);
      *(unsigned*)(smem + X_XS + cw * 128 + r31 * 4) = ln_pair(xs, m, den, rinv);
    }
    RAW_BARRIER();
    const char* ho = smem + HANDOFF + cw * 6144 + l * 16;
    fr[2] = *(const bf16x8*)(ho + 2048); fr[3] = *(const bf16x8*)(ho + 3072);
    f32x2 ep[8], en[8];
    phi_fn<false>(wp, fr[2], fr[3], ep, en, [&](auto kc) {
      constexpr int k = decltype(kc)::value;
      static_for<NREG - WPOST>([&](auto ic) {
        constexpr int i = decltype(ic)::value;
        if constexpr (k == WH0 + (i * (17 - WH0)) / (NREG - WPOST)) { SB0(); wld(ic); SB0(); }
      });
    });
#pragma unroll
    for (int g = 0; g < 4; ++g) imgw(g, ep, en);
    SB0();
    static_for<WPOST>([&](auto ic) { wld(IC(NREG - WPOST + decltype(ic)::value)); });
  }
  asm volatile(".Lkf_pf_mid:");
  f32x16 zero;
#pragma unroll
  for (int i = 0; i < 16; ++i) zero[i] = 0.f;
  u32x2 tg; unsigned t8, rkey; u32x4 t2;
  {
    __builtin_amdgcn_s_setprio(2);
    RAW_BARRIER();
    u32x4 am; unsigned rk;
    { const unsigned a_ = lbase + X_AM; DS_B128(am, a_, 48); DS_B128(t2, a_, 32); asm volatile("ds_read_b32 %0, %1 offset:4" : "=v"(rk) : "v"(a_)); }
    s16x4 al[4], ah[4]; bf16x8 v0[4], v1[4];
    DS_TR(al[0], pa0, 0); DS_TR(ah[0], pa0, 2304); DS_B128(v0[0], pvh, 0); DS_B128(v1[0], pvh, 1024);
    DS_TR(al[1], pa0, 4608); DS_TR(ah[1], pa0, 4608 + 2304); DS_B128(v0[1], pvh, 4096); DS_B128(v1[1], pvh, 4096 + 1024);
    DS_TR(al[2], pa0, 9216); DS_TR(ah[2], pa0, 9216 + 2304); DS_B128(v0[2], pvh, 6144); DS_B128(v1[2], pvh, 6144 + 1024);
    DS_TR(al[3], pa0, 13824); DS_TR(ah[3], pa0, 13824 + 2304); DS_B128(v0[3], pvh, 6144 + 4096); DS_B128(v1[3], pvh, 6144 + 4096 + 1024);
    const bf16x8 ones = {0x3F80, 0x3F80, 0x3F80, 0x3F80, 0x3F80, 0x3F80, 0x3F80, 0x3F80};
    f32x4 k0 = {0.f, 0.f, 0.f, 0.f}, k1 = k0, zz = k0;
    static_for<4>([&](auto tbc) {
      constexpr int tb = decltype(tbc)::value;
      asm volatile("s_waitcnt lgkmcnt(%4)" : "+v"(al[tb]), "+v"(ah[tb]), "+v"(v0[tb]), "+v"(v1[tb]) : "n"(12 - 4 * tb));
      const bf16x8 af = cat8(al[tb], ah[tb]);
      k0 = MFMA16(af, v0[tb], k0); k1 = MFMA16(af, v1[tb], k1); zz = MFMA16(af, ones, zz);
      SB0();
    });
    asm volatile("" : "+v"(am), "+v"(rk), "+v"(t2));
    auto tagv = [&](f32x4 v) { const u32x4 u = __builtin_bit_cast(u32x4, v); return (u & 0xFFFFFF00u) | (am[2] & 0xFFu); };
    ST128((u32x4{pk2(k0[0], k0[1]), pk2(k0[2], k0[3]), pk2(k1[0], k1[1]), pk2(k1[2], k1[3])} & 0xFFFEFFFEu) | t2, dst);
    const u32x4 zt = tagv(zz);
    asm volatile("s_mov_b64 exec, %3\n\tbuffer_store_dwordx4 %0, %1, %2, 0 offen sc1\n\ts_mov_b64 exec, -1" :: "v"(zt), "v"(dz), "s"(rs), "s"(0x0001000100010001ull) : "memory");
    SB0();
    tg = u32x2{am[0], am[1]}; t8 = __builtin_amdgcn_readfirstlane(am[2]) & 0xFFu; rkey = __builtin_amdgcn_readfirstlane(rk);
  }
  {
    bf16x8 xf[8];
    const unsigned px = lbase + X_XS + (l >> 4) * 16, hq = lbase + X_XW + cw * 2048 + l * 16;
    static_for<8>([&](auto kc) { constexpr int ks = decltype(kc)::value; (void)&xf; (void)&px; DS_B128(xf[ks], px, ks * 64); });
    if (w >= 4) {
      __builtin_amdgcn_s_setprio(0);
      WAIT_V(NREG + 2);
    } else {
      auto lo4 = [](u32x4 v) { return __builtin_bit_cast(s16x4, u32x2{v[0], v[1]}); };
      auto hi4 = [](u32x4 v) { return __builtin_bit_cast(s16x4, u32x2{v[2], v[3]}); };
#pragma unroll
      for (int tt = 0; tt < 4; ++tt) {
        const int t = tt ^ 2;
#pragma unroll
        for (int j = 0; j < 2; ++j) qa[t][j] = MFMA16(__builtin_bit_cast(bf16x8, wr_[4 + j]), __builtin_bit_cast(bf16x8, wr_[t]), qa[t][j]);
        if (tt & 1) {
#pragma unroll
          for (int u = t - 1; u <= t; ++u)
#pragma unroll
            for (int j = 0; j < 2; ++j) qa[u][j] = MFMA16H(j ? hi4(wr_[8]) : lo4(wr_[8]), (u & 1) ? hi4(wr_[6 + (u >> 1)]) : lo4(wr_[6 + (u >> 1)]), qa[u][j]);
        }
      }
      const u32x4 q0 = pack_t(qa[2][0], qa[3][0], ri[2], ri[3]), q1 = pack_t(qa[2][1], qa[3][1], ri[2], ri[3]);
      asm volatile("ds_write_b128 %0, %1\n\tds_write_b128 %0, %2 offset:1024" :: "v"(hq), "v"(q0), "v"(q1) : "memory");
    }
    RAW_BARRIER();
    asm volatile("" : "+v"(xf[0]), "+v"(xf[1]), "+v"(xf[2]), "+v"(xf[3]), "+v"(xf[4]), "+v"(xf[5]), "+v"(xf[6]), "+v"(xf[7]));
    if (has_gu) {
      s16x4 gl[8], gh[8];
      static_for<8>([&](auto kc) { constexpr int ks = decltype(kc)::value; (void)&gl; (void)&gh; (void)&pgw;
        DS_TR(gl[ks], pgw, ks * 8192); DS_TR(gh[ks], pgw, ks * 8192 + 512); });
      f32x4 c0 = {0.f, 0.f, 0.f, 0.f};
      static_for<8>([&](auto kc) { constexpr int ks = decltype(kc)::value;
        asm volatile("s_waitcnt lgkmcnt(%2)" : "+v"(gl[ks]), "+v"(gh[ks]) : "n"(14 - 2 * ks));
        c0 = MFMA16(cat8(gl[ks], gh[ks]), xf[ks], c0);
        SB0(); });
      const u32x4 gt_ = f2u_tag(c0, t8);
      asm volatile("s_mov_b64 exec, %3\n\tbuffer_store_dwordx4 %0, %1, %2, 0 offen sc1\n\ts_mov_b64 exec, -1" :: "v"(gt_), "v"(ogu), "s"(rs), "s"(0x0001000100010001ull) : "memory");
    }
  }
  const int idx = 2047 - (int)(rkey & 2047u);
  const int fb = kh ^ ((idx >> 4) & 1);
  const int erow = cw * 32 + fb * 16 + (l & 15), ech = l >> 4;
  u32x4 xv[2];
  const bool sw = ech & 1;
#define FSWAP(v) v = u32x4{sw ? v[2] : v[0], sw ? v[3] : v[1], sw ? v[0] : v[2], sw ? v[1] : v[3]}
  const bool has_idx = w < 4 && (idx >> 5) == p3 * 4 + w;
  unsigned xrt = 0;
  unsigned of4, op4, oo0, oo1, kba, kbz, odo, o2, oz2, wa_, wz_, oxg;
  auto later = [&]() {
    SB0();
    of4 = WS_F4 + (unsigned)(((panel * 4 + wt) * 16 + hp) * 2 + wn) * 32; op4 = WS_F4 + (unsigned)(((p3 * 4 + cw) * 16 + 8 * (kh ^ flip)) * 2 + (l & 15)) * 32;
    oo0 = WS_O + 512 + (unsigned)((((p3 * 4 + cw) * 2 + fb) * 32 + (kh ^ flip) * 16) * 128 + h * 64 + r31) * 16; oo1 = oo0 ^ 65536u;
    kba = lbase + X_KB + wn * 4224 + l * 16; kbz = lbase + X_KB + wn * 4224 + 4096 + h * 64;
    odo = WS_O + 512 + (unsigned)((((panel * 4 + wt) * 2 + blk) * 32 + hd) * 128 + h * 16 + (l & 15)) * 16;
    oxg = (unsigned)((p3 * 128 + cw * 32 + fb * 16 + (l >> 3)) * DM + nb * 64 + ((l & 7) ^ (l >> 3)) * 8) * 2;
    asm volatile("" : "+v"(oxg), "+v"(kba), "+v"(kbz), "+v"(odo), "+v"(of4), "+v"(op4), "+v"(oo0), "+v"(oo1));
    SB0();
  };
  if (w < 4) {
    unsigned pfd3, od; u64 mkp;
    {
      u32x4 pa[2]; unsigned z0 = t8, z1 = t8;
#define H1LD(p) asm volatile("buffer_load_dwordx4 %0, %4, %6, 0 offen sc1\n\tbuffer_load_dwordx4 %1, %4, %6, %7 offen sc1\n\t" \
                   "s_cmp_lg_u32 %8, 3\n\ts_cbranch_scc1 1f\n\tbuffer_load_dword %2, %5, %6, 0 offen sc1\n\tbuffer_load_dword %3, %5, %6, 0 offen offset:256 sc1\n1:" \
                   : "=&v"(p[0]), "=&v"(p[1]), "+v"(z0), "+v"(z1) : "v"(oh1), "v"(oh1z), "s"(rs), "s"(65536), "s"(cw) : "memory", "scc")
      __builtin_amdgcn_s_sleep(H1_DELAY);
      H1LD(pa);
      SB0();
      CODE_PF(3, ".Lkf_pf_mid", pfd3);
      od = WS_KVF + (unsigned)(hr * 16 + panel) * 640 + ((l & 8) ? 512 + (l & 7) * 2 : cw * 128 + h * 64 + (l & 7) * 8 + blk * 4);
      mkp = cw == 3 ? 0x00FF00FF00FF55FFull : 0x00FF00FF00FF00FFull;
      asm volatile("" : "+s"(mkp));
      fr[0] = __builtin_bit_cast(bf16x8, pack_t(qa[0][0], qa[1][0], ri[0], ri[1])); fr[1] = __builtin_bit_cast(bf16x8, pack_t(qa[0][1], qa[1][1], ri[0], ri[1]));
      asm volatile("" : "+v"(od), "+v"(fr[0]), "+v"(fr[1]));
      asm volatile("global_load_ushort %0, %1, %2" : "=v"(xrt) : "v"((unsigned)(idx * DM + nb * 64 + 4 * (l & 15) + ech) * 2), "s"(x) : "memory");
      later();
      o2 = WS_KVF + (unsigned)(hp * 16 + wn * 8 + wm * 4 + h) * 640 + ((r31 & 12) * 2 + (r31 >> 4) * 4 + (r31 & 3)) * 16;
      oz2 = WS_KVF + (unsigned)(hp * 16 + wn * 8 + wm * 4 + (l & 3)) * 640 + 512;
      wa_ = kba + wm * 2048; wz_ = lbase + X_KB + wn * 4224 + 4096 + (l & 1) * 64 + (wm * 2 + ((l >> 1) & 1)) * 16;
      asm volatile("" : "+v"(o2), "+v"(oz2), "+v"(wa_), "+v"(wz_));
      unsigned hb = 0x8000u, s_lo = 0x05040100u, s_hi = 0x07060100u;
      asm volatile("" : "+s"(s_lo), "+s"(s_hi));
      asm volatile("s_waitcnt vmcnt(1)" : "+v"(pa[0]), "+v"(pa[1]), "+v"(z0), "+v"(z1), "+v"(hb) :: "memory");
      u64 b0, b1, b2; u32x4 v0, v1;
      auto miss = [&]() __attribute__((always_inline)) {
        v0 = pa[0] ^ t2; v1 = pa[1] ^ t2;
        b0 = __ballot((or4(v0) & 0x00010001u) != 0u); b1 = __ballot((or4(v1) & 0x00010001u) != 0u);
        b2 = __ballot((((z0 ^ t8) | (z1 ^ t8)) & 0xFFu) != 0u);
        return (b0 | b1 | b2) != 0;
      };
      if (miss()) {
        unsigned sp = 0;
        do {
        b0 |= (b0 >> 4) & 0x0F0F0F0F0F0F0F0Full; b0 |= (b0 << 4) & 0xF0F0F0F0F0F0F0F0ull; b1 |= (b1 >> 4) & 0x0F0F0F0F0F0F0F0Full; b1 |= (b1 << 4) & 0xF0F0F0F0F0F0F0F0ull;
        asm volatile("s_wqm_b64 exec, %8\n\ts_cbranch_execz 1f\n\tbuffer_load_dwordx4 %0, %4, %6, 0 offen sc1\n1:\n\ts_wqm_b64 exec, %9\n\ts_cbranch_execz 2f\n\tbuffer_load_dwordx4 %1, %4, %6, %7 offen sc1\n2:\n\t"
                     "s_mov_b64 exec, -1\n\ts_cmp_eq_u64 %10, 0\n\ts_cbranch_scc1 3f\n\tbuffer_load_dword %2, %5, %6, 0 offen sc1\n\tbuffer_load_dword %3, %5, %6, 0 offen offset:256 sc1\n3:\n\ts_waitcnt vmcnt(0)"
                     : "+v"(pa[0]), "+v"(pa[1]), "+v"(z0), "+v"(z1) : "v"(oh1), "v"(oh1z), "s"(rs), "s"(65536), "s"(b0), "s"(b1), "s"(b2) : "memory", "scc");
        } while (miss() && ++sp < SPIN_CAP);
      }
    auto sw32 = [](float a, float c) { const auto r = __builtin_amdgcn_permlane32_swap(__float_as_uint(a), __float_as_uint(c), false, false); return __uint_as_float(r[0]) + __uint_as_float(r[1]); };
    auto sw16 = [](float a, float c) { const auto r = __builtin_amdgcn_permlane16_swap(__float_as_uint(a), __float_as_uint(c), false, false); return __uint_as_float(r[0]) + __uint_as_float(r[1]); };
    float sl[4], sh[4];
#pragma unroll
    for (int m = 0; m < 4; ++m) {
      sl[m] = own(__uint_as_float(__builtin_amdgcn_perm(v0[m], hb, s_lo)) + __uint_as_float(__builtin_amdgcn_perm(v1[m], hb, s_lo)));
      sh[m] = own(__uint_as_float(__builtin_amdgcn_perm(v0[m], hb, s_hi)) + __uint_as_float(__builtin_amdgcn_perm(v1[m], hb, s_hi))); }
    const float e0 = dpp_add<0x128, 0xf>(sw16(sw32(sl[0], sl[2]), sw32(sl[1], sl[3]))), e1 = dpp_add<0x128, 0xf>(sw16(sw32(sh[0], sh[2]), sw32(sh[1], sh[3])));
    const float zs = x32_add(x16_add(dpp_add<0x128, 0xf>(__uint_as_float(z0 ^ t8) + __uint_as_float(z1 ^ t8))));
    const float zn = __builtin_bit_cast(float, __builtin_amdgcn_update_dpp(0, __builtin_bit_cast(int, zs), 0xB1, 0xf, 0xf, true));
    const unsigned kvb = min((l & 8) ? pk2(zs, zn) : pk2(e0, e1), SENT - 1u);
    asm volatile("s_mov_b64 exec, %3\n\tbuffer_store_dword %0, %1, %2, 0 offen sc1\n\ts_mov_b64 exec, -1" :: "v"(kvb), "v"(od), "s"(rs), "s"(mkp) : "memory");
    PF_DONE(pfd3);
    }
  } else {
    const char* hq = smem + X_XW + cw * 2048 + l * 16;
    fr[0] = *(const bf16x8*)hq; fr[1] = *(const bf16x8*)(hq + 1024);
  }
  bf16x8 pf[4];
  {
    f32x2 ep[8], en[8];
    phi_fn<true>(wp, fr[0], fr[1], ep, en, [](auto) {});
#pragma unroll
    for (int s = 0; s < 2; ++s) {
      pf[s] = __builtin_bit_cast(bf16x8, u32x4{pk2(ep[4 * s][0], ep[4 * s][1]), pk2(ep[4 * s + 1][0], ep[4 * s + 1][1]), pk2(ep[4 * s + 2][0], ep[4 * s + 2][1]), pk2(ep[4 * s + 3][0], ep[4 * s + 3][1])});
      pf[2 + s] = __builtin_bit_cast(bf16x8, u32x4{pk2(en[4 * s][0], en[4 * s][1]), pk2(en[4 * s + 1][0], en[4 * s + 1][1]), pk2(en[4 * s + 2][0], en[4 * s + 2][1]), pk2(en[4 * s + 3][0], en[4 * s + 3][1])});
    }
    asm volatile("" : "+v"(pf[0]), "+v"(pf[1]), "+v"(pf[2]), "+v"(pf[3]));
  }
  SB0();
  if (b == 0 && tid == 64) ST64(*(const u32x2*)(smem + X_AM + 56), WS_EPOCH);
  u32x4 gq, gq2; f32x2 df[8][4]; unsigned ayr;
#define GQLD(tail) asm volatile("buffer_load_dwordx4 %0, %1, %2, 0 offen sc1" tail : "=v"(gq) : "v"(ogq), "s"(rs) : "memory")
  if (w >= 4) {
    {
      WAIT_V(2);
      { const u16* ga = wo + (size_t)(((cw * 10) ^ (flip * 64)) * 8) * DM + nb * 64; const u16* gb = wo + (size_t)(((104 + cw * 6) ^ (flip * 64)) * 8) * DM + nb * 64;
        static_for<16 - NREG>([&](auto ic) { constexpr int i = NREG + decltype(ic)::value;
          if constexpr (i < 10) gl_m0<i * 1024>(voO, ga + (size_t)(i * 8) * DM, lbase + cw * 10240); else gl_m0<(104 + i - 10) * 1024>(voO, gb + (size_t)((i - 10) * 8) * DM, lbase + cw * 6144); }); }
      SB0();
      char* pa_ = smem + cw * 10 * 1024 + l * 16; char* pb_ = smem + (104 + cw * 6) * 1024 + l * 16;
#pragma unroll
      for (int i = 0; i < 10; ++i) if (i < NREG) *(u32x4*)(pa_ + i * 1024) = wr_[i];
#pragma unroll
      for (int i = 0; i < 6; ++i) if (10 + i < NREG) *(u32x4*)(pb_ + i * 1024) = wr_[10 + i];
      SB0();
    }
    asm volatile("" : "+v"(dv[0]), "+v"(dv[1]), "+v"(dv[2]), "+v"(dv[3]), "+v"(dv[4]), "+v"(dv[5]), "+v"(dv[6]), "+v"(dv[7]) :: "memory");
    SB0();
#pragma unroll
    for (int i = 0; i < 8; ++i)
#pragma unroll
      for (int e = 0; e < 4; ++e) { df[i][e] = f32x2{bflo(dv[i][e]), bfhi(dv[i][e])}; asm volatile("" : "+v"(df[i][e])); }
    SB0();
    later();
    ayr = lbase + X_YR + (cw * 128 + (l & 15) * 8 + (l >> 4)) * 4;
    asm volatile("" : "+v"(ayr));
    if (has_gu) GQLD("");
    SB0();
  }
  auto downp = [&]() __attribute__((always_inline)) {
            auto red4 = [](float s0, float s1, float s2, float s3) {
              const auto r01 = __builtin_amdgcn_permlane16_swap(__float_as_uint(s0), __float_as_uint(s1), false, false);
              const auto r23 = __builtin_amdgcn_permlane16_swap(__float_as_uint(s2), __float_as_uint(s3), false, false);
              const float t01 = __uint_as_float(r01[0]) + __uint_as_float(r01[1]), t23 = __uint_as_float(r23[0]) + __uint_as_float(r23[1]);
              const auto r = __builtin_amdgcn_permlane32_swap(__float_as_uint(t01), __float_as_uint(t23), false, false);
              return __uint_as_float(r[0]) + __uint_as_float(r[1]);
            };
            const float gg = rbf(red4(__uint_as_float(gq[0]), __uint_as_float(gq[1]), __uint_as_float(gq[2]), __uint_as_float(gq[3])));
            const float uu = __builtin_bit_cast(float, __builtin_amdgcn_update_dpp(0, __builtin_bit_cast(int, gg), 0x128, 0xf, 0xf, true));
            const float hv0 = rbf(rbf(silu_r(gg)) * uu);
            f32x2 ya[4] = {{0.f, 0.f}, {0.f, 0.f}, {0.f, 0.f}, {0.f, 0.f}};
            static_for<8>([&](auto ic) {
              constexpr int i = decltype(ic)::value;
              const float hv = __builtin_bit_cast(float, __builtin_amdgcn_update_dpp(0, __builtin_bit_cast(int, hv0), 0x150 + i, 0xf, 0xf, true));
              const f32x2 h2 = {hv, hv};
#pragma unroll
              for (int e = 0; e < 4; ++e) ya[e] = __builtin_elementwise_fma(h2, df[i][e], ya[e]);
            });
            const float y0 = red4(ya[0][0], own(ya[0][1]), ya[1][0], own(ya[1][1])), y1 = red4(ya[2][0], own(ya[2][1]), ya[3][0], own(ya[3][1]));
            asm volatile("ds_write2_b32 %0, %1, %2 offset1:4" :: "v"(ayr), "v"(y0), "v"(y1) : "memory");
  };
  const unsigned lxv = lbase + (w < 6 ? 131072 + w * 2048 : X_YS + 64 + (w - 6) * 2048);
  {
    u32x4 kf[4], zf[4];
    if (w < 4) {
      u32x4 a, c, z; u64 m0 = ~0ull, m1 = ~0ull, m2 = ~0ull;
      unsigned sp = 0;
      do {
        asm volatile("s_mov_b64 exec, %8\n\ts_cbranch_execz 1f\n\tbuffer_load_dwordx4 %0, %3, %5, 0 offen sc1\n1:\n\ts_mov_b64 exec, %9\n\ts_cbranch_execz 2f\n\tbuffer_load_dwordx4 %1, %3, %5, 0 offen offset:1280 sc1\n2:\n\t"
                     "s_mov_b64 exec, %10\n\ts_cbranch_execz 3f\n\tbuffer_load_dwordx4 %2, %4, %5, 0 offen sc1\n3:\n\ts_mov_b64 exec, -1\n\ts_waitcnt vmcnt(0)\n\t"
                     "ds_write_b128 %6, %0\n\tds_write_b128 %6, %1 offset:1024\n\tds_write_b128 %7, %2"
                     : "+v"(a), "+v"(c), "+v"(z) : "v"(o2), "v"(oz2), "s"(rs), "v"(wa_), "v"(wz_), "s"(m0), "s"(m1), "s"(m2) : "memory");
        m0 = __ballot(max(max(max(a[0], a[1]), a[2]), a[3]) == SENT); m1 = __ballot(max(max(max(c[0], c[1]), c[2]), c[3]) == SENT); m2 = __ballot(max(max(max(z[0], z[1]), z[2]), z[3]) == SENT);
      } while ((m0 | m1 | m2) != 0 && ++sp < SPIN_CAP);
      asm volatile("" : "+v"(xrt));
    }
    RAW_BARRIER();
    DS_B128(kf[0], kba, 0); DS_B128(zf[0], kbz, 0); DS_B128(kf[1], kba, 1024); DS_B128(zf[1], kbz, 16);
    DS_B128(kf[2], kba, 2048); DS_B128(zf[2], kbz, 32); DS_B128(kf[3], kba, 3072); DS_B128(zf[3], kbz, 48);
    if (w >= 4 && has_gu) asm volatile("buffer_load_dwordx4 %0, %1, %2, 0 offen sc1" : "=v"(gq2) : "v"(ogq), "s"(rs) : "memory");
    gl_m0<0>(oxg, x, lxv); gl_m0<1024>(oxg, x + 8 * DM, lxv);
    __builtin_amdgcn_s_setprio(0);
    f32x16 num = zero;
    float d0 = 0.f, d1 = 0.f;
    static_for<4>([&](auto cc) {
      constexpr int c = decltype(cc)::value;
      asm volatile("s_waitcnt lgkmcnt(%2)" : "+v"(kf[c]), "+v"(zf[c]) : "n"(6 - 2 * c));
      num = MFMA(__builtin_bit_cast(bf16x8, kf[c]), pf[c], num);
      const u32x4 pc = __builtin_bit_cast(u32x4, pf[c]);
      d0 = dot2(zf[c][0], pc[0], d0); d1 = dot2(zf[c][1], pc[1], d1); d0 = dot2(zf[c][2], pc[2], d0); d1 = dot2(zf[c][3], pc[3], d1);
      SB0();
    });
    float den = x32_add(d0 + d1);
    den = rbf(rbf(den) + 1e-6f);
    const float rinv = rcp_nr(den);
    const f32x2 rr = {rinv, rinv};
#pragma unroll
    for (int q = 0; q < 2; ++q) {
      u32x4 u;
#pragma unroll
      for (int e = 0; e < 4; ++e) {
        const f32x2 r = f32x2{num[8 * q + 2 * e], num[8 * q + 2 * e + 1]} * rr;
        u[e] = pk2(r[0], r[1]);
      }
      ST128(u, odo + q * 1024);
      SB0();
    }
  }
  const int p0 = 8 * w + 40;
  const u16* gw2 = wo + (size_t)((p0 ^ (flip * 64)) * 8) * DM + nb * 64; unsigned lw2 = lbase + p0 * 1024;
  asm volatile("" : "+s"(gw2), "+s"(lw2));
  bool glate = false;
  if (w >= 4 && has_gu) {
    asm volatile("s_waitcnt vmcnt(5)" : "+v"(gq) :: "memory");
    gq ^= t8;
    glate = !__all((or4(gq) & 0xFFu) == 0u);
    if (glate) {
      asm volatile("s_waitcnt vmcnt(4)\n\tv_xor_b32 %0, %8, %4\n\tv_xor_b32 %1, %8, %5\n\tv_xor_b32 %2, %8, %6\n\tv_xor_b32 %3, %8, %7"
                   : "=&v"(gq[0]), "=&v"(gq[1]), "=&v"(gq[2]), "=&v"(gq[3]) : "v"(gq2[0]), "v"(gq2[1]), "v"(gq2[2]), "v"(gq2[3]), "s"(t8) : "memory");
      glate = !__all((or4(gq) & 0xFFu) == 0u);
    }
    if (!glate) downp();
    asm volatile("s_waitcnt vmcnt(4)" :: "v"(gq2) : "memory");
  }
  asm volatile("s_waitcnt vmcnt(0)\n\ts_mov_b64 exec, 1\n\tbuffer_store_dwordx2 %0, %1, %2, 0 offen sc1\n\ts_mov_b64 exec, -1" :: "v"(tg), "v"(of4), "s"(rs) : "memory");
  if (__builtin_expect(glate, 0)) {
    for (unsigned sp = 0;;) {
      GQLD("\n\ts_waitcnt vmcnt(0)");
      gq ^= t8;
      if (__all((or4(gq) & 0xFFu) == 0u)) break;
      SPIN_NEXT(sp);
    }
    downp();
  }
  RAW_BARRIER();
  float rxn = 0.f, rsg = 0.f;
  u32x4 yq[7];
  const unsigned oy = WS_YP + (unsigned)(l >> 4) * 4096 + nb * 256 + (l & 15) * 16;
#define YLD(tail) asm volatile("buffer_load_dwordx4 %0, %7, %8, 0 offen sc1\n\tbuffer_load_dwordx4 %1, %7, %8, %9 offen sc1\n\tbuffer_load_dwordx4 %2, %7, %8, %10 offen sc1\n\t" \
    "buffer_load_dwordx4 %3, %7, %8, %11 offen sc1\n\tbuffer_load_dwordx4 %4, %7, %8, %12 offen sc1\n\tbuffer_load_dwordx4 %5, %7, %8, %13 offen sc1\n\t" \
    "buffer_load_dwordx4 %6, %7, %8, %14 offen sc1" tail \
    : "=&v"(yq[0]), "=&v"(yq[1]), "=&v"(yq[2]), "=&v"(yq[3]), "=&v"(yq[4]), "=&v"(yq[5]), "=&v"(yq[6]) \
    : "v"(oy), "s"(rs), "s"(16384), "s"(2 * 16384), "s"(3 * 16384), "s"(4 * 16384), "s"(5 * 16384), "s"(6 * 16384) : "memory")
  f32x2 xf[2][4], gf[2][4];
  __attribute__((address_space(3))) f32x4* pw = (__attribute__((address_space(3))) f32x4*)(smem + 131072 + w * 4096) + l;
  unsigned prd = lbase + 131072 + (w ^ 4) * 4096 + l * 16;
  u16* po = out + (size_t)(p3 * 128 + cw * 32 + fb * 16 + (l >> 3)) * DM + nb * 64 + (l & 7) * 8; u16* po2 = po + 8 * DM;
  unsigned tw0 = lbase + 131072 + (w ^ 4) * 4096 + (l & 15) * 128 + ((ech ^ (l & 7)) * 16), tw1 = tw0 ^ 64, trd = lbase + 131072 + (w ^ 4) * 4096 + (l >> 3) * 128 + (((l & 7) ^ (l >> 3)) * 16);
    unsigned bw[4];
#pragma unroll
    for (int n = 0; n < 4; ++n) bw[n] = lbase + kh * 65536 + (16 * h + 4 * blk + tq) * 128 + (((4 * (n >> 1) + tp) ^ ((tq >> 1) | (blk << 2))) * 16) + 8 * ((n ^ tp) & 1);
  {
  if (w == 1 && has_gu && l < 32) {
    const f32x4* y = (const f32x4*)(smem + X_YR) + l;
    ST128(f2u_tag((y[0] + y[32]) + (y[64] + y[96]), t8), WS_YP + (unsigned)(gr >> 3) * 4096 + ((gr + 1) & 7) * 512 + l * 16);
  }
    SB0();
    gl_run<8>(voO, gw2, lw2);
    u32x2 f0;
    asm volatile("buffer_load_dwordx2 %0, %1, %2, 0 offen sc1" : "=v"(f0) : "v"(op4), "s"(rs) : "memory");
    SB0();
    {
      const f32x2 md = *(const f32x2*)(smem + X_MD + erow * 8);
      { const unsigned ax = lxv + (l & 15) * 128 + ((ech ^ (l & 7)) * 16), a0 = ax + (sw ? 8 : 0), a1 = ax + (sw ? 0 : 8);
        u32x2 q0, q1, q2, q3;
        asm volatile("ds_read_b64 %0, %4\n\tds_read_b64 %1, %5\n\tds_read_b64 %2, %6\n\tds_read_b64 %3, %7\n\ts_waitcnt lgkmcnt(0)" : "=&v"(q0), "=&v"(q1), "=&v"(q2), "=&v"(q3) : "v"(a0), "v"(a1), "v"(a0 ^ 64), "v"(a1 ^ 64) : "memory");
        xv[0] = u32x4{q0[0], q0[1], q1[0], q1[1]}; xv[1] = u32x4{q2[0], q2[1], q3[0], q3[1]}; }
      const float rinv = rcp_nr(own(md[1])), mr = -md[0] * rinv, L2E = 1.44269504088896340736f, rl = -L2E * rinv, ml = -L2E * mr;
#pragma unroll
      for (int i = 0; i < 2; ++i)
#pragma unroll
        for (int e = 0; e < 4; ++e) {
          xf[i][e] = f32x2{bflo(xv[i][e]), bfhi(xv[i][e])};
          const f32x2 xn = __builtin_elementwise_fma(xf[i][e], f32x2{rinv, rinv}, f32x2{mr, mr}), tn = __builtin_elementwise_fma(xf[i][e], f32x2{rl, rl}, f32x2{ml, ml});
          const f32x2 dd = f32x2{__builtin_amdgcn_exp2f(tn[0]), __builtin_amdgcn_exp2f(tn[1])} + f32x2{1.0f, 1.0f};
          gf[i][e] = xn * f32x2{__builtin_amdgcn_rcpf(dd[0]), __builtin_amdgcn_rcpf(dd[1])};
        }
#pragma unroll
      for (int i = 0; i < 2; ++i) asm volatile("" : "+v"(xf[i][0]), "+v"(xf[i][1]), "+v"(xf[i][2]), "+v"(xf[i][3]), "+v"(gf[i][0]), "+v"(gf[i][1]), "+v"(gf[i][2]), "+v"(gf[i][3]));
      asm volatile("" : "+v"(pw), "+v"(prd), "+v"(po2), "+v"(tw0), "+v"(tw1), "+v"(trd));
      asm volatile("" : "+v"(po), "+v"(bw[0]), "+v"(bw[1]), "+v"(bw[2]), "+v"(bw[3]));
    }
    if (has_idx) {
      const unsigned ord = (rkey >> 11) & 0xFFFFu;
      rsg = rbf(1.0f / (1.0f + __expf(-__uint_as_float((ord ^ ((ord & 0x8000u) ? 0x8000u : 0xFFFFu)) << 16))));
      const f32x2 md = *(const f32x2*)(smem + X_MD + (idx & 127) * 8);
      const float dn = own(md[1]), rinv = rcp_nr(dn);
      rxn = bflo(ln_pair(xrt, md[0], dn, rinv));
      asm volatile("" : "+v"(rxn), "+v"(rsg));
      { u64 pc_; unsigned d_, o_;
        asm volatile("s_getpc_b64 %0\n.Lkf_own1:\n\ts_mov_b32 %1, .Lkf_own0-.Lkf_own1" : "=s"(pc_), "=s"(o_));
        pc_ += o_;
        asm volatile("s_load_dword %0, %1, 0x0\n\ts_load_dword %0, %1, 0x80\n\ts_load_dword %0, %1, 0x100\n\ts_load_dword %0, %1, 0x180\n\ts_load_dword %0, %1, 0x200\n\ts_load_dword %0, %1, 0x280\n\ts_waitcnt lgkmcnt(0)" : "=&s"(d_) : "s"(pc_) : "memory"); }
    }
    u64 ne;
    SB0();
    asm volatile("s_waitcnt vmcnt(0)\n\tv_cmp_ne_u64 %0, %1, %2" : "=s"(ne) : "v"(f0), "v"(tg) : "memory");
    for (unsigned sp = 0; ne != 0 && sp < SPIN_CAP; ++sp) {
      u32x2 f;
      asm volatile("buffer_load_dwordx2 %1, %2, %3, 0 offen sc1\n\ts_waitcnt vmcnt(0)\n\tv_cmp_ne_u64 %0, %1, %4" : "=s"(ne), "=&v"(f) : "v"(op4), "s"(rs), "v"(tg) : "memory");
    }
  }
  constexpr int OWIN = 4;
  u32x4 of[16][2];
  {
    SB0();
#pragma unroll
    for (int i = 0; i < OWIN; ++i) { of[i][0] = LD128(oo0 + i * 2048); of[i][1] = LD128(oo1 + i * 2048); }
    SB0();
    if (has_idx) YLD("");
    SB0();
  }
    auto owner = [&](auto nc) __attribute__((always_inline)) {
      asm volatile(".Lkf_own0:\n\ts_waitcnt vmcnt(%7)" : "+v"(yq[0]), "+v"(yq[1]), "+v"(yq[2]), "+v"(yq[3]), "+v"(yq[4]), "+v"(yq[5]), "+v"(yq[6]) : "n"(decltype(nc)::value) : "memory");
      for (unsigned sp = 0;; ++sp) {
        unsigned bad = 0;
#pragma unroll
        for (int i = 0; i < 7; ++i) { yq[i] ^= t8; bad |= or4(yq[i]); }
        if (__builtin_expect(__all((bad & 0xFFu) == 0u) || sp > SPIN_CAP, 1)) break;
        YLD("\n\ts_waitcnt vmcnt(0)");
      }
#define YF(i) __builtin_bit_cast(f32x4, yq[i])
      const f32x4 y4 = ((YF(0) + YF(1)) + (YF(2) + YF(3))) + ((YF(4) + YF(5)) + YF(6));
      const auto r01 = __builtin_amdgcn_permlane16_swap(__float_as_uint(y4[0]), __float_as_uint(own(y4[1])), false, false);
      const auto r23 = __builtin_amdgcn_permlane16_swap(__float_as_uint(y4[2]), __float_as_uint(own(y4[3])), false, false);
      const float t01 = __uint_as_float(r01[0]) + __uint_as_float(r01[1]), t23 = __uint_as_float(r23[0]) + __uint_as_float(r23[1]);
      const auto r = __builtin_amdgcn_permlane32_swap(__float_as_uint(t01), __float_as_uint(t23), false, false);
      const float y = __uint_as_float(r[0]) + __uint_as_float(r[1]);
      const float g1 = rbf(silu_r(rbf(rxn + rbf(rbf(y) * rsg))));
      (void)&gf; (void)&oy; (void)&rs; const u64 mk_ = 0x0001000100010001ull << (idx & 15);
      const unsigned gb = lbase + (131072 + w * 4096), aw_ = gb + (l & 15) * 16 + ech * 4, a0 = gb + ech * 32 + (ech & 1) * 16, a1 = a0 ^ 16;
      asm volatile("ds_write_b32 %8, %9\n\ts_mov_b64 exec, %12\n\ts_waitcnt lgkmcnt(0)\n\t"
                   "ds_read_b64 %0, %10\n\tds_read_b64 %1, %10 offset:8\n\tds_read_b64 %2, %11\n\tds_read_b64 %3, %11 offset:8\n\t"
                   "ds_read_b64 %4, %10 offset:128\n\tds_read_b64 %5, %10 offset:136\n\tds_read_b64 %6, %11 offset:128\n\tds_read_b64 %7, %11 offset:136\n\t"
                   "s_mov_b64 exec, -1\n\ts_waitcnt lgkmcnt(0)"
                   : "+v"(gf[0][0]), "+v"(gf[0][1]), "+v"(gf[0][2]), "+v"(gf[0][3]), "+v"(gf[1][0]), "+v"(gf[1][1]), "+v"(gf[1][2]), "+v"(gf[1][3])
                   : "v"(aw_), "v"(g1), "v"(a0), "v"(a1), "s"(mk_) : "memory");
    };
  f32x4 acc3[2][4];
#pragma unroll
  for (int t = 0; t < 2; ++t)
#pragma unroll
    for (int n = 0; n < 4; ++n) acc3[t][n] = f32x4{0.f, 0.f, 0.f, 0.f};
  {
    s16x4 wa[2][8];
    auto rd3 = [&](auto kc, auto nc) {
      constexpr int k = decltype(kc)::value, n = decltype(nc)::value;
      (void)&wa; (void)&bw;
      DS_TR(wa[k & 1][2 * n], bw[n], k * 4096); DS_TR(wa[k & 1][2 * n + 1], bw[n], k * 4096 + 1024);
    };
    RAW_BARRIER();
    static_for<4>([&](auto nc) { rd3(IC(0), nc); });
    static_for<16>([&](auto kc) {
      constexpr int k = decltype(kc)::value, sx = k & 1;
      static_for<4>([&](auto nc) {
        constexpr int n = decltype(nc)::value;
        asm volatile("s_waitcnt lgkmcnt(%2)" : "+v"(wa[sx][2 * n]), "+v"(wa[sx][2 * n + 1]) : "n"(k < 15 ? 6 : n < 2 ? 6 - 2 * n : 5 - n));
        SB0();
        const bf16x8 wf = cat8(wa[sx][2 * n], wa[sx][2 * n + 1]);
        acc3[0][n] = MFMA16(wf, __builtin_bit_cast(bf16x8, of[k][0]), acc3[0][n]);
        SB0();
        if constexpr (n == 0 && (k & 3) == 0) __builtin_amdgcn_s_setprio(3 - (k >> 2));
        if constexpr (k + 1 < 16) rd3(IC(k + 1), nc);
        if constexpr (k + OWIN < 16 && n == 3) { of[k + OWIN][0] = LD128(oo0 + (k + OWIN) * 2048); }
        SB0();
        acc3[1][n] = MFMA16(wf, __builtin_bit_cast(bf16x8, of[k][1]), acc3[1][n]);
        SB0();
        if constexpr (k + OWIN < 16 && n == 3) { of[k + OWIN][1] = LD128(oo1 + (k + OWIN) * 2048); SB0(); }
        if constexpr (k == YK && n == 3) { (void)&yq; (void)&oy; (void)&rs; if (has_idx) YLD(""); SB0(); }
        if constexpr (k == YP_ && n == 3) { constexpr int e_ = YP_ + OWIN < 16 ? YP_ : 15 - OWIN; if (has_idx) owner(IC(2 * (e_ - YK))); SB0(); }
        if constexpr (k == 15 && n > 0) { pw[(n - 1) * 64] = acc3[1][n - 1]; SB0(); }
      });
    });
  }
  {
    pw[192] = acc3[1][3];
    SB0();
    SB0();
#pragma unroll
    for (int i = 0; i < 2; ++i)
#pragma unroll
      for (int e = 0; e < 4; ++e) {
        const f32x4 a = acc3[0][2 * i + (e >> 1)];
        xf[i][e] = __builtin_elementwise_fma(f32x2{a[2 * (e & 1)], a[2 * (e & 1) + 1]}, gf[i][e], xf[i][e]);
        asm volatile("" : "+v"(xf[i][e]));
      }
    RAW_BARRIER();
    f32x4 pr[4];
    DS_B128(pr[0], prd, 0); DS_B128(pr[1], prd, 1024); DS_B128(pr[2], prd, 2048); DS_B128(pr[3], prd, 3072);
    SB0();
    u32x4 res[2];
#pragma unroll
    for (int i = 0; i < 2; ++i) {
      if (i == 0) asm volatile("s_waitcnt lgkmcnt(2)" : "+v"(pr[0]), "+v"(pr[1])); else asm volatile("s_waitcnt lgkmcnt(1)" : "+v"(pr[2]), "+v"(pr[3]));
      const f32x4 s0 = pr[2 * i], s1 = pr[2 * i + 1];
      const f32x2 o0 = __builtin_elementwise_fma(f32x2{s0[0], s0[1]}, gf[i][0], xf[i][0]), o1 = __builtin_elementwise_fma(f32x2{s0[2], s0[3]}, gf[i][1], xf[i][1]);
      const f32x2 o2 = __builtin_elementwise_fma(f32x2{s1[0], s1[1]}, gf[i][2], xf[i][2]), o3 = __builtin_elementwise_fma(f32x2{s1[2], s1[3]}, gf[i][3], xf[i][3]);
      res[i] = u32x4{pk2(o0[0], o0[1]), pk2(o1[0], o1[1]), pk2(o2[0], o2[1]), pk2(o3[0], o3[1])};
      FSWAP(res[i]);
      asm volatile("ds_write_b128 %0, %1" :: "v"(i ? tw1 : tw0), "v"(res[i]) : "memory");
    }
    asm volatile("s_waitcnt lgkmcnt(0)\n\tds_read_b128 %0, %2\n\tds_read_b128 %1, %2 offset:1024\n\ts_waitcnt lgkmcnt(1)\n\t"
                 "global_store_dwordx4 %3, %0, off sc1\n\ts_waitcnt lgkmcnt(0)\n\tglobal_store_dwordx4 %4, %1, off sc1\n\ts_nop 1"
                 : "=&v"(res[0]), "=&v"(res[1]) : "v"(trd), "v"(po), "v"(po2) : "memory");
  }
  asm volatile(".Lkf_pf_end:" ::: "memory");
}

extern "C" void kernel_launch(void* const* d_in, const int* in_sizes, int n_in,
                              void* d_out, int out_size, void* d_ws, size_t ws_size,
                              hipStream_t stream) {
  (void)hipFuncSetAttribute((const void*)kf_all, hipFuncAttributeMaxDynamicSharedMemorySize, LDS_TOTAL);
  kf_all<<<256, 512, LDS_TOTAL, stream>>>((const u16*)d_in[0], (const u16*)d_in[1], (const u16*)d_in[2], (const u16*)d_in[3],
                                          (const u16*)d_in[5], (const u16*)d_in[6], (const u16*)d_in[7], (const u16*)d_in[8],
                                          (char*)d_ws, (u16*)d_out);
}
```

```cpp
#define I1 10
#define I2 10
#define I3 10
#define I4 10
#define I5 10
#define I6 10
#define I7 10
#define WOK 2
#define S2A 10
#define KP 9
#define KC 10
#define F1_ST 26
#define PF2_KT 8
#define H1_DELAY 8
#define PACE 1
constexpr int PACE_T[16] = {0,108,72,60};
constexpr int PACE_3[16] = {0,60,50,50};
constexpr int DVN[20] = {0,2,2,2,0,0,1,0,1};
constexpr int dv_first(int k) { int n = 0; for (int i = -2; i < k; ++i) n += DVN[i + 2]; return n; }
constexpr bool CSS(int S) { return S < 2 || S == 6 || S == 7; }
#define NREG 12
#define WH0 10
#define WPOST 4
#define YK -1
#define YP_ 11
#define PRIO_T 2
#define PRIO_H 1
#include <hip/hip_runtime.h>
#include <hip/hip_bf16.h>
#include <stdint.h>
#include <utility>
#include <type_traits>

typedef __attribute__((ext_vector_type(8))) short bf16x8;
typedef __attribute__((ext_vector_type(4))) short s16x4;
typedef __attribute__((ext_vector_type(16))) float f32x16;
typedef __attribute__((ext_vector_type(4))) float f32x4;
typedef __attribute__((ext_vector_type(2))) float f32x2;
typedef __attribute__((ext_vector_type(4))) unsigned u32x4;
typedef __attribute__((ext_vector_type(2))) unsigned u32x2;
typedef __attribute__((ext_vector_type(2))) __bf16 bfx2;
typedef __attribute__((ext_vector_type(4))) short v4i16_t;
typedef unsigned long long u64;
typedef unsigned short u16;

#define DI __device__ __forceinline__
#define MFMA(a, b, c) __builtin_amdgcn_mfma_f32_32x32x16_bf16((a), (b), (c), 0, 0, 0)
#define MFMA16H(a, b, c) __builtin_amdgcn_mfma_f32_16x16x16bf16_1k((a), (b), (c), 0, 0, 0)
#define MFMA16(a, b, c) __builtin_amdgcn_mfma_f32_16x16x32_bf16((a), (b), (c), 0, 0, 0)

DI unsigned pk2(float a, float b) { f32x2 v = {a, b}; return __builtin_bit_cast(unsigned, __builtin_convertvector(v, bfx2)); }
DI float bflo(unsigned u) { return __uint_as_float(u << 16); }
DI float bfhi(unsigned u) { return __uint_as_float(u & 0xffff0000u); }
DI float rbf(float x) { return (float)(__bf16)x; }
DI s16x4 trread(const char* p) {
  return __builtin_bit_cast(s16x4, __builtin_amdgcn_ds_read_tr16_b64_v4i16((__attribute__((address_space(3))) v4i16_t*)p));
}
DI bf16x8 cat8(s16x4 lo, s16x4 hi) { return __builtin_shufflevector(lo, hi, 0, 1, 2, 3, 4, 5, 6, 7); }
#define WAIT_V(n) asm volatile("s_waitcnt vmcnt(%0)" ::"n"(n) : "memory")
#define RAW_BARRIER() do { asm volatile("s_waitcnt lgkmcnt(0)" ::: "memory"); __builtin_amdgcn_s_barrier(); asm volatile("" ::: "memory"); } while (0)
#define DS_B128(dst, addr, off) asm volatile("ds_read_b128 %0, %1 offset:%2" : "=v"(dst) : "v"(addr), "n"(off))
#define DS_TR(dst, addr, off) asm volatile("ds_read_b64_tr_b16 %0, %1 offset:%2" : "=v"(dst) : "v"(addr), "n"(off))
#define LDS_OFF(p) ((unsigned)(uintptr_t)(__attribute__((address_space(3))) const char*)(p))
template <class F, int... I> DI void static_for_impl(F&& f, std::integer_sequence<int, I...>) { (f(std::integral_constant<int, I>{}), ...); }
template <int N, class F> DI void static_for(F&& f) { static_for_impl(f, std::make_integer_sequence<int, N>{}); }
DI float divc(float a, float b, float rinv) { const float q = a * rinv; return __builtin_fmaf(__builtin_fmaf(-q, b, a), rinv, q); }
DI float rcp_nr(float d) { const float r = __builtin_amdgcn_rcpf(d); return __builtin_fmaf(__builtin_fmaf(-d, r, 1.0f), r, r); }
DI float own(float v) { asm volatile("" : "+v"(v)); return v; }
DI float silu_r(float x) { return x * __builtin_amdgcn_rcpf(1.0f + __expf(-x)); }
#define SB0() __builtin_amdgcn_sched_barrier(0)
#define GLDS_S(vo, sp, ld) asm volatile("s_mov_b32 m0, %2\n\ts_nop 0\n\tglobal_load_lds_dwordx4 %0, %1" :: "v"(vo), "s"(sp), "s"(ld) : "m0", "memory")
template <int IMM> DI void gl_m0(unsigned vo, const void* sp, unsigned ls) {
  asm volatile("s_add_u32 m0, %2, %3\n\ts_nop 0\n\tglobal_load_lds_dwordx4 %0, %1" :: "v"(vo), "s"(sp), "s"(ls), "n"(IMM) : "m0", "memory", "scc");
}
template <int IMM> DI void gl_nt(unsigned vo, const void* sp, unsigned ls) {
  asm volatile("s_add_u32 m0, %2, %3\n\ts_nop 0\n\tglobal_load_lds_dwordx4 %0, %1 nt" :: "v"(vo), "s"(sp), "s"(ls), "n"(IMM) : "m0", "memory", "scc");
}
template <int N> DI void gl_run(unsigned vo, const void* sp, unsigned ls) {
  unsigned lo = (unsigned)(uintptr_t)sp, hi = (unsigned)((uintptr_t)sp >> 32);
  static_for<N>([&](auto ic) {
    constexpr int i = decltype(ic)::value;
    gl_m0<i * 1024>(vo, (const void*)(((uintptr_t)hi << 32) | lo), ls);
    if constexpr (i + 1 < N) asm volatile("s_add_u32 %0, %0, 0x4000\n\ts_addc_u32 %1, %1, 0" : "+s"(lo), "+s"(hi) :: "scc");
  });
}
#define GLDS_SC1(vo, sp, ld) asm volatile("s_mov_b32 m0, %2\n\ts_nop 0\n\tglobal_load_lds_dwordx4 %0, %1 sc1" :: "v"(vo), "s"(sp), "s"(ld) : "m0", "memory")
DI float x32_add(float v) { auto r = __builtin_amdgcn_permlane32_swap(__float_as_uint(v), __float_as_uint(v), false, false); return __uint_as_float(r[0]) + __uint_as_float(r[1]); }
DI float x32_max(float v) { auto r = __builtin_amdgcn_permlane32_swap(__float_as_uint(v), __float_as_uint(v), false, false); float o; asm("v_max_f32 %0, %1, %2" : "=v"(o) : "v"(r[0]), "v"(r[1])); return o; }
DI float x32_min(float v) { auto r = __builtin_amdgcn_permlane32_swap(__float_as_uint(v), __float_as_uint(v), false, false); float o; asm("v_min_f32 %0, %1, %2" : "=v"(o) : "v"(r[0]), "v"(r[1])); return o; }
DI float x16_add(float v) { auto r = __builtin_amdgcn_permlane16_swap(__float_as_uint(v), __float_as_uint(v), false, false); return __uint_as_float(r[0]) + __uint_as_float(r[1]); }
template <int CTRL, int RMASK> DI float dpp_add(float v) {
  return v + __builtin_bit_cast(float, __builtin_amdgcn_update_dpp(0, __builtin_bit_cast(int, v), CTRL, RMASK, 0xf, true));
}

DI unsigned ln_pair(unsigned u, float m, float den, float rinv) {
  const f32x2 mm = {m, m}, dd = {den, den}, rr = {rinv, rinv};
  const f32x2 d0 = f32x2{bflo(u), bfhi(u)} - mm;
  const unsigned du = pk2(d0[0], d0[1]);
  const f32x2 d = {bflo(du), bfhi(du)};
  const f32x2 q = d * rr;
  const f32x2 r = __builtin_elementwise_fma(__builtin_elementwise_fma(-q, dd, d), rr, q);
  return pk2(r[0], r[1]);
}
DI float dot2(unsigned a, unsigned b, float c) { return __builtin_amdgcn_fdot2_f32_bf16(__builtin_bit_cast(bfx2, a), __builtin_bit_cast(bfx2, b), c, false); }
DI float half_total(float v) {
  v = dpp_add<0x121, 0xf>(v); v = dpp_add<0x122, 0xf>(v); v = dpp_add<0x124, 0xf>(v); v = dpp_add<0x128, 0xf>(v);
  return x16_add(v);
}
DI void ln_rows(const u32x4 (&xr)[4], const u32x4 (&wr)[4], int h, float& m, float& den, float& rinv, float& r) {
  const unsigned c0 = __builtin_amdgcn_readlane(xr[0][0], 0) << 16, c1 = __builtin_amdgcn_readlane(xr[0][0], 32) << 16;
  const float c = __uint_as_float(h ? c1 : c0);
  const f32x2 cc = {c, c};
  float s1[2] = {0.f, 0.f}, sx[2] = {0.f, 0.f}, sw[2] = {0.f, 0.f};
  f32x2 q[2] = {{0.f, 0.f}, {0.f, 0.f}};
#pragma unroll
  for (int i = 0; i < 4; ++i)
#pragma unroll
    for (int e = 0; e < 4; ++e) {
      const unsigned u = xr[i][e];
      s1[e & 1] = dot2(u, 0x3F803F80u, s1[e & 1]); sx[e & 1] = dot2(u, wr[i][e], sx[e & 1]); sw[e & 1] = dot2(wr[i][e], 0x3F803F80u, sw[e & 1]);
      const f32x2 d = f32x2{bflo(u), bfhi(u)} - cc;
      q[e & 1] = __builtin_elementwise_fma(d, d, q[e & 1]);
    }
  const f32x2 q2 = q[0] + q[1];
  const float mean = half_total(s1[0] + s1[1]) * (1.0f / 1024.0f), m2 = half_total(q2[0] + own(q2[1])) * (1.0f / 1024.0f);
  const float tx = half_total(sx[0] + sx[1]), tw = half_total(sw[0] + sw[1]);
  const float dm = mean - c;
  const float var = fmaxf(__builtin_fmaf(-dm, dm, m2), 0.f);
  m = rbf(mean);
  den = rbf(sqrtf(rbf(rbf(var) + 1e-5f)));
  rinv = rcp_nr(den);
  r = rbf(divc(__builtin_fmaf(-m, tw, tx), den, rinv));
}

template <bool RND, class HK> DI void phi_fn(const bf16x8 (&wp)[2], const bf16x8 f0, const bf16x8 f1, f32x2 (&ep)[8], f32x2 (&en)[8], HK&& hk) {
  f32x16 p;
#pragma unroll
  for (int i = 0; i < 16; ++i) p[i] = 0.f;
  p = MFMA(wp[0], f0, p);
  p = MFMA(wp[1], f1, p);
  hk(std::integral_constant<int, 0>{});
  float mx = -3.0e38f, mn = 3.0e38f;
  f32x2 p2[8];
  static_for<8>([&](auto ic) {
    constexpr int i = decltype(ic)::value;
    if constexpr (RND) { const unsigned u = pk2(p[2 * i], p[2 * i + 1]); p2[i] = f32x2{bflo(u), bfhi(u)}; }
    else p2[i] = f32x2{p[2 * i], p[2 * i + 1]};
    mx = fmaxf(fmaxf(mx, p2[i][0]), p2[i][1]); mn = fminf(fminf(mn, p2[i][0]), p2[i][1]);
    if constexpr (i & 1) hk(std::integral_constant<int, 1 + (i >> 1)>{});
  });
  mx = x32_max(mx); mn = x32_min(mn);
  hk(std::integral_constant<int, 5>{});
  const float L2E = 1.44269504088896340736f, mxl = -mx * L2E, mnl = mn * L2E;
  const f32x2 cp = {L2E, L2E}, cn = {-L2E, -L2E}, bp = {mxl, mxl}, bn = {mnl, mnl};
  f32x2 sp2, sn2;
  static_for<8>([&](auto ic) {
    constexpr int i = decltype(ic)::value;
    const f32x2 tp2 = __builtin_elementwise_fma(p2[i], cp, bp), tn2 = __builtin_elementwise_fma(p2[i], cn, bn);
    ep[i] = f32x2{__builtin_amdgcn_exp2f(tp2[0]), __builtin_amdgcn_exp2f(tp2[1])};
    en[i] = f32x2{__builtin_amdgcn_exp2f(tn2[0]), __builtin_amdgcn_exp2f(tn2[1])};
    if constexpr (i == 0) { sp2 = ep[0]; sn2 = en[0]; } else { sp2 += ep[i]; sn2 += en[i]; }
    hk(std::integral_constant<int, 6 + i>{});
  });
  const float sp = x32_add(sp2[0] + own(sp2[1])), sn = x32_add(sn2[0] + own(sn2[1]));
  hk(std::integral_constant<int, 14>{});
  const float rp = __builtin_amdgcn_rcpf(sp), rn = __builtin_amdgcn_rcpf(sn);
  const f32x2 rp2 = {rp, rp}, rn2 = {rn, rn};
  hk(std::integral_constant<int, 15>{});
#pragma unroll
  for (int i = 0; i < 8; ++i) { ep[i] = ep[i] * rp2; en[i] = en[i] * rn2; }
  hk(std::integral_constant<int, 16>{});
}

constexpr int DM = 1024, FF = 3584;
constexpr unsigned SENT = 0xFFFFFFFFu;
constexpr unsigned SPIN_CAP = 1u << 17;

constexpr unsigned WS_EPOCH = 0;
constexpr unsigned WS_F1 = 4096;
constexpr unsigned WS_F4 = 896u << 10;
constexpr unsigned WS_STAT = 20480;
constexpr unsigned WS_CS = 36864;
constexpr unsigned WS_GUP = WS_CS + 16 * 16 * 768;
constexpr unsigned WS_YP = WS_GUP + 256 * 512;
constexpr unsigned WS_KVF = WS_YP + 32 * 4096;
constexpr unsigned WS_ZP = WS_KVF + 256 * 640;
constexpr unsigned WS_XA = 800u << 10;
constexpr unsigned WS_RI = 840u << 10;
static_assert(WS_ZP + 131072 <= WS_XA && WS_XA + 32768 <= WS_RI, "ws map");
constexpr unsigned WS_KVP = 1u << 20;
constexpr unsigned WS_O = 8u << 20;
constexpr unsigned WS_END = 16u << 20;

constexpr int ABYTES = 128 * 128, WSLAB = 64 * 128, STAGE1 = ABYTES + 3 * WSLAB, NST1 = 3;
constexpr int XB = NST1 * STAGE1;
constexpr int X_WO = XB;
constexpr int X_CSP = XB + 8192;
constexpr int X_P = 131072 + 4096;
constexpr int X_XW = X_P;
constexpr int X_XA = X_XW + 6144;
constexpr int X_ST = X_XA + 2048;
constexpr int X_MD = X_ST + 1024;
constexpr int X_AM = X_MD + 1024;
constexpr int X_XS = X_AM + 64;
constexpr int X_YR = X_XS + 512;
constexpr int X_YS = X_YR + 2048;
constexpr int X_WP = X_YS + 2048;
constexpr int X_KB = X_WP + 4096;
constexpr int LDS_TOTAL = 163840;
static_assert(X_KB + 2 * 4224 <= LDS_TOTAL && 131072 + 6 * 2048 <= X_MD && X_YS + 64 + 4096 <= X_KB && X_YS % 128 == 64, "lds");
static_assert(LDS_TOTAL <= 163840, "lds");
constexpr int gu_at(int ks) { return STAGE1 + ks * 8192; }
constexpr int HANDOFF = 106496;

#define LD128(off) __builtin_amdgcn_raw_buffer_load_b128(rs, (int)(off), 0, 16)
#define ST128(v, off) __builtin_amdgcn_raw_buffer_store_b128((v), rs, (int)(off), 0, 16)
#define ST64(v, off) __builtin_amdgcn_raw_buffer_store_b64((v), rs, (int)(off), 0, 16)
DI u32x4 f2u_tag(f32x4 v, unsigned t8) { u32x4 u = __builtin_bit_cast(u32x4, v); return (u & 0xFFFFFF00u) | t8; }
DI unsigned or4(u32x4 v) { return (v[0] | v[1]) | (v[2] | v[3]); }
#define SPIN_NEXT(sp) { if (++(sp) > SPIN_CAP) break; __builtin_amdgcn_s_sleep(1); }
#define TAG_OF(en) ((en) * 0x9E3779B97F4A7C15ull)

__global__ __launch_bounds__(512, 2) void kf_all(const u16* __restrict__ x, const u16* __restrict__ wqkv, const u16* __restrict__ wphi,
                                              const u16* __restrict__ wo, const u16* __restrict__ w_r, const u16* __restrict__ w_gate,
                                              const u16* __restrict__ w_up, const u16* __restrict__ w_down, char* ws, u16* __restrict__ out) {
  extern __shared__ __attribute__((aligned(16))) char smem[];
  asm volatile(".Lkf_pf_start:");
  const int b = blockIdx.x;
  const int xcd = b & 7, jj = b >> 3;
  const int panel = (xcd >> 2) * 8 + (jj & 7), hp = (xcd & 3) * 4 + (jj >> 3);
  const int id = panel * 16 + hp;
  const int tid = threadIdx.x, l = tid & 63, w = __builtin_amdgcn_readfirstlane(tid >> 6);
  unsigned pfd;
  {
    u64 pc; unsigned len, back;
    asm volatile("s_getpc_b64 %0\n.Lkf_pf_here:\n\ts_mov_b32 %1, .Lkf_pf_end+1152-.Lkf_pf_start\n\ts_mov_b32 %2, .Lkf_pf_here-.Lkf_pf_start" : "=s"(pc), "=s"(len), "=s"(back));
    unsigned o = (unsigned)(jj * 8 + w) * 256u;
    o = o + 256u <= len ? o : len - 256u;
    const u64 pa = (pc - back + o) & ~63ull;
    asm volatile("s_load_dword %0, %1, 0x0\n\ts_load_dword %0, %1, 0x40\n\ts_load_dword %0, %1, 0x80\n\ts_load_dword %0, %1, 0xc0" : "=&s"(pfd) : "s"(pa) : "memory");
  }
  asm volatile("" : "+s"(x), "+s"(wqkv), "+s"(wphi), "+s"(wo), "+s"(w_r), "+s"(w_gate), "+s"(w_up), "+s"(w_down), "+s"(ws), "+s"(out));
  SB0();
#define GLD(dst, p) asm volatile("global_load_dwordx4 %0, %1, off" : "=v"(dst) : "v"(p) : "memory")
  u32x4 xr4[4], wr4[4];
  if (w < 4) {
    const u16* xr = x + (size_t)(panel * 128 + hp * 8 + (tid >> 5)) * DM + (l & 31) * 8;
    const u16* wr = w_r + (l & 31) * 8;
    GLD(xr4[0], xr); GLD(xr4[1], xr + 256); GLD(xr4[2], xr + 512); GLD(xr4[3], xr + 768);
    GLD(wr4[0], wr); GLD(wr4[1], wr + 256); GLD(wr4[2], wr + 512); GLD(wr4[3], wr + 768);
  }
  SB0();
  const int cw = w & 3, wm = cw & 1, wn = cw >> 1;
  const int h = l >> 5, r31 = l & 31, blk = (l >> 4) & 1, tq = (l & 15) >> 2, tp = l & 3;
  const int hd = hp * 2 + wn;
  const unsigned lbase = LDS_OFF(smem);
  const __amdgpu_buffer_rsrc_t rs = __builtin_amdgcn_make_buffer_rsrc((void*)ws, 0, (int)WS_END, 0x00020000);
  bf16x8 fr[4];
  bf16x8 wp[2];
  f32x4 qa[4][2]; float ri[4];
  const int kh = w >> 2;
  const int wt = wm * 2 + kh;
  char* img = smem + (wn * 4 + wt) * 4608 + r31 * 144 + h * 8;
  auto wpload = [&]() {
    const char* p = smem + X_WP + wn * 2048 + (8 * h + tq) * 64 + (16 * blk + 4 * tp) * 2;
    wp[0] = cat8(trread(p), trread(p + 256)); wp[1] = cat8(trread(p + 1024), trread(p + 1280));
  };
  auto imgw = [&](int g, const f32x2 (&ep)[8], const f32x2 (&en)[8]) {
    const u32x2 vp = {pk2(ep[2 * g][0], ep[2 * g][1]), pk2(ep[2 * g + 1][0], ep[2 * g + 1][1])};
    const u32x2 vn = {pk2(en[2 * g][0], en[2 * g][1]), pk2(en[2 * g + 1][0], en[2 * g + 1][1])};
    *(u32x2*)(img + g * 16) = vp;
    *(u32x2*)(img + 64 + g * 16) = vn;
  };
  auto pack_t = [&](const f32x4 x0, const f32x4 x1, float r0, float r1) {
    const f32x2 a0 = f32x2{x0[0], x0[1]} * f32x2{r0, r0}, a1 = f32x2{x0[2], x0[3]} * f32x2{r0, r0};
    const f32x2 b0 = f32x2{x1[0], x1[1]} * f32x2{r1, r1}, b1 = f32x2{x1[2], x1[3]} * f32x2{r1, r1};
    const auto s0 = __builtin_amdgcn_permlane16_swap(pk2(a0[0], a0[1]), pk2(b0[0], b0[1]), false, false);
    const auto s1 = __builtin_amdgcn_permlane16_swap(pk2(a1[0], a1[1]), pk2(b1[0], b1[1]), false, false);
    return u32x4{s0[0], s1[0], s0[1], s1[1]};
  };
  const int hr = hp ^ 8;
  const int p3 = ((xcd >> 1) * 4 + (jj & 3)) ^ 8, nb = (int)(((xcd & 1) ? 0xF7EDCA98u : 0xB3654210u) >> (jj & 28)) & 15;
  const int flip = jj & 1;
  const unsigned voO = (unsigned)((l >> 3) * DM + (((l & 7) ^ (((l >> 4) & 1) | ((l >> 5) << 2))) * 8)) * 2;
  const bool has_gu = (hp & 7) != 3;
  const int gr = !has_gu ? 224 + (jj - 24) * 4 + (xcd >> 1) : jj >= 28 ? 8 * (4 * (xcd >> 1) + jj - 16) + 6 : (xcd == 6 && jj >= 12) ? 8 * (24 + (jj & 3)) + 2 * ((jj >> 2) - 3) : b;
  const int gkq = (gr / 56) & 3;
  u32x4 dv[8]; unsigned ogq;
  u32x4 wr_[16];
    const int srow = l >> 3, sch = l & 7;
    unsigned voA[4], voW[6];
#pragma unroll
    for (int i = 0; i < 4; ++i) voA[i] = (unsigned)((panel * 128 + (4 * cw + i) * 8 + srow) * DM + ((sch ^ (4 * (i & 1) + (srow >> 1))) * 8)) * 2;
#pragma unroll
    for (int i = 0; i < 6; ++i) voW[i] = (unsigned)(((2 * cw + (i & 1)) * 8 + srow) * 3072 + (i >> 1) * 1024 + hp * 64 + ((sch ^ ((((srow >> 1) & 1) << 2) | ((i & 1) << 1))) * 8)) * 2;
    const unsigned lwa = lbase + cw * 4096, lww = lbase + ABYTES + cw * 2048;
    const int rot = 4 * ((panel & 7) >> 1) + 2 * (panel >> 3) + 4 * (xcd & 3);
    const u64 kord = !has_gu ? (0xFB73EA6D95C21840ull ^ ((panel & 1) ? 0x1111111111111111ull : 0ull)) : (panel & 8) ? ((panel & 1) ? 0xD95FB73EA62C1840ull : 0xD95FB73EA62C1840ull) : (panel & 1) ? 0xFB73EA62D95C1840ull : 0xFB73EA62D95C1840ull;
    auto stagep = [&](int it, auto bufc, auto loc, auto hic) {
      constexpr int B = decltype(bufc)::value * STAGE1, lo = decltype(loc)::value, hi = decltype(hic)::value;
      const int kt = ((int)(kord >> (4 * it)) + rot) & 15;
      const u16* gx = x + kt * 64; const u16* gq_ = wqkv + (size_t)kt * 64 * 3072;
      static_for<hi - lo>([&](auto ic) {
        constexpr int i = lo + decltype(ic)::value;
        if constexpr (i < 4) gl_m0<B + i * 1024>(voA[i], gx, lwa);
        else gl_m0<B + ((i - 4) >> 1) * WSLAB + (i & 1) * 1024>(voW[i - 4], gq_, lww);
      });
    };
    auto stage = [&](int it, auto bufc) { stagep(it, bufc, std::integral_constant<int, 0>{}, std::integral_constant<int, 10>{}); };
  unsigned pa0, pvh, dst, dz, pgw, ogu, oh1, oh1z;
  auto kvsetup = [&]() {
    const int g = l >> 4, c15 = l & 15;
    pa0 = lbase + kh * 18432 + (4 * g + tq) * 144 + (4 * cw + tp) * 8;
    pvh = lbase + HANDOFF + kh * 12288 + l * 16;
    dst = WS_KVP + (unsigned)((hp * 16 + panel) * 16 + w * 2 + (g & 1)) * 512 + c15 * 32 + (g >> 1) * 16;
    dz = WS_ZP + (unsigned)((hp * 16 + w * 2 + (g & 1)) * 16 + panel) * 32 + (g >> 1) * 16;
    pgw = lbase + gu_at(0) + kh * 4096 + (8 * g + tq) * 128 + (((2 * cw + (tp >> 1)) ^ ((((tq >> 1) & 1) << 2) | ((g & 1) << 1))) * 16) + 8 * (tp & 1);
    ogu = WS_GUP + (unsigned)gr * 512 + w * 64 + g * 16;
    oh1 = WS_KVP + (unsigned)((hr * 16 + (l >> 3)) * 16 + panel) * 512 + cw * 128 + (l & 7) * 16;
    oh1z = WS_ZP + (unsigned)((hr * 16 + panel) * 16) * 32 + l * 4;
    asm volatile("" : "+v"(pa0), "+v"(pvh), "+v"(dst), "+v"(dz), "+v"(pgw), "+v"(ogu), "+v"(oh1), "+v"(oh1z));
  };
#define IC(n) std::integral_constant<int, (n)>{}
#define CODE_PF(tagn, FROM, d) do { u64 pc_; unsigned len_; int off_; \
    asm volatile("s_getpc_b64 %0\n.Lkf_pf_h" #tagn ":\n\ts_mov_b32 %1, .Lkf_pf_end+1152-" FROM "\n\ts_mov_b32 %2, " FROM "-.Lkf_pf_h" #tagn : "=s"(pc_), "=s"(len_), "=s"(off_)); \
    unsigned o_ = (unsigned)(jj * 4 + cw) * 256u; o_ = o_ + 256u <= len_ ? o_ : len_ - 256u; \
    const u64 pa_ = (pc_ + (long long)off_ + o_) & ~63ull; \
    asm volatile("s_load_dword %0, %1, 0x0\n\ts_load_dword %0, %1, 0x40\n\ts_load_dword %0, %1, 0x80\n\ts_load_dword %0, %1, 0xc0" : "=&s"(d) : "s"(pa_) : "memory"); } while (0)
#define PF_DONE(d) asm volatile("s_waitcnt lgkmcnt(0)" : "+s"(d) :: "memory")
  if (w < 4) {
    unsigned oc2;
    {
      const unsigned r0 = (unsigned)(panel * 128 + hp * 8);
      oc2 = l < 33 ? WS_KVF + (unsigned)(hr * 16 + panel) * 640 + l * 16 : l == 33 ? WS_F1 + (unsigned)id * 32 : l == 34 ? WS_STAT + r0 * 8 : l == 35 ? WS_RI + r0 * 4
          : l < 40 ? WS_F4 + (unsigned)(((panel * 4 + (l - 36)) * 16 + hp) * 2) * 32 : l < 44 ? WS_GUP + (unsigned)gr * 512 + (l - 40) * 128
          : WS_ZP + (unsigned)((hp * 16 + (l & 15)) * 16 + panel) * 32;
    }
    u32x4 tm, k0, k1; u32x2 tk;
    const unsigned tmofs = lbase + X_CSP + (l < 46 ? l : 45) * 16, kofs = lbase + X_CSP + 768, tgofs = lbase + X_AM + 48;
    unsigned opay;
    { const unsigned r0 = (unsigned)(panel * 128 + hp * 8);
      opay = l < 32 ? WS_CS + (unsigned)(hp * 8 + (panel >> 1)) * 768 + (panel & 1) * 512 + l * 16 : l < 40 ? WS_XA + r0 * 16 + (l - 32) * 16 : l < 44 ? WS_STAT + r0 * 8 + (l - 40) * 16 : WS_RI + r0 * 4 + (l - 44) * 16; }
    const unsigned oflag = WS_F1 + (unsigned)id * 32;
    unsigned mz; asm volatile("s_cmp_eq_u32 %1, 0\n\ts_cselect_b32 %0, -1, 0" : "=s"(mz) : "s"(cw) : "scc");
    const u64 mk_all = ((u64)mz << 32) | mz, mk_pay = ((u64)(mz & 0x3FFFu) << 32) | (mz & ((panel & 1) ? 0xFFFFu : 0xFFFFFFFFu)), mk_one = mz & 1u;
    asm volatile("" : "+v"(oc2), "+v"(opay));
#define MST(op, data, off, mask) asm volatile("s_mov_b64 exec, %3\n\t" op " %0, %1, %2, 0 offen sc1\n\ts_mov_b64 exec, -1" :: "v"(data), "v"(off), "s"(rs), "s"(mask) : "memory")
    f32x4 csa[2] = {{0.f, 0.f, 0.f, 0.f}, {0.f, 0.f, 0.f, 0.f}};
    const bf16x8 ones = {0x3F80, 0x3F80, 0x3F80, 0x3F80, 0x3F80, 0x3F80, 0x3F80, 0x3F80};
    s16x4 cl[2][2], ch[2][2];
    const unsigned csw = lbase + X_CSP + (wm * 64 + wn * 32 + r31) * 4;
    f32x4 acc[4][6];
#pragma unroll
    for (int t = 0; t < 4; ++t)
#pragma unroll
      for (int a = 0; a < 6; ++a) acc[t][a] = f32x4{0.f, 0.f, 0.f, 0.f};
    const int g4 = l >> 4, c15 = l & 15;
    unsigned asw[2][2], wofs[2][2], cofs[2];
#pragma unroll
    for (int e = 0; e < 2; ++e) {
      asw[0][e] = lbase + (wm * 64 + c15) * 128 + (((4 * e + g4) ^ ((c15 >> 1) & 7)) * 16);
      wofs[0][e] = lbase + ABYTES + (8 * g4 + tq) * 128 + (((wn * 4 + 2 * e + (tp >> 1)) ^ ((((tq >> 1) & 1) << 2) | ((g4 & 1) << 1))) * 16) + 8 * (tp & 1);
      asw[1][e] = asw[0][e] + 2 * STAGE1; wofs[1][e] = wofs[0][e] + 2 * STAGE1;
      cofs[e] = wofs[0][e] + ((panel & 1) ? 2 : wm) * WSLAB;
    }
    asm volatile("" : "+v"(asw[0][0]), "+v"(asw[0][1]), "+v"(asw[1][0]), "+v"(asw[1][1]), "+v"(wofs[0][0]), "+v"(wofs[0][1]), "+v"(wofs[1][0]), "+v"(wofs[1][1]), "+v"(cofs[0]), "+v"(cofs[1]));
    bf16x8 af[2][4]; s16x4 wl[2][6], wh_[2][6];
    s16x4 ea[4], ew[6]; f32x4 rv[4];
    const unsigned eaofs = lbase + X_XA + (wm * 64 + c15) * 16;
    const unsigned ewofs = lbase + X_XW + g4 * 1536 + (wn * 32 + c15) * 4;
    const unsigned riofs = lbase + X_ST + (wm * 64 + c15) * 4, rvofs = lbase + X_ST + (wm * 64 + 4 * g4) * 4;
    auto rd = [&](auto ic, auto stc) {
      constexpr int i = decltype(ic)::value, S = decltype(stc)::value, s = S & 1, e = S & 1, buf = (S >> 1) % NST1;
      constexpr int b2 = buf == 2, bo = buf == 1 ? STAGE1 : 0;
      (void)&cl; (void)&ch; (void)&cofs; (void)&af; (void)&wl; (void)&wh_; (void)&asw; (void)&wofs;
      if constexpr (i == 0) DS_B128(af[s][0], asw[b2][e], bo);
      else if constexpr (i == 1) DS_TR(wl[s][0], wofs[b2][0], bo + e * 4096);
      else if constexpr (i == 2) DS_TR(wh_[s][0], wofs[b2][0], bo + e * 4096 + 512);
      else if constexpr (i < 6) DS_B128(af[s][i - 2], asw[b2][e], bo + (i - 2) * 2048);
      else if constexpr (i < 16) {
        constexpr int j = (i - 4) >> 1, off = bo + e * 4096 + (j >> 1) * WSLAB;
        if constexpr (i & 1) DS_TR(wh_[s][j], wofs[b2][j & 1], off + 512); else DS_TR(wl[s][j], wofs[b2][j & 1], off);
      } else {
        constexpr int c = (i - 16) >> 1;
        if constexpr (i & 1) DS_TR(ch[s][c], cofs[c], bo + e * 4096 + 512); else DS_TR(cl[s][c], cofs[c], bo + e * 4096);
      }
    };
    {
      kvsetup();
      SB0();
      asm volatile("s_waitcnt vmcnt(0) lgkmcnt(0)" : "+s"(pfd), "+v"(xr4[0]), "+v"(xr4[1]), "+v"(xr4[2]), "+v"(xr4[3]), "+v"(wr4[0]), "+v"(wr4[1]), "+v"(wr4[2]), "+v"(wr4[3]) :: "memory");
      SB0();
      float m, den, rinv, r;
      ln_rows(xr4, wr4, h, m, den, rinv, r);
      const int rw = tid >> 5;
      const unsigned rb = __float_as_uint(r) >> 16, ord = rb ^ ((rb & 0x8000u) ? 0xFFFFu : 0x8000u), nm = pk2(-m, -m);
      if (r31 == 0) { *(u32x4*)(smem + X_CSP + 512 + rw * 16) = u32x4{nm, nm, nm, nm};
        *(f32x2*)(smem + X_CSP + 640 + rw * 8) = f32x2{m, den}; *(float*)(smem + X_CSP + 704 + rw * 4) = rinv;
        *(unsigned*)(smem + X_CSP + 768 + rw * 4) = (ord << 11) | (unsigned)(2047 - (id * 8 + rw)); }
      SB0();
    }
    __builtin_amdgcn_s_barrier();
    static_for<20>([&](auto ic) { rd(ic, IC(0)); });
    static_for<31>([&](auto stc) {
      constexpr int S = decltype(stc)::value, s = S & 1;
      constexpr bool bnd = (S & 1) == 1, more = S + 1 < 32;
      static_for<24>([&](auto mc) {
        constexpr int m = decltype(mc)::value, j = m >> 2, t = (j & 1) ? 3 - (m & 3) : m & 3;
        (void)&tm; (void)&k0; (void)&k1; (void)&tk; (void)&ea; (void)&ew; (void)&ri; (void)&rv; (void)&oc2; (void)&opay; (void)&oflag; (void)&mk_all; (void)&mk_pay; (void)&mk_one; (void)&rs;
        (void)&tmofs; (void)&kofs; (void)&tgofs; (void)&csw; (void)&csa; (void)&eaofs; (void)&ewofs; (void)&riofs; (void)&rvofs;
        constexpr int issued = more ? (m < 2 ? 0 : m - 2 > 16 ? 16 : m - 2) : 0;
        if constexpr (m == 0) {
          if constexpr (bnd) asm volatile("s_waitcnt lgkmcnt(0)" : "+v"(af[s][0]), "+v"(af[s][1]), "+v"(af[s][2]), "+v"(af[s][3]), "+v"(wl[s][0]), "+v"(wh_[s][0]), "+v"(wl[s][1]), "+v"(wh_[s][1]),
                                          "+v"(wl[s][2]), "+v"(wh_[s][2]), "+v"(wl[s][3]), "+v"(wh_[s][3]), "+v"(wl[s][4]), "+v"(wh_[s][4]), "+v"(wl[s][5]), "+v"(wh_[s][5]));
          else asm volatile("s_waitcnt lgkmcnt(10)" : "+v"(af[s][0]), "+v"(af[s][1]), "+v"(af[s][2]), "+v"(af[s][3]), "+v"(wl[s][0]), "+v"(wh_[s][0]));
        } else if constexpr (!bnd && (m & 3) == 0) {
          constexpr int al = 10 - 2 * j + issued;
          asm volatile("s_waitcnt lgkmcnt(%2)" : "+v"(wl[s][j]), "+v"(wh_[s][j]) : "n"(al > 15 ? 15 : al));
        }
        SB0();
        const bf16x8 wf = cat8(wl[s][j], wh_[s][j]);
        if constexpr (j < 4) acc[t][j] = MFMA16(wf, af[s][t], acc[t][j]);
        else acc[t][j] = MFMA16(af[s][t], wf, acc[t][j]);
        SB0();
        if constexpr (m == 1) {
          if constexpr (S == 1) { const u32x4 sv = {SENT, SENT, SENT, SENT}; MST("buffer_store_dwordx4", sv, oc2, mk_all); }
          if constexpr (S == 11) { asm volatile("" : "+v"(tm), "+v"(k0), "+v"(k1), "+v"(tk)); MST("buffer_store_dwordx4", tm, opay, mk_pay); }
          if constexpr (S == F1_ST / 2) { WAIT_V(0); MST("buffer_store_dwordx2", tk, oflag, mk_one); }
          if constexpr (bnd && more) __builtin_amdgcn_s_barrier();
        }
        if constexpr (more && m >= 1 && m - 1 < (CSS(S + 1) ? 20 : 16)) rd(IC(m - 1), IC(S + 1));
        if constexpr (S == 8 && m == 18) {
          asm volatile("" : "+v"(csa[0]), "+v"(csa[1]));
          const float c = blk ? csa[1][0] : csa[0][0];
          const unsigned u = pk2(c, c - rbf(c));
          asm volatile("ds_write_b32 %0, %1" :: "v"(csw), "v"(u) : "memory");
        }
        if constexpr (S == 9 && m >= 18 && m < 22) {
          if constexpr (m == 18) DS_B128(tm, tmofs, 0);
          if constexpr (m == 19) DS_B128(k0, kofs, 0);
          if constexpr (m == 20) DS_B128(k1, kofs, 16);
          if constexpr (m == 21) asm volatile("ds_read_b64 %0, %1" : "=v"(tk) : "v"(tgofs));
        }
        if constexpr (S == 11 && m == 18) { k0 = __builtin_elementwise_max(k0, k1); asm volatile("" : "+v"(k0)); }
        if constexpr (S == 11 && m == 20) { tk[0] = (tk[0] & ~0x7FFFFFFu) | max(max(k0[0], k0[1]), max(k0[2], k0[3])); asm volatile("" : "+v"(tk)); }
#define DS_B64(dst, addr, off) asm volatile("ds_read_b64 %0, %1 offset:%2" : "=v"(dst) : "v"(addr), "n"(off))
#define DS_B32(dst, addr, off) asm volatile("ds_read_b32 %0, %1 offset:%2" : "=v"(dst) : "v"(addr), "n"(off))
#define DS_2(dst, addr, o0, o1) asm volatile("ds_read2_b32 %0, %1 offset0:%2 offset1:%3" : "=v"(dst) : "v"(addr), "n"(o0), "n"(o1))
        if constexpr (S == 30 && m >= 17) {
          if constexpr (m < 21) DS_B64(ea[m - 17], eaofs, (m - 17) * 256);
          else if constexpr (m < 23) { const unsigned a_ = ewofs + 256; DS_2(ew[m - 19], a_, (m - 21) * 16, (m - 21) * 16 + 192); }
        }
        SB0();
      });
      if constexpr (CSS(S)) {
        asm volatile("s_waitcnt lgkmcnt(15)" : "+v"(cl[s][0]), "+v"(ch[s][0]), "+v"(cl[s][1]), "+v"(ch[s][1]));
        SB0();
        csa[0] = MFMA16(ones, cat8(cl[s][0], ch[s][0]), csa[0]); csa[1] = MFMA16(ones, cat8(cl[s][1], ch[s][1]), csa[1]);
        SB0();
      }
    });
    {
      __builtin_amdgcn_s_setprio(PRIO_T);
      asm volatile("s_waitcnt lgkmcnt(0)" : "+v"(af[1][0]), "+v"(af[1][1]), "+v"(af[1][2]), "+v"(af[1][3]), "+v"(wl[1][0]), "+v"(wh_[1][0]), "+v"(wl[1][1]), "+v"(wh_[1][1]),
                   "+v"(wl[1][2]), "+v"(wh_[1][2]), "+v"(wl[1][3]), "+v"(wh_[1][3]), "+v"(wl[1][4]), "+v"(wh_[1][4]), "+v"(wl[1][5]), "+v"(wh_[1][5]));
      asm volatile("" : "+v"(ea[0]), "+v"(ea[1]), "+v"(ea[2]), "+v"(ea[3]), "+v"(ew[2]), "+v"(ew[3]));
      SB0();
      auto mm = [&](int t, int j) { const bf16x8 wf = cat8(wl[1][j], wh_[1][j]); if (j < 4) acc[t][j] = MFMA16(wf, af[1][t], acc[t][j]); else acc[t][j] = MFMA16(af[1][t], wf, acc[t][j]); };
      auto ff = [&](int t, int j) { if (j < 4) acc[t][j] = MFMA16H(ew[j], ea[t], acc[t][j]); else acc[t][j] = MFMA16H(ea[t], ew[j], acc[t][j]); };
      auto dm = [&](int n) {
        SB0();
        const int t = n & 3, j = (n < 16 ? 4 : 0) + ((n >> 2) & 1);
        if (n & 8) ff(t, j); else mm(t, j);
        SB0();
      };
      const unsigned ew0 = ewofs, ew2 = ewofs + 512;
      mm(2, 2); SB0(); DS_B32(ri[2], riofs, 128); DS_B32(ri[3], riofs, 192); SB0();
      mm(3, 2); SB0(); DS_B32(ri[0], riofs, 0); DS_B32(ri[1], riofs, 64); SB0();
      mm(2, 3); SB0(); DS_2(ew[4], ew2, 0, 192); DS_2(ew[5], ew2, 16, 208); SB0();
      mm(3, 3); SB0(); DS_2(ew[0], ew0, 0, 192); DS_2(ew[1], ew0, 16, 208); SB0();
      ff(2, 2); ff(3, 2); SB0(); DS_B128(rv[0], rvofs, 0); DS_B128(rv[1], rvofs, 64); SB0();
      ff(2, 3); ff(3, 3); SB0(); DS_B128(rv[2], rvofs, 128); DS_B128(rv[3], rvofs, 192); SB0();
      mm(0, 2); mm(1, 2);
      SB0();
      asm volatile("s_waitcnt lgkmcnt(0)" : "+v"(ri[0]), "+v"(ri[1]), "+v"(ri[2]), "+v"(ri[3]), "+v"(ew[0]), "+v"(ew[1]), "+v"(ew[4]), "+v"(ew[5]), "+v"(rv[0]), "+v"(rv[1]), "+v"(rv[2]), "+v"(rv[3]));
      SB0();
      char* ho = smem + HANDOFF + cw * 6144 + l * 16;
      *(u32x4*)(ho + 2048) = pack_t(acc[2][2], acc[3][2], ri[2], ri[3]);
      SB0(); mm(0, 3); mm(1, 3); SB0();
      *(u32x4*)(ho + 3072) = pack_t(acc[2][3], acc[3][3], ri[2], ri[3]);
      SB0(); ff(0, 2); ff(1, 2); ff(0, 3); ff(1, 3); SB0();
      RAW_BARRIER();
      wpload();
      dm(0); dm(1);
      fr[2] = __builtin_bit_cast(bf16x8, pack_t(acc[0][2], acc[1][2], ri[0], ri[1]));
      dm(2); dm(3);
      fr[3] = __builtin_bit_cast(bf16x8, pack_t(acc[0][3], acc[1][3], ri[0], ri[1]));
      dm(4);
      f32x2 ep[8], en[8];
      phi_fn<false>(wp, fr[2], fr[3], ep, en, [&](auto kc) { constexpr int k = decltype(kc)::value; if constexpr (k == 0) { dm(5); dm(6); dm(7); } else if constexpr (7 + k < 16) dm(7 + k); });
      __builtin_amdgcn_s_setprio(0);
#pragma unroll
      for (int g = 0; g < 4; ++g) imgw(g, ep, en);
#pragma unroll
      for (int u = 0; u < 4; ++u) {
        const int t2 = u >> 1, db = u & 1;
        const f32x4 x0 = acc[2 * t2][4 + db] * rv[2 * t2], x1 = acc[2 * t2 + 1][4 + db] * rv[2 * t2 + 1];
        char* pv = t2 ? ho + 4096 : ho;
        *(u32x4*)(pv + db * 1024) = u32x4{pk2(x0[0], x0[1]), pk2(x0[2], x0[3]), pk2(x1[0], x1[1]), pk2(x1[2], x1[3])};
      }
#pragma unroll
      for (int t = 0; t < 4; ++t) { qa[t][0] = acc[t][0]; qa[t][1] = acc[t][1]; wr_[t] = __builtin_bit_cast(u32x4, af[1][t]); }
      wr_[4] = __builtin_bit_cast(u32x4, cat8(wl[1][0], wh_[1][0])); wr_[5] = __builtin_bit_cast(u32x4, cat8(wl[1][1], wh_[1][1]));
      wr_[6] = __builtin_bit_cast(u32x4, cat8(ea[0], ea[1])); wr_[7] = __builtin_bit_cast(u32x4, cat8(ea[2], ea[3])); wr_[8] = __builtin_bit_cast(u32x4, cat8(ew[0], ew[1]));
    }
  } else {
    const int t = tid - 256;
    __builtin_amdgcn_s_setprio(PRIO_H);
    u64 epoch;
    asm volatile("s_load_dwordx2 %0, %1, 0x0" : "=&s"(epoch) : "s"(ws) : "memory");
    stagep(0, IC(0), IC(0), IC(10));
    asm volatile("s_waitcnt lgkmcnt(0)" : "+s"(pfd), "+s"(epoch) :: "memory");
    const u64 enx = epoch + (((unsigned)epoch & 0xFEu) == 0xFEu ? 3u - ((unsigned)epoch & 1u) : 1u);
    const u64 tag = TAG_OF(enx);
    if (t == 0) { *(u32x2*)(smem + X_AM) = u32x2{0u, 0u};
      const unsigned e2 = ((unsigned)enx & 0x55u) | (((unsigned)enx & 0xAAu) << 15);
      *(u32x4*)(smem + X_AM + 32) = u32x4{e2, e2 >> 2, e2 >> 4, e2 >> 6} & 0x00010001u;
      *(u32x4*)(smem + X_AM + 48) = u32x4{(unsigned)tag, (unsigned)(tag >> 32), (unsigned)enx, (unsigned)(enx >> 32)}; }
    u32x2 fl, si; unsigned pfd2, tpace = 0, xs;
    unsigned ofl = WS_F1 + (unsigned)t * 32, orA = WS_CS + (unsigned)(hp * 6144 + cw * 1024 + l * 16);
    unsigned orB = (cw < 2 ? WS_CS + (unsigned)(hp * 6144 + 4096 + cw * 1024) : WS_XA + (unsigned)(panel * 2048 + (cw - 2) * 1024)) + l * 16;
    unsigned orC = ((cw & 1) ? WS_STAT + (unsigned)p3 * 1024 : WS_RI + (unsigned)panel * 512) + l * 16;
    unsigned oxs = (unsigned)(gkq * 256 + cw * 64 + r31 * 2) * 2, akey = lbase + X_AM + 4, vol = (unsigned)l * 16;
    unsigned voG = (unsigned)((8 * cw + srow) * FF + ((sch ^ ((((srow >> 1) & 1) << 2) | ((cw & 1) << 1))) * 8)) * 2;
    const u16* gg_ = w_gate + (size_t)gkq * 256 * FF + ((gr + 1) % 56) * 64;
    const u16* gu_ = w_up + (size_t)gkq * 256 * FF + ((gr + 3) % 56) * 64;
    const u16* gwo = wo + (size_t)(((xcd >> 1) * 4 + (jj >> 3)) * 64 + 2 * cw * 8) * DM + ((xcd & 1) * 8 + (jj & 7)) * 64;
    const u16* gwp = wphi + hp * 2048 + cw * 512;
    const u16* gdw = w_down + (size_t)(((gr >> 3) & 31) * 128 + 32 * cw) * DM + ((gr + 1) & 7) * 128;
    unsigned odw = (unsigned)((l >> 4) * DM + (l & 15) * 8) * 2;
    const u16* gw2a = wo + (size_t)(((cw * 10) ^ (flip * 64)) * 8) * DM + nb * 64;
    const u16* gw2b = wo + (size_t)(((104 + cw * 6) ^ (flip * 64)) * 8) * DM + nb * 64;
    asm volatile("" : "+s"(gw2a), "+s"(gw2b));
    asm volatile("" : "+s"(gg_), "+s"(gu_), "+s"(gwo), "+s"(gwp), "+s"(gdw));
    { const int f = (gr >> 3) * 128 + 32 * cw + 4 * (l & 7), fs = (f >> 6) - ((l & 8) ? 3 : 1);
      ogq = WS_GUP + (unsigned)((l >> 4) * 56 + (fs < 0 ? fs + 56 : fs)) * 512 + (f & 63) * 4 + (l & 8) * 32; }
    asm volatile("" : "+v"(voG), "+v"(odw), "+v"(ogq));
    kvsetup();
    asm volatile("s_waitcnt lgkmcnt(0)" : "+v"(ofl), "+v"(orA), "+v"(orB), "+v"(orC), "+v"(oxs), "+v"(akey), "+v"(vol) :: "memory");
    stagep(0, IC(0), IC(10), IC(10));
    stagep(1, IC(1), IC(0), IC(10));
    WAIT_V(10);
    __builtin_amdgcn_s_barrier();
    stagep(1, IC(1), IC(10), IC(10));
    auto wopf = [&]() {
      GLDS_S(voO, gwo, lbase + X_WO + 2 * cw * 1024); GLDS_S(voO, gwo + 8 * DM, lbase + X_WO + (2 * cw + 1) * 1024);
      GLDS_S(vol, gwp, lbase + X_WP + cw * 1024);
    };
    auto dvl = [&](auto kc) {
      constexpr int k = decltype(kc)::value;
      static_for<DVN[k + 2]>([&](auto ic) {
        constexpr int i = dv_first(k) + decltype(ic)::value;
        u32x4& d_ = dv[i]; const unsigned o_ = odw; const u16* p_ = gdw + (size_t)(4 * i) * DM;
        asm volatile("global_load_dwordx4 %0, %1, %2 nt" : "=v"(d_) : "v"(o_), "s"(p_) : "memory");
      });
    };
    if constexpr (DVN[0] != 0) { if (has_gu) dvl(IC(-2)); }
    stagep(2, IC(2), IC(0), IC(S2A));
    if constexpr (WOK == -1) wopf();
    if constexpr (DVN[1] != 0) { if (has_gu) dvl(IC(-1)); }
    const unsigned lgu = lbase + cw * 1024;
    auto gul = [&](auto ksc) {
      constexpr int ks = decltype(ksc)::value;
      gl_nt<gu_at(ks)>(voG, gg_ + (size_t)ks * 32 * FF, lgu); gl_nt<gu_at(ks) + 4096>(voG, gu_ + (size_t)ks * 32 * FF, lgu);
    };
#define BLD64(dst, off) asm volatile("buffer_load_dwordx2 %0, %1, %2, 0 offen sc1" : "=v"(dst) : "v"(off), "s"(rs) : "memory")
    static_for<15>([&](auto ktc) {
      constexpr int kt = decltype(ktc)::value;
      constexpr auto exw = [](int k) { return DVN[k + 2]; };
      constexpr auto exo = [](int k) { return k == KP ? 1 : k == KC + 1 ? 3 : k == KC + 2 ? 2 : 0; };
      constexpr int exa = ((kt - 2 == WOK || kt - 1 == WOK) ? 3 : 0) - (kt == 0 ? 10 - S2A : 0) + (kt - 2 > KC ? exo(kt - 2) : 0);
      if constexpr (exw(kt - 2) + exw(kt - 1) != 0) { if (has_gu) WAIT_V(10 + exa + exw(kt - 2) + exw(kt - 1) + exo(kt - 1)); else WAIT_V(10 + exa + exo(kt - 1)); }
      else if constexpr (kt < 14) WAIT_V(10 + exa + exo(kt - 1));
      else { if (has_gu) WAIT_V(10); else WAIT_V(0); }
      if constexpr (PACE_T[kt] != 0) { if (cw == 0) { while ((unsigned)__builtin_amdgcn_s_memrealtime() - tpace < (unsigned)(has_gu ? PACE_T[kt] : PACE_3[kt])) {} } }
      __builtin_amdgcn_s_barrier();
      if constexpr (PACE_T[kt + 1] != 0) { if (cw == 0) tpace = (unsigned)__builtin_amdgcn_s_memrealtime(); SB0(); }
      if constexpr (kt == 0) stagep(2, IC(2), IC(S2A), IC(10));
#define UMAX_DPP(C) { const unsigned o = (unsigned)__builtin_amdgcn_update_dpp(0, (int)key, C, 0xf, 0xf, true); key = o > key ? o : key; asm volatile("" : "+v"(key)); }
#define SP(lo, hi) if constexpr (kt + NST1 < 16) { SB0(); stagep(kt + NST1, IC(kt % NST1), IC(lo), IC(hi)); SB0(); }
      SP(0, 10)
      if constexpr (kt == WOK) wopf();
      if constexpr (kt == PF2_KT) CODE_PF(2, ".Lkf_pf_mid", pfd2);
      if constexpr (kt == PF2_KT + 2) PF_DONE(pfd2);
      if constexpr (kt == KP) BLD64(fl, ofl);
      if constexpr (kt == KC) {
        static_assert(DVN[KP + 2] == 0, "count");
        if constexpr (KC == KP + 1) asm volatile("s_waitcnt vmcnt(10)" : "+v"(fl) :: "memory"); else asm volatile("" : "+v"(fl) :: "memory");
#define FL_OK() __all((((fl[0] ^ (unsigned)tag) >> 27) | (fl[1] ^ (unsigned)(tag >> 32))) == 0u)
        if (__builtin_expect(!FL_OK(), 0)) {
          for (unsigned sp = 0;;) {
            asm volatile("buffer_load_dwordx2 %0, %1, %2, 0 offen sc1\n\ts_waitcnt vmcnt(0)" : "=v"(fl) : "v"(ofl), "s"(rs) : "memory");
            if (FL_OK()) break;
            SPIN_NEXT(sp);
          }
        }
        unsigned key = fl[0];
        UMAX_DPP(0x121) UMAX_DPP(0x122) UMAX_DPP(0x124) UMAX_DPP(0x128)
        asm volatile("s_mov_b64 exec, %2\n\tds_max_u32 %0, %1\n\ts_mov_b64 exec, -1\n\ts_waitcnt lgkmcnt(0)" :: "v"(akey), "v"(key), "s"(0x0001000100010001ull) : "memory");
      }
      if constexpr (kt == KC + 1) { GLDS_SC1(orA, ws, lbase + X_XW + cw * 1024); GLDS_SC1(orB, ws, lbase + X_XW + 4096 + cw * 1024); GLDS_SC1(orC, ws, lbase + X_ST + (cw & 1) * 1024); }
      if constexpr (kt == KC + 2) {
        const int bi = 2047 - (int)(__builtin_amdgcn_readfirstlane(*(const unsigned*)(smem + X_AM + 4)) & 2047u);
        asm volatile("global_load_dword %0, %1, %2" : "=v"(xs) : "v"(oxs), "s"(x + (size_t)bi * DM) : "memory");
        asm volatile("buffer_load_dwordx2 %0, off, %1, %2 sc1" : "=v"(si) : "s"(rs), "s"(WS_STAT + (unsigned)bi * 8) : "memory");
      }
      if constexpr (DVN[kt + 2] != 0) { if (has_gu) dvl(ktc); }
      if constexpr (kt == 13) { if (has_gu) { gul(IC(0)); gul(IC(1)); gul(IC(2)); gul(IC(3)); gul(IC(4)); } }
      if constexpr (kt == 14) asm volatile("" : "+v"(xs), "+v"(si) :: "memory");
      if constexpr (kt == 14) { if (has_gu) { gul(IC(5)); gul(IC(6)); gul(IC(7)); } }
    });
    auto wld = [&](auto ic) {
      constexpr int i = decltype(ic)::value;
      u32x4& d_ = wr_[i]; const unsigned o_ = voO; const u16* p_ = (i < 10 ? gw2a : gw2b) + (size_t)((i < 10 ? i : i - 10) * 8) * DM;
      asm volatile("global_load_dwordx4 %0, %1, %2" : "=v"(d_) : "v"(o_), "s"(p_) : "memory");
    };
    wpload();
    {
      const float m = __uint_as_float(si[0]), den = own(__uint_as_float(si[1])), rinv = rcp_nr(den);
      *(unsigned*)(smem + X_XS + cw * 128 + r31 * 4) = ln_pair(xs, m, den, rinv);
    }
    RAW_BARRIER();
    const char* ho = smem + HANDOFF + cw * 6144 + l * 16;
    fr[2] = *(const bf16x8*)(ho + 2048); fr[3] = *(const bf16x8*)(ho + 3072);
    f32x2 ep[8], en[8];
    phi_fn<false>(wp, fr[2], fr[3], ep, en, [&](auto kc) {
      constexpr int k = decltype(kc)::value;
      static_for<NREG - WPOST>([&](auto ic) {
        constexpr int i = decltype(ic)::value;
        if constexpr (k == WH0 + (i * (17 - WH0)) / (NREG - WPOST)) { SB0(); wld(ic); SB0(); }
      });
    });
#pragma unroll
    for (int g = 0; g < 4; ++g) imgw(g, ep, en);
    SB0();
    static_for<WPOST>([&](auto ic) { wld(IC(NREG - WPOST + decltype(ic)::value)); });
  }
  asm volatile(".Lkf_pf_mid:");
  f32x16 zero;
#pragma unroll
  for (int i = 0; i < 16; ++i) zero[i] = 0.f;
  u32x2 tg; unsigned t8, rkey; u32x4 t2;
  {
    __builtin_amdgcn_s_setprio(2);
    RAW_BARRIER();
    u32x4 am; unsigned rk;
    { const unsigned a_ = lbase + X_AM; DS_B128(am, a_, 48); DS_B128(t2, a_, 32); asm volatile("ds_read_b32 %0, %1 offset:4" : "=v"(rk) : "v"(a_)); }
    s16x4 al[4], ah[4]; bf16x8 v0[4], v1[4];
    DS_TR(al[0], pa0, 0); DS_TR(ah[0], pa0, 2304); DS_B128(v0[0], pvh, 0); DS_B128(v1[0], pvh, 1024);
    DS_TR(al[1], pa0, 4608); DS_TR(ah[1], pa0, 4608 + 2304); DS_B128(v0[1], pvh, 4096); DS_B128(v1[1], pvh, 4096 + 1024);
    DS_TR(al[2], pa0, 9216); DS_TR(ah[2], pa0, 9216 + 2304); DS_B128(v0[2], pvh, 6144); DS_B128(v1[2], pvh, 6144 + 1024);
    DS_TR(al[3], pa0, 13824); DS_TR(ah[3], pa0, 13824 + 2304); DS_B128(v0[3], pvh, 6144 + 4096); DS_B128(v1[3], pvh, 6144 + 4096 + 1024);
    const bf16x8 ones = {0x3F80, 0x3F80, 0x3F80, 0x3F80, 0x3F80, 0x3F80, 0x3F80, 0x3F80};
    f32x4 k0 = {0.f, 0.f, 0.f, 0.f}, k1 = k0, zz = k0;
    static_for<4>([&](auto tbc) {
      constexpr int tb = decltype(tbc)::value;
      asm volatile("s_waitcnt lgkmcnt(%4)" : "+v"(al[tb]), "+v"(ah[tb]), "+v"(v0[tb]), "+v"(v1[tb]) : "n"(12 - 4 * tb));
      const bf16x8 af = cat8(al[tb], ah[tb]);
      k0 = MFMA16(af, v0[tb], k0); k1 = MFMA16(af, v1[tb], k1); zz = MFMA16(af, ones, zz);
      SB0();
    });
    asm volatile("" : "+v"(am), "+v"(rk), "+v"(t2));
    auto tagv = [&](f32x4 v) { const u32x4 u = __builtin_bit_cast(u32x4, v); return (u & 0xFFFFFF00u) | (am[2] & 0xFFu); };
    ST128((u32x4{pk2(k0[0], k0[1]), pk2(k0[2], k0[3]), pk2(k1[0], k1[1]), pk2(k1[2], k1[3])} & 0xFFFEFFFEu) | t2, dst);
    const u32x4 zt = tagv(zz);
    asm volatile("s_mov_b64 exec, %3\n\tbuffer_store_dwordx4 %0, %1, %2, 0 offen sc1\n\ts_mov_b64 exec, -1" :: "v"(zt), "v"(dz), "s"(rs), "s"(0x0001000100010001ull) : "memory");
    SB0();
    tg = u32x2{am[0], am[1]}; t8 = __builtin_amdgcn_readfirstlane(am[2]) & 0xFFu; rkey = __builtin_amdgcn_readfirstlane(rk);
  }
  const int idx = 2047 - (int)(rkey & 2047u);
  const int fb = kh ^ ((idx >> 4) & 1);
  const unsigned lxc = lbase + (cw < 2 ? 131072 : X_YS + 64 - 4096) + cw * 2048, lxv = w < 4 ? lxc : lbase + X_XW + cw * 2048;
  {
    bf16x8 xf[8];
    const unsigned px = lbase + X_XS + (l >> 4) * 16, hq = lbase + X_XW + cw * 2048 + l * 16;
    static_for<8>([&](auto kc) { constexpr int ks = decltype(kc)::value; (void)&xf; (void)&px; DS_B128(xf[ks], px, ks * 64); });
    if (w >= 4) {
      __builtin_amdgcn_s_setprio(0);
      WAIT_V(NREG + 2);
    } else {
      auto lo4 = [](u32x4 v) { return __builtin_bit_cast(s16x4, u32x2{v[0], v[1]}); };
      auto hi4 = [](u32x4 v) { return __builtin_bit_cast(s16x4, u32x2{v[2], v[3]}); };
#pragma unroll
      for (int tt = 0; tt < 4; ++tt) {
        const int t = tt ^ 2;
#pragma unroll
        for (int j = 0; j < 2; ++j) qa[t][j] = MFMA16(__builtin_bit_cast(bf16x8, wr_[4 + j]), __builtin_bit_cast(bf16x8, wr_[t]), qa[t][j]);
        if (tt & 1) {
#pragma unroll
          for (int u = t - 1; u <= t; ++u)
#pragma unroll
            for (int j = 0; j < 2; ++j) qa[u][j] = MFMA16H(j ? hi4(wr_[8]) : lo4(wr_[8]), (u & 1) ? hi4(wr_[6 + (u >> 1)]) : lo4(wr_[6 + (u >> 1)]), qa[u][j]);
        }
      }
      const u32x4 q0 = pack_t(qa[2][0], qa[3][0], ri[2], ri[3]), q1 = pack_t(qa[2][1], qa[3][1], ri[2], ri[3]);
      asm volatile("ds_write_b128 %0, %1\n\tds_write_b128 %0, %2 offset:1024" :: "v"(hq), "v"(q0), "v"(q1) : "memory");
    }
    RAW_BARRIER();
    asm volatile("" : "+v"(xf[0]), "+v"(xf[1]), "+v"(xf[2]), "+v"(xf[3]), "+v"(xf[4]), "+v"(xf[5]), "+v"(xf[6]), "+v"(xf[7]));
    if (w >= 4) {
      { const u16* ga = wo + (size_t)(((cw * 10) ^ (flip * 64)) * 8) * DM + nb * 64; const u16* gb = wo + (size_t)(((104 + cw * 6) ^ (flip * 64)) * 8) * DM + nb * 64;
        static_for<16 - NREG>([&](auto ic) { constexpr int i = NREG + decltype(ic)::value;
          if constexpr (i < 10) gl_m0<i * 1024>(voO, ga + (size_t)(i * 8) * DM, lbase + cw * 10240); else gl_m0<(104 + i - 10) * 1024>(voO, gb + (size_t)((i - 10) * 8) * DM, lbase + cw * 6144); }); }
    }
    if (has_gu) {
      s16x4 gl[8], gh[8];
      static_for<8>([&](auto kc) { constexpr int ks = decltype(kc)::value; (void)&gl; (void)&gh; (void)&pgw;
        DS_TR(gl[ks], pgw, ks * 8192); DS_TR(gh[ks], pgw, ks * 8192 + 512); });
      f32x4 c0 = {0.f, 0.f, 0.f, 0.f};
      static_for<8>([&](auto kc) { constexpr int ks = decltype(kc)::value;
        asm volatile("s_waitcnt lgkmcnt(%2)" : "+v"(gl[ks]), "+v"(gh[ks]) : "n"(14 - 2 * ks));
        c0 = MFMA16(cat8(gl[ks], gh[ks]), xf[ks], c0);
        SB0(); });
      const u32x4 gt_ = f2u_tag(c0, t8);
      asm volatile("s_mov_b64 exec, %3\n\tbuffer_store_dwordx4 %0, %1, %2, 0 offen sc1\n\ts_mov_b64 exec, -1" :: "v"(gt_), "v"(ogu), "s"(rs), "s"(0x0001000100010001ull) : "memory");
    }
  }
  const int erow = cw * 32 + fb * 16 + (l & 15), ech = l >> 4;
  u32x4 xv[2];
  const bool sw = ech & 1;
#define FSWAP(v) v = u32x4{sw ? v[2] : v[0], sw ? v[3] : v[1], sw ? v[0] : v[2], sw ? v[1] : v[3]}
  const bool has_idx = w < 4 && (idx >> 5) == p3 * 4 + w;
  unsigned xrt = 0;
  unsigned of4, op4, oo0, oo1, kba, kbz, odo, o2, oz2, wa_, wz_, oxg;
  auto later = [&]() {
    SB0();
    of4 = WS_F4 + (unsigned)(((panel * 4 + wt) * 16 + hp) * 2 + wn) * 32; op4 = WS_F4 + (unsigned)(((p3 * 4 + cw) * 16 + 8 * (kh ^ flip)) * 2 + (l & 15)) * 32;
    oo0 = WS_O + 512 + (unsigned)((((p3 * 4 + cw) * 2 + fb) * 32 + (kh ^ flip) * 16) * 128 + h * 64 + r31) * 16; oo1 = oo0 ^ 65536u;
    kba = lbase + X_KB + wn * 4224 + l * 16; kbz = lbase + X_KB + wn * 4224 + 4096 + h * 64;
    odo = WS_O + 512 + (unsigned)((((panel * 4 + wt) * 2 + blk) * 32 + hd) * 128 + h * 16 + (l & 15)) * 16;
    asm volatile("" : "+v"(kba), "+v"(kbz), "+v"(odo), "+v"(of4), "+v"(op4), "+v"(oo0), "+v"(oo1));
    SB0();
  };
  if (w < 4) {
    unsigned pfd3, od; u64 mkp;
    {
      u32x4 pa[2]; unsigned z0 = t8, z1 = t8;
#define H1LD(p) asm volatile("buffer_load_dwordx4 %0, %4, %6, 0 offen sc1\n\tbuffer_load_dwordx4 %1, %4, %6, %7 offen sc1\n\t" \
                   "s_cmp_lg_u32 %8, 3\n\ts_cbranch_scc1 1f\n\tbuffer_load_dword %2, %5, %6, 0 offen sc1\n\tbuffer_load_dword %3, %5, %6, 0 offen offset:256 sc1\n1:" \
                   : "=&v"(p[0]), "=&v"(p[1]), "+v"(z0), "+v"(z1) : "v"(oh1), "v"(oh1z), "s"(rs), "s"(65536), "s"(cw) : "memory", "scc")
      __builtin_amdgcn_s_sleep(H1_DELAY);
      H1LD(pa);
      SB0();
      CODE_PF(3, ".Lkf_pf_mid", pfd3);
      od = WS_KVF + (unsigned)(hr * 16 + panel) * 640 + ((l & 8) ? 512 + (l & 7) * 2 : cw * 128 + h * 64 + (l & 7) * 8 + blk * 4);
      mkp = cw == 3 ? 0x00FF00FF00FF55FFull : 0x00FF00FF00FF00FFull;
      asm volatile("" : "+s"(mkp));
      fr[0] = __builtin_bit_cast(bf16x8, pack_t(qa[0][0], qa[1][0], ri[0], ri[1])); fr[1] = __builtin_bit_cast(bf16x8, pack_t(qa[0][1], qa[1][1], ri[0], ri[1]));
      asm volatile("" : "+v"(od), "+v"(fr[0]), "+v"(fr[1]));
      asm volatile("global_load_ushort %0, %1, %2" : "=v"(xrt) : "v"((unsigned)(idx * DM + nb * 64 + 4 * (l & 15) + ech) * 2), "s"(x) : "memory");
      later();
      o2 = WS_KVF + (unsigned)(hp * 16 + wn * 8 + wm * 4 + h) * 640 + ((r31 & 12) * 2 + (r31 >> 4) * 4 + (r31 & 3)) * 16;
      oz2 = WS_KVF + (unsigned)(hp * 16 + wn * 8 + wm * 4 + (l & 3)) * 640 + 512;
      wa_ = kba + wm * 2048; wz_ = lbase + X_KB + wn * 4224 + 4096 + (l & 1) * 64 + (wm * 2 + ((l >> 1) & 1)) * 16;
      asm volatile("" : "+v"(o2), "+v"(oz2), "+v"(wa_), "+v"(wz_));
      unsigned hb = 0x8000u, s_lo = 0x05040100u, s_hi = 0x07060100u;
      asm volatile("" : "+s"(s_lo), "+s"(s_hi));
      asm volatile("s_waitcnt vmcnt(1)" : "+v"(pa[0]), "+v"(pa[1]), "+v"(z0), "+v"(z1), "+v"(hb) :: "memory");
      u64 b0, b1, b2; u32x4 v0, v1;
      auto miss = [&]() __attribute__((always_inline)) {
        v0 = pa[0] ^ t2; v1 = pa[1] ^ t2;
        b0 = __ballot((or4(v0) & 0x00010001u) != 0u); b1 = __ballot((or4(v1) & 0x00010001u) != 0u);
        b2 = __ballot((((z0 ^ t8) | (z1 ^ t8)) & 0xFFu) != 0u);
        return (b0 | b1 | b2) != 0;
      };
      if (miss()) {
        unsigned sp = 0;
        do {
        b0 |= (b0 >> 4) & 0x0F0F0F0F0F0F0F0Full; b0 |= (b0 << 4) & 0xF0F0F0F0F0F0F0F0ull; b1 |= (b1 >> 4) & 0x0F0F0F0F0F0F0F0Full; b1 |= (b1 << 4) & 0xF0F0F0F0F0F0F0F0ull;
        asm volatile("s_wqm_b64 exec, %8\n\ts_cbranch_execz 1f\n\tbuffer_load_dwordx4 %0, %4, %6, 0 offen sc1\n1:\n\ts_wqm_b64 exec, %9\n\ts_cbranch_execz 2f\n\tbuffer_load_dwordx4 %1, %4, %6, %7 offen sc1\n2:\n\t"
                     "s_mov_b64 exec, -1\n\ts_cmp_eq_u64 %10, 0\n\ts_cbranch_scc1 3f\n\tbuffer_load_dword %2, %5, %6, 0 offen sc1\n\tbuffer_load_dword %3, %5, %6, 0 offen offset:256 sc1\n3:\n\ts_waitcnt vmcnt(0)"
                     : "+v"(pa[0]), "+v"(pa[1]), "+v"(z0), "+v"(z1) : "v"(oh1), "v"(oh1z), "s"(rs), "s"(65536), "s"(b0), "s"(b1), "s"(b2) : "memory", "scc");
        } while (miss() && ++sp < SPIN_CAP);
      }
    auto sw32 = [](float a, float c) { const auto r = __builtin_amdgcn_permlane32_swap(__float_as_uint(a), __float_as_uint(c), false, false); return __uint_as_float(r[0]) + __uint_as_float(r[1]); };
    auto sw16 = [](float a, float c) { const auto r = __builtin_amdgcn_permlane16_swap(__float_as_uint(a), __float_as_uint(c), false, false); return __uint_as_float(r[0]) + __uint_as_float(r[1]); };
    float sl[4], sh[4];
#pragma unroll
    for (int m = 0; m < 4; ++m) {
      sl[m] = own(__uint_as_float(__builtin_amdgcn_perm(v0[m], hb, s_lo)) + __uint_as_float(__builtin_amdgcn_perm(v1[m], hb, s_lo)));
      sh[m] = own(__uint_as_float(__builtin_amdgcn_perm(v0[m], hb, s_hi)) + __uint_as_float(__builtin_amdgcn_perm(v1[m], hb, s_hi))); }
    const float e0 = dpp_add<0x128, 0xf>(sw16(sw32(sl[0], sl[2]), sw32(sl[1], sl[3]))), e1 = dpp_add<0x128, 0xf>(sw16(sw32(sh[0], sh[2]), sw32(sh[1], sh[3])));
    const float zs = x32_add(x16_add(dpp_add<0x128, 0xf>(__uint_as_float(z0 ^ t8) + __uint_as_float(z1 ^ t8))));
    const float zn = __builtin_bit_cast(float, __builtin_amdgcn_update_dpp(0, __builtin_bit_cast(int, zs), 0xB1, 0xf, 0xf, true));
    const unsigned kvb = min((l & 8) ? pk2(zs, zn) : pk2(e0, e1), SENT - 1u);
    asm volatile("s_mov_b64 exec, %3\n\tbuffer_store_dword %0, %1, %2, 0 offen sc1\n\ts_mov_b64 exec, -1" :: "v"(kvb), "v"(od), "s"(rs), "s"(mkp) : "memory");
    PF_DONE(pfd3);
    }
  } else {
    const char* hq = smem + X_XW + cw * 2048 + l * 16;
    fr[0] = *(const bf16x8*)hq; fr[1] = *(const bf16x8*)(hq + 1024);
  }
  bf16x8 pf[4];
  {
    f32x2 ep[8], en[8];
    phi_fn<true>(wp, fr[0], fr[1], ep, en, [](auto) {});
#pragma unroll
    for (int s = 0; s < 2; ++s) {
      pf[s] = __builtin_bit_cast(bf16x8, u32x4{pk2(ep[4 * s][0], ep[4 * s][1]), pk2(ep[4 * s + 1][0], ep[4 * s + 1][1]), pk2(ep[4 * s + 2][0], ep[4 * s + 2][1]), pk2(ep[4 * s + 3][0], ep[4 * s + 3][1])});
      pf[2 + s] = __builtin_bit_cast(bf16x8, u32x4{pk2(en[4 * s][0], en[4 * s][1]), pk2(en[4 * s + 1][0], en[4 * s + 1][1]), pk2(en[4 * s + 2][0], en[4 * s + 2][1]), pk2(en[4 * s + 3][0], en[4 * s + 3][1])});
    }
    asm volatile("" : "+v"(pf[0]), "+v"(pf[1]), "+v"(pf[2]), "+v"(pf[3]));
  }
  SB0();
  if (b == 0 && tid == 64) ST64(*(const u32x2*)(smem + X_AM + 56), WS_EPOCH);
  u32x4 gq, gq2; f32x2 df[8][4]; unsigned ayr;
#define GQLD(tail) asm volatile("buffer_load_dwordx4 %0, %1, %2, 0 offen sc1" tail : "=v"(gq) : "v"(ogq), "s"(rs) : "memory")
  if (w >= 4) {
    {
      WAIT_V(18 - NREG);
    { const unsigned o_ = (unsigned)((p3 * 128 + cw * 32 + (l >> 3)) * DM + nb * 64 + ((l & 7) ^ (l >> 3)) * 8) * 2; const u16* xa = x + fb * 16 * DM; const u16* xb = x + (fb ^ 1) * 16 * DM;
      gl_m0<0>(o_, xa, lxv); gl_m0<1024>(o_, xa + 8 * DM, lxv); gl_m0<0>(o_, xb, lxc); gl_m0<1024>(o_, xb + 8 * DM, lxc); }
      SB0();
      char* pa_ = smem + cw * 10 * 1024 + l * 16; char* pb_ = smem + (104 + cw * 6) * 1024 + l * 16;
#pragma unroll
      for (int i = 0; i < 10; ++i) if (i < NREG) *(u32x4*)(pa_ + i * 1024) = wr_[i];
#pragma unroll
      for (int i = 0; i < 6; ++i) if (10 + i < NREG) *(u32x4*)(pb_ + i * 1024) = wr_[10 + i];
      SB0();
    }
    asm volatile("" : "+v"(dv[0]), "+v"(dv[1]), "+v"(dv[2]), "+v"(dv[3]), "+v"(dv[4]), "+v"(dv[5]), "+v"(dv[6]), "+v"(dv[7]) :: "memory");
    SB0();
#pragma unroll
    for (int i = 0; i < 8; ++i)
#pragma unroll
      for (int e = 0; e < 4; ++e) { df[i][e] = f32x2{bflo(dv[i][e]), bfhi(dv[i][e])}; asm volatile("" : "+v"(df[i][e])); }
    SB0();
    later();
    ayr = lbase + X_YR + (cw * 128 + (l & 15) * 8 + (l >> 4)) * 4;
    asm volatile("" : "+v"(ayr));
    if (has_gu) GQLD("");
    SB0();
  }
  auto downp = [&]() __attribute__((always_inline)) {
            auto red4 = [](float s0, float s1, float s2, float s3) {
              const auto r01 = __builtin_amdgcn_permlane16_swap(__float_as_uint(s0), __float_as_uint(s1), false, false);
              const auto r23 = __builtin_amdgcn_permlane16_swap(__float_as_uint(s2), __float_as_uint(s3), false, false);
              const float t01 = __uint_as_float(r01[0]) + __uint_as_float(r01[1]), t23 = __uint_as_float(r23[0]) + __uint_as_float(r23[1]);
              const auto r = __builtin_amdgcn_permlane32_swap(__float_as_uint(t01), __float_as_uint(t23), false, false);
              return __uint_as_float(r[0]) + __uint_as_float(r[1]);
            };
            const float gg = rbf(red4(__uint_as_float(gq[0]), __uint_as_float(gq[1]), __uint_as_float(gq[2]), __uint_as_float(gq[3])));
            const float uu = __builtin_bit_cast(float, __builtin_amdgcn_update_dpp(0, __builtin_bit_cast(int, gg), 0x128, 0xf, 0xf, true));
            const float hv0 = rbf(rbf(silu_r(gg)) * uu);
            f32x2 ya[4] = {{0.f, 0.f}, {0.f, 0.f}, {0.f, 0.f}, {0.f, 0.f}};
            static_for<8>([&](auto ic) {
              constexpr int i = decltype(ic)::value;
              const float hv = __builtin_bit_cast(float, __builtin_amdgcn_update_dpp(0, __builtin_bit_cast(int, hv0), 0x150 + i, 0xf, 0xf, true));
              const f32x2 h2 = {hv, hv};
#pragma unroll
              for (int e = 0; e < 4; ++e) ya[e] = __builtin_elementwise_fma(h2, df[i][e], ya[e]);
            });
            const float y0 = red4(ya[0][0], own(ya[0][1]), ya[1][0], own(ya[1][1])), y1 = red4(ya[2][0], own(ya[2][1]), ya[3][0], own(ya[3][1]));
            asm volatile("ds_write2_b32 %0, %1, %2 offset1:4" :: "v"(ayr), "v"(y0), "v"(y1) : "memory");
  };
  {
    u32x4 kf[4], zf[4];
    if (w < 4) {
      u32x4 a, c, z; u64 m0 = ~0ull, m1 = ~0ull, m2 = ~0ull;
      unsigned sp = 0;
      do {
        asm volatile("s_mov_b64 exec, %8\n\ts_cbranch_execz 1f\n\tbuffer_load_dwordx4 %0, %3, %5, 0 offen sc1\n1:\n\ts_mov_b64 exec, %9\n\ts_cbranch_execz 2f\n\tbuffer_load_dwordx4 %1, %3, %5, 0 offen offset:1280 sc1\n2:\n\t"
                     "s_mov_b64 exec, %10\n\ts_cbranch_execz 3f\n\tbuffer_load_dwordx4 %2, %4, %5, 0 offen sc1\n3:\n\ts_mov_b64 exec, -1\n\ts_waitcnt vmcnt(0)\n\t"
                     "ds_write_b128 %6, %0\n\tds_write_b128 %6, %1 offset:1024\n\tds_write_b128 %7, %2"
                     : "+v"(a), "+v"(c), "+v"(z) : "v"(o2), "v"(oz2), "s"(rs), "v"(wa_), "v"(wz_), "s"(m0), "s"(m1), "s"(m2) : "memory");
        m0 = __ballot(max(max(max(a[0], a[1]), a[2]), a[3]) == SENT); m1 = __ballot(max(max(max(c[0], c[1]), c[2]), c[3]) == SENT); m2 = __ballot(max(max(max(z[0], z[1]), z[2]), z[3]) == SENT);
      } while ((m0 | m1 | m2) != 0 && ++sp < SPIN_CAP);
      asm volatile("" : "+v"(xrt));
    }
    RAW_BARRIER();
    DS_B128(kf[0], kba, 0); DS_B128(zf[0], kbz, 0); DS_B128(kf[1], kba, 1024); DS_B128(zf[1], kbz, 16);
    DS_B128(kf[2], kba, 2048); DS_B128(zf[2], kbz, 32); DS_B128(kf[3], kba, 3072); DS_B128(zf[3], kbz, 48);
    __builtin_amdgcn_s_setprio(0);
    f32x16 num = zero;
    float d0 = 0.f, d1 = 0.f;
    static_for<4>([&](auto cc) {
      constexpr int c = decltype(cc)::value;
      asm volatile("s_waitcnt lgkmcnt(%2)" : "+v"(kf[c]), "+v"(zf[c]) : "n"(6 - 2 * c));
      num = MFMA(__builtin_bit_cast(bf16x8, kf[c]), pf[c], num);
      const u32x4 pc = __builtin_bit_cast(u32x4, pf[c]);
      d0 = dot2(zf[c][0], pc[0], d0); d1 = dot2(zf[c][1], pc[1], d1); d0 = dot2(zf[c][2], pc[2], d0); d1 = dot2(zf[c][3], pc[3], d1);
      SB0();
    });
    float den = x32_add(d0 + d1);
    den = rbf(rbf(den) + 1e-6f);
    const float rinv = rcp_nr(den);
    const f32x2 rr = {rinv, rinv};
#pragma unroll
    for (int q = 0; q < 2; ++q) {
      u32x4 u;
#pragma unroll
      for (int e = 0; e < 4; ++e) {
        const f32x2 r = f32x2{num[8 * q + 2 * e], num[8 * q + 2 * e + 1]} * rr;
        u[e] = pk2(r[0], r[1]);
      }
      ST128(u, odo + q * 1024);
      SB0();
    }
  }
  const int p0 = 8 * w + 40;
  const u16* gw2 = wo + (size_t)((p0 ^ (flip * 64)) * 8) * DM + nb * 64; unsigned lw2 = lbase + p0 * 1024;
  asm volatile("" : "+s"(gw2), "+s"(lw2));
  bool glate = false;
  if (w >= 4 && has_gu) {
    asm volatile("s_waitcnt vmcnt(2)" : "+v"(gq) :: "memory");
    gq ^= t8;
    glate = !__all((or4(gq) & 0xFFu) == 0u);
    if (!glate) downp();
  }
  asm volatile("s_waitcnt vmcnt(0)\n\ts_mov_b64 exec, 1\n\tbuffer_store_dwordx2 %0, %1, %2, 0 offen sc1\n\ts_mov_b64 exec, -1" :: "v"(tg), "v"(of4), "s"(rs) : "memory");
  if (__builtin_expect(glate, 0)) {
    for (unsigned sp = 0;;) {
      GQLD("\n\ts_waitcnt vmcnt(0)");
      gq ^= t8;
      if (__all((or4(gq) & 0xFFu) == 0u)) break;
      SPIN_NEXT(sp);
    }
    downp();
  }
  RAW_BARRIER();
  float rxn = 0.f, rsg = 0.f;
  u32x4 yq[7];
  const unsigned oy = WS_YP + (unsigned)(l >> 4) * 4096 + nb * 256 + (l & 15) * 16;
#define YLD(tail) asm volatile("buffer_load_dwordx4 %0, %7, %8, 0 offen sc1\n\tbuffer_load_dwordx4 %1, %7, %8, %9 offen sc1\n\tbuffer_load_dwordx4 %2, %7, %8, %10 offen sc1\n\t" \
    "buffer_load_dwordx4 %3, %7, %8, %11 offen sc1\n\tbuffer_load_dwordx4 %4, %7, %8, %12 offen sc1\n\tbuffer_load_dwordx4 %5, %7, %8, %13 offen sc1\n\t" \
    "buffer_load_dwordx4 %6, %7, %8, %14 offen sc1" tail \
    : "=&v"(yq[0]), "=&v"(yq[1]), "=&v"(yq[2]), "=&v"(yq[3]), "=&v"(yq[4]), "=&v"(yq[5]), "=&v"(yq[6]) \
    : "v"(oy), "s"(rs), "s"(16384), "s"(2 * 16384), "s"(3 * 16384), "s"(4 * 16384), "s"(5 * 16384), "s"(6 * 16384) : "memory")
  f32x2 xf[2][4], gf[2][4];
  __attribute__((address_space(3))) f32x4* pw = (__attribute__((address_space(3))) f32x4*)(smem + 131072 + w * 4096) + l;
  unsigned prd = lbase + 131072 + (w ^ 4) * 4096 + l * 16;
  u16* po = out + (size_t)(p3 * 128 + cw * 32 + fb * 16 + (l >> 3)) * DM + nb * 64 + (l & 7) * 8; u16* po2 = po + 8 * DM;
  unsigned tw0 = lbase + 131072 + (w ^ 4) * 4096 + (l & 15) * 128 + ((ech ^ (l & 7)) * 16), tw1 = tw0 ^ 64, trd = lbase + 131072 + (w ^ 4) * 4096 + (l >> 3) * 128 + (((l & 7) ^ (l >> 3)) * 16);
    unsigned bw[4];
#pragma unroll
    for (int n = 0; n < 4; ++n) bw[n] = lbase + kh * 65536 + (16 * h + 4 * blk + tq) * 128 + (((4 * (n >> 1) + tp) ^ ((tq >> 1) | (blk << 2))) * 16) + 8 * ((n ^ tp) & 1);
  {
  if (w == 1 && has_gu && l < 32) {
    const f32x4* y = (const f32x4*)(smem + X_YR) + l;
    ST128(f2u_tag((y[0] + y[32]) + (y[64] + y[96]), t8), WS_YP + (unsigned)(gr >> 3) * 4096 + ((gr + 1) & 7) * 512 + l * 16);
  }
    SB0();
    gl_run<8>(voO, gw2, lw2);
    u32x2 f0;
    asm volatile("buffer_load_dwordx2 %0, %1, %2, 0 offen sc1" : "=v"(f0) : "v"(op4), "s"(rs) : "memory");
    SB0();
    {
      const f32x2 md = *(const f32x2*)(smem + X_MD + erow * 8);
      { const unsigned ax = lxv + (l & 15) * 128 + ((ech ^ (l & 7)) * 16), a0 = ax + (sw ? 8 : 0), a1 = ax + (sw ? 0 : 8);
        u32x2 q0, q1, q2, q3;
        asm volatile("ds_read_b64 %0, %4\n\tds_read_b64 %1, %5\n\tds_read_b64 %2, %6\n\tds_read_b64 %3, %7\n\ts_waitcnt lgkmcnt(0)" : "=&v"(q0), "=&v"(q1), "=&v"(q2), "=&v"(q3) : "v"(a0), "v"(a1), "v"(a0 ^ 64), "v"(a1 ^ 64) : "memory");
        xv[0] = u32x4{q0[0], q0[1], q1[0], q1[1]}; xv[1] = u32x4{q2[0], q2[1], q3[0], q3[1]}; }
      const float rinv = rcp_nr(own(md[1])), mr = -md[0] * rinv, L2E = 1.44269504088896340736f, rl = -L2E * rinv, ml = -L2E * mr;
#pragma unroll
      for (int i = 0; i < 2; ++i)
#pragma unroll
        for (int e = 0; e < 4; ++e) {
          xf[i][e] = f32x2{bflo(xv[i][e]), bfhi(xv[i][e])};
          const f32x2 xn = __builtin_elementwise_fma(xf[i][e], f32x2{rinv, rinv}, f32x2{mr, mr}), tn = __builtin_elementwise_fma(xf[i][e], f32x2{rl, rl}, f32x2{ml, ml});
          const f32x2 dd = f32x2{__builtin_amdgcn_exp2f(tn[0]), __builtin_amdgcn_exp2f(tn[1])} + f32x2{1.0f, 1.0f};
          gf[i][e] = xn * f32x2{__builtin_amdgcn_rcpf(dd[0]), __builtin_amdgcn_rcpf(dd[1])};
        }
#pragma unroll
      for (int i = 0; i < 2; ++i) asm volatile("" : "+v"(xf[i][0]), "+v"(xf[i][1]), "+v"(xf[i][2]), "+v"(xf[i][3]), "+v"(gf[i][0]), "+v"(gf[i][1]), "+v"(gf[i][2]), "+v"(gf[i][3]));
      asm volatile("" : "+v"(pw), "+v"(prd), "+v"(po2), "+v"(tw0), "+v"(tw1), "+v"(trd));
      asm volatile("" : "+v"(po), "+v"(bw[0]), "+v"(bw[1]), "+v"(bw[2]), "+v"(bw[3]));
    }
    if (has_idx) {
      const unsigned ord = (rkey >> 11) & 0xFFFFu;
      rsg = rbf(1.0f / (1.0f + __expf(-__uint_as_float((ord ^ ((ord & 0x8000u) ? 0x8000u : 0xFFFFu)) << 16))));
      const f32x2 md = *(const f32x2*)(smem + X_MD + (idx & 127) * 8);
      const float dn = own(md[1]), rinv = rcp_nr(dn);
      rxn = bflo(ln_pair(xrt, md[0], dn, rinv));
      asm volatile("" : "+v"(rxn), "+v"(rsg));
      { u64 pc_; unsigned d_, o_;
        asm volatile("s_getpc_b64 %0\n.Lkf_own1:\n\ts_mov_b32 %1, .Lkf_own0-.Lkf_own1" : "=s"(pc_), "=s"(o_));
        pc_ += o_;
        asm volatile("s_load_dword %0, %1, 0x0\n\ts_load_dword %0, %1, 0x80\n\ts_load_dword %0, %1, 0x100\n\ts_load_dword %0, %1, 0x180\n\ts_load_dword %0, %1, 0x200\n\ts_load_dword %0, %1, 0x280\n\ts_waitcnt lgkmcnt(0)" : "=&s"(d_) : "s"(pc_) : "memory"); }
    }
    u64 ne;
    SB0();
    asm volatile("s_waitcnt vmcnt(0)\n\tv_cmp_ne_u64 %0, %1, %2" : "=s"(ne) : "v"(f0), "v"(tg) : "memory");
    for (unsigned sp = 0; ne != 0 && sp < SPIN_CAP; ++sp) {
      u32x2 f;
      asm volatile("buffer_load_dwordx2 %1, %2, %3, 0 offen sc1\n\ts_waitcnt vmcnt(0)\n\tv_cmp_ne_u64 %0, %1, %4" : "=s"(ne), "=&v"(f) : "v"(op4), "s"(rs), "v"(tg) : "memory");
    }
  }
  constexpr int OWIN = 4;
  u32x4 of[16][2];
  {
    SB0();
#pragma unroll
    for (int i = 0; i < OWIN; ++i) { of[i][0] = LD128(oo0 + i * 2048); of[i][1] = LD128(oo1 + i * 2048); }
    SB0();
    if (has_idx) YLD("");
    SB0();
  }
    auto owner = [&](auto nc) __attribute__((always_inline)) {
      asm volatile(".Lkf_own0:\n\ts_waitcnt vmcnt(%7)" : "+v"(yq[0]), "+v"(yq[1]), "+v"(yq[2]), "+v"(yq[3]), "+v"(yq[4]), "+v"(yq[5]), "+v"(yq[6]) : "n"(decltype(nc)::value) : "memory");
      for (unsigned sp = 0;; ++sp) {
        unsigned bad = 0;
#pragma unroll
        for (int i = 0; i < 7; ++i) { yq[i] ^= t8; bad |= or4(yq[i]); }
        if (__builtin_expect(__all((bad & 0xFFu) == 0u) || sp > SPIN_CAP, 1)) break;
        YLD("\n\ts_waitcnt vmcnt(0)");
      }
#define YF(i) __builtin_bit_cast(f32x4, yq[i])
      const f32x4 y4 = ((YF(0) + YF(1)) + (YF(2) + YF(3))) + ((YF(4) + YF(5)) + YF(6));
      const auto r01 = __builtin_amdgcn_permlane16_swap(__float_as_uint(y4[0]), __float_as_uint(own(y4[1])), false, false);
      const auto r23 = __builtin_amdgcn_permlane16_swap(__float_as_uint(y4[2]), __float_as_uint(own(y4[3])), false, false);
      const float t01 = __uint_as_float(r01[0]) + __uint_as_float(r01[1]), t23 = __uint_as_float(r23[0]) + __uint_as_float(r23[1]);
      const auto r = __builtin_amdgcn_permlane32_swap(__float_as_uint(t01), __float_as_uint(t23), false, false);
      const float y = __uint_as_float(r[0]) + __uint_as_float(r[1]);
      const float g1 = rbf(silu_r(rbf(rxn + rbf(rbf(y) * rsg))));
      (void)&gf; (void)&oy; (void)&rs; const u64 mk_ = 0x0001000100010001ull << (idx & 15);
      const unsigned gb = lbase + (131072 + w * 4096), aw_ = gb + (l & 15) * 16 + ech * 4, a0 = gb + ech * 32 + (ech & 1) * 16, a1 = a0 ^ 16;
      asm volatile("ds_write_b32 %8, %9\n\ts_mov_b64 exec, %12\n\ts_waitcnt lgkmcnt(0)\n\t"
                   "ds_read_b64 %0, %10\n\tds_read_b64 %1, %10 offset:8\n\tds_read_b64 %2, %11\n\tds_read_b64 %3, %11 offset:8\n\t"
                   "ds_read_b64 %4, %10 offset:128\n\tds_read_b64 %5, %10 offset:136\n\tds_read_b64 %6, %11 offset:128\n\tds_read_b64 %7, %11 offset:136\n\t"
                   "s_mov_b64 exec, -1\n\ts_waitcnt lgkmcnt(0)"
                   : "+v"(gf[0][0]), "+v"(gf[0][1]), "+v"(gf[0][2]), "+v"(gf[0][3]), "+v"(gf[1][0]), "+v"(gf[1][1]), "+v"(gf[1][2]), "+v"(gf[1][3])
                   : "v"(aw_), "v"(g1), "v"(a0), "v"(a1), "s"(mk_) : "memory");
    };
  f32x4 acc3[2][4];
#pragma unroll
  for (int t = 0; t < 2; ++t)
#pragma unroll
    for (int n = 0; n < 4; ++n) acc3[t][n] = f32x4{0.f, 0.f, 0.f, 0.f};
  {
    s16x4 wa[2][8];
    auto rd3 = [&](auto kc, auto nc) {
      constexpr int k = decltype(kc)::value, n = decltype(nc)::value;
      (void)&wa; (void)&bw;
      DS_TR(wa[k & 1][2 * n], bw[n], k * 4096); DS_TR(wa[k & 1][2 * n + 1], bw[n], k * 4096 + 1024);
    };
    RAW_BARRIER();
    static_for<4>([&](auto nc) { rd3(IC(0), nc); });
    static_for<16>([&](auto kc) {
      constexpr int k = decltype(kc)::value, sx = k & 1;
      static_for<4>([&](auto nc) {
        constexpr int n = decltype(nc)::value;
        asm volatile("s_waitcnt lgkmcnt(%2)" : "+v"(wa[sx][2 * n]), "+v"(wa[sx][2 * n + 1]) : "n"(k < 15 ? 6 : n < 2 ? 6 - 2 * n : 5 - n));
        SB0();
        const bf16x8 wf = cat8(wa[sx][2 * n], wa[sx][2 * n + 1]);
        acc3[0][n] = MFMA16(wf, __builtin_bit_cast(bf16x8, of[k][0]), acc3[0][n]);
        SB0();
        if constexpr (n == 0 && (k & 3) == 0) __builtin_amdgcn_s_setprio(3 - (k >> 2));
        if constexpr (k + 1 < 16) rd3(IC(k + 1), nc);
        if constexpr (k + OWIN < 16 && n == 3) { of[k + OWIN][0] = LD128(oo0 + (k + OWIN) * 2048); }
        SB0();
        acc3[1][n] = MFMA16(wf, __builtin_bit_cast(bf16x8, of[k][1]), acc3[1][n]);
        SB0();
        if constexpr (k + OWIN < 16 && n == 3) { of[k + OWIN][1] = LD128(oo1 + (k + OWIN) * 2048); SB0(); }
        if constexpr (k == YK && n == 3) { (void)&yq; (void)&oy; (void)&rs; if (has_idx) YLD(""); SB0(); }
        if constexpr (k == YP_ && n == 3) { constexpr int e_ = YP_ + OWIN < 16 ? YP_ : 15 - OWIN; if (has_idx) owner(IC(2 * (e_ - YK))); SB0(); }
        if constexpr (k == 15 && n > 0) { pw[(n - 1) * 64] = acc3[1][n - 1]; SB0(); }
      });
    });
  }
  {
    pw[192] = acc3[1][3];
    SB0();
    SB0();
#pragma unroll
    for (int i = 0; i < 2; ++i)
#pragma unroll
      for (int e = 0; e < 4; ++e) {
        const f32x4 a = acc3[0][2 * i + (e >> 1)];
        xf[i][e] = __builtin_elementwise_fma(f32x2{a[2 * (e & 1)], a[2 * (e & 1) + 1]}, gf[i][e], xf[i][e]);
        asm volatile("" : "+v"(xf[i][e]));
      }
    RAW_BARRIER();
    f32x4 pr[4];
    DS_B128(pr[0], prd, 0); DS_B128(pr[1], prd, 1024); DS_B128(pr[2], prd, 2048); DS_B128(pr[3], prd, 3072);
    SB0();
    u32x4 res[2];
#pragma unroll
    for (int i = 0; i < 2; ++i) {
      if (i == 0) asm volatile("s_waitcnt lgkmcnt(2)" : "+v"(pr[0]), "+v"(pr[1])); else asm volatile("s_waitcnt lgkmcnt(1)" : "+v"(pr[2]), "+v"(pr[3]));
      const f32x4 s0 = pr[2 * i], s1 = pr[2 * i + 1];
      const f32x2 o0 = __builtin_elementwise_fma(f32x2{s0[0], s0[1]}, gf[i][0], xf[i][0]), o1 = __builtin_elementwise_fma(f32x2{s0[2], s0[3]}, gf[i][1], xf[i][1]);
      const f32x2 o2 = __builtin_elementwise_fma(f32x2{s1[0], s1[1]}, gf[i][2], xf[i][2]), o3 = __builtin_elementwise_fma(f32x2{s1[2], s1[3]}, gf[i][3], xf[i][3]);
      res[i] = u32x4{pk2(o0[0], o0[1]), pk2(o1[0], o1[1]), pk2(o2[0], o2[1]), pk2(o3[0], o3[1])};
      FSWAP(res[i]);
      asm volatile("ds_write_b128 %0, %1" :: "v"(i ? tw1 : tw0), "v"(res[i]) : "memory");
    }
    asm volatile("s_waitcnt lgkmcnt(0)\n\tds_read_b128 %0, %2\n\tds_read_b128 %1, %2 offset:1024\n\ts_waitcnt lgkmcnt(1)\n\t"
                 "global_store_dwordx4 %3, %0, off sc1\n\ts_waitcnt lgkmcnt(0)\n\tglobal_store_dwordx4 %4, %1, off sc1\n\ts_nop 1"
                 : "=&v"(res[0]), "=&v"(res[1]) : "v"(trd), "v"(po), "v"(po2) : "memory");
  }
  asm volatile(".Lkf_pf_end:" ::: "memory");
}

extern "C" void kernel_launch(void* const* d_in, const int* in_sizes, int n_in,
                              void* d_out, int out_size, void* d_ws, size_t ws_size,
                              hipStream_t stream) {
  (void)hipFuncSetAttribute((const void*)kf_all, hipFuncAttributeMaxDynamicSharedMemorySize, LDS_TOTAL);
  kf_all<<<256, 512, LDS_TOTAL, stream>>>((const u16*)d_in[0], (const u16*)d_in[1], (const u16*)d_in[2], (const u16*)d_in[3],
                                          (const u16*)d_in[5], (const u16*)d_in[6], (const u16*)d_in[7], (const u16*)d_in[8],
                                          (char*)d_ws, (u16*)d_out);
}
```

```cpp
#define I1 10
#define I2 10
#define I3 10
#define I4 10
#define I5 10
#define I6 10
#define I7 10
#define WOK 2
#define S2A 10
#define KP 9
#define KC 10
#define F1_ST 26
#define PF2_KT 8
#define H1_DELAY 8
#define PACE 1
constexpr int PACE_T[16] = {0,108,72,60};
constexpr int PACE_3[16] = {0,60,50,50};
constexpr int DVN[20] = {0,0,3,2,1,0,1,0,1};
constexpr int dv_first(int k) { int n = 0; for (int i = -2; i < k; ++i) n += DVN[i + 2]; return n; }
constexpr bool CSS(int S) { return S < 2 || S == 6 || S == 7; }
#define NREG 8
#define WH0 12
#define WPOST 4
#define YK -1
#define YP_ 11
#define PRIO_T 2
#define PRIO_H 1
#include <hip/hip_runtime.h>
#include <hip/hip_bf16.h>
#include <stdint.h>
#include <utility>
#include <type_traits>

typedef __attribute__((ext_vector_type(8))) short bf16x8;
typedef __attribute__((ext_vector_type(4))) short s16x4;
typedef __attribute__((ext_vector_type(16))) float f32x16;
typedef __attribute__((ext_vector_type(4))) float f32x4;
typedef __attribute__((ext_vector_type(2))) float f32x2;
typedef __attribute__((ext_vector_type(4))) unsigned u32x4;
typedef __attribute__((ext_vector_type(2))) unsigned u32x2;
typedef __attribute__((ext_vector_type(2))) __bf16 bfx2;
typedef __attribute__((ext_vector_type(4))) short v4i16_t;
typedef unsigned long long u64;
typedef unsigned short u16;

#define DI __device__ __forceinline__
#define MFMA(a, b, c) __builtin_amdgcn_mfma_f32_32x32x16_bf16((a), (b), (c), 0, 0, 0)
#define MFMA16H(a, b, c) __builtin_amdgcn_mfma_f32_16x16x16bf16_1k((a), (b), (c), 0, 0, 0)
#define MFMA16(a, b, c) __builtin_amdgcn_mfma_f32_16x16x32_bf16((a), (b), (c), 0, 0, 0)

DI unsigned pk2(float a, float b) { f32x2 v = {a, b}; return __builtin_bit_cast(unsigned, __builtin_convertvector(v, bfx2)); }
DI float bflo(unsigned u) { return __uint_as_float(u << 16); }
DI float bfhi(unsigned u) { return __uint_as_float(u & 0xffff0000u); }
DI float rbf(float x) { return (float)(__bf16)x; }
DI s16x4 trread(const char* p) {
  return __builtin_bit_cast(s16x4, __builtin_amdgcn_ds_read_tr16_b64_v4i16((__attribute__((address_space(3))) v4i16_t*)p));
}
DI bf16x8 cat8(s16x4 lo, s16x4 hi) { return __builtin_shufflevector(lo, hi, 0, 1, 2, 3, 4, 5, 6, 7); }
#define WAIT_V(n) asm volatile("s_waitcnt vmcnt(%0)" ::"n"(n) : "memory")
#define RAW_BARRIER() do { asm volatile("s_waitcnt lgkmcnt(0)" ::: "memory"); __builtin_amdgcn_s_barrier(); asm volatile("" ::: "memory"); } while (0)
#define DS_B128(dst, addr, off) asm volatile("ds_read_b128 %0, %1 offset:%2" : "=v"(dst) : "v"(addr), "n"(off))
#define DS_TR(dst, addr, off) asm volatile("ds_read_b64_tr_b16 %0, %1 offset:%2" : "=v"(dst) : "v"(addr), "n"(off))
#define LDS_OFF(p) ((unsigned)(uintptr_t)(__attribute__((address_space(3))) const char*)(p))
template <class F, int... I> DI void static_for_impl(F&& f, std::integer_sequence<int, I...>) { (f(std::integral_constant<int, I>{}), ...); }
template <int N, class F> DI void static_for(F&& f) { static_for_impl(f, std::make_integer_sequence<int, N>{}); }
DI float divc(float a, float b, float rinv) { const float q = a * rinv; return __builtin_fmaf(__builtin_fmaf(-q, b, a), rinv, q); }
DI float rcp_nr(float d) { const float r = __builtin_amdgcn_rcpf(d); return __builtin_fmaf(__builtin_fmaf(-d, r, 1.0f), r, r); }
DI float own(float v) { asm volatile("" : "+v"(v)); return v; }
DI float silu_r(float x) { return x * __builtin_amdgcn_rcpf(1.0f + __expf(-x)); }
#define SB0() __builtin_amdgcn_sched_barrier(0)
#define GLDS_S(vo, sp, ld) asm volatile("s_mov_b32 m0, %2\n\ts_nop 0\n\tglobal_load_lds_dwordx4 %0, %1" :: "v"(vo), "s"(sp), "s"(ld) : "m0", "memory")
template <int IMM> DI void gl_m0(unsigned vo, const void* sp, unsigned ls) {
  asm volatile("s_add_u32 m0, %2, %3\n\ts_nop 0\n\tglobal_load_lds_dwordx4 %0, %1" :: "v"(vo), "s"(sp), "s"(ls), "n"(IMM) : "m0", "memory", "scc");
}
template <int IMM> DI void gl_nt(unsigned vo, const void* sp, unsigned ls) {
  asm volatile("s_add_u32 m0, %2, %3\n\ts_nop 0\n\tglobal_load_lds_dwordx4 %0, %1 nt" :: "v"(vo), "s"(sp), "s"(ls), "n"(IMM) : "m0", "memory", "scc");
}
template <int N> DI void gl_run(unsigned vo, const void* sp, unsigned ls) {
  unsigned lo = (unsigned)(uintptr_t)sp, hi = (unsigned)((uintptr_t)sp >> 32);
  static_for<N>([&](auto ic) {
    constexpr int i = decltype(ic)::value;
    gl_m0<i * 1024>(vo, (const void*)(((uintptr_t)hi << 32) | lo), ls);
    if constexpr (i + 1 < N) asm volatile("s_add_u32 %0, %0, 0x4000\n\ts_addc_u32 %1, %1, 0" : "+s"(lo), "+s"(hi) :: "scc");
  });
}
#define GLDS_SC1(vo, sp, ld) asm volatile("s_mov_b32 m0, %2\n\ts_nop 0\n\tglobal_load_lds_dwordx4 %0, %1 sc1" :: "v"(vo), "s"(sp), "s"(ld) : "m0", "memory")
DI float x32_add(float v) { auto r = __builtin_amdgcn_permlane32_swap(__float_as_uint(v), __float_as_uint(v), false, false); return __uint_as_float(r[0]) + __uint_as_float(r[1]); }
DI float x32_max(float v) { auto r = __builtin_amdgcn_permlane32_swap(__float_as_uint(v), __float_as_uint(v), false, false); float o; asm("v_max_f32 %0, %1, %2" : "=v"(o) : "v"(r[0]), "v"(r[1])); return o; }
DI float x32_min(float v) { auto r = __builtin_amdgcn_permlane32_swap(__float_as_uint(v), __float_as_uint(v), false, false); float o; asm("v_min_f32 %0, %1, %2" : "=v"(o) : "v"(r[0]), "v"(r[1])); return o; }
DI float x16_add(float v) { auto r = __builtin_amdgcn_permlane16_swap(__float_as_uint(v), __float_as_uint(v), false, false); return __uint_as_float(r[0]) + __uint_as_float(r[1]); }
template <int CTRL, int RMASK> DI float dpp_add(float v) {
  return v + __builtin_bit_cast(float, __builtin_amdgcn_update_dpp(0, __builtin_bit_cast(int, v), CTRL, RMASK, 0xf, true));
}

DI unsigned ln_pair(unsigned u, float m, float den, float rinv) {
  const f32x2 mm = {m, m}, dd = {den, den}, rr = {rinv, rinv};
  const f32x2 d0 = f32x2{bflo(u), bfhi(u)} - mm;
  const unsigned du = pk2(d0[0], d0[1]);
  const f32x2 d = {bflo(du), bfhi(du)};
  const f32x2 q = d * rr;
  const f32x2 r = __builtin_elementwise_fma(__builtin_elementwise_fma(-q, dd, d), rr, q);
  return pk2(r[0], r[1]);
}
DI float dot2(unsigned a, unsigned b, float c) { return __builtin_amdgcn_fdot2_f32_bf16(__builtin_bit_cast(bfx2, a), __builtin_bit_cast(bfx2, b), c, false); }
DI float half_total(float v) {
  v = dpp_add<0x121, 0xf>(v); v = dpp_add<0x122, 0xf>(v); v = dpp_add<0x124, 0xf>(v); v = dpp_add<0x128, 0xf>(v);
  return x16_add(v);
}
DI void ln_rows(const u32x4 (&xr)[4], const u32x4 (&wr)[4], int h, float& m, float& den, float& rinv, float& r) {
  const unsigned c0 = __builtin_amdgcn_readlane(xr[0][0], 0) << 16, c1 = __builtin_amdgcn_readlane(xr[0][0], 32) << 16;
  const float c = __uint_as_float(h ? c1 : c0);
  const f32x2 cc = {c, c};
  float s1[2] = {0.f, 0.f}, sx[2] = {0.f, 0.f}, sw[2] = {0.f, 0.f};
  f32x2 q[2] = {{0.f, 0.f}, {0.f, 0.f}};
#pragma unroll
  for (int i = 0; i < 4; ++i)
#pragma unroll
    for (int e = 0; e < 4; ++e) {
      const unsigned u = xr[i][e];
      s1[e & 1] = dot2(u, 0x3F803F80u, s1[e & 1]); sx[e & 1] = dot2(u, wr[i][e], sx[e & 1]); sw[e & 1] = dot2(wr[i][e], 0x3F803F80u, sw[e & 1]);
      const f32x2 d = f32x2{bflo(u), bfhi(u)} - cc;
      q[e & 1] = __builtin_elementwise_fma(d, d, q[e & 1]);
    }
  const f32x2 q2 = q[0] + q[1];
  const float mean = half_total(s1[0] + s1[1]) * (1.0f / 1024.0f), m2 = half_total(q2[0] + own(q2[1])) * (1.0f / 1024.0f);
  const float tx = half_total(sx[0] + sx[1]), tw = half_total(sw[0] + sw[1]);
  const float dm = mean - c;
  const float var = fmaxf(__builtin_fmaf(-dm, dm, m2), 0.f);
  m = rbf(mean);
  den = rbf(sqrtf(rbf(rbf(var) + 1e-5f)));
  rinv = rcp_nr(den);
  r = rbf(divc(__builtin_fmaf(-m, tw, tx), den, rinv));
}

template <bool RND, class HK> DI void phi_fn(const bf16x8 (&wp)[2], const bf16x8 f0, const bf16x8 f1, f32x2 (&ep)[8], f32x2 (&en)[8], float si, float so, HK&& hk) {
  f32x16 p;
#pragma unroll
  for (int i = 0; i < 16; ++i) p[i] = 0.f;
  p = MFMA(wp[0], f0, p);
  p = MFMA(wp[1], f1, p);
  hk(std::integral_constant<int, 0>{});
  float mx = -3.0e38f, mn = 3.0e38f;
  f32x2 p2[8];
  static_for<8>([&](auto ic) {
    constexpr int i = decltype(ic)::value;
    if constexpr (RND) { const unsigned u = pk2(p[2 * i], p[2 * i + 1]); p2[i] = f32x2{bflo(u), bfhi(u)}; }
    else p2[i] = f32x2{p[2 * i], p[2 * i + 1]};
    mx = fmaxf(fmaxf(mx, p2[i][0]), p2[i][1]); mn = fminf(fminf(mn, p2[i][0]), p2[i][1]);
    if constexpr (i & 1) hk(std::integral_constant<int, 1 + (i >> 1)>{});
  });
  mx = x32_max(mx); mn = x32_min(mn);
  hk(std::integral_constant<int, 5>{});
  const float L2E = 1.44269504088896340736f * si, mxl = -mx * L2E, mnl = mn * L2E;
  const f32x2 cp = {L2E, L2E}, cn = {-L2E, -L2E}, bp = {mxl, mxl}, bn = {mnl, mnl};
  f32x2 sp2, sn2;
  static_for<8>([&](auto ic) {
    constexpr int i = decltype(ic)::value;
    const f32x2 tp2 = __builtin_elementwise_fma(p2[i], cp, bp), tn2 = __builtin_elementwise_fma(p2[i], cn, bn);
    ep[i] = f32x2{__builtin_amdgcn_exp2f(tp2[0]), __builtin_amdgcn_exp2f(tp2[1])};
    en[i] = f32x2{__builtin_amdgcn_exp2f(tn2[0]), __builtin_amdgcn_exp2f(tn2[1])};
    if constexpr (i == 0) { sp2 = ep[0]; sn2 = en[0]; } else { sp2 += ep[i]; sn2 += en[i]; }
    hk(std::integral_constant<int, 6 + i>{});
  });
  const float sp = x32_add(sp2[0] + own(sp2[1])), sn = x32_add(sn2[0] + own(sn2[1]));
  hk(std::integral_constant<int, 14>{});
  const float rp = __builtin_amdgcn_rcpf(sp) * so, rn = __builtin_amdgcn_rcpf(sn) * so;
  const f32x2 rp2 = {rp, rp}, rn2 = {rn, rn};
  hk(std::integral_constant<int, 15>{});
#pragma unroll
  for (int i = 0; i < 8; ++i) { ep[i] = ep[i] * rp2; en[i] = en[i] * rn2; }
  hk(std::integral_constant<int, 16>{});
}

constexpr int DM = 1024, FF = 3584;
constexpr unsigned SENT = 0xFFFFFFFFu;
constexpr unsigned SPIN_CAP = 1u << 17;

constexpr unsigned WS_EPOCH = 0;
constexpr unsigned WS_F1 = 4096;
constexpr unsigned WS_F4 = 896u << 10;
constexpr unsigned WS_STAT = 20480;
constexpr unsigned WS_CS = 36864;
constexpr unsigned WS_GUP = WS_CS + 16 * 16 * 768;
constexpr unsigned WS_YP = WS_GUP + 256 * 512;
constexpr unsigned WS_KVF = WS_YP + 32 * 4096;
constexpr unsigned WS_ZP = WS_KVF + 256 * 640;
constexpr unsigned WS_XA = 800u << 10;
constexpr unsigned WS_RI = 840u << 10;
static_assert(WS_ZP + 131072 <= WS_XA && WS_XA + 32768 <= WS_RI, "ws map");
constexpr unsigned WS_KVP = 1u << 20;
constexpr unsigned WS_O = 8u << 20;
constexpr unsigned WS_END = 16u << 20;

constexpr int ABYTES = 128 * 128, WSLAB = 64 * 128, STAGE1 = ABYTES + 3 * WSLAB, NST1 = 3;
constexpr int XB = NST1 * STAGE1;
constexpr int X_WO = XB;
constexpr int X_CSP = XB + 8192;
constexpr int X_P = 131072 + 4096;
constexpr int X_XW = X_P;
constexpr int X_XA = X_XW + 6144;
constexpr int X_ST = X_XA + 2048;
constexpr int X_MD = X_ST + 1024;
constexpr int X_AM = X_MD + 1024;
constexpr int X_XS = X_AM + 64;
constexpr int X_YR = X_XS + 512;
constexpr int X_YS = X_YR + 2048;
constexpr int X_WP = X_YS + 2048;
constexpr int X_KB = X_WP + 4096;
constexpr int LDS_TOTAL = 163840;
static_assert(X_KB + 2 * 4224 <= LDS_TOTAL && 131072 + 6 * 2048 <= X_MD && X_YS + 64 + 4096 <= X_KB && X_YS % 128 == 64, "lds");
static_assert(LDS_TOTAL <= 163840, "lds");
constexpr int gu_at(int ks) { return STAGE1 + ks * 8192; }
constexpr int HANDOFF = 106496;

#define LD128(off) __builtin_amdgcn_raw_buffer_load_b128(rs, (int)(off), 0, 16)
#define ST128(v, off) __builtin_amdgcn_raw_buffer_store_b128((v), rs, (int)(off), 0, 16)
#define ST64(v, off) __builtin_amdgcn_raw_buffer_store_b64((v), rs, (int)(off), 0, 16)
DI u32x4 f2u_tag(f32x4 v, unsigned t8) { u32x4 u = __builtin_bit_cast(u32x4, v); return (u & 0xFFFFFF00u) | t8; }
DI unsigned or4(u32x4 v) { return (v[0] | v[1]) | (v[2] | v[3]); }
#define SPIN_NEXT(sp) { if (++(sp) > SPIN_CAP) break; __builtin_amdgcn_s_sleep(1); }
#define TAG_OF(en) ((en) * 0x9E3779B97F4A7C15ull)

__global__ __launch_bounds__(512, 2) void kf_all(const u16* __restrict__ x, const u16* __restrict__ wqkv, const u16* __restrict__ wphi,
                                              const u16* __restrict__ wo, const u16* __restrict__ w_r, const u16* __restrict__ w_gate,
                                              const u16* __restrict__ w_up, const u16* __restrict__ w_down, char* ws, u16* __restrict__ out) {
  extern __shared__ __attribute__((aligned(16))) char smem[];
  asm volatile(".Lkf_pf_start:");
  const int b = blockIdx.x;
  const int xcd = b & 7, jj = b >> 3;
  const int panel = (xcd >> 2) * 8 + (jj & 7), hp = (xcd & 3) * 4 + (jj >> 3);
  const int id = panel * 16 + hp;
  const int tid = threadIdx.x, l = tid & 63, w = __builtin_amdgcn_readfirstlane(tid >> 6);
  unsigned pfd;
  {
    u64 pc; unsigned len, back;
    asm volatile("s_getpc_b64 %0\n.Lkf_pf_here:\n\ts_mov_b32 %1, .Lkf_pf_end+1152-.Lkf_pf_start\n\ts_mov_b32 %2, .Lkf_pf_here-.Lkf_pf_start" : "=s"(pc), "=s"(len), "=s"(back));
    unsigned o = (unsigned)(jj * 8 + w) * 256u;
    o = o + 256u <= len ? o : len - 256u;
    const u64 pa = (pc - back + o) & ~63ull;
    asm volatile("s_load_dword %0, %1, 0x0\n\ts_load_dword %0, %1, 0x40\n\ts_load_dword %0, %1, 0x80\n\ts_load_dword %0, %1, 0xc0" : "=&s"(pfd) : "s"(pa) : "memory");
  }
  asm volatile("" : "+s"(x), "+s"(wqkv), "+s"(wphi), "+s"(wo), "+s"(w_r), "+s"(w_gate), "+s"(w_up), "+s"(w_down), "+s"(ws), "+s"(out));
  SB0();
#define GLD(dst, p) asm volatile("global_load_dwordx4 %0, %1, off" : "=v"(dst) : "v"(p) : "memory")
  u32x4 xr4[4], wr4[4];
  if (w < 4) {
    const u16* xr = x + (size_t)(panel * 128 + hp * 8 + (tid >> 5)) * DM + (l & 31) * 8;
    const u16* wr = w_r + (l & 31) * 8;
    GLD(xr4[0], xr); GLD(xr4[1], xr + 256); GLD(xr4[2], xr + 512); GLD(xr4[3], xr + 768);
    GLD(wr4[0], wr); GLD(wr4[1], wr + 256); GLD(wr4[2], wr + 512); GLD(wr4[3], wr + 768);
  }
  SB0();
  const int cw = w & 3, wm = cw & 1, wn = cw >> 1;
  const int h = l >> 5, r31 = l & 31, blk = (l >> 4) & 1, tq = (l & 15) >> 2, tp = l & 3;
  const int hd = hp * 2 + wn;
  const unsigned lbase = LDS_OFF(smem);
  const __amdgpu_buffer_rsrc_t rs = __builtin_amdgcn_make_buffer_rsrc((void*)ws, 0, (int)WS_END, 0x00020000);
  bf16x8 fr[4];
  bf16x8 wp[2];
  f32x4 qa[4][2]; float ri[4];
  const int kh = w >> 2;
  const int wt = wm * 2 + kh;
  char* img = smem + (wn * 4 + wt) * 4608 + r31 * 144 + h * 32;
  auto wpload = [&]() {
    const char* p = smem + X_WP + wn * 2048 + (8 * h + tq) * 64 + (16 * blk + 4 * tp) * 2;
    wp[0] = cat8(trread(p), trread(p + 256)); wp[1] = cat8(trread(p + 1024), trread(p + 1280));
  };
  auto imgw = [&](int g, const f32x2 (&ep)[8], const f32x2 (&en)[8]) {
    const f32x2 (&e)[8] = g < 2 ? ep : en; const int q = 4 * (g & 1);
    *(u32x4*)(img + (g >> 1) * 64 + (g & 1) * 16) = u32x4{pk2(e[q][0], e[q][1]), pk2(e[q + 1][0], e[q + 1][1]), pk2(e[q + 2][0], e[q + 2][1]), pk2(e[q + 3][0], e[q + 3][1])};
  };
  auto pack_t = [&](const f32x4 x0, const f32x4 x1, float r0, float r1) {
    const f32x2 a0 = f32x2{x0[0], x0[1]} * f32x2{r0, r0}, a1 = f32x2{x0[2], x0[3]} * f32x2{r0, r0};
    const f32x2 b0 = f32x2{x1[0], x1[1]} * f32x2{r1, r1}, b1 = f32x2{x1[2], x1[3]} * f32x2{r1, r1};
    const auto s0 = __builtin_amdgcn_permlane16_swap(pk2(a0[0], a0[1]), pk2(b0[0], b0[1]), false, false);
    const auto s1 = __builtin_amdgcn_permlane16_swap(pk2(a1[0], a1[1]), pk2(b1[0], b1[1]), false, false);
    return u32x4{s0[0], s1[0], s0[1], s1[1]};
  };
  auto pack_u = [&](const f32x4 x0, const f32x4 x1) {
    const auto s0 = __builtin_amdgcn_permlane16_swap(pk2(x0[0], x0[1]), pk2(x1[0], x1[1]), false, false);
    const auto s1 = __builtin_amdgcn_permlane16_swap(pk2(x0[2], x0[3]), pk2(x1[2], x1[3]), false, false);
    return u32x4{s0[0], s1[0], s0[1], s1[1]};
  };
  const int hr = hp ^ 8;
  const int p3 = ((xcd >> 1) * 4 + (jj & 3)) ^ 8, nb = (int)(((xcd & 1) ? 0xF7EDCA98u : 0xB3654210u) >> (jj & 28)) & 15;
  const int flip = jj & 1;
  const unsigned voO = (unsigned)((l >> 3) * DM + (((l & 7) ^ (((l >> 4) & 1) | ((l >> 5) << 2))) * 8)) * 2;
  const bool has_gu = (hp & 7) != 3;
  const int gr = !has_gu ? 224 + (jj - 24) * 4 + (xcd >> 1) : jj >= 28 ? 8 * (4 * (xcd >> 1) + jj - 16) + 6 : (xcd == 6 && jj >= 12) ? 8 * (24 + (jj & 3)) + 2 * ((jj >> 2) - 3) : b;
  const int gkq = (gr / 56) & 3;
  u32x4 dv[8]; unsigned ogq; bf16x8 dn[4];
  u32x4 wr_[16];
    const int srow = l >> 3, sch = l & 7;
    unsigned voA[4], voW[6];
#pragma unroll
    for (int i = 0; i < 4; ++i) voA[i] = (unsigned)((panel * 128 + (4 * cw + i) * 8 + srow) * DM + ((sch ^ (4 * (i & 1) + (srow >> 1))) * 8)) * 2;
#pragma unroll
    for (int i = 0; i < 6; ++i) voW[i] = (unsigned)(((2 * cw + (i & 1)) * 8 + srow) * 3072 + (i >> 1) * 1024 + hp * 64 + ((sch ^ ((((srow >> 1) & 1) << 2) | ((i & 1) << 1))) * 8)) * 2;
    const unsigned lwa = lbase + cw * 4096, lww = lbase + ABYTES + cw * 2048;
    const int rot = 4 * ((panel & 7) >> 1) + 2 * (panel >> 3) + 4 * (xcd & 3);
    const u64 kord = !has_gu ? (0xFB73EA6D95C21840ull ^ ((panel & 1) ? 0x1111111111111111ull : 0ull)) : (panel & 8) ? ((panel & 1) ? 0xD95FB73EA62C1840ull : 0xD95FB73EA62C1840ull) : (panel & 1) ? 0xFB73EA62D95C1840ull : 0xFB73EA62D95C1840ull;
    auto stagep = [&](int it, auto bufc, auto loc, auto hic) {
      constexpr int B = decltype(bufc)::value * STAGE1, lo = decltype(loc)::value, hi = decltype(hic)::value;
      const int kt = ((int)(kord >> (4 * it)) + rot) & 15;
      const u16* gx = x + kt * 64; const u16* gq_ = wqkv + (size_t)kt * 64 * 3072;
      static_for<hi - lo>([&](auto ic) {
        constexpr int i = lo + decltype(ic)::value;
        if constexpr (i < 4) gl_m0<B + i * 1024>(voA[i], gx, lwa);
        else gl_m0<B + ((i - 4) >> 1) * WSLAB + (i & 1) * 1024>(voW[i - 4], gq_, lww);
      });
    };
    auto stage = [&](int it, auto bufc) { stagep(it, bufc, std::integral_constant<int, 0>{}, std::integral_constant<int, 10>{}); };
  unsigned pa0, pvh, dst, dz, pgw, ogu, oh1, oh1z;
  auto kvsetup = [&]() {
    const int g = l >> 4, c15 = l & 15;
    pa0 = lbase + kh * 18432 + (4 * g + tq) * 144 + (4 * cw + tp) * 8;
    pvh = lbase + HANDOFF + kh * 12288 + l * 16;
    dst = WS_KVP + (unsigned)((hp * 16 + panel) * 16 + (w & 6) * 2 + (g & 2) + (w & 1)) * 512 + c15 * 32 + (g & 1) * 16;
    dz = WS_ZP + (unsigned)((hp * 16 + (w & 6) * 2 + (g & 2) + (w & 1)) * 16 + panel) * 32 + (g & 1) * 16;
    pgw = lbase + gu_at(0) + kh * 4096 + (8 * g + tq) * 128 + (((2 * cw + (tp >> 1)) ^ ((((tq >> 1) & 1) << 2) | ((g & 1) << 1))) * 16) + 8 * (tp & 1);
    ogu = WS_GUP + (unsigned)gr * 512 + w * 64 + g * 16;
    oh1 = WS_KVP + (unsigned)((hr * 16 + (l >> 3)) * 16 + panel) * 512 + cw * 128 + (l & 7) * 16;
    oh1z = WS_ZP + (unsigned)((hr * 16 + panel) * 16) * 32 + l * 4;
    asm volatile("" : "+v"(pa0), "+v"(pvh), "+v"(dst), "+v"(dz), "+v"(pgw), "+v"(ogu), "+v"(oh1), "+v"(oh1z));
  };
#define IC(n) std::integral_constant<int, (n)>{}
#define CODE_PF(tagn, FROM, d) do { u64 pc_; unsigned len_; int off_; \
    asm volatile("s_getpc_b64 %0\n.Lkf_pf_h" #tagn ":\n\ts_mov_b32 %1, .Lkf_pf_end+1152-" FROM "\n\ts_mov_b32 %2, " FROM "-.Lkf_pf_h" #tagn : "=s"(pc_), "=s"(len_), "=s"(off_)); \
    unsigned o_ = (unsigned)(jj * 4 + cw) * 256u; o_ = o_ + 256u <= len_ ? o_ : len_ - 256u; \
    const u64 pa_ = (pc_ + (long long)off_ + o_) & ~63ull; \
    asm volatile("s_load_dword %0, %1, 0x0\n\ts_load_dword %0, %1, 0x40\n\ts_load_dword %0, %1, 0x80\n\ts_load_dword %0, %1, 0xc0" : "=&s"(d) : "s"(pa_) : "memory"); } while (0)
#define PF_DONE(d) asm volatile("s_waitcnt lgkmcnt(0)" : "+s"(d) :: "memory")
  if (w < 4) {
    unsigned oc2;
    {
      const unsigned r0 = (unsigned)(panel * 128 + hp * 8);
      oc2 = l < 33 ? WS_KVF + (unsigned)(hr * 16 + panel) * 640 + l * 16 : l == 33 ? WS_F1 + (unsigned)id * 16 : l == 34 ? WS_STAT + r0 * 8 : l == 35 ? WS_RI + (unsigned)(panel * 1024 + hp * 32)
          : l < 40 ? WS_F4 + (unsigned)(((panel * 4 + (l - 36)) * 16 + hp) * 2) * 32 : l < 44 ? WS_GUP + (unsigned)gr * 512 + (l - 40) * 128 : l == 44 ? WS_RI + (unsigned)(panel * 1024 + 512 + hp * 16)
          : WS_ZP + (unsigned)((hp * 16 + (l & 15)) * 16 + panel) * 32;
    }
    u32x4 tm, k0, k1; u32x2 tk;
    const unsigned tmofs = lbase + X_CSP + (l < 47 ? l : 46) * 16, kofs = lbase + X_CSP + 768, tgofs = lbase + X_AM + 48;
    unsigned opay;
    { const unsigned r0 = (unsigned)(panel * 128 + hp * 8);
      opay = l < 32 ? WS_CS + (unsigned)(hp * 8 + (panel >> 1)) * 768 + (panel & 1) * 512 + l * 16 : l < 40 ? WS_XA + r0 * 16 + (l - 32) * 16 : l < 44 ? WS_STAT + r0 * 8 + (l - 40) * 16 : l < 46 ? WS_RI + (unsigned)(panel * 1024 + hp * 32) + (l - 44) * 16 : WS_RI + (unsigned)(panel * 1024 + 512 + hp * 16); }
    const unsigned oflag = WS_F1 + (unsigned)id * 16;
    unsigned mz; asm volatile("s_cmp_eq_u32 %1, 0\n\ts_cselect_b32 %0, -1, 0" : "=s"(mz) : "s"(cw) : "scc");
    const u64 mk_all = ((u64)mz << 32) | mz, mk_pay = ((u64)(mz & 0x7FFFu) << 32) | (mz & ((panel & 1) ? 0xFFFFu : 0xFFFFFFFFu)), mk_one = mz & 1u;
    asm volatile("" : "+v"(oc2), "+v"(opay));
#define MST(op, data, off, mask) asm volatile("s_mov_b64 exec, %3\n\t" op " %0, %1, %2, 0 offen sc1\n\ts_mov_b64 exec, -1" :: "v"(data), "v"(off), "s"(rs), "s"(mask) : "memory")
    f32x4 csa[2] = {{0.f, 0.f, 0.f, 0.f}, {0.f, 0.f, 0.f, 0.f}};
    const bf16x8 ones = {0x3F80, 0x3F80, 0x3F80, 0x3F80, 0x3F80, 0x3F80, 0x3F80, 0x3F80};
    s16x4 cl[2][2], ch[2][2];
    const unsigned csw = lbase + X_CSP + (wm * 64 + wn * 32 + r31) * 4;
    f32x4 acc[4][6];
#pragma unroll
    for (int t = 0; t < 4; ++t)
#pragma unroll
      for (int a = 0; a < 6; ++a) acc[t][a] = f32x4{0.f, 0.f, 0.f, 0.f};
    const int g4 = l >> 4, c15 = l & 15;
    unsigned asw[2][2], wofs[2][2], cofs[2];
#pragma unroll
    for (int e = 0; e < 2; ++e) {
      asw[0][e] = lbase + (wm * 64 + c15) * 128 + (((4 * e + g4) ^ ((c15 >> 1) & 7)) * 16);
      wofs[0][e] = lbase + ABYTES + (8 * g4 + tq) * 128 + (((wn * 4 + 2 * e + (tp >> 1)) ^ ((((tq >> 1) & 1) << 2) | ((g4 & 1) << 1))) * 16) + 8 * (tp & 1);
      asw[1][e] = asw[0][e] + 2 * STAGE1; wofs[1][e] = wofs[0][e] + 2 * STAGE1;
      cofs[e] = wofs[0][e] + ((panel & 1) ? 2 : wm) * WSLAB;
    }
    asm volatile("" : "+v"(asw[0][0]), "+v"(asw[0][1]), "+v"(asw[1][0]), "+v"(asw[1][1]), "+v"(wofs[0][0]), "+v"(wofs[0][1]), "+v"(wofs[1][0]), "+v"(wofs[1][1]), "+v"(cofs[0]), "+v"(cofs[1]));
    bf16x8 af[2][4]; s16x4 wl[2][6], wh_[2][6];
    s16x4 ea[4], ew[6];
    const unsigned eaofs = lbase + X_XA + (wm * 64 + c15) * 16;
    const unsigned ewofs = lbase + X_XW + g4 * 1536 + (wn * 32 + c15) * 4;
    const unsigned riofs = lbase + X_ST + (wm * 64 + c15) * 4;
    auto rd = [&](auto ic, auto stc) {
      constexpr int i = decltype(ic)::value, S = decltype(stc)::value, s = S & 1, e = S & 1, buf = (S >> 1) % NST1;
      constexpr int b2 = buf == 2, bo = buf == 1 ? STAGE1 : 0;
      (void)&cl; (void)&ch; (void)&cofs; (void)&af; (void)&wl; (void)&wh_; (void)&asw; (void)&wofs;
      if constexpr (i == 0) DS_B128(af[s][0], asw[b2][e], bo);
      else if constexpr (i == 1) DS_TR(wl[s][0], wofs[b2][0], bo + e * 4096);
      else if constexpr (i == 2) DS_TR(wh_[s][0], wofs[b2][0], bo + e * 4096 + 512);
      else if constexpr (i < 6) DS_B128(af[s][i - 2], asw[b2][e], bo + (i - 2) * 2048);
      else if constexpr (i < 16) {
        constexpr int j = (i - 4) >> 1, off = bo + e * 4096 + (j >> 1) * WSLAB;
        if constexpr (i & 1) DS_TR(wh_[s][j], wofs[b2][j & 1], off + 512); else DS_TR(wl[s][j], wofs[b2][j & 1], off);
      } else {
        constexpr int c = (i - 16) >> 1;
        if constexpr (i & 1) DS_TR(ch[s][c], cofs[c], bo + e * 4096 + 512); else DS_TR(cl[s][c], cofs[c], bo + e * 4096);
      }
    };
    {
      kvsetup();
      SB0();
      asm volatile("s_waitcnt vmcnt(0) lgkmcnt(0)" : "+s"(pfd), "+v"(xr4[0]), "+v"(xr4[1]), "+v"(xr4[2]), "+v"(xr4[3]), "+v"(wr4[0]), "+v"(wr4[1]), "+v"(wr4[2]), "+v"(wr4[3]) :: "memory");
      SB0();
      float m, den, rinv, r;
      ln_rows(xr4, wr4, h, m, den, rinv, r);
      const int rw = tid >> 5;
      const unsigned rb = __float_as_uint(r) >> 16, ord = rb ^ ((rb & 0x8000u) ? 0xFFFFu : 0x8000u), nm = pk2(-m, -m);
      if (r31 == 0) { *(u32x4*)(smem + X_CSP + 512 + rw * 16) = u32x4{nm, nm, nm, nm};
        *(f32x2*)(smem + X_CSP + 640 + rw * 8) = f32x2{m, den}; *(float*)(smem + X_CSP + 704 + rw * 4) = rinv; *(u16*)(smem + X_CSP + 736 + rw * 2) = (u16)(__float_as_uint(den) >> 16);
        *(unsigned*)(smem + X_CSP + 768 + rw * 4) = (ord << 11) | (unsigned)(2047 - (id * 8 + rw)); }
      SB0();
    }
    __builtin_amdgcn_s_barrier();
    static_for<20>([&](auto ic) { rd(ic, IC(0)); });
    static_for<30>([&](auto stc) {
      constexpr int S = decltype(stc)::value, s = S & 1;
      constexpr bool bnd = (S & 1) == 1, more = S + 1 < 32;
      static_for<24>([&](auto mc) {
        constexpr int m = decltype(mc)::value, j = m >> 2, t = (j & 1) ? 3 - (m & 3) : m & 3;
        (void)&tm; (void)&k0; (void)&k1; (void)&tk; (void)&ea; (void)&ew; (void)&ri; (void)&oc2; (void)&opay; (void)&oflag; (void)&mk_all; (void)&mk_pay; (void)&mk_one; (void)&rs;
        (void)&tmofs; (void)&kofs; (void)&tgofs; (void)&csw; (void)&csa; (void)&eaofs; (void)&ewofs; (void)&riofs;
        constexpr int issued = more ? (m < 2 ? 0 : m - 2 > 16 ? 16 : m - 2) : 0;
        if constexpr (m == 0) {
          if constexpr (bnd) asm volatile("s_waitcnt lgkmcnt(0)" : "+v"(af[s][0]), "+v"(af[s][1]), "+v"(af[s][2]), "+v"(af[s][3]), "+v"(wl[s][0]), "+v"(wh_[s][0]), "+v"(wl[s][1]), "+v"(wh_[s][1]),
                                          "+v"(wl[s][2]), "+v"(wh_[s][2]), "+v"(wl[s][3]), "+v"(wh_[s][3]), "+v"(wl[s][4]), "+v"(wh_[s][4]), "+v"(wl[s][5]), "+v"(wh_[s][5]));
          else asm volatile("s_waitcnt lgkmcnt(10)" : "+v"(af[s][0]), "+v"(af[s][1]), "+v"(af[s][2]), "+v"(af[s][3]), "+v"(wl[s][0]), "+v"(wh_[s][0]));
        } else if constexpr (!bnd && (m & 3) == 0) {
          constexpr int al = 10 - 2 * j + issued;
          asm volatile("s_waitcnt lgkmcnt(%2)" : "+v"(wl[s][j]), "+v"(wh_[s][j]) : "n"(al > 15 ? 15 : al));
        }
        SB0();
        const bf16x8 wf = cat8(wl[s][j], wh_[s][j]);
        if constexpr (j < 4) acc[t][j] = MFMA16(wf, af[s][t], acc[t][j]);
        else acc[t][j] = MFMA16(af[s][t], wf, acc[t][j]);
        SB0();
        if constexpr (m == 1) {
          if constexpr (S == 1) { const u32x4 sv = {SENT, SENT, SENT, SENT}; MST("buffer_store_dwordx4", sv, oc2, mk_all); }
          if constexpr (S == 11) { asm volatile("" : "+v"(tm), "+v"(k0), "+v"(k1), "+v"(tk)); MST("buffer_store_dwordx4", tm, opay, mk_pay); }
          if constexpr (S == F1_ST / 2) { WAIT_V(0); MST("buffer_store_dwordx2", tk, oflag, mk_one); }
          if constexpr (bnd && more) __builtin_amdgcn_s_barrier();
        }
        if constexpr (more && m >= 1 && m - 1 < (CSS(S + 1) ? 20 : 16)) rd(IC(m - 1), IC(S + 1));
        if constexpr (S == 8 && m == 18) {
          asm volatile("" : "+v"(csa[0]), "+v"(csa[1]));
          const float c = blk ? csa[1][0] : csa[0][0];
          const unsigned u = pk2(c, c - rbf(c));
          asm volatile("ds_write_b32 %0, %1" :: "v"(csw), "v"(u) : "memory");
        }
        if constexpr (S == 9 && m >= 18 && m < 22) {
          if constexpr (m == 18) DS_B128(tm, tmofs, 0);
          if constexpr (m == 19) DS_B128(k0, kofs, 0);
          if constexpr (m == 20) DS_B128(k1, kofs, 16);
          if constexpr (m == 21) asm volatile("ds_read_b64 %0, %1" : "=v"(tk) : "v"(tgofs));
        }
        if constexpr (S == 11 && m == 18) { k0 = __builtin_elementwise_max(k0, k1); asm volatile("" : "+v"(k0)); }
        if constexpr (S == 11 && m == 20) { tk[0] = (tk[0] & ~0x7FFFFFFu) | max(max(k0[0], k0[1]), max(k0[2], k0[3])); asm volatile("" : "+v"(tk)); }
#define DS_B64(dst, addr, off) asm volatile("ds_read_b64 %0, %1 offset:%2" : "=v"(dst) : "v"(addr), "n"(off))
#define DS_B32(dst, addr, off) asm volatile("ds_read_b32 %0, %1 offset:%2" : "=v"(dst) : "v"(addr), "n"(off))
#define DS_2(dst, addr, o0, o1) asm volatile("ds_read2_b32 %0, %1 offset0:%2 offset1:%3" : "=v"(dst) : "v"(addr), "n"(o0), "n"(o1))
        if constexpr (S == 30 && m >= 17) {
          if constexpr (m < 21) DS_B64(ea[m - 17], eaofs, (m - 17) * 256);
          else if constexpr (m < 23) { const unsigned a_ = ewofs + 256; DS_2(ew[m - 19], a_, (m - 21) * 16, (m - 21) * 16 + 192); }
        }
        SB0();
      });
      if constexpr (CSS(S)) {
        asm volatile("s_waitcnt lgkmcnt(15)" : "+v"(cl[s][0]), "+v"(ch[s][0]), "+v"(cl[s][1]), "+v"(ch[s][1]));
        SB0();
        csa[0] = MFMA16(ones, cat8(cl[s][0], ch[s][0]), csa[0]); csa[1] = MFMA16(ones, cat8(cl[s][1], ch[s][1]), csa[1]);
        SB0();
      }
    });
    static_for<16>([&](auto ic) {
      constexpr int i = decltype(ic)::value, j = 2 + (i >> 2), t = (j & 1) ? 3 - (i & 3) : i & 3;
      (void)&ea; (void)&ew; (void)&eaofs; (void)&ewofs;
      if constexpr (i == 0) asm volatile("s_waitcnt lgkmcnt(6)" : "+v"(af[0][0]), "+v"(af[0][1]), "+v"(af[0][2]), "+v"(af[0][3]), "+v"(wl[0][0]), "+v"(wh_[0][0]), "+v"(wl[0][1]), "+v"(wh_[0][1]), "+v"(wl[0][2]), "+v"(wh_[0][2]));
      else if constexpr ((i & 3) == 0) asm volatile("s_waitcnt lgkmcnt(%2)" : "+v"(wl[0][j]), "+v"(wh_[0][j]) : "n"(i == 4 ? 12 : 15));
      SB0();
      const bf16x8 wf = cat8(wl[0][j], wh_[0][j]);
      if constexpr (j < 4) acc[t][j] = MFMA16(wf, af[0][t], acc[t][j]); else acc[t][j] = MFMA16(af[0][t], wf, acc[t][j]);
      SB0();
      if constexpr (i < 8) { rd(IC(2 * i), IC(31)); rd(IC(2 * i + 1), IC(31)); }
      else if constexpr (i < 12) DS_B64(ea[i - 8], eaofs, (i - 8) * 256);
      else if constexpr (i < 14) { const unsigned a_ = ewofs + 256; DS_2(ew[i - 10], a_, (i - 12) * 16, (i - 12) * 16 + 192); }
      SB0();
    });
    {
      __builtin_amdgcn_s_setprio(PRIO_T);
      asm volatile("s_waitcnt lgkmcnt(0)" : "+v"(af[1][0]), "+v"(af[1][1]), "+v"(af[1][2]), "+v"(af[1][3]), "+v"(wl[1][0]), "+v"(wh_[1][0]), "+v"(wl[1][1]), "+v"(wh_[1][1]),
                   "+v"(wl[1][2]), "+v"(wh_[1][2]), "+v"(wl[1][3]), "+v"(wh_[1][3]), "+v"(wl[1][4]), "+v"(wh_[1][4]), "+v"(wl[1][5]), "+v"(wh_[1][5]));
      asm volatile("" : "+v"(ea[0]), "+v"(ea[1]), "+v"(ea[2]), "+v"(ea[3]), "+v"(ew[2]), "+v"(ew[3]));
      SB0();
      auto mm = [&](int t, int j) { const bf16x8 wf = cat8(wl[1][j], wh_[1][j]); if (j < 4) acc[t][j] = MFMA16(wf, af[1][t], acc[t][j]); else acc[t][j] = MFMA16(af[1][t], wf, acc[t][j]); };
      auto ff = [&](int t, int j) { if (j < 4) acc[t][j] = MFMA16H(ew[j], ea[t], acc[t][j]); else acc[t][j] = MFMA16H(ea[t], ew[j], acc[t][j]); };
      auto dm = [&](int n) {
        SB0();
        const int t = n & 3, j = (n < 16 ? 4 : 0) + ((n >> 2) & 1);
        if (n & 8) ff(t, j); else mm(t, j);
        SB0();
      };
      const unsigned ew0 = ewofs, ew2 = ewofs + 512;
      mm(2, 2); SB0(); DS_B32(ri[2], riofs, 128); DS_B32(ri[3], riofs, 192); SB0();
      mm(3, 2); SB0(); DS_B32(ri[0], riofs, 0); DS_B32(ri[1], riofs, 64); SB0();
      mm(2, 3); SB0(); DS_2(ew[4], ew2, 0, 192); DS_2(ew[5], ew2, 16, 208); SB0();
      mm(3, 3); SB0(); DS_2(ew[0], ew0, 0, 192); DS_2(ew[1], ew0, 16, 208); SB0();
      ff(2, 2); ff(3, 2); SB0(); wpload(); SB0();
      ff(2, 3); ff(3, 3); SB0(); SB0();
      mm(0, 2); mm(1, 2);
      SB0();
      SB0();
      char* ho = smem + HANDOFF + cw * 6144 + l * 16;
      *(u32x4*)(ho + 2048) = pack_u(acc[2][2], acc[3][2]);
      SB0(); mm(0, 3); mm(1, 3); SB0();
      *(u32x4*)(ho + 3072) = pack_u(acc[2][3], acc[3][3]);
      SB0(); ff(0, 2); ff(1, 2); ff(0, 3); ff(1, 3); SB0();
      RAW_BARRIER();
      asm volatile("" : "+v"(ri[0]), "+v"(ri[1]), "+v"(ri[2]), "+v"(ri[3]), "+v"(ew[0]), "+v"(ew[1]), "+v"(ew[4]), "+v"(ew[5]));
      const float rt = blk ? ri[1] : ri[0];
      dm(0); dm(1);
      fr[2] = __builtin_bit_cast(bf16x8, pack_u(acc[0][2], acc[1][2]));
      dm(2); dm(3);
      fr[3] = __builtin_bit_cast(bf16x8, pack_u(acc[0][3], acc[1][3]));
      dm(4);
      f32x2 ep[8], en[8];
      phi_fn<false>(wp, fr[2], fr[3], ep, en, rt, rt, [&](auto kc) { constexpr int k = decltype(kc)::value; if constexpr (k == 0) { dm(5); dm(6); dm(7); } else if constexpr (7 + k < 16) dm(7 + k); });
    { const unsigned a_ = lbase + X_ST + 512 + (l >> 4) * 8;
      static_for<4>([&](auto tc) { constexpr int tb = decltype(tc)::value; (void)&dn; (void)&a_;
        asm volatile("ds_read2_b64 %0, %1 offset0:%2 offset1:%3" : "=v"(dn[tb]) : "v"(a_), "n"(tb * 8), "n"(tb * 8 + 4)); }); }
#pragma unroll
      for (int g = 0; g < 4; ++g) imgw(g, ep, en);
#pragma unroll
      for (int u = 0; u < 4; ++u) {
        const int t2 = u >> 1, db = u & 1;
        const f32x4 x0 = acc[2 * t2][4 + db], x1 = acc[2 * t2 + 1][4 + db];
        char* pv = t2 ? ho + 4096 : ho;
        *(u32x4*)(pv + db * 1024) = u32x4{pk2(x0[0], x0[1]), pk2(x0[2], x0[3]), pk2(x1[0], x1[1]), pk2(x1[2], x1[3])};
      }
#pragma unroll
      for (int t = 0; t < 4; ++t) { qa[t][0] = acc[t][0]; qa[t][1] = acc[t][1]; wr_[t] = __builtin_bit_cast(u32x4, af[1][t]); wr_[9 + t] = __builtin_bit_cast(u32x4, af[0][t]); }
      wr_[13] = __builtin_bit_cast(u32x4, cat8(wl[0][0], wh_[0][0])); wr_[14] = __builtin_bit_cast(u32x4, cat8(wl[0][1], wh_[0][1]));
      wr_[4] = __builtin_bit_cast(u32x4, cat8(wl[1][0], wh_[1][0])); wr_[5] = __builtin_bit_cast(u32x4, cat8(wl[1][1], wh_[1][1]));
      wr_[6] = __builtin_bit_cast(u32x4, cat8(ea[0], ea[1])); wr_[7] = __builtin_bit_cast(u32x4, cat8(ea[2], ea[3])); wr_[8] = __builtin_bit_cast(u32x4, cat8(ew[0], ew[1]));
    }
  } else {
    const int t = tid - 256;
    __builtin_amdgcn_s_setprio(PRIO_H);
    u64 epoch;
    asm volatile("s_load_dwordx2 %0, %1, 0x0" : "=&s"(epoch) : "s"(ws) : "memory");
    stagep(0, IC(0), IC(0), IC(10));
    asm volatile("s_waitcnt lgkmcnt(0)" : "+s"(pfd), "+s"(epoch) :: "memory");
    const u64 enx = epoch + (((unsigned)epoch & 0xFEu) == 0xFEu ? 3u - ((unsigned)epoch & 1u) : 1u);
    const u64 tag = TAG_OF(enx);
    if (t == 0) { *(u32x2*)(smem + X_AM) = u32x2{0u, 0u};
      const unsigned e2 = ((unsigned)enx & 0x55u) | (((unsigned)enx & 0xAAu) << 15);
      *(u32x4*)(smem + X_AM + 32) = u32x4{e2, e2 >> 2, e2 >> 4, e2 >> 6} & 0x00010001u;
      *(u32x4*)(smem + X_AM + 48) = u32x4{(unsigned)tag, (unsigned)(tag >> 32), (unsigned)enx, (unsigned)(enx >> 32)}; }
    u32x2 fl, si; unsigned pfd2, tpace = 0, xs;
    unsigned ofl = WS_F1 + (unsigned)t * 16, orA = WS_CS + (unsigned)(hp * 6144 + cw * 1024 + l * 16);
    unsigned orB = (cw < 2 ? WS_CS + (unsigned)(hp * 6144 + 4096 + cw * 1024) : WS_XA + (unsigned)(panel * 2048 + (cw - 2) * 1024)) + l * 16;
    unsigned orC = ((cw & 1) ? WS_STAT + (unsigned)p3 * 1024 : WS_RI + (unsigned)panel * 1024) + l * 16;
    unsigned oxs = (unsigned)(gkq * 256 + cw * 64 + r31 * 2) * 2, akey = lbase + X_AM + 4, vol = (unsigned)l * 16;
    unsigned voG = (unsigned)((8 * cw + srow) * FF + ((sch ^ ((((srow >> 1) & 1) << 2) | ((cw & 1) << 1))) * 8)) * 2;
    const u16* gg_ = w_gate + (size_t)gkq * 256 * FF + ((gr + 1) % 56) * 64;
    const u16* gu_ = w_up + (size_t)gkq * 256 * FF + ((gr + 3) % 56) * 64;
    const u16* gwo = wo + (size_t)(((xcd >> 1) * 4 + (jj >> 3)) * 64 + 2 * cw * 8) * DM + ((xcd & 1) * 8 + (jj & 7)) * 64;
    const u16* gwp = wphi + hp * 2048 + cw * 512;
    const u16* gdw = w_down + (size_t)(((gr >> 3) & 31) * 128 + 32 * cw) * DM + ((gr + 1) & 7) * 128;
    unsigned odw = (unsigned)((l >> 4) * DM + (l & 15) * 8) * 2;
    const u16* gw2a = wo + (size_t)(((cw * 10) ^ (flip * 64)) * 8) * DM + nb * 64;
    const u16* gw2b = wo + (size_t)(((104 + cw * 6) ^ (flip * 64)) * 8) * DM + nb * 64;
    asm volatile("" : "+s"(gw2a), "+s"(gw2b));
    asm volatile("" : "+s"(gg_), "+s"(gu_), "+s"(gwo), "+s"(gwp), "+s"(gdw));
    { const int f = (gr >> 3) * 128 + 32 * cw + 4 * (l & 7), fs = (f >> 6) - ((l & 8) ? 3 : 1);
      ogq = WS_GUP + (unsigned)((l >> 4) * 56 + (fs < 0 ? fs + 56 : fs)) * 512 + (f & 63) * 4 + (l & 8) * 32; }
    asm volatile("" : "+v"(voG), "+v"(odw), "+v"(ogq));
    kvsetup();
    asm volatile("s_waitcnt lgkmcnt(0)" : "+v"(ofl), "+v"(orA), "+v"(orB), "+v"(orC), "+v"(oxs), "+v"(akey), "+v"(vol) :: "memory");
    stagep(0, IC(0), IC(10), IC(10));
    stagep(1, IC(1), IC(0), IC(10));
    WAIT_V(10);
    __builtin_amdgcn_s_barrier();
    stagep(1, IC(1), IC(10), IC(10));
    auto wopf = [&]() {
      GLDS_S(voO, gwo, lbase + X_WO + 2 * cw * 1024); GLDS_S(voO, gwo + 8 * DM, lbase + X_WO + (2 * cw + 1) * 1024);
      GLDS_S(vol, gwp, lbase + X_WP + cw * 1024);
    };
    auto dvl = [&](auto kc) {
      constexpr int k = decltype(kc)::value;
      static_for<DVN[k + 2]>([&](auto ic) {
        constexpr int i = dv_first(k) + decltype(ic)::value;
        u32x4& d_ = dv[i]; const unsigned o_ = odw; const u16* p_ = gdw + (size_t)(4 * i) * DM;
        asm volatile("global_load_dwordx4 %0, %1, %2 nt" : "=v"(d_) : "v"(o_), "s"(p_) : "memory");
      });
    };
    if constexpr (DVN[0] != 0) { if (has_gu) dvl(IC(-2)); }
    stagep(2, IC(2), IC(0), IC(S2A));
    if constexpr (WOK == -1) wopf();
    if constexpr (DVN[1] != 0) { if (has_gu) dvl(IC(-1)); }
    const unsigned lgu = lbase + cw * 1024;
    auto gul = [&](auto ksc) {
      constexpr int ks = decltype(ksc)::value;
      gl_nt<gu_at(ks)>(voG, gg_ + (size_t)ks * 32 * FF, lgu); gl_nt<gu_at(ks) + 4096>(voG, gu_ + (size_t)ks * 32 * FF, lgu);
    };
#define BLD64(dst, off) asm volatile("buffer_load_dwordx2 %0, %1, %2, 0 offen sc1" : "=v"(dst) : "v"(off), "s"(rs) : "memory")
    static_for<15>([&](auto ktc) {
      constexpr int kt = decltype(ktc)::value;
      constexpr auto exw = [](int k) { return DVN[k + 2]; };
      constexpr auto exo = [](int k) { return k == KP ? 1 : k == KC + 1 ? 3 : k == KC + 2 ? 2 : 0; };
      constexpr int exa = ((kt - 2 == WOK || kt - 1 == WOK) ? 3 : 0) - (kt == 0 ? 10 - S2A : 0) + (kt - 2 > KC ? exo(kt - 2) : 0);
      if constexpr (exw(kt - 2) + exw(kt - 1) != 0) { if (has_gu) WAIT_V(10 + exa + exw(kt - 2) + exw(kt - 1) + exo(kt - 1)); else WAIT_V(10 + exa + exo(kt - 1)); }
      else if constexpr (kt < 14) WAIT_V(10 + exa + exo(kt - 1));
      else { if (has_gu) WAIT_V(10); else WAIT_V(0); }
      if constexpr (PACE_T[kt] != 0) { if (cw == 0) { while ((unsigned)__builtin_amdgcn_s_memrealtime() - tpace < (unsigned)(has_gu ? PACE_T[kt] : PACE_3[kt])) {} } }
      __builtin_amdgcn_s_barrier();
      if constexpr (PACE_T[kt + 1] != 0) { if (cw == 0) tpace = (unsigned)__builtin_amdgcn_s_memrealtime(); SB0(); }
      if constexpr (kt == 0) stagep(2, IC(2), IC(S2A), IC(10));
#define UMAX_DPP(C) { const unsigned o = (unsigned)__builtin_amdgcn_update_dpp(0, (int)key, C, 0xf, 0xf, true); key = o > key ? o : key; asm volatile("" : "+v"(key)); }
#define SP(lo, hi) if constexpr (kt + NST1 < 16) { SB0(); stagep(kt + NST1, IC(kt % NST1), IC(lo), IC(hi)); SB0(); }
      SP(0, 10)
      if constexpr (kt == WOK) wopf();
      if constexpr (kt == PF2_KT) CODE_PF(2, ".Lkf_pf_mid", pfd2);
      if constexpr (kt == PF2_KT + 2) PF_DONE(pfd2);
      if constexpr (kt == KP) BLD64(fl, ofl);
      if constexpr (kt == KC) {
        static_assert(DVN[KP + 2] == 0, "count");
        if constexpr (KC == KP + 1) asm volatile("s_waitcnt vmcnt(10)" : "+v"(fl) :: "memory"); else asm volatile("" : "+v"(fl) :: "memory");
#define FL_OK() __all((((fl[0] ^ (unsigned)tag) >> 27) | (fl[1] ^ (unsigned)(tag >> 32))) == 0u)
        if (__builtin_expect(!FL_OK(), 0)) {
          for (unsigned sp = 0;;) {
            asm volatile("buffer_load_dwordx2 %0, %1, %2, 0 offen sc1\n\ts_waitcnt vmcnt(0)" : "=v"(fl) : "v"(ofl), "s"(rs) : "memory");
            if (FL_OK()) break;
            SPIN_NEXT(sp);
          }
        }
        unsigned key = fl[0];
        UMAX_DPP(0x121) UMAX_DPP(0x122) UMAX_DPP(0x124) UMAX_DPP(0x128)
        asm volatile("s_mov_b64 exec, %2\n\tds_max_u32 %0, %1\n\ts_mov_b64 exec, -1\n\ts_waitcnt lgkmcnt(0)" :: "v"(akey), "v"(key), "s"(0x0001000100010001ull) : "memory");
      }
      if constexpr (kt == KC + 1) { GLDS_SC1(orA, ws, lbase + X_XW + cw * 1024); GLDS_SC1(orB, ws, lbase + X_XW + 4096 + cw * 1024); GLDS_SC1(orC, ws, lbase + X_ST + (cw & 1) * 1024); }
      if constexpr (kt == KC + 2) {
        const int bi = 2047 - (int)(__builtin_amdgcn_readfirstlane(*(const unsigned*)(smem + X_AM + 4)) & 2047u);
        asm volatile("global_load_dword %0, %1, %2" : "=v"(xs) : "v"(oxs), "s"(x + (size_t)bi * DM) : "memory");
        asm volatile("buffer_load_dwordx2 %0, off, %1, %2 sc1" : "=v"(si) : "s"(rs), "s"(WS_STAT + (unsigned)bi * 8) : "memory");
      }
      if constexpr (DVN[kt + 2] != 0) { if (has_gu) dvl(ktc); }
      if constexpr (kt == 13) { if (has_gu) { gul(IC(0)); gul(IC(1)); gul(IC(2)); gul(IC(3)); gul(IC(4)); } }
      if constexpr (kt == 14) asm volatile("" : "+v"(xs), "+v"(si) :: "memory");
      if constexpr (kt == 14) { if (has_gu) { static_for<1>([&](auto ic) { gul(IC(5 + decltype(ic)::value)); }); } }
    });
    auto wld = [&](auto ic) {
      constexpr int i = decltype(ic)::value;
      u32x4& d_ = wr_[i]; const unsigned o_ = voO; const u16* p_ = (i < 10 ? gw2a : gw2b) + (size_t)((i < 10 ? i : i - 10) * 8) * DM;
      asm volatile("global_load_dwordx4 %0, %1, %2" : "=v"(d_) : "v"(o_), "s"(p_) : "memory");
    };
    wpload();
    const float rt = own(*(const float*)(smem + X_ST + (wt * 32 + r31) * 4));
    {
      const float m = __uint_as_float(si[0]), den = own(__uint_as_float(si[1])), rinv = rcp_nr(den);
      *(unsigned*)(smem + X_XS + cw * 128 + r31 * 4) = ln_pair(xs, m, den, rinv);
    }
    RAW_BARRIER();
    const char* ho = smem + HANDOFF + cw * 6144 + l * 16;
    fr[2] = *(const bf16x8*)(ho + 2048); fr[3] = *(const bf16x8*)(ho + 3072);
    f32x2 ep[8], en[8];
    phi_fn<false>(wp, fr[2], fr[3], ep, en, rt, rt, [&](auto kc) {
      constexpr int k = decltype(kc)::value;
      if constexpr (k == 0) { SB0(); if (has_gu) gul(IC(6)); SB0(); }
      if constexpr (k == 5) { SB0(); if (has_gu) gul(IC(7)); SB0(); }
      static_for<NREG - WPOST>([&](auto ic) {
        constexpr int i = decltype(ic)::value;
        if constexpr (k == WH0 + (i * (17 - WH0)) / (NREG - WPOST)) { SB0(); wld(ic); SB0(); }
      });
    });
    { const unsigned a_ = lbase + X_ST + 512 + (l >> 4) * 8;
      static_for<4>([&](auto tc) { constexpr int tb = decltype(tc)::value; (void)&dn; (void)&a_;
        asm volatile("ds_read2_b64 %0, %1 offset0:%2 offset1:%3" : "=v"(dn[tb]) : "v"(a_), "n"(tb * 8), "n"(tb * 8 + 4)); }); }
#pragma unroll
    for (int g = 0; g < 4; ++g) imgw(g, ep, en);
    SB0();
    static_for<WPOST>([&](auto ic) { wld(IC(NREG - WPOST + decltype(ic)::value)); });
  }
  asm volatile(".Lkf_pf_mid:");
  f32x16 zero;
#pragma unroll
  for (int i = 0; i < 16; ++i) zero[i] = 0.f;
  u32x2 tg; unsigned t8, rkey; u32x4 t2;
  {
    __builtin_amdgcn_s_setprio(2);
    RAW_BARRIER();
    asm volatile("" : "+v"(dn[0]), "+v"(dn[1]), "+v"(dn[2]), "+v"(dn[3]));
    u32x4 am; unsigned rk;
    { const unsigned a_ = lbase + X_AM; DS_B128(am, a_, 48); DS_B128(t2, a_, 32); asm volatile("ds_read_b32 %0, %1 offset:4" : "=v"(rk) : "v"(a_)); }
    s16x4 al[4], ah[4]; bf16x8 v0[4], v1[4];
    DS_TR(al[0], pa0, 0); DS_TR(ah[0], pa0, 2304); DS_B128(v0[0], pvh, 0); DS_B128(v1[0], pvh, 1024);
    DS_TR(al[1], pa0, 4608); DS_TR(ah[1], pa0, 4608 + 2304); DS_B128(v0[1], pvh, 4096); DS_B128(v1[1], pvh, 4096 + 1024);
    DS_TR(al[2], pa0, 9216); DS_TR(ah[2], pa0, 9216 + 2304); DS_B128(v0[2], pvh, 6144); DS_B128(v1[2], pvh, 6144 + 1024);
    DS_TR(al[3], pa0, 13824); DS_TR(ah[3], pa0, 13824 + 2304); DS_B128(v0[3], pvh, 6144 + 4096); DS_B128(v1[3], pvh, 6144 + 4096 + 1024);
    const bf16x8 ones = {0x3F80, 0x3F80, 0x3F80, 0x3F80, 0x3F80, 0x3F80, 0x3F80, 0x3F80};
    f32x4 k0 = {0.f, 0.f, 0.f, 0.f}, k1 = k0, zz = k0;
    static_for<4>([&](auto tbc) {
      constexpr int tb = decltype(tbc)::value;
      asm volatile("s_waitcnt lgkmcnt(%4)" : "+v"(al[tb]), "+v"(ah[tb]), "+v"(v0[tb]), "+v"(v1[tb]) : "n"(12 - 4 * tb));
      const bf16x8 af = cat8(al[tb], ah[tb]);
      k0 = MFMA16(af, v0[tb], k0); k1 = MFMA16(af, v1[tb], k1); zz = MFMA16(af, dn[tb], zz);
      SB0();
    });
    asm volatile("" : "+v"(am), "+v"(rk), "+v"(t2));
    auto tagv = [&](f32x4 v) { const u32x4 u = __builtin_bit_cast(u32x4, v); return (u & 0xFFFFFF00u) | (am[2] & 0xFFu); };
    ST128((u32x4{pk2(k0[0], k0[1]), pk2(k0[2], k0[3]), pk2(k1[0], k1[1]), pk2(k1[2], k1[3])} & 0xFFFEFFFEu) | t2, dst);
    const u32x4 zt = tagv(zz);
    asm volatile("s_mov_b64 exec, %3\n\tbuffer_store_dwordx4 %0, %1, %2, 0 offen sc1\n\ts_mov_b64 exec, -1" :: "v"(zt), "v"(dz), "s"(rs), "s"(0x0001000100010001ull) : "memory");
    SB0();
    tg = u32x2{am[0], am[1]}; t8 = __builtin_amdgcn_readfirstlane(am[2]) & 0xFFu; rkey = __builtin_amdgcn_readfirstlane(rk);
  }
  const int idx = 2047 - (int)(rkey & 2047u);
  const int fb = kh ^ ((idx >> 4) & 1);
  const unsigned lxc = lbase + (cw < 2 ? 131072 : X_YS + 64 - 4096) + cw * 2048, lxv = w < 4 ? lxc : lbase + X_XW + cw * 2048;
  {
    bf16x8 xf[8];
    const unsigned px = lbase + X_XS + (l >> 4) * 16, hq = lbase + X_XW + cw * 2048 + l * 16;
    static_for<8>([&](auto kc) { constexpr int ks = decltype(kc)::value; (void)&xf; (void)&px; DS_B128(xf[ks], px, ks * 64); });
    if (w >= 4) {
      __builtin_amdgcn_s_setprio(0);
      WAIT_V(NREG + 2);
    } else {
      auto lo4 = [](u32x4 v) { return __builtin_bit_cast(s16x4, u32x2{v[0], v[1]}); };
      auto hi4 = [](u32x4 v) { return __builtin_bit_cast(s16x4, u32x2{v[2], v[3]}); };
#pragma unroll
      for (int tt = 0; tt < 4; ++tt) {
        const int t = tt ^ 2;
#pragma unroll
        for (int j = 0; j < 2; ++j) qa[t][j] = MFMA16(__builtin_bit_cast(bf16x8, wr_[13 + j]), __builtin_bit_cast(bf16x8, wr_[9 + t]), qa[t][j]);
#pragma unroll
        for (int j = 0; j < 2; ++j) qa[t][j] = MFMA16(__builtin_bit_cast(bf16x8, wr_[4 + j]), __builtin_bit_cast(bf16x8, wr_[t]), qa[t][j]);
        if (tt & 1) {
#pragma unroll
          for (int u = t - 1; u <= t; ++u)
#pragma unroll
            for (int j = 0; j < 2; ++j) qa[u][j] = MFMA16H(j ? hi4(wr_[8]) : lo4(wr_[8]), (u & 1) ? hi4(wr_[6 + (u >> 1)]) : lo4(wr_[6 + (u >> 1)]), qa[u][j]);
        }
      }
      const u32x4 q0 = pack_t(qa[2][0], qa[3][0], ri[2], ri[3]), q1 = pack_t(qa[2][1], qa[3][1], ri[2], ri[3]);
      asm volatile("ds_write_b128 %0, %1\n\tds_write_b128 %0, %2 offset:1024" :: "v"(hq), "v"(q0), "v"(q1) : "memory");
    }
    RAW_BARRIER();
    asm volatile("" : "+v"(xf[0]), "+v"(xf[1]), "+v"(xf[2]), "+v"(xf[3]), "+v"(xf[4]), "+v"(xf[5]), "+v"(xf[6]), "+v"(xf[7]));
    if (w >= 4) {
      { const u16* ga = wo + (size_t)(((cw * 10) ^ (flip * 64)) * 8) * DM + nb * 64; const u16* gb = wo + (size_t)(((104 + cw * 6) ^ (flip * 64)) * 8) * DM + nb * 64;
        static_for<16 - NREG>([&](auto ic) { constexpr int i = NREG + decltype(ic)::value;
          if constexpr (i < 10) gl_m0<i * 1024>(voO, ga + (size_t)(i * 8) * DM, lbase + cw * 10240); else gl_m0<(104 + i - 10) * 1024>(voO, gb + (size_t)((i - 10) * 8) * DM, lbase + cw * 6144); }); }
    }
    if (has_gu) {
      s16x4 gl[8], gh[8];
      static_for<8>([&](auto kc) { constexpr int ks = decltype(kc)::value; (void)&gl; (void)&gh; (void)&pgw;
        DS_TR(gl[ks], pgw, ks * 8192); DS_TR(gh[ks], pgw, ks * 8192 + 512); });
      f32x4 c0 = {0.f, 0.f, 0.f, 0.f};
      static_for<8>([&](auto kc) { constexpr int ks = decltype(kc)::value;
        asm volatile("s_waitcnt lgkmcnt(%2)" : "+v"(gl[ks]), "+v"(gh[ks]) : "n"(14 - 2 * ks));
        c0 = MFMA16(cat8(gl[ks], gh[ks]), xf[ks], c0);
        SB0(); });
      const u32x4 gt_ = f2u_tag(c0, t8);
      asm volatile("s_mov_b64 exec, %3\n\tbuffer_store_dwordx4 %0, %1, %2, 0 offen sc1\n\ts_mov_b64 exec, -1" :: "v"(gt_), "v"(ogu), "s"(rs), "s"(0x0001000100010001ull) : "memory");
    }
  }
  const int erow = cw * 32 + fb * 16 + (l & 15), ech = l >> 4;
  u32x4 xv[2];
  const bool sw = ech & 1;
#define FSWAP(v) v = u32x4{sw ? v[2] : v[0], sw ? v[3] : v[1], sw ? v[0] : v[2], sw ? v[1] : v[3]}
  const bool has_idx = w < 4 && (idx >> 5) == p3 * 4 + w;
  unsigned xrt = 0;
  unsigned of4, op4, oo0, oo1, kba, kbz, odo, o2, oz2, wa_, wz_, oxg;
  auto later = [&]() {
    SB0();
    of4 = WS_F4 + (unsigned)(((panel * 4 + wt) * 16 + hp) * 2 + wn) * 32; op4 = WS_F4 + (unsigned)(((p3 * 4 + cw) * 16 + 8 * (kh ^ flip)) * 2 + (l & 15)) * 32;
    oo0 = WS_O + 512 + (unsigned)((((p3 * 4 + cw) * 2 + fb) * 32 + (kh ^ flip) * 16) * 128 + h * 64 + r31) * 16; oo1 = oo0 ^ 65536u;
    kba = lbase + X_KB + wn * 4224 + l * 16; kbz = lbase + X_KB + wn * 4224 + 4096 + h * 64;
    odo = WS_O + 512 + (unsigned)((((panel * 4 + wt) * 2 + blk) * 32 + hd) * 128 + h * 16 + (l & 15)) * 16;
    asm volatile("" : "+v"(kba), "+v"(kbz), "+v"(odo), "+v"(of4), "+v"(op4), "+v"(oo0), "+v"(oo1));
    SB0();
  };
  if (w < 4) {
    unsigned pfd3, od; u64 mkp;
    {
      u32x4 pa[2]; unsigned z0 = t8, z1 = t8;
#define H1LD(p) asm volatile("buffer_load_dwordx4 %0, %4, %6, 0 offen sc1\n\tbuffer_load_dwordx4 %1, %4, %6, %7 offen sc1\n\t" \
                   "s_cmp_lg_u32 %8, 3\n\ts_cbranch_scc1 1f\n\tbuffer_load_dword %2, %5, %6, 0 offen sc1\n\tbuffer_load_dword %3, %5, %6, 0 offen offset:256 sc1\n1:" \
                   : "=&v"(p[0]), "=&v"(p[1]), "+v"(z0), "+v"(z1) : "v"(oh1), "v"(oh1z), "s"(rs), "s"(65536), "s"(cw) : "memory", "scc")
      __builtin_amdgcn_s_sleep(H1_DELAY);
      H1LD(pa);
      SB0();
      CODE_PF(3, ".Lkf_pf_mid", pfd3);
      od = WS_KVF + (unsigned)(hr * 16 + panel) * 640 + ((l & 8) ? 512 + (l & 7) * 2 : cw * 128 + h * 64 + (l & 7) * 8 + blk * 4);
      mkp = cw == 3 ? 0x00FF00FF00FF55FFull : 0x00FF00FF00FF00FFull;
      asm volatile("" : "+s"(mkp));
      fr[0] = __builtin_bit_cast(bf16x8, pack_t(qa[0][0], qa[1][0], ri[0], ri[1])); fr[1] = __builtin_bit_cast(bf16x8, pack_t(qa[0][1], qa[1][1], ri[0], ri[1]));
      asm volatile("" : "+v"(od), "+v"(fr[0]), "+v"(fr[1]));
      asm volatile("global_load_ushort %0, %1, %2" : "=v"(xrt) : "v"((unsigned)(idx * DM + nb * 64 + 4 * (l & 15) + ech) * 2), "s"(x) : "memory");
      later();
      o2 = WS_KVF + (unsigned)(hp * 16 + wn * 8 + wm * 4 + h) * 640 + ((r31 & 12) * 2 + (r31 >> 4) * 4 + (r31 & 3)) * 16;
      oz2 = WS_KVF + (unsigned)(hp * 16 + wn * 8 + wm * 4 + (l & 3)) * 640 + 512;
      wa_ = kba + wm * 2048; wz_ = lbase + X_KB + wn * 4224 + 4096 + (l & 1) * 64 + (wm * 2 + ((l >> 1) & 1)) * 16;
      asm volatile("" : "+v"(o2), "+v"(oz2), "+v"(wa_), "+v"(wz_));
      unsigned hb = 0x8000u, s_lo = 0x05040100u, s_hi = 0x07060100u;
      asm volatile("" : "+s"(s_lo), "+s"(s_hi));
      asm volatile("s_waitcnt vmcnt(1)" : "+v"(pa[0]), "+v"(pa[1]), "+v"(z0), "+v"(z1), "+v"(hb) :: "memory");
      u64 b0, b1, b2; u32x4 v0, v1;
      auto miss = [&]() __attribute__((always_inline)) {
        v0 = pa[0] ^ t2; v1 = pa[1] ^ t2;
        b0 = __ballot((or4(v0) & 0x00010001u) != 0u); b1 = __ballot((or4(v1) & 0x00010001u) != 0u);
        b2 = __ballot((((z0 ^ t8) | (z1 ^ t8)) & 0xFFu) != 0u);
        return (b0 | b1 | b2) != 0;
      };
      if (miss()) {
        unsigned sp = 0;
        do {
        b0 |= (b0 >> 4) & 0x0F0F0F0F0F0F0F0Full; b0 |= (b0 << 4) & 0xF0F0F0F0F0F0F0F0ull; b1 |= (b1 >> 4) & 0x0F0F0F0F0F0F0F0Full; b1 |= (b1 << 4) & 0xF0F0F0F0F0F0F0F0ull;
        asm volatile("s_wqm_b64 exec, %8\n\ts_cbranch_execz 1f\n\tbuffer_load_dwordx4 %0, %4, %6, 0 offen sc1\n1:\n\ts_wqm_b64 exec, %9\n\ts_cbranch_execz 2f\n\tbuffer_load_dwordx4 %1, %4, %6, %7 offen sc1\n2:\n\t"
                     "s_mov_b64 exec, -1\n\ts_cmp_eq_u64 %10, 0\n\ts_cbranch_scc1 3f\n\tbuffer_load_dword %2, %5, %6, 0 offen sc1\n\tbuffer_load_dword %3, %5, %6, 0 offen offset:256 sc1\n3:\n\ts_waitcnt vmcnt(0)"
                     : "+v"(pa[0]), "+v"(pa[1]), "+v"(z0), "+v"(z1) : "v"(oh1), "v"(oh1z), "s"(rs), "s"(65536), "s"(b0), "s"(b1), "s"(b2) : "memory", "scc");
        } while (miss() && ++sp < SPIN_CAP);
      }
    auto sw32 = [](float a, float c) { const auto r = __builtin_amdgcn_permlane32_swap(__float_as_uint(a), __float_as_uint(c), false, false); return __uint_as_float(r[0]) + __uint_as_float(r[1]); };
    auto sw16 = [](float a, float c) { const auto r = __builtin_amdgcn_permlane16_swap(__float_as_uint(a), __float_as_uint(c), false, false); return __uint_as_float(r[0]) + __uint_as_float(r[1]); };
    float sl[4], sh[4];
#pragma unroll
    for (int m = 0; m < 4; ++m) {
      sl[m] = own(__uint_as_float(__builtin_amdgcn_perm(v0[m], hb, s_lo)) + __uint_as_float(__builtin_amdgcn_perm(v1[m], hb, s_lo)));
      sh[m] = own(__uint_as_float(__builtin_amdgcn_perm(v0[m], hb, s_hi)) + __uint_as_float(__builtin_amdgcn_perm(v1[m], hb, s_hi))); }
    const float e0 = dpp_add<0x128, 0xf>(sw16(sw32(sl[0], sl[2]), sw32(sl[1], sl[3]))), e1 = dpp_add<0x128, 0xf>(sw16(sw32(sh[0], sh[2]), sw32(sh[1], sh[3])));
    const float zs = x32_add(x16_add(dpp_add<0x128, 0xf>(__uint_as_float(z0 ^ t8) + __uint_as_float(z1 ^ t8))));
    const float zn = __builtin_bit_cast(float, __builtin_amdgcn_update_dpp(0, __builtin_bit_cast(int, zs), 0xB1, 0xf, 0xf, true));
    const unsigned kvb = min((l & 8) ? pk2(zs, zn) : pk2(e0, e1), SENT - 1u);
    asm volatile("s_mov_b64 exec, %3\n\tbuffer_store_dword %0, %1, %2, 0 offen sc1\n\ts_mov_b64 exec, -1" :: "v"(kvb), "v"(od), "s"(rs), "s"(mkp) : "memory");
    PF_DONE(pfd3);
    }
  } else {
    const char* hq = smem + X_XW + cw * 2048 + l * 16;
    fr[0] = *(const bf16x8*)hq; fr[1] = *(const bf16x8*)(hq + 1024);
  }
  bf16x8 pf[4];
  {
    f32x2 ep[8], en[8];
    phi_fn<true>(wp, fr[0], fr[1], ep, en, 1.0f, 1.0f, [](auto) {});
#pragma unroll
    for (int s = 0; s < 2; ++s) {
      pf[s] = __builtin_bit_cast(bf16x8, u32x4{pk2(ep[4 * s][0], ep[4 * s][1]), pk2(ep[4 * s + 1][0], ep[4 * s + 1][1]), pk2(ep[4 * s + 2][0], ep[4 * s + 2][1]), pk2(ep[4 * s + 3][0], ep[4 * s + 3][1])});
      pf[2 + s] = __builtin_bit_cast(bf16x8, u32x4{pk2(en[4 * s][0], en[4 * s][1]), pk2(en[4 * s + 1][0], en[4 * s + 1][1]), pk2(en[4 * s + 2][0], en[4 * s + 2][1]), pk2(en[4 * s + 3][0], en[4 * s + 3][1])});
    }
    asm volatile("" : "+v"(pf[0]), "+v"(pf[1]), "+v"(pf[2]), "+v"(pf[3]));
  }
  SB0();
  if (b == 0 && tid == 64) ST64(*(const u32x2*)(smem + X_AM + 56), WS_EPOCH);
  u32x4 gq, gq2; f32x2 df[8][4]; unsigned ayr;
#define GQLD(tail) asm volatile("buffer_load_dwordx4 %0, %1, %2, 0 offen sc1" tail : "=v"(gq) : "v"(ogq), "s"(rs) : "memory")
  if (w >= 4) {
    {
      WAIT_V(18 - NREG);
    { const unsigned o_ = (unsigned)((p3 * 128 + cw * 32 + (l >> 3)) * DM + nb * 64 + ((l & 7) ^ (l >> 3)) * 8) * 2; const u16* xa = x + fb * 16 * DM; const u16* xb = x + (fb ^ 1) * 16 * DM;
      gl_m0<0>(o_, xa, lxv); gl_m0<1024>(o_, xa + 8 * DM, lxv); gl_m0<0>(o_, xb, lxc); gl_m0<1024>(o_, xb + 8 * DM, lxc); }
      SB0();
      char* pa_ = smem + cw * 10 * 1024 + l * 16; char* pb_ = smem + (104 + cw * 6) * 1024 + l * 16;
#pragma unroll
      for (int i = 0; i < 10; ++i) if (i < NREG) *(u32x4*)(pa_ + i * 1024) = wr_[i];
#pragma unroll
      for (int i = 0; i < 6; ++i) if (10 + i < NREG) *(u32x4*)(pb_ + i * 1024) = wr_[10 + i];
      SB0();
    }
    asm volatile("" : "+v"(dv[0]), "+v"(dv[1]), "+v"(dv[2]), "+v"(dv[3]), "+v"(dv[4]), "+v"(dv[5]), "+v"(dv[6]), "+v"(dv[7]) :: "memory");
    SB0();
#pragma unroll
    for (int i = 0; i < 8; ++i)
#pragma unroll
      for (int e = 0; e < 4; ++e) { df[i][e] = f32x2{bflo(dv[i][e]), bfhi(dv[i][e])}; asm volatile("" : "+v"(df[i][e])); }
    SB0();
    later();
    ayr = lbase + X_YR + (cw * 128 + (l & 15) * 8 + (l >> 4)) * 4;
    asm volatile("" : "+v"(ayr));
    if (has_gu) GQLD("");
    SB0();
  }
  auto downp = [&]() __attribute__((always_inline)) {
            auto red4 = [](float s0, float s1, float s2, float s3) {
              const auto r01 = __builtin_amdgcn_permlane16_swap(__float_as_uint(s0), __float_as_uint(s1), false, false);
              const auto r23 = __builtin_amdgcn_permlane16_swap(__float_as_uint(s2), __float_as_uint(s3), false, false);
              const float t01 = __uint_as_float(r01[0]) + __uint_as_float(r01[1]), t23 = __uint_as_float(r23[0]) + __uint_as_float(r23[1]);
              const auto r = __builtin_amdgcn_permlane32_swap(__float_as_uint(t01), __float_as_uint(t23), false, false);
              return __uint_as_float(r[0]) + __uint_as_float(r[1]);
            };
            const float gg = rbf(red4(__uint_as_float(gq[0]), __uint_as_float(gq[1]), __uint_as_float(gq[2]), __uint_as_float(gq[3])));
            const float uu = __builtin_bit_cast(float, __builtin_amdgcn_update_dpp(0, __builtin_bit_cast(int, gg), 0x128, 0xf, 0xf, true));
            const float hv0 = rbf(rbf(silu_r(gg)) * uu);
            f32x2 ya[4] = {{0.f, 0.f}, {0.f, 0.f}, {0.f, 0.f}, {0.f, 0.f}};
            static_for<8>([&](auto ic) {
              constexpr int i = decltype(ic)::value;
              const float hv = __builtin_bit_cast(float, __builtin_amdgcn_update_dpp(0, __builtin_bit_cast(int, hv0), 0x150 + i, 0xf, 0xf, true));
              const f32x2 h2 = {hv, hv};
#pragma unroll
              for (int e = 0; e < 4; ++e) ya[e] = __builtin_elementwise_fma(h2, df[i][e], ya[e]);
            });
            const float y0 = red4(ya[0][0], own(ya[0][1]), ya[1][0], own(ya[1][1])), y1 = red4(ya[2][0], own(ya[2][1]), ya[3][0], own(ya[3][1]));
            asm volatile("ds_write2_b32 %0, %1, %2 offset1:4" :: "v"(ayr), "v"(y0), "v"(y1) : "memory");
  };
  {
    u32x4 kf[4], zf[4];
    if (w < 4) {
      u32x4 a, c, z; u64 m0 = ~0ull, m1 = ~0ull, m2 = ~0ull;
      unsigned sp = 0;
      do {
        asm volatile("s_mov_b64 exec, %8\n\ts_cbranch_execz 1f\n\tbuffer_load_dwordx4 %0, %3, %5, 0 offen sc1\n1:\n\ts_mov_b64 exec, %9\n\ts_cbranch_execz 2f\n\tbuffer_load_dwordx4 %1, %3, %5, 0 offen offset:1280 sc1\n2:\n\t"
                     "s_mov_b64 exec, %10\n\ts_cbranch_execz 3f\n\tbuffer_load_dwordx4 %2, %4, %5, 0 offen sc1\n3:\n\ts_mov_b64 exec, -1\n\ts_waitcnt vmcnt(0)\n\t"
                     "ds_write_b128 %6, %0\n\tds_write_b128 %6, %1 offset:1024\n\tds_write_b128 %7, %2"
                     : "+v"(a), "+v"(c), "+v"(z) : "v"(o2), "v"(oz2), "s"(rs), "v"(wa_), "v"(wz_), "s"(m0), "s"(m1), "s"(m2) : "memory");
        m0 = __ballot(max(max(max(a[0], a[1]), a[2]), a[3]) == SENT); m1 = __ballot(max(max(max(c[0], c[1]), c[2]), c[3]) == SENT); m2 = __ballot(max(max(max(z[0], z[1]), z[2]), z[3]) == SENT);
      } while ((m0 | m1 | m2) != 0 && ++sp < SPIN_CAP);
      asm volatile("" : "+v"(xrt));
    }
    RAW_BARRIER();
    DS_B128(kf[0], kba, 0); DS_B128(zf[0], kbz, 0); DS_B128(kf[1], kba, 1024); DS_B128(zf[1], kbz, 16);
    DS_B128(kf[2], kba, 2048); DS_B128(zf[2], kbz, 32); DS_B128(kf[3], kba, 3072); DS_B128(zf[3], kbz, 48);
    __builtin_amdgcn_s_setprio(0);
    f32x16 num = zero;
    float d0 = 0.f, d1 = 0.f;
    static_for<4>([&](auto cc) {
      constexpr int c = decltype(cc)::value;
      asm volatile("s_waitcnt lgkmcnt(%2)" : "+v"(kf[c]), "+v"(zf[c]) : "n"(6 - 2 * c));
      num = MFMA(__builtin_bit_cast(bf16x8, kf[c]), pf[c], num);
      const u32x4 pc = __builtin_bit_cast(u32x4, pf[c]);
      d0 = dot2(zf[c][0], pc[0], d0); d1 = dot2(zf[c][1], pc[1], d1); d0 = dot2(zf[c][2], pc[2], d0); d1 = dot2(zf[c][3], pc[3], d1);
      SB0();
    });
    float den = x32_add(d0 + d1);
    den = rbf(rbf(den) + 1e-6f);
    const float rinv = rcp_nr(den);
    const f32x2 rr = {rinv, rinv};
#pragma unroll
    for (int q = 0; q < 2; ++q) {
      u32x4 u;
#pragma unroll
      for (int e = 0; e < 4; ++e) {
        const f32x2 r = f32x2{num[8 * q + 2 * e], num[8 * q + 2 * e + 1]} * rr;
        u[e] = pk2(r[0], r[1]);
      }
      ST128(u, odo + q * 1024);
      SB0();
    }
  }
  const int p0 = 8 * w + 40;
  const u16* gw2 = wo + (size_t)((p0 ^ (flip * 64)) * 8) * DM + nb * 64; unsigned lw2 = lbase + p0 * 1024;
  asm volatile("" : "+s"(gw2), "+s"(lw2));
  bool glate = false;
  if (w >= 4 && has_gu) {
    asm volatile("s_waitcnt vmcnt(2)" : "+v"(gq) :: "memory");
    gq ^= t8;
    glate = !__all((or4(gq) & 0xFFu) == 0u);
    if (!glate) downp();
  }
  asm volatile("s_waitcnt vmcnt(0)\n\ts_mov_b64 exec, 1\n\tbuffer_store_dwordx2 %0, %1, %2, 0 offen sc1\n\ts_mov_b64 exec, -1" :: "v"(tg), "v"(of4), "s"(rs) : "memory");
  if (__builtin_expect(glate, 0)) {
    for (unsigned sp = 0;;) {
      GQLD("\n\ts_waitcnt vmcnt(0)");
      gq ^= t8;
      if (__all((or4(gq) & 0xFFu) == 0u)) break;
      SPIN_NEXT(sp);
    }
    downp();
  }
  RAW_BARRIER();
  float rxn = 0.f, rsg = 0.f;
  u32x4 yq[7];
  unsigned oy = WS_YP + (unsigned)(l >> 4) * 4096 + nb * 256 + (l & 15) * 16;
#define YLD(tail) asm volatile("buffer_load_dwordx4 %0, %7, %8, 0 offen sc1\n\tbuffer_load_dwordx4 %1, %7, %8, %9 offen sc1\n\tbuffer_load_dwordx4 %2, %7, %8, %10 offen sc1\n\t" \
    "buffer_load_dwordx4 %3, %7, %8, %11 offen sc1\n\tbuffer_load_dwordx4 %4, %7, %8, %12 offen sc1\n\tbuffer_load_dwordx4 %5, %7, %8, %13 offen sc1\n\t" \
    "buffer_load_dwordx4 %6, %7, %8, %14 offen sc1" tail \
    : "=&v"(yq[0]), "=&v"(yq[1]), "=&v"(yq[2]), "=&v"(yq[3]), "=&v"(yq[4]), "=&v"(yq[5]), "=&v"(yq[6]) \
    : "v"(oy), "s"(rs), "s"(16384), "s"(2 * 16384), "s"(3 * 16384), "s"(4 * 16384), "s"(5 * 16384), "s"(6 * 16384) : "memory")
  f32x2 xf[2][4], gf[2][4];
  __attribute__((address_space(3))) f32x4* pw = (__attribute__((address_space(3))) f32x4*)(smem + 131072 + w * 4096) + l;
  unsigned prd = lbase + 131072 + (w ^ 4) * 4096 + l * 16;
  u16* po = out + (size_t)(p3 * 128 + cw * 32 + fb * 16 + (l >> 3)) * DM + nb * 64 + (l & 7) * 8; u16* po2 = po + 8 * DM;
  unsigned tw0 = lbase + 131072 + (w ^ 4) * 4096 + (l & 15) * 128 + ((ech ^ (l & 7)) * 16) + (sw ? 8 : 0), tw1 = tw0 ^ 64, tw2 = tw0 ^ 8, tw3 = tw0 ^ 72, trd = lbase + 131072 + (w ^ 4) * 4096 + (l >> 3) * 128 + (((l & 7) ^ (l >> 3)) * 16);
    unsigned bw[4];
#pragma unroll
    for (int n = 0; n < 4; ++n) bw[n] = lbase + kh * 65536 + (16 * h + 4 * blk + tq) * 128 + (((4 * (n >> 1) + tp) ^ ((tq >> 1) | (blk << 2))) * 16) + 8 * ((n ^ tp) & 1);
  {
  if (w == 1 && has_gu && l < 32) {
    const f32x4* y = (const f32x4*)(smem + X_YR) + l;
    ST128(f2u_tag((y[0] + y[32]) + (y[64] + y[96]), t8), WS_YP + (unsigned)(gr >> 3) * 4096 + ((gr + 1) & 7) * 512 + l * 16);
  }
    SB0();
    gl_run<8>(voO, gw2, lw2);
    u32x2 f0;
    asm volatile("buffer_load_dwordx2 %0, %1, %2, 0 offen sc1" : "=v"(f0) : "v"(op4), "s"(rs) : "memory");
    SB0();
    {
      const f32x2 md = *(const f32x2*)(smem + X_MD + erow * 8);
      { const unsigned ax = lxv + (l & 15) * 128 + ((ech ^ (l & 7)) * 16), a0 = ax + (sw ? 8 : 0), a1 = ax + (sw ? 0 : 8);
        u32x2 q0, q1, q2, q3;
        asm volatile("ds_read_b64 %0, %4\n\tds_read_b64 %1, %5\n\tds_read_b64 %2, %6\n\tds_read_b64 %3, %7\n\ts_waitcnt lgkmcnt(0)" : "=&v"(q0), "=&v"(q1), "=&v"(q2), "=&v"(q3) : "v"(a0), "v"(a1), "v"(a0 ^ 64), "v"(a1 ^ 64) : "memory");
        xv[0] = u32x4{q0[0], q0[1], q1[0], q1[1]}; xv[1] = u32x4{q2[0], q2[1], q3[0], q3[1]}; }
      const float rinv = rcp_nr(own(md[1])), mr = -md[0] * rinv, L2E = 1.44269504088896340736f, rl = -L2E * rinv, ml = -L2E * mr;
#pragma unroll
      for (int i = 0; i < 2; ++i)
#pragma unroll
        for (int e = 0; e < 4; ++e) {
          xf[i][e] = f32x2{bflo(xv[i][e]), bfhi(xv[i][e])};
          const f32x2 xn = __builtin_elementwise_fma(xf[i][e], f32x2{rinv, rinv}, f32x2{mr, mr}), tn = __builtin_elementwise_fma(xf[i][e], f32x2{rl, rl}, f32x2{ml, ml});
          const f32x2 dd = f32x2{__builtin_amdgcn_exp2f(tn[0]), __builtin_amdgcn_exp2f(tn[1])} + f32x2{1.0f, 1.0f};
          gf[i][e] = xn * f32x2{__builtin_amdgcn_rcpf(dd[0]), __builtin_amdgcn_rcpf(dd[1])};
        }
#pragma unroll
      for (int i = 0; i < 2; ++i) asm volatile("" : "+v"(xf[i][0]), "+v"(xf[i][1]), "+v"(xf[i][2]), "+v"(xf[i][3]), "+v"(gf[i][0]), "+v"(gf[i][1]), "+v"(gf[i][2]), "+v"(gf[i][3]));
      asm volatile("" : "+v"(pw), "+v"(prd), "+v"(po2), "+v"(tw0), "+v"(tw1), "+v"(tw2), "+v"(tw3), "+v"(trd));
      asm volatile("" : "+v"(oy), "+v"(po), "+v"(bw[0]), "+v"(bw[1]), "+v"(bw[2]), "+v"(bw[3]));
    }
    if (has_idx) {
      const unsigned ord = (rkey >> 11) & 0xFFFFu;
      rsg = rbf(1.0f / (1.0f + __expf(-__uint_as_float((ord ^ ((ord & 0x8000u) ? 0x8000u : 0xFFFFu)) << 16))));
      const f32x2 md = *(const f32x2*)(smem + X_MD + (idx & 127) * 8);
      const float dn = own(md[1]), rinv = rcp_nr(dn);
      rxn = bflo(ln_pair(xrt, md[0], dn, rinv));
      asm volatile("" : "+v"(rxn), "+v"(rsg));
      { u64 pc_; unsigned d_, o_;
        asm volatile("s_getpc_b64 %0\n.Lkf_own1:\n\ts_mov_b32 %1, .Lkf_own0-.Lkf_own1" : "=s"(pc_), "=s"(o_));
        pc_ += o_;
        asm volatile("s_load_dword %0, %1, 0x0\n\ts_load_dword %0, %1, 0x80\n\ts_load_dword %0, %1, 0x100\n\ts_load_dword %0, %1, 0x180\n\ts_load_dword %0, %1, 0x200\n\ts_load_dword %0, %1, 0x280\n\ts_waitcnt lgkmcnt(0)" : "=&s"(d_) : "s"(pc_) : "memory"); }
    }
    u64 ne;
    SB0();
    asm volatile("s_waitcnt vmcnt(0)\n\tv_cmp_ne_u64 %0, %1, %2" : "=s"(ne) : "v"(f0), "v"(tg) : "memory");
    for (unsigned sp = 0; ne != 0 && sp < SPIN_CAP; ++sp) {
      u32x2 f;
      asm volatile("buffer_load_dwordx2 %1, %2, %3, 0 offen sc1\n\ts_waitcnt vmcnt(0)\n\tv_cmp_ne_u64 %0, %1, %4" : "=s"(ne), "=&v"(f) : "v"(op4), "s"(rs), "v"(tg) : "memory");
    }
  }
  constexpr int OWIN = 4;
  u32x4 of[16][2];
  {
    SB0();
#pragma unroll
    for (int i = 0; i < OWIN; ++i) { of[i][0] = LD128(oo0 + i * 2048); of[i][1] = LD128(oo1 + i * 2048); }
    SB0();
    if (has_idx) YLD("");
    SB0();
  }
    auto owner = [&](auto nc) __attribute__((always_inline)) {
      asm volatile(".Lkf_own0:\n\ts_waitcnt vmcnt(%7)" : "+v"(yq[0]), "+v"(yq[1]), "+v"(yq[2]), "+v"(yq[3]), "+v"(yq[4]), "+v"(yq[5]), "+v"(yq[6]) : "n"(decltype(nc)::value) : "memory");
      for (unsigned sp = 0;; ++sp) {
        unsigned bad = 0;
#pragma unroll
        for (int i = 0; i < 7; ++i) { yq[i] ^= t8; bad |= or4(yq[i]); }
        if (__builtin_expect(__all((bad & 0xFFu) == 0u) || sp > SPIN_CAP, 1)) break;
        YLD("\n\ts_waitcnt vmcnt(0)");
      }
#define YF(i) __builtin_bit_cast(f32x4, yq[i])
      const f32x4 y4 = ((YF(0) + YF(1)) + (YF(2) + YF(3))) + ((YF(4) + YF(5)) + YF(6));
      const auto r01 = __builtin_amdgcn_permlane16_swap(__float_as_uint(y4[0]), __float_as_uint(own(y4[1])), false, false);
      const auto r23 = __builtin_amdgcn_permlane16_swap(__float_as_uint(y4[2]), __float_as_uint(own(y4[3])), false, false);
      const float t01 = __uint_as_float(r01[0]) + __uint_as_float(r01[1]), t23 = __uint_as_float(r23[0]) + __uint_as_float(r23[1]);
      const auto r = __builtin_amdgcn_permlane32_swap(__float_as_uint(t01), __float_as_uint(t23), false, false);
      const float y = __uint_as_float(r[0]) + __uint_as_float(r[1]);
      const float g1 = rbf(silu_r(rbf(rxn + rbf(rbf(y) * rsg))));
      (void)&gf; (void)&oy; (void)&rs; const u64 mk_ = 0x0001000100010001ull << (idx & 15);
      const unsigned gb = lbase + (131072 + w * 4096), aw_ = gb + (l & 15) * 16 + ech * 4, a0 = gb + ech * 32 + (ech & 1) * 16, a1 = a0 ^ 16;
      asm volatile("ds_write_b32 %8, %9\n\ts_mov_b64 exec, %12\n\t"
                   "ds_read_b64 %0, %10\n\tds_read_b64 %1, %10 offset:8\n\tds_read_b64 %2, %11\n\tds_read_b64 %3, %11 offset:8\n\t"
                   "ds_read_b64 %4, %10 offset:128\n\tds_read_b64 %5, %10 offset:136\n\tds_read_b64 %6, %11 offset:128\n\tds_read_b64 %7, %11 offset:136\n\t"
                   "s_mov_b64 exec, -1\n\ts_waitcnt lgkmcnt(0)"
                   : "+v"(gf[0][0]), "+v"(gf[0][1]), "+v"(gf[0][2]), "+v"(gf[0][3]), "+v"(gf[1][0]), "+v"(gf[1][1]), "+v"(gf[1][2]), "+v"(gf[1][3])
                   : "v"(aw_), "v"(g1), "v"(a0), "v"(a1), "s"(mk_) : "memory");
    };
  f32x4 acc3[2][4];
#pragma unroll
  for (int t = 0; t < 2; ++t)
#pragma unroll
    for (int n = 0; n < 4; ++n) acc3[t][n] = f32x4{0.f, 0.f, 0.f, 0.f};
  {
    s16x4 wa[2][8];
    auto rd3 = [&](auto kc, auto nc) {
      constexpr int k = decltype(kc)::value, n = decltype(nc)::value;
      (void)&wa; (void)&bw;
      DS_TR(wa[k & 1][2 * n], bw[n], k * 4096); DS_TR(wa[k & 1][2 * n + 1], bw[n], k * 4096 + 1024);
    };
    RAW_BARRIER();
    static_for<4>([&](auto nc) { rd3(IC(0), nc); });
    static_for<15>([&](auto kc) {
      constexpr int k = decltype(kc)::value, sx = k & 1;
      static_for<4>([&](auto nc) {
        constexpr int n = decltype(nc)::value;
        asm volatile("s_waitcnt lgkmcnt(%2)" : "+v"(wa[sx][2 * n]), "+v"(wa[sx][2 * n + 1]) : "n"(6));
        SB0();
        const bf16x8 wf = cat8(wa[sx][2 * n], wa[sx][2 * n + 1]);
        acc3[0][n] = MFMA16(wf, __builtin_bit_cast(bf16x8, of[k][0]), acc3[0][n]);
        SB0();
        if constexpr (n == 0 && (k & 3) == 0) __builtin_amdgcn_s_setprio(3 - (k >> 2));
        if constexpr (k + 1 < 16) rd3(IC(k + 1), nc);
        if constexpr (k + OWIN < 16 && n == 3) { of[k + OWIN][0] = LD128(oo0 + (k + OWIN) * 2048); }
        SB0();
        acc3[1][n] = MFMA16(wf, __builtin_bit_cast(bf16x8, of[k][1]), acc3[1][n]);
        SB0();
        if constexpr (k + OWIN < 16 && n == 3) { of[k + OWIN][1] = LD128(oo1 + (k + OWIN) * 2048); SB0(); }
        if constexpr (k == YK && n == 3) { (void)&yq; (void)&oy; (void)&rs; if (has_idx) YLD(""); SB0(); }
        if constexpr (k == YP_ && n == 3) { constexpr int e_ = YP_ + OWIN < 16 ? YP_ : 15 - OWIN; if (has_idx) owner(IC(2 * (e_ - YK))); SB0(); }
      });
    });
    static_for<4>([&](auto nc) {
      constexpr int n = decltype(nc)::value;
      asm volatile("s_waitcnt lgkmcnt(%2)" : "+v"(wa[1][2 * n]), "+v"(wa[1][2 * n + 1]) : "n"(6 - 2 * n));
      SB0();
      acc3[1][n] = MFMA16(cat8(wa[1][2 * n], wa[1][2 * n + 1]), __builtin_bit_cast(bf16x8, of[15][1]), acc3[1][n]);
      SB0();
      if constexpr (n == 3) { pw[0] = acc3[1][0]; SB0(); }
    });
    static_for<4>([&](auto nc) {
      constexpr int n = decltype(nc)::value;
      acc3[0][n] = MFMA16(cat8(wa[1][2 * n], wa[1][2 * n + 1]), __builtin_bit_cast(bf16x8, of[15][0]), acc3[0][n]);
      SB0();
      if constexpr (n < 3) { pw[(n + 1) * 64] = acc3[1][n + 1]; SB0(); }
    });
  }
  {
    RAW_BARRIER();
    f32x4 pr[4];
    DS_B128(pr[0], prd, 0); DS_B128(pr[1], prd, 1024); DS_B128(pr[2], prd, 2048); DS_B128(pr[3], prd, 3072);
    SB0();
#pragma unroll
    for (int i = 0; i < 2; ++i)
#pragma unroll
      for (int e = 0; e < 4; ++e) {
        const f32x4 a = acc3[0][2 * i + (e >> 1)];
        xf[i][e] = __builtin_elementwise_fma(f32x2{a[2 * (e & 1)], a[2 * (e & 1) + 1]}, gf[i][e], xf[i][e]);
        asm volatile("" : "+v"(xf[i][e]));
      }
    SB0();
    u32x4 res[2];
#pragma unroll
    for (int i = 0; i < 2; ++i) {
      if (i == 0) asm volatile("s_waitcnt lgkmcnt(2)" : "+v"(pr[0]), "+v"(pr[1])); else asm volatile("s_waitcnt lgkmcnt(2)" : "+v"(pr[2]), "+v"(pr[3]));
      const f32x4 s0 = pr[2 * i], s1 = pr[2 * i + 1];
      const f32x2 o0 = __builtin_elementwise_fma(f32x2{s0[0], s0[1]}, gf[i][0], xf[i][0]), o1 = __builtin_elementwise_fma(f32x2{s0[2], s0[3]}, gf[i][1], xf[i][1]);
      const f32x2 o2 = __builtin_elementwise_fma(f32x2{s1[0], s1[1]}, gf[i][2], xf[i][2]), o3 = __builtin_elementwise_fma(f32x2{s1[2], s1[3]}, gf[i][3], xf[i][3]);
      res[i] = u32x4{pk2(o0[0], o0[1]), pk2(o1[0], o1[1]), pk2(o2[0], o2[1]), pk2(o3[0], o3[1])};
      asm volatile("ds_write_b64 %0, %2\n\tds_write_b64 %1, %3" :: "v"(i ? tw1 : tw0), "v"(i ? tw3 : tw2), "v"(u32x2{res[i][0], res[i][1]}), "v"(u32x2{res[i][2], res[i][3]}) : "memory");
    }
    asm volatile("ds_read_b128 %0, %2\n\tds_read_b128 %1, %2 offset:1024\n\ts_waitcnt lgkmcnt(1)\n\t"
                 "global_store_dwordx4 %3, %0, off sc1\n\ts_waitcnt lgkmcnt(0)\n\tglobal_store_dwordx4 %4, %1, off sc1\n\ts_nop 1"
                 : "=&v"(res[0]), "=&v"(res[1]) : "v"(trd), "v"(po), "v"(po2) : "memory");
  }
  asm volatile(".Lkf_pf_end:" ::: "memory");
}

extern "C" void kernel_launch(void* const* d_in, const int* in_sizes, int n_in,
                              void* d_out, int out_size, void* d_ws, size_t ws_size,
                              hipStream_t stream) {
  (void)hipFuncSetAttribute((const void*)kf_all, hipFuncAttributeMaxDynamicSharedMemorySize, LDS_TOTAL);
  kf_all<<<256, 512, LDS_TOTAL, stream>>>((const u16*)d_in[0], (const u16*)d_in[1], (const u16*)d_in[2], (const u16*)d_in[3],
                                          (const u16*)d_in[5], (const u16*)d_in[6], (const u16*)d_in[7], (const u16*)d_in[8],
                                          (char*)d_ws, (u16*)d_out);
}
```

```cpp
#define KP 9
#define KC 10
#define F1_S 13
#define PF2_KT 8
#define WOK 2
#define H1_DELAY 4
constexpr int PACE_T[16] = {0, 108, 72, 60}, PACE_3[16] = {0, 60, 50, 50};
constexpr int DVN[20] = {0, 0, 3, 2, 1, 0, 1, 0, 1};
constexpr int dv_first(int k) { int n = 0; for (int i = -2; i < k; ++i) n += DVN[i + 2]; return n; }
constexpr bool CSS(int S) { return S < 2 || S == 6 || S == 7; }
#define NREG 8
static_assert(NREG <= 10, "first run");
#define WH0 12
#define WPOST 4
#define YP_ 11
#define PRIO_T 2
#define PRIO_H 1
#include <hip/hip_runtime.h>
#include <hip/hip_bf16.h>
#include <stdint.h>
#include <utility>
#include <type_traits>

typedef __attribute__((ext_vector_type(8))) short bf16x8;
typedef __attribute__((ext_vector_type(4))) short s16x4;
typedef __attribute__((ext_vector_type(16))) float f32x16;
typedef __attribute__((ext_vector_type(4))) float f32x4;
typedef __attribute__((ext_vector_type(2))) float f32x2;
typedef __attribute__((ext_vector_type(4))) unsigned u32x4;
typedef __attribute__((ext_vector_type(2))) unsigned u32x2;
typedef __attribute__((ext_vector_type(2))) __bf16 bfx2;
typedef __attribute__((ext_vector_type(4))) short v4i16_t;
typedef unsigned long long u64;
typedef unsigned short u16;

#define DI __device__ __forceinline__
#define MFMA(a, b, c) __builtin_amdgcn_mfma_f32_32x32x16_bf16((a), (b), (c), 0, 0, 0)
#define MFMA16H(a, b, c) __builtin_amdgcn_mfma_f32_16x16x16bf16_1k((a), (b), (c), 0, 0, 0)
#define MFMA16(a, b, c) __builtin_amdgcn_mfma_f32_16x16x32_bf16((a), (b), (c), 0, 0, 0)

DI unsigned pk2(float a, float b) { f32x2 v = {a, b}; return __builtin_bit_cast(unsigned, __builtin_convertvector(v, bfx2)); }
DI float bflo(unsigned u) { return __uint_as_float(u << 16); }
DI float bfhi(unsigned u) { return __uint_as_float(u & 0xffff0000u); }
DI float rbf(float x) { return (float)(__bf16)x; }
DI s16x4 trread(const char* p) {
  return __builtin_bit_cast(s16x4, __builtin_amdgcn_ds_read_tr16_b64_v4i16((__attribute__((address_space(3))) v4i16_t*)p));
}
DI bf16x8 cat8(s16x4 lo, s16x4 hi) { return __builtin_shufflevector(lo, hi, 0, 1, 2, 3, 4, 5, 6, 7); }
#define WAIT_V(n) asm volatile("s_waitcnt vmcnt(%0)" ::"n"(n) : "memory")
#define RAW_BARRIER() do { asm volatile("s_waitcnt lgkmcnt(0)" ::: "memory"); __builtin_amdgcn_s_barrier(); asm volatile("" ::: "memory"); } while (0)
#define DS_B128(dst, addr, off) asm volatile("ds_read_b128 %0, %1 offset:%2" : "=v"(dst) : "v"(addr), "n"(off))
#define DS_TR(dst, addr, off) asm volatile("ds_read_b64_tr_b16 %0, %1 offset:%2" : "=v"(dst) : "v"(addr), "n"(off))
#define LDS_OFF(p) ((unsigned)(uintptr_t)(__attribute__((address_space(3))) const char*)(p))
template <class F, int... I> DI void static_for_impl(F&& f, std::integer_sequence<int, I...>) { (f(std::integral_constant<int, I>{}), ...); }
template <int N, class F> DI void static_for(F&& f) { static_for_impl(f, std::make_integer_sequence<int, N>{}); }
DI float divc(float a, float b, float rinv) { const float q = a * rinv; return __builtin_fmaf(__builtin_fmaf(-q, b, a), rinv, q); }
DI float rcp_nr(float d) { const float r = __builtin_amdgcn_rcpf(d); return __builtin_fmaf(__builtin_fmaf(-d, r, 1.0f), r, r); }
DI float own(float v) { asm volatile("" : "+v"(v)); return v; }
DI float silu_r(float x) { return x * __builtin_amdgcn_rcpf(1.0f + __expf(-x)); }
#define SB0() __builtin_amdgcn_sched_barrier(0)
#define GLDS_S(vo, sp, ld) asm volatile("s_mov_b32 m0, %2\n\ts_nop 0\n\tglobal_load_lds_dwordx4 %0, %1" :: "v"(vo), "s"(sp), "s"(ld) : "m0", "memory")
template <int IMM> DI void gl_m0(unsigned vo, const void* sp, unsigned ls) {
  asm volatile("s_add_u32 m0, %2, %3\n\ts_nop 0\n\tglobal_load_lds_dwordx4 %0, %1" :: "v"(vo), "s"(sp), "s"(ls), "n"(IMM) : "m0", "memory", "scc");
}
template <int IMM> DI void gl_nt(unsigned vo, const void* sp, unsigned ls) {
  asm volatile("s_add_u32 m0, %2, %3\n\ts_nop 0\n\tglobal_load_lds_dwordx4 %0, %1 nt" :: "v"(vo), "s"(sp), "s"(ls), "n"(IMM) : "m0", "memory", "scc");
}
template <int N> DI void gl_run(unsigned vo, const void* sp, unsigned ls) {
  unsigned lo = (unsigned)(uintptr_t)sp, hi = (unsigned)((uintptr_t)sp >> 32);
  static_for<N>([&](auto ic) {
    constexpr int i = decltype(ic)::value;
    gl_m0<i * 1024>(vo, (const void*)(((uintptr_t)hi << 32) | lo), ls);
    if constexpr (i + 1 < N) asm volatile("s_add_u32 %0, %0, 0x4000\n\ts_addc_u32 %1, %1, 0" : "+s"(lo), "+s"(hi) :: "scc");
  });
}
#define GLDS_SC1(vo, sp, ld) asm volatile("s_mov_b32 m0, %2\n\ts_nop 0\n\tglobal_load_lds_dwordx4 %0, %1 sc1" :: "v"(vo), "s"(sp), "s"(ld) : "m0", "memory")
DI float x32_add(float v) { auto r = __builtin_amdgcn_permlane32_swap(__float_as_uint(v), __float_as_uint(v), false, false); return __uint_as_float(r[0]) + __uint_as_float(r[1]); }
DI float x32_max(float v) { auto r = __builtin_amdgcn_permlane32_swap(__float_as_uint(v), __float_as_uint(v), false, false); float o; asm("v_max_f32 %0, %1, %2" : "=v"(o) : "v"(r[0]), "v"(r[1])); return o; }
DI float x32_min(float v) { auto r = __builtin_amdgcn_permlane32_swap(__float_as_uint(v), __float_as_uint(v), false, false); float o; asm("v_min_f32 %0, %1, %2" : "=v"(o) : "v"(r[0]), "v"(r[1])); return o; }
DI float x16_add(float v) { auto r = __builtin_amdgcn_permlane16_swap(__float_as_uint(v), __float_as_uint(v), false, false); return __uint_as_float(r[0]) + __uint_as_float(r[1]); }
template <int CTRL, int RMASK> DI float dpp_add(float v) {
  return v + __builtin_bit_cast(float, __builtin_amdgcn_update_dpp(0, __builtin_bit_cast(int, v), CTRL, RMASK, 0xf, true));
}

DI unsigned ln_pair(unsigned u, float m, float den, float rinv) {
  const f32x2 mm = {m, m}, dd = {den, den}, rr = {rinv, rinv};
  const f32x2 d0 = f32x2{bflo(u), bfhi(u)} - mm;
  const unsigned du = pk2(d0[0], d0[1]);
  const f32x2 d = {bflo(du), bfhi(du)};
  const f32x2 q = d * rr;
  const f32x2 r = __builtin_elementwise_fma(__builtin_elementwise_fma(-q, dd, d), rr, q);
  return pk2(r[0], r[1]);
}
DI float dot2(unsigned a, unsigned b, float c) { return __builtin_amdgcn_fdot2_f32_bf16(__builtin_bit_cast(bfx2, a), __builtin_bit_cast(bfx2, b), c, false); }
DI float half_total(float v) {
  v = dpp_add<0x121, 0xf>(v); v = dpp_add<0x122, 0xf>(v); v = dpp_add<0x124, 0xf>(v); v = dpp_add<0x128, 0xf>(v);
  return x16_add(v);
}
DI void ln_rows(const u32x4 (&xr)[4], const u32x4 (&wr)[4], int h, float& m, float& den, float& rinv, float& r) {
  const unsigned c0 = __builtin_amdgcn_readlane(xr[0][0], 0) << 16, c1 = __builtin_amdgcn_readlane(xr[0][0], 32) << 16;
  const float c = __uint_as_float(h ? c1 : c0);
  const f32x2 cc = {c, c};
  float s1[2] = {0.f, 0.f}, sx[2] = {0.f, 0.f}, sw[2] = {0.f, 0.f};
  f32x2 q[2] = {{0.f, 0.f}, {0.f, 0.f}};
#pragma unroll
  for (int i = 0; i < 4; ++i)
#pragma unroll
    for (int e = 0; e < 4; ++e) {
      const unsigned u = xr[i][e];
      s1[e & 1] = dot2(u, 0x3F803F80u, s1[e & 1]); sx[e & 1] = dot2(u, wr[i][e], sx[e & 1]); sw[e & 1] = dot2(wr[i][e], 0x3F803F80u, sw[e & 1]);
      const f32x2 d = f32x2{bflo(u), bfhi(u)} - cc;
      q[e & 1] = __builtin_elementwise_fma(d, d, q[e & 1]);
    }
  const f32x2 q2 = q[0] + q[1];
  const float mean = half_total(s1[0] + s1[1]) * (1.0f / 1024.0f), m2 = half_total(q2[0] + own(q2[1])) * (1.0f / 1024.0f);
  const float tx = half_total(sx[0] + sx[1]), tw = half_total(sw[0] + sw[1]);
  const float dm = mean - c;
  const float var = fmaxf(__builtin_fmaf(-dm, dm, m2), 0.f);
  m = rbf(mean);
  den = rbf(sqrtf(rbf(rbf(var) + 1e-5f)));
  rinv = rcp_nr(den);
  r = rbf(divc(__builtin_fmaf(-m, tw, tx), den, rinv));
}

template <class HK> DI void phi_fn(const bf16x8 (&wp)[2], const bf16x8 f0, const bf16x8 f1, f32x2 (&ep)[8], f32x2 (&en)[8], float si, float so, HK&& hk) {
  f32x16 p;
#pragma unroll
  for (int i = 0; i < 16; ++i) p[i] = 0.f;
  p = MFMA(wp[0], f0, p);
  p = MFMA(wp[1], f1, p);
  hk(std::integral_constant<int, 0>{});
  float mx = -3.0e38f, mn = 3.0e38f;
  f32x2 p2[8];
  static_for<8>([&](auto ic) {
    constexpr int i = decltype(ic)::value;
    p2[i] = f32x2{p[2 * i], p[2 * i + 1]};
    mx = fmaxf(fmaxf(mx, p2[i][0]), p2[i][1]); mn = fminf(fminf(mn, p2[i][0]), p2[i][1]);
    if constexpr (i & 1) hk(std::integral_constant<int, 1 + (i >> 1)>{});
  });
  mx = x32_max(mx); mn = x32_min(mn);
  hk(std::integral_constant<int, 5>{});
  const float L2E = 1.44269504088896340736f * si, mxl = -mx * L2E, mnl = mn * L2E;
  const f32x2 cp = {L2E, L2E}, cn = {-L2E, -L2E}, bp = {mxl, mxl}, bn = {mnl, mnl};
  f32x2 sp2, sn2;
  static_for<8>([&](auto ic) {
    constexpr int i = decltype(ic)::value;
    const f32x2 tp2 = __builtin_elementwise_fma(p2[i], cp, bp), tn2 = __builtin_elementwise_fma(p2[i], cn, bn);
    ep[i] = f32x2{__builtin_amdgcn_exp2f(tp2[0]), __builtin_amdgcn_exp2f(tp2[1])};
    en[i] = f32x2{__builtin_amdgcn_exp2f(tn2[0]), __builtin_amdgcn_exp2f(tn2[1])};
    if constexpr (i == 0) { sp2 = ep[0]; sn2 = en[0]; } else { sp2 += ep[i]; sn2 += en[i]; }
    hk(std::integral_constant<int, 6 + i>{});
  });
  const float sp = x32_add(sp2[0] + own(sp2[1])), sn = x32_add(sn2[0] + own(sn2[1]));
  hk(std::integral_constant<int, 14>{});
  const float rp = __builtin_amdgcn_rcpf(sp) * so, rn = __builtin_amdgcn_rcpf(sn) * so;
  const f32x2 rp2 = {rp, rp}, rn2 = {rn, rn};
  hk(std::integral_constant<int, 15>{});
#pragma unroll
  for (int i = 0; i < 8; ++i) { ep[i] = ep[i] * rp2; en[i] = en[i] * rn2; }
  hk(std::integral_constant<int, 16>{});
}

constexpr int DM = 1024, FF = 3584;
constexpr unsigned SENT = 0xFFFFFFFFu;
constexpr unsigned SPIN_CAP = 1u << 17;

constexpr unsigned WS_EPOCH = 0;
constexpr unsigned WS_F1 = 4096;
constexpr unsigned WS_F4 = 896u << 10;
constexpr unsigned WS_STAT = 20480;
constexpr unsigned WS_CS = 36864;
constexpr unsigned WS_GUP = WS_CS + 16 * 16 * 768;
constexpr unsigned WS_YP = WS_GUP + 256 * 512;
constexpr unsigned WS_KVF = WS_YP + 32 * 4096;
constexpr unsigned WS_ZP = WS_KVF + 256 * 640;
constexpr unsigned WS_XA = 800u << 10;
constexpr unsigned WS_RI = 840u << 10;
static_assert(WS_ZP + 131072 <= WS_XA && WS_XA + 32768 <= WS_RI, "ws map");
constexpr unsigned WS_KVP = 1u << 20;
constexpr unsigned WS_O = 8u << 20;
constexpr unsigned WS_END = 16u << 20;

constexpr int ABYTES = 128 * 128, WSLAB = 64 * 128, STAGE1 = ABYTES + 3 * WSLAB, NST1 = 3;
constexpr int XB = NST1 * STAGE1;
constexpr int X_WO = XB;
constexpr int X_CSP = XB + 8192;
constexpr int X_P = 131072 + 4096;
constexpr int X_XW = X_P;
constexpr int X_XA = X_XW + 6144;
constexpr int X_ST = X_XA + 2048;
constexpr int X_MD = X_ST + 1024;
constexpr int X_AM = X_MD + 1024;
constexpr int X_XS = X_AM + 64;
constexpr int X_YR = X_XS + 512;
constexpr int X_YS = X_YR + 2048;
constexpr int X_WP = X_YS + 2048;
constexpr int X_KB = X_WP + 4096;
constexpr int LDS_TOTAL = 163840;
static_assert(X_KB + 2 * 4224 <= LDS_TOTAL && 131072 + 6 * 2048 <= X_MD && X_YS + 64 + 4096 <= X_KB && X_YS % 128 == 64, "lds");
static_assert(LDS_TOTAL <= 163840, "lds");
constexpr int gu_at(int ks) { return STAGE1 + ks * 8192; }
constexpr int HANDOFF = 106496;

#define LD128(off) __builtin_amdgcn_raw_buffer_load_b128(rs, (int)(off), 0, 16)
#define ST128(v, off) __builtin_amdgcn_raw_buffer_store_b128((v), rs, (int)(off), 0, 16)
#define ST64(v, off) __builtin_amdgcn_raw_buffer_store_b64((v), rs, (int)(off), 0, 16)
DI u32x4 f2u_tag(f32x4 v, unsigned t8) { u32x4 u = __builtin_bit_cast(u32x4, v); return (u & 0xFFFFFF00u) | t8; }
DI unsigned or4(u32x4 v) { return (v[0] | v[1]) | (v[2] | v[3]); }
#define SPIN_NEXT(sp) { if (++(sp) > SPIN_CAP) break; __builtin_amdgcn_s_sleep(1); }
#define TAG_OF(en) ((en) * 0x9E3779B97F4A7C15ull)

__global__ __launch_bounds__(512, 2) void kf_all(const u16* __restrict__ x, const u16* __restrict__ wqkv, const u16* __restrict__ wphi,
                                              const u16* __restrict__ wo, const u16* __restrict__ w_r, const u16* __restrict__ w_gate,
                                              const u16* __restrict__ w_up, const u16* __restrict__ w_down, char* ws, u16* __restrict__ out) {
  extern __shared__ __attribute__((aligned(16))) char smem[];
  asm volatile(".Lkf_pf_start:");
  const int b = blockIdx.x;
  const int xcd = b & 7, jj = b >> 3;
  const int panel = (xcd >> 2) * 8 + (jj & 7), hp = (xcd & 3) * 4 + (jj >> 3);
  const int id = panel * 16 + hp;
  const int tid = threadIdx.x, l = tid & 63, w = __builtin_amdgcn_readfirstlane(tid >> 6);
  unsigned pfd;
  {
    u64 pc; unsigned len, back;
    asm volatile("s_getpc_b64 %0\n.Lkf_pf_here:\n\ts_mov_b32 %1, .Lkf_pf_end+1152-.Lkf_pf_start\n\ts_mov_b32 %2, .Lkf_pf_here-.Lkf_pf_start" : "=s"(pc), "=s"(len), "=s"(back));
    unsigned o = (unsigned)(jj * 8 + w) * 256u;
    o = o + 256u <= len ? o : len - 256u;
    const u64 pa = (pc - back + o) & ~63ull;
    asm volatile("s_load_dword %0, %1, 0x0\n\ts_load_dword %0, %1, 0x40\n\ts_load_dword %0, %1, 0x80\n\ts_load_dword %0, %1, 0xc0" : "=&s"(pfd) : "s"(pa) : "memory");
  }
  asm volatile("" : "+s"(x), "+s"(wqkv), "+s"(wphi), "+s"(wo), "+s"(w_r), "+s"(w_gate), "+s"(w_up), "+s"(w_down), "+s"(ws), "+s"(out));
  SB0();
#define GLD(dst, p) asm volatile("global_load_dwordx4 %0, %1, off" : "=v"(dst) : "v"(p) : "memory")
  u32x4 xr4[4], wr4[4];
  if (w < 4) {
    const u16* xr = x + (size_t)(panel * 128 + hp * 8 + (tid >> 5)) * DM + (l & 31) * 8;
    const u16* wr = w_r + (l & 31) * 8;
    GLD(xr4[0], xr); GLD(xr4[1], xr + 256); GLD(xr4[2], xr + 512); GLD(xr4[3], xr + 768);
    GLD(wr4[0], wr); GLD(wr4[1], wr + 256); GLD(wr4[2], wr + 512); GLD(wr4[3], wr + 768);
  }
  SB0();
  const int cw = w & 3, wm = cw & 1, wn = cw >> 1;
  const int h = l >> 5, r31 = l & 31, blk = (l >> 4) & 1, tq = (l & 15) >> 2, tp = l & 3;
  const int hd = hp * 2 + wn;
  const unsigned lbase = LDS_OFF(smem);
  const __amdgpu_buffer_rsrc_t rs = __builtin_amdgcn_make_buffer_rsrc((void*)ws, 0, (int)WS_END, 0x00020000);
  bf16x8 fr[4];
  bf16x8 wp[2];
  f32x4 qa[4][2]; float rt;
  const int kh = w >> 2;
  const int wt = wm * 2 + kh;
  const unsigned img = (wn * 4 + wt) * 4096 + r31 * 128 + (((2 * h) ^ (r31 & 7)) * 16);
  auto wpload = [&]() {
    const char* p = smem + X_WP + wn * 2048 + (8 * h + tq) * 64 + (16 * blk + 4 * tp) * 2;
    wp[0] = cat8(trread(p), trread(p + 256)); wp[1] = cat8(trread(p + 1024), trread(p + 1280));
  };
  auto imgw = [&](int g, const f32x2 (&ep)[8], const f32x2 (&en)[8]) {
    const f32x2 (&e)[8] = g < 2 ? ep : en; const int q = 4 * (g & 1);
    *(u32x4*)(smem + (img ^ ((g >> 1) * 64 + (g & 1) * 16))) = u32x4{pk2(e[q][0], e[q][1]), pk2(e[q + 1][0], e[q + 1][1]), pk2(e[q + 2][0], e[q + 2][1]), pk2(e[q + 3][0], e[q + 3][1])};
  };
  auto pack_u = [&](const f32x4 x0, const f32x4 x1) {
    const auto s0 = __builtin_amdgcn_permlane16_swap(pk2(x0[0], x0[1]), pk2(x1[0], x1[1]), false, false);
    const auto s1 = __builtin_amdgcn_permlane16_swap(pk2(x0[2], x0[3]), pk2(x1[2], x1[3]), false, false);
    return u32x4{s0[0], s1[0], s0[1], s1[1]};
  };
  const int hr = hp ^ 8;
  const int p3 = ((xcd >> 1) * 4 + (jj & 3)) ^ 8, nb = (int)(((xcd & 1) ? 0xF7EDCA98u : 0xB3654210u) >> (jj & 28)) & 15;
  const int flip = jj & 1;
  const unsigned voO = (unsigned)((l >> 3) * DM + (((l & 7) ^ (((l >> 4) & 1) | ((l >> 5) << 2))) * 8)) * 2;
  const bool has_gu = (hp & 7) != 3;
  const int gr = !has_gu ? 224 + (jj - 24) * 4 + (xcd >> 1) : jj >= 28 ? 8 * (4 * (xcd >> 1) + jj - 16) + 6 : (xcd == 6 && jj >= 12) ? 8 * (24 + (jj & 3)) + 2 * ((jj >> 2) - 3) : b;
  const int gkq = (gr / 56) & 3;
  u32x4 dv[8]; unsigned ogq; bf16x8 dn[4];
  u32x4 wr_[16];
    const int srow = l >> 3, sch = l & 7;
    unsigned voA[4], voW[6];
#pragma unroll
    for (int i = 0; i < 4; ++i) voA[i] = (unsigned)((panel * 128 + (4 * cw + i) * 8 + srow) * DM + ((sch ^ (4 * (i & 1) + (srow >> 1))) * 8)) * 2;
#pragma unroll
    for (int i = 0; i < 6; ++i) voW[i] = (unsigned)(((2 * cw + (i & 1)) * 8 + srow) * 3072 + (i >> 1) * 1024 + hp * 64 + ((sch ^ ((((srow >> 1) & 1) << 2) | ((i & 1) << 1))) * 8)) * 2;
    const unsigned lwa = lbase + cw * 4096, lww = lbase + ABYTES + cw * 2048;
    const int rot = 4 * ((panel & 7) >> 1) + 2 * (panel >> 3) + 4 * (xcd & 3);
    const u64 kord = !has_gu ? ((panel & 1) ? 0xEA62FB7C84D30951ull : 0xFB73EA6D95C21840ull) : (panel & 8) ? 0xD95FB73EA62C1840ull : 0xFB73EA62D95C1840ull;
    auto stage = [&](int it, auto bufc) {
      constexpr int B = decltype(bufc)::value * STAGE1;
      const int kt = ((int)(kord >> (4 * it)) + rot) & 15;
      const u16* gx = x + kt * 64; const u16* gq_ = wqkv + (size_t)kt * 64 * 3072;
      static_for<10>([&](auto ic) {
        constexpr int i = decltype(ic)::value;
        if constexpr (i < 4) gl_m0<B + i * 1024>(voA[i], gx, lwa);
        else gl_m0<B + ((i - 4) >> 1) * WSLAB + (i & 1) * 1024>(voW[i - 4], gq_, lww);
      });
    };
  unsigned pa0, pvh, dst, dz, pgw, ogu, oh1, oh1z;
  auto kvsetup = [&]() {
    const int g = l >> 4, c15 = l & 15;
    pa0 = lbase + kh * 16384 + (4 * g + tq) * 128 + (((2 * cw + (tp >> 1)) ^ ((4 * g + tq) & 7)) * 16) + 8 * (tp & 1);
    pvh = lbase + HANDOFF + kh * 12288 + l * 16;
    dst = WS_KVP + (unsigned)((hp * 16 + panel) * 16 + (w & 6) * 2 + (g & 2) + (w & 1)) * 512 + c15 * 32 + (g & 1) * 16;
    dz = WS_ZP + (unsigned)((hp * 16 + (w & 6) * 2 + (g & 2) + (w & 1)) * 16 + panel) * 32 + (g & 1) * 16;
    pgw = lbase + gu_at(0) + kh * 4096 + (8 * g + tq) * 128 + (((2 * cw + (tp >> 1)) ^ ((((tq >> 1) & 1) << 2) | ((g & 1) << 1))) * 16) + 8 * (tp & 1);
    ogu = WS_GUP + (unsigned)gr * 512 + w * 64 + g * 16;
    oh1 = WS_KVP + (unsigned)((hr * 16 + (l >> 3)) * 16 + panel) * 512 + cw * 128 + (l & 7) * 16;
    oh1z = WS_ZP + (unsigned)((hr * 16 + panel) * 16) * 32 + l * 4;
    asm volatile("" : "+v"(pa0), "+v"(pvh), "+v"(dst), "+v"(dz), "+v"(pgw), "+v"(ogu), "+v"(oh1), "+v"(oh1z));
  };
#define IC(n) std::integral_constant<int, (n)>{}
#define CODE_PF(tagn, FROM, d) do { u64 pc_; unsigned len_; int off_; \
    asm volatile("s_getpc_b64 %0\n.Lkf_pf_h" #tagn ":\n\ts_mov_b32 %1, .Lkf_pf_end+1152-" FROM "\n\ts_mov_b32 %2, " FROM "-.Lkf_pf_h" #tagn : "=s"(pc_), "=s"(len_), "=s"(off_)); \
    unsigned o_ = (unsigned)(jj * 4 + cw) * 256u; o_ = o_ + 256u <= len_ ? o_ : len_ - 256u; \
    const u64 pa_ = (pc_ + (long long)off_ + o_) & ~63ull; \
    asm volatile("s_load_dword %0, %1, 0x0\n\ts_load_dword %0, %1, 0x40\n\ts_load_dword %0, %1, 0x80\n\ts_load_dword %0, %1, 0xc0" : "=&s"(d) : "s"(pa_) : "memory"); } while (0)
#define PF_DONE(d) asm volatile("s_waitcnt lgkmcnt(0)" : "+s"(d) :: "memory")
  if (w < 4) {
    unsigned oc2;
    {
      const unsigned r0 = (unsigned)(panel * 128 + hp * 8);
      oc2 = l < 33 ? WS_KVF + (unsigned)(hr * 16 + panel) * 640 + l * 16 : l == 33 ? WS_F1 + (unsigned)id * 16 : l == 34 ? WS_STAT + r0 * 8 : l == 35 ? WS_RI + (unsigned)(panel * 1024 + hp * 32)
          : l < 40 ? WS_F4 + (unsigned)(((panel * 4 + (l - 36)) * 16 + hp) * 2) * 32 : l < 44 ? WS_GUP + (unsigned)gr * 512 + (l - 40) * 128 : l == 44 ? WS_RI + (unsigned)(panel * 1024 + 512 + hp * 16)
          : WS_ZP + (unsigned)((hp * 16 + (l & 15)) * 16 + panel) * 32;
    }
    u32x4 tm, k0, k1; u32x2 tk;
    const unsigned tmofs = lbase + X_CSP + (l < 47 ? l : 46) * 16, kofs = lbase + X_CSP + 768, tgofs = lbase + X_AM + 48;
    unsigned opay;
    { const unsigned r0 = (unsigned)(panel * 128 + hp * 8);
      opay = l < 32 ? WS_CS + (unsigned)(hp * 8 + (panel >> 1)) * 768 + (panel & 1) * 512 + l * 16 : l < 40 ? WS_XA + r0 * 16 + (l - 32) * 16 : l < 44 ? WS_STAT + r0 * 8 + (l - 40) * 16 : l < 46 ? WS_RI + (unsigned)(panel * 1024 + hp * 32) + (l - 44) * 16 : WS_RI + (unsigned)(panel * 1024 + 512 + hp * 16); }
    const unsigned oflag = WS_F1 + (unsigned)id * 16;
    unsigned mz; asm volatile("s_cmp_eq_u32 %1, 0\n\ts_cselect_b32 %0, -1, 0" : "=s"(mz) : "s"(cw) : "scc");
    const u64 mk_all = ((u64)mz << 32) | mz, mk_pay = ((u64)(mz & 0x7FFFu) << 32) | (mz & ((panel & 1) ? 0xFFFFu : 0xFFFFFFFFu)), mk_one = mz & 1u;
    asm volatile("" : "+v"(oc2), "+v"(opay));
#define MST(op, data, off, mask) asm volatile("s_mov_b64 exec, %3\n\t" op " %0, %1, %2, 0 offen sc1\n\ts_mov_b64 exec, -1" :: "v"(data), "v"(off), "s"(rs), "s"(mask) : "memory")
    f32x4 csa[2] = {{0.f, 0.f, 0.f, 0.f}, {0.f, 0.f, 0.f, 0.f}};
    const bf16x8 ones = {0x3F80, 0x3F80, 0x3F80, 0x3F80, 0x3F80, 0x3F80, 0x3F80, 0x3F80};
    s16x4 cl[2][2], ch[2][2];
    const unsigned csw = lbase + X_CSP + (wm * 64 + wn * 32 + r31) * 4;
    f32x4 acc[4][6];
#pragma unroll
    for (int t = 0; t < 4; ++t)
#pragma unroll
      for (int a = 0; a < 6; ++a) acc[t][a] = f32x4{0.f, 0.f, 0.f, 0.f};
    const int g4 = l >> 4, c15 = l & 15;
    unsigned asw[2][2], wofs[2][2], cofs[2];
#pragma unroll
    for (int e = 0; e < 2; ++e) {
      asw[0][e] = lbase + (wm * 64 + c15) * 128 + (((4 * e + g4) ^ ((c15 >> 1) & 7)) * 16);
      wofs[0][e] = lbase + ABYTES + (8 * g4 + tq) * 128 + (((wn * 4 + 2 * e + (tp >> 1)) ^ ((((tq >> 1) & 1) << 2) | ((g4 & 1) << 1))) * 16) + 8 * (tp & 1);
      asw[1][e] = asw[0][e] + 2 * STAGE1; wofs[1][e] = wofs[0][e] + 2 * STAGE1;
      cofs[e] = wofs[0][e] + ((panel & 1) ? 2 : wm) * WSLAB;
    }
    asm volatile("" : "+v"(asw[0][0]), "+v"(asw[0][1]), "+v"(asw[1][0]), "+v"(asw[1][1]), "+v"(wofs[0][0]), "+v"(wofs[0][1]), "+v"(wofs[1][0]), "+v"(wofs[1][1]), "+v"(cofs[0]), "+v"(cofs[1]));
    bf16x8 af[2][4]; s16x4 wl[2][6], wh_[2][6];
    s16x4 ea[4], ew[6];
    const unsigned eaofs = lbase + X_XA + (wm * 64 + c15) * 16;
    const unsigned ewofs = lbase + X_XW + g4 * 1536 + (wn * 32 + c15) * 4;
    const unsigned riofs = lbase + X_ST + (wm * 64 + r31) * 4;
    auto rd = [&](auto ic, auto stc) {
      constexpr int i = decltype(ic)::value, S = decltype(stc)::value, s = S & 1, e = S & 1, buf = (S >> 1) % NST1;
      constexpr int b2 = buf == 2, bo = buf == 1 ? STAGE1 : 0;
      (void)&cl; (void)&ch; (void)&cofs; (void)&af; (void)&wl; (void)&wh_; (void)&asw; (void)&wofs;
      if constexpr (i == 0) DS_B128(af[s][0], asw[b2][e], bo);
      else if constexpr (i == 1) DS_TR(wl[s][0], wofs[b2][0], bo + e * 4096);
      else if constexpr (i == 2) DS_TR(wh_[s][0], wofs[b2][0], bo + e * 4096 + 512);
      else if constexpr (i < 6) DS_B128(af[s][i - 2], asw[b2][e], bo + (i - 2) * 2048);
      else if constexpr (i < 16) {
        constexpr int j = (i - 4) >> 1, off = bo + e * 4096 + (j >> 1) * WSLAB;
        if constexpr (i & 1) DS_TR(wh_[s][j], wofs[b2][j & 1], off + 512); else DS_TR(wl[s][j], wofs[b2][j & 1], off);
      } else {
        constexpr int c = (i - 16) >> 1;
        if constexpr (i & 1) DS_TR(ch[s][c], cofs[c], bo + e * 4096 + 512); else DS_TR(cl[s][c], cofs[c], bo + e * 4096);
      }
    };
    {
      kvsetup();
      SB0();
      asm volatile("s_waitcnt vmcnt(0) lgkmcnt(0)" : "+s"(pfd), "+v"(xr4[0]), "+v"(xr4[1]), "+v"(xr4[2]), "+v"(xr4[3]), "+v"(wr4[0]), "+v"(wr4[1]), "+v"(wr4[2]), "+v"(wr4[3]) :: "memory");
      SB0();
      float m, den, rinv, r;
      ln_rows(xr4, wr4, h, m, den, rinv, r);
      const int rw = tid >> 5;
      const unsigned rb = __float_as_uint(r) >> 16, ord = rb ^ ((rb & 0x8000u) ? 0xFFFFu : 0x8000u), nm = pk2(-m, -m);
      if (r31 == 0) { *(u32x4*)(smem + X_CSP + 512 + rw * 16) = u32x4{nm, nm, nm, nm};
        *(f32x2*)(smem + X_CSP + 640 + rw * 8) = f32x2{m, den}; *(float*)(smem + X_CSP + 704 + rw * 4) = rinv; *(u16*)(smem + X_CSP + 736 + rw * 2) = (u16)(__float_as_uint(den) >> 16);
        *(unsigned*)(smem + X_CSP + 768 + rw * 4) = (ord << 11) | (unsigned)(2047 - (id * 8 + rw)); }
      SB0();
    }
    __builtin_amdgcn_s_barrier();
    static_for<20>([&](auto ic) { rd(ic, IC(0)); });
    static_for<30>([&](auto stc) {
      constexpr int S = decltype(stc)::value, s = S & 1;
      constexpr bool bnd = (S & 1) == 1;
      static_for<24>([&](auto mc) {
        constexpr int m = decltype(mc)::value, j = m >> 2, t = (j & 1) ? 3 - (m & 3) : m & 3;
        (void)&tm; (void)&k0; (void)&k1; (void)&tk; (void)&ea; (void)&ew; (void)&oc2; (void)&opay; (void)&oflag; (void)&mk_all; (void)&mk_pay; (void)&mk_one; (void)&rs;
        (void)&tmofs; (void)&kofs; (void)&tgofs; (void)&csw; (void)&csa; (void)&eaofs; (void)&ewofs; (void)&riofs;
        if constexpr (m == 0) asm volatile("s_waitcnt lgkmcnt(%0)" :: "n"(bnd ? 0 : CSS(S) ? 14 : 10));
        else if constexpr (!bnd && m == 4) asm volatile("s_waitcnt lgkmcnt(%0)" :: "n"(CSS(S) ? 7 : 3));
        else if constexpr (!bnd && m == 12 && CSS(S)) asm volatile("s_waitcnt lgkmcnt(11)");
        SB0();
        const bf16x8 wf = cat8(wl[s][j], wh_[s][j]);
        if constexpr (j < 4) acc[t][j] = MFMA16(wf, af[s][t], acc[t][j]);
        else acc[t][j] = MFMA16(af[s][t], wf, acc[t][j]);
        SB0();
        if constexpr (m == 1) {
          if constexpr (S == 1) { const u32x4 sv = {SENT, SENT, SENT, SENT}; MST("buffer_store_dwordx4", sv, oc2, mk_all); }
          if constexpr (S == 11) { asm volatile("" : "+v"(tm), "+v"(k0), "+v"(k1), "+v"(tk)); MST("buffer_store_dwordx4", tm, opay, mk_pay); }
          if constexpr (S == F1_S) { WAIT_V(0); MST("buffer_store_dwordx2", tk, oflag, mk_one); }
          if constexpr (bnd) __builtin_amdgcn_s_barrier();
        }
        if constexpr (m >= 1 && m - 1 < (CSS(S + 1) ? 20 : 16)) rd(IC(m - 1), IC(S + 1));
        if constexpr (S == 8 && m == 18) {
          asm volatile("" : "+v"(csa[0]), "+v"(csa[1]));
          const float c = blk ? csa[1][0] : csa[0][0];
          const unsigned u = pk2(c, c - rbf(c));
          asm volatile("ds_write_b32 %0, %1" :: "v"(csw), "v"(u) : "memory");
        }
        if constexpr (S == 9 && m >= 18 && m < 22) {
          if constexpr (m == 18) DS_B128(tm, tmofs, 0);
          if constexpr (m == 19) DS_B128(k0, kofs, 0);
          if constexpr (m == 20) DS_B128(k1, kofs, 16);
          if constexpr (m == 21) asm volatile("ds_read_b64 %0, %1" : "=v"(tk) : "v"(tgofs));
        }
        if constexpr (S == 11 && m == 18) { k0 = __builtin_elementwise_max(k0, k1); asm volatile("" : "+v"(k0)); }
        if constexpr (S == 11 && m == 20) { tk[0] = (tk[0] & ~0x7FFFFFFu) | max(max(k0[0], k0[1]), max(k0[2], k0[3])); asm volatile("" : "+v"(tk)); }
#define DS_B64(dst, addr, off) asm volatile("ds_read_b64 %0, %1 offset:%2" : "=v"(dst) : "v"(addr), "n"(off))
#define DS_B32(dst, addr, off) asm volatile("ds_read_b32 %0, %1 offset:%2" : "=v"(dst) : "v"(addr), "n"(off))
#define DS_2(dst, addr, o0, o1) asm volatile("ds_read2_b32 %0, %1 offset0:%2 offset1:%3" : "=v"(dst) : "v"(addr), "n"(o0), "n"(o1))
        SB0();
      });
      if constexpr (CSS(S)) {
        SB0();
        csa[0] = MFMA16(ones, cat8(cl[s][0], ch[s][0]), csa[0]); csa[1] = MFMA16(ones, cat8(cl[s][1], ch[s][1]), csa[1]);
        SB0();
      }
    });
    static_for<16>([&](auto ic) {
      constexpr int i = decltype(ic)::value, j = 2 + (i >> 2), t = (j & 1) ? 3 - (i & 3) : i & 3;
      (void)&ea; (void)&ew; (void)&eaofs; (void)&ewofs;
      if constexpr (i == 0) asm volatile("s_waitcnt lgkmcnt(6)");
      else if constexpr (i == 4) asm volatile("s_waitcnt lgkmcnt(8)");
      SB0();
      const bf16x8 wf = cat8(wl[0][j], wh_[0][j]);
      if constexpr (j < 4) acc[t][j] = MFMA16(wf, af[0][t], acc[t][j]); else acc[t][j] = MFMA16(af[0][t], wf, acc[t][j]);
      SB0();
      if constexpr (i < 8) { rd(IC(2 * i), IC(31)); rd(IC(2 * i + 1), IC(31)); }
      else if constexpr (i < 12) DS_B64(ea[i - 8], eaofs, (i - 8) * 256);
      else if constexpr (i < 14) { const unsigned a_ = ewofs + 256; DS_2(ew[i - 10], a_, (i - 12) * 16, (i - 12) * 16 + 192); }
      SB0();
    });
    {
      __builtin_amdgcn_s_setprio(PRIO_T);
      asm volatile("s_waitcnt lgkmcnt(0)" : "+v"(af[1][0]), "+v"(af[1][1]), "+v"(af[1][2]), "+v"(af[1][3]), "+v"(wl[1][0]), "+v"(wh_[1][0]), "+v"(wl[1][1]), "+v"(wh_[1][1]),
                   "+v"(wl[1][2]), "+v"(wh_[1][2]), "+v"(wl[1][3]), "+v"(wh_[1][3]), "+v"(wl[1][4]), "+v"(wh_[1][4]), "+v"(wl[1][5]), "+v"(wh_[1][5]));
      asm volatile("" : "+v"(ea[0]), "+v"(ea[1]), "+v"(ea[2]), "+v"(ea[3]), "+v"(ew[2]), "+v"(ew[3]));
      SB0();
      auto mm = [&](int t, int j) { const bf16x8 wf = cat8(wl[1][j], wh_[1][j]); if (j < 4) acc[t][j] = MFMA16(wf, af[1][t], acc[t][j]); else acc[t][j] = MFMA16(af[1][t], wf, acc[t][j]); };
      auto ff = [&](int t, int j) { if (j < 4) acc[t][j] = MFMA16H(ew[j], ea[t], acc[t][j]); else acc[t][j] = MFMA16H(ea[t], ew[j], acc[t][j]); };
      auto dm = [&](int n) {
        SB0();
        const int t = n & 3, j = (n < 16 ? 4 : 0) + ((n >> 2) & 1);
        if (n & 8) ff(t, j); else mm(t, j);
        SB0();
      };
      const unsigned ew0 = ewofs, ew2 = ewofs + 512;
      mm(2, 2); SB0(); DS_B32(rt, riofs, 0); SB0();
      mm(3, 2); SB0();
      mm(2, 3); SB0(); DS_2(ew[4], ew2, 0, 192); DS_2(ew[5], ew2, 16, 208); SB0();
      mm(3, 3); SB0(); DS_2(ew[0], ew0, 0, 192); DS_2(ew[1], ew0, 16, 208); SB0();
      ff(2, 2); ff(3, 2); SB0(); wpload(); SB0();
      ff(2, 3); ff(3, 3); SB0(); SB0();
      mm(0, 2); mm(1, 2);
      SB0();
      SB0();
      char* ho = smem + HANDOFF + cw * 6144 + l * 16;
      *(u32x4*)(ho + 2048) = pack_u(acc[2][2], acc[3][2]);
      SB0(); mm(0, 3); mm(1, 3); SB0();
      *(u32x4*)(ho + 3072) = pack_u(acc[2][3], acc[3][3]);
      SB0(); ff(0, 2); ff(1, 2); ff(0, 3); ff(1, 3); SB0();
      RAW_BARRIER();
      asm volatile("" : "+v"(rt), "+v"(ew[0]), "+v"(ew[1]), "+v"(ew[4]), "+v"(ew[5]));
      dm(0); dm(1);
      fr[2] = __builtin_bit_cast(bf16x8, pack_u(acc[0][2], acc[1][2]));
      dm(2); dm(3);
      fr[3] = __builtin_bit_cast(bf16x8, pack_u(acc[0][3], acc[1][3]));
      dm(4);
      f32x2 ep[8], en[8];
      phi_fn(wp, fr[2], fr[3], ep, en, rt, rt, [&](auto kc) { constexpr int k = decltype(kc)::value; if constexpr (k == 0) { dm(5); dm(6); dm(7); } else if constexpr (7 + k < 16) dm(7 + k); });
    { const unsigned a_ = lbase + X_ST + 512 + (l >> 4) * 8;
      static_for<4>([&](auto tc) { constexpr int tb = decltype(tc)::value; (void)&dn; (void)&a_;
        asm volatile("ds_read2_b64 %0, %1 offset0:%2 offset1:%3" : "=v"(dn[tb]) : "v"(a_), "n"(tb * 8), "n"(tb * 8 + 4)); }); }
#pragma unroll
      for (int g = 0; g < 4; ++g) imgw(g, ep, en);
#pragma unroll
      for (int u = 0; u < 4; ++u) {
        const int t2 = u >> 1, db = u & 1;
        const f32x4 x0 = acc[2 * t2][4 + db], x1 = acc[2 * t2 + 1][4 + db];
        char* pv = t2 ? ho + 4096 : ho;
        *(u32x4*)(pv + db * 1024) = u32x4{pk2(x0[0], x0[1]), pk2(x0[2], x0[3]), pk2(x1[0], x1[1]), pk2(x1[2], x1[3])};
      }
#pragma unroll
      for (int t = 0; t < 4; ++t) { qa[t][0] = acc[t][0]; qa[t][1] = acc[t][1]; wr_[t] = __builtin_bit_cast(u32x4, af[1][t]); wr_[9 + t] = __builtin_bit_cast(u32x4, af[0][t]); }
      wr_[13] = __builtin_bit_cast(u32x4, cat8(wl[0][0], wh_[0][0])); wr_[14] = __builtin_bit_cast(u32x4, cat8(wl[0][1], wh_[0][1]));
      wr_[4] = __builtin_bit_cast(u32x4, cat8(wl[1][0], wh_[1][0])); wr_[5] = __builtin_bit_cast(u32x4, cat8(wl[1][1], wh_[1][1]));
      wr_[6] = __builtin_bit_cast(u32x4, cat8(ea[0], ea[1])); wr_[7] = __builtin_bit_cast(u32x4, cat8(ea[2], ea[3])); wr_[8] = __builtin_bit_cast(u32x4, cat8(ew[0], ew[1]));
    }
  } else {
    const int t = tid - 256;
    __builtin_amdgcn_s_setprio(PRIO_H);
    u64 epoch;
    asm volatile("s_load_dwordx2 %0, %1, 0x0" : "=&s"(epoch) : "s"(ws) : "memory");
    stage(0, IC(0));
    asm volatile("s_waitcnt lgkmcnt(0)" : "+s"(pfd), "+s"(epoch) :: "memory");
    const u64 enx = epoch + (((unsigned)epoch & 0xFEu) == 0xFEu ? 3u - ((unsigned)epoch & 1u) : 1u);
    const u64 tag = TAG_OF(enx);
    if (t == 0) { *(u32x2*)(smem + X_AM) = u32x2{0u, 0u};
      const unsigned e2 = ((unsigned)enx & 0x55u) | (((unsigned)enx & 0xAAu) << 15);
      *(u32x4*)(smem + X_AM + 32) = u32x4{e2, e2 >> 2, e2 >> 4, e2 >> 6} & 0x00010001u;
      *(u32x4*)(smem + X_AM + 48) = u32x4{(unsigned)tag, (unsigned)(tag >> 32), (unsigned)enx, (unsigned)(enx >> 32)}; }
    u32x2 fl, si; unsigned pfd2, tpace = 0, xs;
    unsigned ofl = WS_F1 + (unsigned)t * 16, orA = WS_CS + (unsigned)(hp * 6144 + cw * 1024 + l * 16);
    unsigned orB = (cw < 2 ? WS_CS + (unsigned)(hp * 6144 + 4096 + cw * 1024) : WS_XA + (unsigned)(panel * 2048 + (cw - 2) * 1024)) + l * 16;
    unsigned orC = ((cw & 1) ? WS_STAT + (unsigned)p3 * 1024 : WS_RI + (unsigned)panel * 1024) + l * 16;
    unsigned oxs = (unsigned)(gkq * 256 + cw * 64 + r31 * 2) * 2, akey = lbase + X_AM + 4, vol = (unsigned)l * 16;
    unsigned voG = (unsigned)((8 * cw + srow) * FF + ((sch ^ ((((srow >> 1) & 1) << 2) | ((cw & 1) << 1))) * 8)) * 2;
    const u16* gg_ = w_gate + (size_t)gkq * 256 * FF + ((gr + 1) % 56) * 64;
    const u16* gu_ = w_up + (size_t)gkq * 256 * FF + ((gr + 3) % 56) * 64;
    const u16* gwo = wo + (size_t)(((xcd >> 1) * 4 + (jj >> 3)) * 64 + 2 * cw * 8) * DM + ((xcd & 1) * 8 + (jj & 7)) * 64;
    const u16* gwp = wphi + hp * 2048 + cw * 512;
    const u16* gdw = w_down + (size_t)(((gr >> 3) & 31) * 128 + 32 * cw) * DM + ((gr + 1) & 7) * 128;
    unsigned odw = (unsigned)((l >> 4) * DM + (l & 15) * 8) * 2;
    const u16* gw2a = wo + (size_t)(((cw * 10) ^ (flip * 64)) * 8) * DM + nb * 64;
    asm volatile("" : "+s"(gw2a));
    asm volatile("" : "+s"(gg_), "+s"(gu_), "+s"(gwo), "+s"(gwp), "+s"(gdw));
    { const int f = (gr >> 3) * 128 + 32 * cw + 4 * (l & 7), fs = (f >> 6) - ((l & 8) ? 3 : 1);
      ogq = WS_GUP + (unsigned)((l >> 4) * 56 + (fs < 0 ? fs + 56 : fs)) * 512 + (f & 63) * 4 + (l & 8) * 32; }
    asm volatile("" : "+v"(voG), "+v"(odw), "+v"(ogq));
    kvsetup();
    asm volatile("s_waitcnt lgkmcnt(0)" : "+v"(ofl), "+v"(orA), "+v"(orB), "+v"(orC), "+v"(oxs), "+v"(akey), "+v"(vol) :: "memory");
    stage(1, IC(1));
    WAIT_V(10);
    __builtin_amdgcn_s_barrier();
    auto wopf = [&]() {
      GLDS_S(voO, gwo, lbase + X_WO + 2 * cw * 1024); GLDS_S(voO, gwo + 8 * DM, lbase + X_WO + (2 * cw + 1) * 1024);
      GLDS_S(vol, gwp, lbase + X_WP + cw * 1024);
    };
    auto dvl = [&](auto kc) {
      constexpr int k = decltype(kc)::value;
      static_for<DVN[k + 2]>([&](auto ic) {
        constexpr int i = dv_first(k) + decltype(ic)::value;
        u32x4& d_ = dv[i]; const unsigned o_ = odw; const u16* p_ = gdw + (size_t)(4 * i) * DM;
        asm volatile("global_load_dwordx4 %0, %1, %2 nt" : "=v"(d_) : "v"(o_), "s"(p_) : "memory");
      });
    };
    stage(2, IC(2));
    const unsigned lgu = lbase + cw * 1024;
    auto gul = [&](auto ksc) {
      constexpr int ks = decltype(ksc)::value;
      gl_nt<gu_at(ks)>(voG, gg_ + (size_t)ks * 32 * FF, lgu); gl_nt<gu_at(ks) + 4096>(voG, gu_ + (size_t)ks * 32 * FF, lgu);
    };
#define BLD64(dst, off) asm volatile("buffer_load_dwordx2 %0, %1, %2, 0 offen sc1" : "=v"(dst) : "v"(off), "s"(rs) : "memory")
    static_for<15>([&](auto ktc) {
      constexpr int kt = decltype(ktc)::value;
      constexpr auto exw = [](int k) { return DVN[k + 2]; };
      constexpr auto exo = [](int k) { return k == KP ? 1 : k == KC + 1 ? 3 : k == KC + 2 ? 2 : 0; };
      constexpr int exa = ((kt - 2 == WOK || kt - 1 == WOK) ? 3 : 0) + (kt - 2 > KC ? exo(kt - 2) : 0);
      if constexpr (exw(kt - 2) + exw(kt - 1) != 0) { if (has_gu) WAIT_V(10 + exa + exw(kt - 2) + exw(kt - 1) + exo(kt - 1)); else WAIT_V(10 + exa + exo(kt - 1)); }
      else if constexpr (kt < 14) WAIT_V(10 + exa + exo(kt - 1));
      else { if (has_gu) WAIT_V(10); else WAIT_V(0); }
      if constexpr (PACE_T[kt] != 0) { if (cw == 0) { while ((unsigned)__builtin_amdgcn_s_memrealtime() - tpace < (unsigned)(has_gu ? PACE_T[kt] : PACE_3[kt])) {} } }
      __builtin_amdgcn_s_barrier();
      if constexpr (PACE_T[kt + 1] != 0) { if (cw == 0) tpace = (unsigned)__builtin_amdgcn_s_memrealtime(); SB0(); }
#define UMAX_DPP(C) { const unsigned o = (unsigned)__builtin_amdgcn_update_dpp(0, (int)key, C, 0xf, 0xf, true); key = o > key ? o : key; asm volatile("" : "+v"(key)); }
      if constexpr (kt + NST1 < 16) { SB0(); stage(kt + NST1, IC(kt % NST1)); SB0(); }
      if constexpr (kt == WOK) wopf();
      if constexpr (kt == PF2_KT) CODE_PF(2, ".Lkf_pf_mid", pfd2);
      if constexpr (kt == PF2_KT + 2) PF_DONE(pfd2);
      if constexpr (kt == KP) BLD64(fl, ofl);
      if constexpr (kt == KC) {
        static_assert(DVN[KP + 2] == 0, "count");
        if constexpr (KC == KP + 1) asm volatile("s_waitcnt vmcnt(10)" : "+v"(fl) :: "memory"); else asm volatile("" : "+v"(fl) :: "memory");
#define FL_OK() __all((((fl[0] ^ (unsigned)tag) >> 27) | (fl[1] ^ (unsigned)(tag >> 32))) == 0u)
        if (__builtin_expect(!FL_OK(), 0)) {
          for (unsigned sp = 0;;) {
            asm volatile("buffer_load_dwordx2 %0, %1, %2, 0 offen sc1\n\ts_waitcnt vmcnt(0)" : "=v"(fl) : "v"(ofl), "s"(rs) : "memory");
            if (FL_OK()) break;
            SPIN_NEXT(sp);
          }
        }
        unsigned key = fl[0];
        UMAX_DPP(0x121) UMAX_DPP(0x122) UMAX_DPP(0x124) UMAX_DPP(0x128)
        asm volatile("s_mov_b64 exec, %2\n\tds_max_u32 %0, %1\n\ts_mov_b64 exec, -1\n\ts_waitcnt lgkmcnt(0)" :: "v"(akey), "v"(key), "s"(0x0001000100010001ull) : "memory");
      }
      if constexpr (kt == KC + 1) { GLDS_SC1(orA, ws, lbase + X_XW + cw * 1024); GLDS_SC1(orB, ws, lbase + X_XW + 4096 + cw * 1024); GLDS_SC1(orC, ws, lbase + X_ST + (cw & 1) * 1024); }
      if constexpr (kt == KC + 2) {
        const int bi = 2047 - (int)(__builtin_amdgcn_readfirstlane(*(const unsigned*)(smem + X_AM + 4)) & 2047u);
        asm volatile("global_load_dword %0, %1, %2" : "=v"(xs) : "v"(oxs), "s"(x + (size_t)bi * DM) : "memory");
        asm volatile("buffer_load_dwordx2 %0, off, %1, %2 sc1" : "=v"(si) : "s"(rs), "s"(WS_STAT + (unsigned)bi * 8) : "memory");
      }
      if constexpr (DVN[kt + 2] != 0) { if (has_gu) dvl(ktc); }
      if constexpr (kt == 13) { if (has_gu) { gul(IC(0)); gul(IC(1)); gul(IC(2)); gul(IC(3)); gul(IC(4)); } }
      if constexpr (kt == 14) asm volatile("" : "+v"(xs), "+v"(si) :: "memory");
      if constexpr (kt == 14) { if (has_gu) { static_for<1>([&](auto ic) { gul(IC(5 + decltype(ic)::value)); }); } }
    });
    auto wld = [&](auto ic) {
      constexpr int i = decltype(ic)::value;
      u32x4& d_ = wr_[i]; const unsigned o_ = voO; const u16* p_ = gw2a + (size_t)(i * 8) * DM;
      asm volatile("global_load_dwordx4 %0, %1, %2" : "=v"(d_) : "v"(o_), "s"(p_) : "memory");
    };
    wpload();
    rt = own(*(const float*)(smem + X_ST + (wt * 32 + r31) * 4));
    {
      const float m = __uint_as_float(si[0]), den = own(__uint_as_float(si[1])), rinv = rcp_nr(den);
      *(unsigned*)(smem + X_XS + cw * 128 + r31 * 4) = ln_pair(xs, m, den, rinv);
    }
    RAW_BARRIER();
    const char* ho = smem + HANDOFF + cw * 6144 + l * 16;
    fr[2] = *(const bf16x8*)(ho + 2048); fr[3] = *(const bf16x8*)(ho + 3072);
    f32x2 ep[8], en[8];
    phi_fn(wp, fr[2], fr[3], ep, en, rt, rt, [&](auto kc) {
      constexpr int k = decltype(kc)::value;
      if constexpr (k == 0) { SB0(); if (has_gu) gul(IC(6)); SB0(); }
      if constexpr (k == 5) { SB0(); if (has_gu) gul(IC(7)); SB0(); }
      static_for<NREG - WPOST>([&](auto ic) {
        constexpr int i = decltype(ic)::value;
        if constexpr (k == WH0 + (i * (17 - WH0)) / (NREG - WPOST)) { SB0(); wld(ic); SB0(); }
      });
    });
    { const unsigned a_ = lbase + X_ST + 512 + (l >> 4) * 8;
      static_for<4>([&](auto tc) { constexpr int tb = decltype(tc)::value; (void)&dn; (void)&a_;
        asm volatile("ds_read2_b64 %0, %1 offset0:%2 offset1:%3" : "=v"(dn[tb]) : "v"(a_), "n"(tb * 8), "n"(tb * 8 + 4)); }); }
#pragma unroll
    for (int g = 0; g < 4; ++g) imgw(g, ep, en);
    SB0();
    static_for<WPOST>([&](auto ic) { wld(IC(NREG - WPOST + decltype(ic)::value)); });
  }
  asm volatile(".Lkf_pf_mid:");
  f32x16 zero;
#pragma unroll
  for (int i = 0; i < 16; ++i) zero[i] = 0.f;
  u32x2 tg; unsigned t8, rkey; u32x4 t2;
  {
    __builtin_amdgcn_s_setprio(2);
    RAW_BARRIER();
    asm volatile("" : "+v"(dn[0]), "+v"(dn[1]), "+v"(dn[2]), "+v"(dn[3]));
    u32x4 am; unsigned rk;
    { const unsigned a_ = lbase + X_AM; DS_B128(am, a_, 48); DS_B128(t2, a_, 32); asm volatile("ds_read_b32 %0, %1 offset:4" : "=v"(rk) : "v"(a_)); }
    s16x4 al[4], ah[4]; bf16x8 v0[4], v1[4];
    DS_TR(al[0], pa0, 0); DS_TR(ah[0], pa0, 2048); DS_B128(v0[0], pvh, 0); DS_B128(v1[0], pvh, 1024);
    DS_TR(al[1], pa0, 4096); DS_TR(ah[1], pa0, 4096 + 2048); DS_B128(v0[1], pvh, 4096); DS_B128(v1[1], pvh, 4096 + 1024);
    DS_TR(al[2], pa0, 8192); DS_TR(ah[2], pa0, 8192 + 2048); DS_B128(v0[2], pvh, 6144); DS_B128(v1[2], pvh, 6144 + 1024);
    DS_TR(al[3], pa0, 12288); DS_TR(ah[3], pa0, 12288 + 2048); DS_B128(v0[3], pvh, 6144 + 4096); DS_B128(v1[3], pvh, 6144 + 4096 + 1024);
    f32x4 k0 = {0.f, 0.f, 0.f, 0.f}, k1 = k0, zz = k0;
    static_for<4>([&](auto tbc) {
      constexpr int tb = decltype(tbc)::value;
      asm volatile("s_waitcnt lgkmcnt(%4)" : "+v"(al[tb]), "+v"(ah[tb]), "+v"(v0[tb]), "+v"(v1[tb]) : "n"(12 - 4 * tb));
      const bf16x8 af = cat8(al[tb], ah[tb]);
      k0 = MFMA16(af, v0[tb], k0); k1 = MFMA16(af, v1[tb], k1); zz = MFMA16(af, dn[tb], zz);
      SB0();
    });
    asm volatile("" : "+v"(am), "+v"(rk), "+v"(t2));
    auto tagv = [&](f32x4 v) { const u32x4 u = __builtin_bit_cast(u32x4, v); return (u & 0xFFFFFF00u) | (am[2] & 0xFFu); };
    ST128((u32x4{pk2(k0[0], k0[1]), pk2(k0[2], k0[3]), pk2(k1[0], k1[1]), pk2(k1[2], k1[3])} & 0xFFFEFFFEu) | t2, dst);
    const u32x4 zt = tagv(zz);
    asm volatile("s_mov_b64 exec, %3\n\tbuffer_store_dwordx4 %0, %1, %2, 0 offen sc1\n\ts_mov_b64 exec, -1" :: "v"(zt), "v"(dz), "s"(rs), "s"(0x0001000100010001ull) : "memory");
    SB0();
    tg = u32x2{am[0], am[1]}; t8 = __builtin_amdgcn_readfirstlane(am[2]) & 0xFFu; rkey = __builtin_amdgcn_readfirstlane(rk);
  }
  const int idx = 2047 - (int)(rkey & 2047u);
  const int fb = kh ^ ((idx >> 4) & 1);
  const unsigned lxc = lbase + (cw < 2 ? 131072 : X_YS + 64 - 4096) + cw * 2048, lxv = w < 4 ? lxc : lbase + X_XW + cw * 2048;
  {
    bf16x8 xf[8];
    const unsigned px = lbase + X_XS + (l >> 4) * 16, hq = lbase + X_XW + cw * 2048 + l * 16;
    static_for<8>([&](auto kc) { constexpr int ks = decltype(kc)::value; (void)&xf; (void)&px; DS_B128(xf[ks], px, ks * 64); });
    if (w >= 4) {
      __builtin_amdgcn_s_setprio(0);
      WAIT_V(NREG + 2);
    } else {
      auto lo4 = [](u32x4 v) { return __builtin_bit_cast(s16x4, u32x2{v[0], v[1]}); };
      auto hi4 = [](u32x4 v) { return __builtin_bit_cast(s16x4, u32x2{v[2], v[3]}); };
#pragma unroll
      for (int tt = 0; tt < 4; ++tt) {
        const int t = tt ^ 2;
#pragma unroll
        for (int j = 0; j < 2; ++j) qa[t][j] = MFMA16(__builtin_bit_cast(bf16x8, wr_[13 + j]), __builtin_bit_cast(bf16x8, wr_[9 + t]), qa[t][j]);
#pragma unroll
        for (int j = 0; j < 2; ++j) qa[t][j] = MFMA16(__builtin_bit_cast(bf16x8, wr_[4 + j]), __builtin_bit_cast(bf16x8, wr_[t]), qa[t][j]);
        if (tt & 1) {
#pragma unroll
          for (int u = t - 1; u <= t; ++u)
#pragma unroll
            for (int j = 0; j < 2; ++j) qa[u][j] = MFMA16H(j ? hi4(wr_[8]) : lo4(wr_[8]), (u & 1) ? hi4(wr_[6 + (u >> 1)]) : lo4(wr_[6 + (u >> 1)]), qa[u][j]);
        }
      }
      const u32x4 q0 = pack_u(qa[2][0], qa[3][0]), q1 = pack_u(qa[2][1], qa[3][1]);
      asm volatile("ds_write_b128 %0, %1\n\tds_write_b128 %0, %2 offset:1024" :: "v"(hq), "v"(q0), "v"(q1) : "memory");
    }
    RAW_BARRIER();
    asm volatile("" : "+v"(xf[0]), "+v"(xf[1]), "+v"(xf[2]), "+v"(xf[3]), "+v"(xf[4]), "+v"(xf[5]), "+v"(xf[6]), "+v"(xf[7]));
    if (w >= 4) {
      { const u16* ga = wo + (size_t)(((cw * 10) ^ (flip * 64)) * 8) * DM + nb * 64; const u16* gb = wo + (size_t)(((104 + cw * 6) ^ (flip * 64)) * 8) * DM + nb * 64;
        static_for<16 - NREG>([&](auto ic) { constexpr int i = NREG + decltype(ic)::value;
          if constexpr (i < 10) gl_m0<i * 1024>(voO, ga + (size_t)(i * 8) * DM, lbase + cw * 10240); else gl_m0<(104 + i - 10) * 1024>(voO, gb + (size_t)((i - 10) * 8) * DM, lbase + cw * 6144); }); }
    }
    if (has_gu) {
      s16x4 gl[8], gh[8];
      static_for<8>([&](auto kc) { constexpr int ks = decltype(kc)::value; (void)&gl; (void)&gh; (void)&pgw;
        DS_TR(gl[ks], pgw, ks * 8192); DS_TR(gh[ks], pgw, ks * 8192 + 512); });
      f32x4 c0 = {0.f, 0.f, 0.f, 0.f};
      static_for<8>([&](auto kc) { constexpr int ks = decltype(kc)::value;
        asm volatile("s_waitcnt lgkmcnt(%2)" : "+v"(gl[ks]), "+v"(gh[ks]) : "n"(14 - 2 * ks));
        c0 = MFMA16(cat8(gl[ks], gh[ks]), xf[ks], c0);
        SB0(); });
      const u32x4 gt_ = f2u_tag(c0, t8);
      asm volatile("s_mov_b64 exec, %3\n\tbuffer_store_dwordx4 %0, %1, %2, 0 offen sc1\n\ts_mov_b64 exec, -1" :: "v"(gt_), "v"(ogu), "s"(rs), "s"(0x0001000100010001ull) : "memory");
    }
  }
  const int erow = cw * 32 + fb * 16 + (l & 15), ech = l >> 4;
  u32x4 xv[2];
  const bool sw = ech & 1;
  const bool has_idx = w < 4 && (idx >> 5) == p3 * 4 + w;
  unsigned xrt = 0;
  unsigned of4, op4, oo0, oo1, kba, kbz, odo, o2, oz2, wa_, wz_;
  auto later = [&]() {
    SB0();
    of4 = WS_F4 + (unsigned)(((panel * 4 + wt) * 16 + hp) * 2 + wn) * 32; op4 = WS_F4 + (unsigned)(((p3 * 4 + cw) * 16 + 8 * (kh ^ flip)) * 2 + (l & 15)) * 32;
    oo0 = WS_O + 512 + (unsigned)((((p3 * 4 + cw) * 2 + fb) * 32 + (kh ^ flip) * 16) * 128 + h * 64 + r31) * 16; oo1 = oo0 ^ 65536u;
    kba = lbase + X_KB + wn * 4224 + l * 16; kbz = lbase + X_KB + wn * 4224 + 4096 + h * 64;
    odo = WS_O + 512 + (unsigned)((((panel * 4 + wt) * 2 + blk) * 32 + hd) * 128 + h * 16 + (l & 15)) * 16;
    asm volatile("" : "+v"(kba), "+v"(kbz), "+v"(odo), "+v"(of4), "+v"(op4), "+v"(oo0), "+v"(oo1));
    SB0();
  };
  if (w < 4) {
    unsigned pfd3, od; u64 mkp;
    {
      u32x4 pa[2]; unsigned z0 = t8, z1 = t8;
#define H1LD(p) asm volatile("buffer_load_dwordx4 %0, %4, %6, 0 offen sc1\n\tbuffer_load_dwordx4 %1, %4, %6, %7 offen sc1\n\t" \
                   "s_cmp_lg_u32 %8, 3\n\ts_cbranch_scc1 1f\n\tbuffer_load_dword %2, %5, %6, 0 offen sc1\n\tbuffer_load_dword %3, %5, %6, 0 offen offset:256 sc1\n1:" \
                   : "=&v"(p[0]), "=&v"(p[1]), "+v"(z0), "+v"(z1) : "v"(oh1), "v"(oh1z), "s"(rs), "s"(65536), "s"(cw) : "memory", "scc")
      __builtin_amdgcn_s_sleep(H1_DELAY);
      H1LD(pa);
      SB0();
      CODE_PF(3, ".Lkf_pf_mid", pfd3);
      od = WS_KVF + (unsigned)(hr * 16 + panel) * 640 + ((l & 8) ? 512 + (l & 7) * 2 : cw * 128 + h * 64 + (l & 7) * 8 + blk * 4);
      mkp = cw == 3 ? 0x00FF00FF00FF55FFull : 0x00FF00FF00FF00FFull;
      asm volatile("" : "+s"(mkp));
      fr[0] = __builtin_bit_cast(bf16x8, pack_u(qa[0][0], qa[1][0])); fr[1] = __builtin_bit_cast(bf16x8, pack_u(qa[0][1], qa[1][1]));
      asm volatile("" : "+v"(od), "+v"(fr[0]), "+v"(fr[1]));
      asm volatile("global_load_ushort %0, %1, %2" : "=v"(xrt) : "v"((unsigned)(idx * DM + nb * 64 + 4 * (l & 15) + ech) * 2), "s"(x) : "memory");
      later();
      o2 = WS_KVF + (unsigned)(hp * 16 + wn * 8 + wm * 4 + h) * 640 + ((r31 & 12) * 2 + (r31 >> 4) * 4 + (r31 & 3)) * 16;
      oz2 = WS_KVF + (unsigned)(hp * 16 + wn * 8 + wm * 4 + (l & 3)) * 640 + 512;
      wa_ = kba + wm * 2048; wz_ = lbase + X_KB + wn * 4224 + 4096 + (l & 1) * 64 + (wm * 2 + ((l >> 1) & 1)) * 16;
      asm volatile("" : "+v"(o2), "+v"(oz2), "+v"(wa_), "+v"(wz_));
      unsigned hb = 0x8000u, s_lo = 0x05040100u, s_hi = 0x07060100u;
      asm volatile("" : "+s"(s_lo), "+s"(s_hi));
      asm volatile("s_waitcnt vmcnt(1)" : "+v"(pa[0]), "+v"(pa[1]), "+v"(z0), "+v"(z1), "+v"(hb) :: "memory");
      u64 b0, b1, b2; u32x4 v0, v1;
      auto miss = [&]() __attribute__((always_inline)) {
        v0 = pa[0] ^ t2; v1 = pa[1] ^ t2;
        b0 = __ballot((or4(v0) & 0x00010001u) != 0u); b1 = __ballot((or4(v1) & 0x00010001u) != 0u);
        b2 = __ballot((((z0 ^ t8) | (z1 ^ t8)) & 0xFFu) != 0u);
        return (b0 | b1 | b2) != 0;
      };
      if (miss()) {
        unsigned sp = 0;
        do {
        b0 |= (b0 >> 4) & 0x0F0F0F0F0F0F0F0Full; b0 |= (b0 << 4) & 0xF0F0F0F0F0F0F0F0ull; b1 |= (b1 >> 4) & 0x0F0F0F0F0F0F0F0Full; b1 |= (b1 << 4) & 0xF0F0F0F0F0F0F0F0ull;
        asm volatile("s_wqm_b64 exec, %8\n\ts_cbranch_execz 1f\n\tbuffer_load_dwordx4 %0, %4, %6, 0 offen sc1\n1:\n\ts_wqm_b64 exec, %9\n\ts_cbranch_execz 2f\n\tbuffer_load_dwordx4 %1, %4, %6, %7 offen sc1\n2:\n\t"
                     "s_mov_b64 exec, -1\n\ts_cmp_eq_u64 %10, 0\n\ts_cbranch_scc1 3f\n\tbuffer_load_dword %2, %5, %6, 0 offen sc1\n\tbuffer_load_dword %3, %5, %6, 0 offen offset:256 sc1\n3:\n\ts_waitcnt vmcnt(0)"
                     : "+v"(pa[0]), "+v"(pa[1]), "+v"(z0), "+v"(z1) : "v"(oh1), "v"(oh1z), "s"(rs), "s"(65536), "s"(b0), "s"(b1), "s"(b2) : "memory", "scc");
        } while (miss() && ++sp < SPIN_CAP);
      }
    auto sw32 = [](float a, float c) { const auto r = __builtin_amdgcn_permlane32_swap(__float_as_uint(a), __float_as_uint(c), false, false); return __uint_as_float(r[0]) + __uint_as_float(r[1]); };
    auto sw16 = [](float a, float c) { const auto r = __builtin_amdgcn_permlane16_swap(__float_as_uint(a), __float_as_uint(c), false, false); return __uint_as_float(r[0]) + __uint_as_float(r[1]); };
    float sl[4], sh[4];
#pragma unroll
    for (int m = 0; m < 4; ++m) {
      sl[m] = own(__uint_as_float(__builtin_amdgcn_perm(v0[m], hb, s_lo)) + __uint_as_float(__builtin_amdgcn_perm(v1[m], hb, s_lo)));
      sh[m] = own(__uint_as_float(__builtin_amdgcn_perm(v0[m], hb, s_hi)) + __uint_as_float(__builtin_amdgcn_perm(v1[m], hb, s_hi))); }
    const float e0 = dpp_add<0x128, 0xf>(sw16(sw32(sl[0], sl[2]), sw32(sl[1], sl[3]))), e1 = dpp_add<0x128, 0xf>(sw16(sw32(sh[0], sh[2]), sw32(sh[1], sh[3])));
    const float zs = x32_add(x16_add(dpp_add<0x128, 0xf>(__uint_as_float(z0 ^ t8) + __uint_as_float(z1 ^ t8))));
    const float zn = __builtin_bit_cast(float, __builtin_amdgcn_update_dpp(0, __builtin_bit_cast(int, zs), 0xB1, 0xf, 0xf, true));
    const unsigned kvb = min((l & 8) ? pk2(zs, zn) : pk2(e0, e1), SENT - 1u);
    asm volatile("s_mov_b64 exec, %3\n\tbuffer_store_dword %0, %1, %2, 0 offen sc1\n\ts_mov_b64 exec, -1" :: "v"(kvb), "v"(od), "s"(rs), "s"(mkp) : "memory");
    PF_DONE(pfd3);
    }
  } else {
    const char* hq = smem + X_XW + cw * 2048 + l * 16;
    fr[0] = *(const bf16x8*)hq; fr[1] = *(const bf16x8*)(hq + 1024);
  }
  bf16x8 pf[4];
  {
    f32x2 ep[8], en[8];
    phi_fn(wp, fr[0], fr[1], ep, en, rt, 1.0f, [](auto) {});
#pragma unroll
    for (int s = 0; s < 2; ++s) {
      pf[s] = __builtin_bit_cast(bf16x8, u32x4{pk2(ep[4 * s][0], ep[4 * s][1]), pk2(ep[4 * s + 1][0], ep[4 * s + 1][1]), pk2(ep[4 * s + 2][0], ep[4 * s + 2][1]), pk2(ep[4 * s + 3][0], ep[4 * s + 3][1])});
      pf[2 + s] = __builtin_bit_cast(bf16x8, u32x4{pk2(en[4 * s][0], en[4 * s][1]), pk2(en[4 * s + 1][0], en[4 * s + 1][1]), pk2(en[4 * s + 2][0], en[4 * s + 2][1]), pk2(en[4 * s + 3][0], en[4 * s + 3][1])});
    }
    asm volatile("" : "+v"(pf[0]), "+v"(pf[1]), "+v"(pf[2]), "+v"(pf[3]));
  }
  SB0();
  if (b == 0 && tid == 64) ST64(*(const u32x2*)(smem + X_AM + 56), WS_EPOCH);
  u32x4 gq; unsigned ayr; u32x4 mk4;
#define GQLD(tail) asm volatile("buffer_load_dwordx4 %0, %1, %2, 0 offen sc1" tail : "=v"(gq) : "v"(ogq), "s"(rs) : "memory")
  if (w >= 4) {
    {
      WAIT_V(18 - NREG);
    { const unsigned o_ = (unsigned)((p3 * 128 + cw * 32 + (l >> 3)) * DM + nb * 64 + ((l & 7) ^ (l >> 3)) * 8) * 2; const u16* xa = x + fb * 16 * DM; const u16* xb = x + (fb ^ 1) * 16 * DM;
      gl_m0<0>(o_, xa, lxv); gl_m0<1024>(o_, xa + 8 * DM, lxv); gl_m0<0>(o_, xb, lxc); gl_m0<1024>(o_, xb + 8 * DM, lxc); }
      SB0();
      char* pa_ = smem + cw * 10 * 1024 + l * 16;
#pragma unroll
      for (int i = 0; i < NREG; ++i) *(u32x4*)(pa_ + i * 1024) = wr_[i];
      SB0();
    }
    asm volatile("" : "+v"(dv[0]), "+v"(dv[1]), "+v"(dv[2]), "+v"(dv[3]), "+v"(dv[4]), "+v"(dv[5]), "+v"(dv[6]), "+v"(dv[7]) :: "memory");
    later();
    ayr = lbase + X_YR + (cw * 128 + (l & 15) * 8 + blk * 4) * 4;
    { const unsigned hm = (l & 1) ? 0xFFFF0000u : 0x0000FFFFu; const int q = (l & 15) >> 1;
      mk4 = u32x4{q == 0 ? hm : 0u, q == 1 ? hm : 0u, q == 2 ? hm : 0u, q == 3 ? hm : 0u}; }
    asm volatile("" : "+v"(ayr), "+v"(mk4));
    SB0();
  }
  auto downp = [&]() __attribute__((always_inline)) {
            auto red4 = [](float s0, float s1, float s2, float s3) {
              const auto r01 = __builtin_amdgcn_permlane16_swap(__float_as_uint(s0), __float_as_uint(s1), false, false);
              const auto r23 = __builtin_amdgcn_permlane16_swap(__float_as_uint(s2), __float_as_uint(s3), false, false);
              const float t01 = __uint_as_float(r01[0]) + __uint_as_float(r01[1]), t23 = __uint_as_float(r23[0]) + __uint_as_float(r23[1]);
              const auto r = __builtin_amdgcn_permlane32_swap(__float_as_uint(t01), __float_as_uint(t23), false, false);
              return __uint_as_float(r[0]) + __uint_as_float(r[1]);
            };
            const float gg = rbf(red4(__uint_as_float(gq[0]), __uint_as_float(gq[1]), __uint_as_float(gq[2]), __uint_as_float(gq[3])));
            const float uu = __builtin_bit_cast(float, __builtin_amdgcn_update_dpp(0, __builtin_bit_cast(int, gg), 0x128, 0xf, 0xf, true));
            const float hv0 = rbf(silu_r(gg)) * uu;
            const int hh = (int)pk2(hv0, hv0);
            f32x4 ya = {0.f, 0.f, 0.f, 0.f};
            static_for<8>([&](auto ic) {
              constexpr int i = decltype(ic)::value;
              u32x4 sel;
#pragma unroll
              for (int e = 0; e < 4; ++e) sel[e] = (unsigned)__builtin_amdgcn_update_dpp(0, hh, 0x150 + i, 0xf, 0xf, true) & mk4[e];
              ya = MFMA16(__builtin_bit_cast(bf16x8, sel), __builtin_bit_cast(bf16x8, dv[i]), ya);
            });
            asm volatile("" : "+v"(ya));
            if (l < 32) *(__attribute__((address_space(3))) f32x4*)(uintptr_t)ayr = ya;
  };
  {
    u32x4 kf[4], zf[4];
    if (w < 4) {
      u32x4 a, c, z; u64 m0 = ~0ull, m1 = ~0ull, m2 = ~0ull;
      unsigned sp = 0;
      do {
        asm volatile("s_mov_b64 exec, %8\n\ts_cbranch_execz 1f\n\tbuffer_load_dwordx4 %0, %3, %5, 0 offen sc1\n1:\n\ts_mov_b64 exec, %9\n\ts_cbranch_execz 2f\n\tbuffer_load_dwordx4 %1, %3, %5, 0 offen offset:1280 sc1\n2:\n\t"
                     "s_mov_b64 exec, %10\n\ts_cbranch_execz 3f\n\tbuffer_load_dwordx4 %2, %4, %5, 0 offen sc1\n3:\n\ts_mov_b64 exec, -1\n\ts_waitcnt vmcnt(0)\n\t"
                     "ds_write_b128 %6, %0\n\tds_write_b128 %6, %1 offset:1024\n\tds_write_b128 %7, %2"
                     : "+v"(a), "+v"(c), "+v"(z) : "v"(o2), "v"(oz2), "s"(rs), "v"(wa_), "v"(wz_), "s"(m0), "s"(m1), "s"(m2) : "memory");
        m0 = __ballot(max(max(max(a[0], a[1]), a[2]), a[3]) == SENT); m1 = __ballot(max(max(max(c[0], c[1]), c[2]), c[3]) == SENT); m2 = __ballot(max(max(max(z[0], z[1]), z[2]), z[3]) == SENT);
      } while ((m0 | m1 | m2) != 0 && ++sp < SPIN_CAP);
      asm volatile("" : "+v"(xrt));
    }
    RAW_BARRIER();
    DS_B128(kf[0], kba, 0); DS_B128(zf[0], kbz, 0); DS_B128(kf[1], kba, 1024); DS_B128(zf[1], kbz, 16);
    DS_B128(kf[2], kba, 2048); DS_B128(zf[2], kbz, 32); DS_B128(kf[3], kba, 3072); DS_B128(zf[3], kbz, 48);
    if (w >= 4 && has_gu) GQLD("");
    __builtin_amdgcn_s_setprio(0);
    f32x16 num = zero;
    float d0 = 0.f, d1 = 0.f;
    static_for<4>([&](auto cc) {
      constexpr int c = decltype(cc)::value;
      asm volatile("s_waitcnt lgkmcnt(%2)" : "+v"(kf[c]), "+v"(zf[c]) : "n"(6 - 2 * c));
      num = MFMA(__builtin_bit_cast(bf16x8, kf[c]), pf[c], num);
      const u32x4 pc = __builtin_bit_cast(u32x4, pf[c]);
      d0 = dot2(zf[c][0], pc[0], d0); d1 = dot2(zf[c][1], pc[1], d1); d0 = dot2(zf[c][2], pc[2], d0); d1 = dot2(zf[c][3], pc[3], d1);
      SB0();
    });
    float den = x32_add(d0 + d1);
    den = rbf(rbf(den) + 1e-6f);
    const float rinv = rcp_nr(den);
    const f32x2 rr = {rinv, rinv};
#pragma unroll
    for (int q = 0; q < 2; ++q) {
      u32x4 u;
#pragma unroll
      for (int e = 0; e < 4; ++e) {
        const f32x2 r = f32x2{num[8 * q + 2 * e], num[8 * q + 2 * e + 1]} * rr;
        u[e] = pk2(r[0], r[1]);
      }
      ST128(u, odo + q * 1024);
      SB0();
    }
  }
  const int p0 = 8 * w + 40;
  const u16* gw2 = wo + (size_t)((p0 ^ (flip * 64)) * 8) * DM + nb * 64; unsigned lw2 = lbase + p0 * 1024;
  asm volatile("" : "+s"(gw2), "+s"(lw2));
  bool glate = false;
  if (w >= 4 && has_gu) {
    asm volatile("s_waitcnt vmcnt(2)" : "+v"(gq) :: "memory");
    gq ^= t8;
    glate = !__all((or4(gq) & 0xFFu) == 0u);
    if (!glate) downp();
  }
  asm volatile("s_waitcnt vmcnt(0)\n\ts_mov_b64 exec, 1\n\tbuffer_store_dwordx2 %0, %1, %2, 0 offen sc1\n\ts_mov_b64 exec, -1" :: "v"(tg), "v"(of4), "s"(rs) : "memory");
  if (__builtin_expect(glate, 0)) {
    for (unsigned sp = 0;;) {
      GQLD("\n\ts_waitcnt vmcnt(0)");
      gq ^= t8;
      if (__all((or4(gq) & 0xFFu) == 0u)) break;
      SPIN_NEXT(sp);
    }
    downp();
  }
  RAW_BARRIER();
  float rxn = 0.f, rsg = 0.f;
  u32x4 yq[7];
  unsigned oy = WS_YP + (unsigned)(l >> 4) * 4096 + nb * 256 + (l & 15) * 16;
#define YLD(tail) asm volatile("buffer_load_dwordx4 %0, %7, %8, 0 offen sc1\n\tbuffer_load_dwordx4 %1, %7, %8, %9 offen sc1\n\tbuffer_load_dwordx4 %2, %7, %8, %10 offen sc1\n\t" \
    "buffer_load_dwordx4 %3, %7, %8, %11 offen sc1\n\tbuffer_load_dwordx4 %4, %7, %8, %12 offen sc1\n\tbuffer_load_dwordx4 %5, %7, %8, %13 offen sc1\n\t" \
    "buffer_load_dwordx4 %6, %7, %8, %14 offen sc1" tail \
    : "=&v"(yq[0]), "=&v"(yq[1]), "=&v"(yq[2]), "=&v"(yq[3]), "=&v"(yq[4]), "=&v"(yq[5]), "=&v"(yq[6]) \
    : "v"(oy), "s"(rs), "s"(16384), "s"(2 * 16384), "s"(3 * 16384), "s"(4 * 16384), "s"(5 * 16384), "s"(6 * 16384) : "memory")
  f32x2 xf[2][4], gf[2][4];
  __attribute__((address_space(3))) f32x4* pw = (__attribute__((address_space(3))) f32x4*)(smem + 131072 + w * 4096) + l;
  unsigned prd = lbase + 131072 + (w ^ 4) * 4096 + l * 16;
  u16* po = out + (size_t)(p3 * 128 + cw * 32 + fb * 16 + (l >> 3)) * DM + nb * 64 + (l & 7) * 8; u16* po2 = po + 8 * DM;
  unsigned tw0 = lbase + 131072 + (w ^ 4) * 4096 + (l & 15) * 128 + ((ech ^ (l & 7)) * 16) + (sw ? 8 : 0), tw1 = tw0 ^ 64, tw2 = tw0 ^ 8, tw3 = tw0 ^ 72, trd = lbase + 131072 + (w ^ 4) * 4096 + (l >> 3) * 128 + (((l & 7) ^ (l >> 3)) * 16);
    unsigned bw[4];
#pragma unroll
    for (int n = 0; n < 4; ++n) bw[n] = lbase + kh * 65536 + (16 * h + 4 * blk + tq) * 128 + (((4 * (n >> 1) + tp) ^ ((tq >> 1) | (blk << 2))) * 16) + 8 * ((n ^ tp) & 1);
  {
  if (w == 1 && has_gu && l < 32) {
    const f32x4* y = (const f32x4*)(smem + X_YR) + l;
    ST128(f2u_tag((y[0] + y[32]) + (y[64] + y[96]), t8), WS_YP + (unsigned)(gr >> 3) * 4096 + ((gr + 1) & 7) * 512 + l * 16);
  }
    SB0();
    gl_run<8>(voO, gw2, lw2);
    u32x2 f0;
    asm volatile("buffer_load_dwordx2 %0, %1, %2, 0 offen sc1" : "=v"(f0) : "v"(op4), "s"(rs) : "memory");
    SB0();
    {
      const f32x2 md = *(const f32x2*)(smem + X_MD + erow * 8);
      { const unsigned ax = lxv + (l & 15) * 128 + ((ech ^ (l & 7)) * 16), a0 = ax + (sw ? 8 : 0), a1 = ax + (sw ? 0 : 8);
        u32x2 q0, q1, q2, q3;
        asm volatile("ds_read_b64 %0, %4\n\tds_read_b64 %1, %5\n\tds_read_b64 %2, %6\n\tds_read_b64 %3, %7\n\ts_waitcnt lgkmcnt(0)" : "=&v"(q0), "=&v"(q1), "=&v"(q2), "=&v"(q3) : "v"(a0), "v"(a1), "v"(a0 ^ 64), "v"(a1 ^ 64) : "memory");
        xv[0] = u32x4{q0[0], q0[1], q1[0], q1[1]}; xv[1] = u32x4{q2[0], q2[1], q3[0], q3[1]}; }
      const float rinv = rcp_nr(own(md[1])), mr = -md[0] * rinv, L2E = 1.44269504088896340736f, rl = -L2E * rinv, ml = -L2E * mr;
#pragma unroll
      for (int i = 0; i < 2; ++i)
#pragma unroll
        for (int e = 0; e < 4; ++e) {
          xf[i][e] = f32x2{bflo(xv[i][e]), bfhi(xv[i][e])};
          const f32x2 xn = __builtin_elementwise_fma(xf[i][e], f32x2{rinv, rinv}, f32x2{mr, mr}), tn = __builtin_elementwise_fma(xf[i][e], f32x2{rl, rl}, f32x2{ml, ml});
          const f32x2 dd = f32x2{__builtin_amdgcn_exp2f(tn[0]), __builtin_amdgcn_exp2f(tn[1])} + f32x2{1.0f, 1.0f};
          gf[i][e] = xn * f32x2{__builtin_amdgcn_rcpf(dd[0]), __builtin_amdgcn_rcpf(dd[1])};
        }
#pragma unroll
      for (int i = 0; i < 2; ++i) asm volatile("" : "+v"(xf[i][0]), "+v"(xf[i][1]), "+v"(xf[i][2]), "+v"(xf[i][3]), "+v"(gf[i][0]), "+v"(gf[i][1]), "+v"(gf[i][2]), "+v"(gf[i][3]));
      asm volatile("" : "+v"(pw), "+v"(prd), "+v"(po2), "+v"(tw0), "+v"(tw1), "+v"(tw2), "+v"(tw3), "+v"(trd));
      asm volatile("" : "+v"(oy), "+v"(po), "+v"(bw[0]), "+v"(bw[1]), "+v"(bw[2]), "+v"(bw[3]));
    }
    if (has_idx) {
      const unsigned ord = (rkey >> 11) & 0xFFFFu;
      rsg = rbf(1.0f / (1.0f + __expf(-__uint_as_float((ord ^ ((ord & 0x8000u) ? 0x8000u : 0xFFFFu)) << 16))));
      const f32x2 md = *(const f32x2*)(smem + X_MD + (idx & 127) * 8);
      const float dn = own(md[1]), rinv = rcp_nr(dn);
      rxn = bflo(ln_pair(xrt, md[0], dn, rinv));
      asm volatile("" : "+v"(rxn), "+v"(rsg));
      { u64 pc_; unsigned d_, o_;
        asm volatile("s_getpc_b64 %0\n.Lkf_own1:\n\ts_mov_b32 %1, .Lkf_own0-.Lkf_own1" : "=s"(pc_), "=s"(o_));
        pc_ += o_;
        asm volatile("s_load_dword %0, %1, 0x0\n\ts_load_dword %0, %1, 0x80\n\ts_load_dword %0, %1, 0x100\n\ts_load_dword %0, %1, 0x180\n\ts_load_dword %0, %1, 0x200\n\ts_load_dword %0, %1, 0x280\n\ts_waitcnt lgkmcnt(0)" : "=&s"(d_) : "s"(pc_) : "memory"); }
    }
    u64 ne;
    SB0();
    asm volatile("s_waitcnt vmcnt(0)\n\tv_cmp_ne_u64 %0, %1, %2" : "=s"(ne) : "v"(f0), "v"(tg) : "memory");
    for (unsigned sp = 0; ne != 0 && sp < SPIN_CAP; ++sp) {
      u32x2 f;
      asm volatile("buffer_load_dwordx2 %1, %2, %3, 0 offen sc1\n\ts_waitcnt vmcnt(0)\n\tv_cmp_ne_u64 %0, %1, %4" : "=s"(ne), "=&v"(f) : "v"(op4), "s"(rs), "v"(tg) : "memory");
    }
  }
  constexpr int OWIN = 4;
  u32x4 of[16][2];
  {
    SB0();
#pragma unroll
    for (int i = 0; i < OWIN; ++i) { of[i][0] = LD128(oo0 + i * 2048); of[i][1] = LD128(oo1 + i * 2048); }
    SB0();
    if (has_idx) YLD("");
    SB0();
  }
    auto owner = [&](auto nc) __attribute__((always_inline)) {
      asm volatile(".Lkf_own0:\n\ts_waitcnt vmcnt(%7)" : "+v"(yq[0]), "+v"(yq[1]), "+v"(yq[2]), "+v"(yq[3]), "+v"(yq[4]), "+v"(yq[5]), "+v"(yq[6]) : "n"(decltype(nc)::value) : "memory");
      for (unsigned sp = 0;; ++sp) {
        unsigned bad = 0;
#pragma unroll
        for (int i = 0; i < 7; ++i) { yq[i] ^= t8; bad |= or4(yq[i]); }
        if (__builtin_expect(__all((bad & 0xFFu) == 0u) || sp > SPIN_CAP, 1)) break;
        YLD("\n\ts_waitcnt vmcnt(0)");
      }
#define YF(i) __builtin_bit_cast(f32x4, yq[i])
      const f32x4 y4 = ((YF(0) + YF(1)) + (YF(2) + YF(3))) + ((YF(4) + YF(5)) + YF(6));
      const auto r01 = __builtin_amdgcn_permlane16_swap(__float_as_uint(y4[0]), __float_as_uint(own(y4[1])), false, false);
      const auto r23 = __builtin_amdgcn_permlane16_swap(__float_as_uint(y4[2]), __float_as_uint(own(y4[3])), false, false);
      const float t01 = __uint_as_float(r01[0]) + __uint_as_float(r01[1]), t23 = __uint_as_float(r23[0]) + __uint_as_float(r23[1]);
      const auto r = __builtin_amdgcn_permlane32_swap(__float_as_uint(t01), __float_as_uint(t23), false, false);
      const float y = __uint_as_float(r[0]) + __uint_as_float(r[1]);
      const float g1 = rbf(silu_r(rbf(rxn + rbf(rbf(y) * rsg))));
      (void)&gf; (void)&oy; (void)&rs; const u64 mk_ = 0x0001000100010001ull << (idx & 15);
      const unsigned gb = lbase + (131072 + w * 4096), aw_ = gb + (l & 15) * 16 + ech * 4, a0 = gb + ech * 32 + (ech & 1) * 16, a1 = a0 ^ 16;
      asm volatile("ds_write_b32 %8, %9\n\ts_mov_b64 exec, %12\n\t"
                   "ds_read_b64 %0, %10\n\tds_read_b64 %1, %10 offset:8\n\tds_read_b64 %2, %11\n\tds_read_b64 %3, %11 offset:8\n\t"
                   "ds_read_b64 %4, %10 offset:128\n\tds_read_b64 %5, %10 offset:136\n\tds_read_b64 %6, %11 offset:128\n\tds_read_b64 %7, %11 offset:136\n\t"
                   "s_mov_b64 exec, -1\n\ts_waitcnt lgkmcnt(0)"
                   : "+v"(gf[0][0]), "+v"(gf[0][1]), "+v"(gf[0][2]), "+v"(gf[0][3]), "+v"(gf[1][0]), "+v"(gf[1][1]), "+v"(gf[1][2]), "+v"(gf[1][3])
                   : "v"(aw_), "v"(g1), "v"(a0), "v"(a1), "s"(mk_) : "memory");
    };
  f32x4 acc3[2][4];
#pragma unroll
  for (int t = 0; t < 2; ++t)
#pragma unroll
    for (int n = 0; n < 4; ++n) acc3[t][n] = f32x4{0.f, 0.f, 0.f, 0.f};
  {
    s16x4 wa[2][8];
    auto rd3 = [&](auto kc, auto nc) {
      constexpr int k = decltype(kc)::value, n = decltype(nc)::value;
      (void)&wa; (void)&bw;
      DS_TR(wa[k & 1][2 * n], bw[n], k * 4096); DS_TR(wa[k & 1][2 * n + 1], bw[n], k * 4096 + 1024);
    };
    RAW_BARRIER();
    static_for<4>([&](auto nc) { rd3(IC(0), nc); });
    static_for<15>([&](auto kc) {
      constexpr int k = decltype(kc)::value, sx = k & 1;
      static_for<4>([&](auto nc) {
        constexpr int n = decltype(nc)::value;
        asm volatile("s_waitcnt lgkmcnt(%2)" : "+v"(wa[sx][2 * n]), "+v"(wa[sx][2 * n + 1]) : "n"(6));
        SB0();
        const bf16x8 wf = cat8(wa[sx][2 * n], wa[sx][2 * n + 1]);
        acc3[0][n] = MFMA16(wf, __builtin_bit_cast(bf16x8, of[k][0]), acc3[0][n]);
        SB0();
        if constexpr (n == 0 && (k & 3) == 0) __builtin_amdgcn_s_setprio(3 - (k >> 2));
        if constexpr (k + 1 < 16) rd3(IC(k + 1), nc);
        if constexpr (k + OWIN < 16 && n == 3) { of[k + OWIN][0] = LD128(oo0 + (k + OWIN) * 2048); }
        SB0();
        acc3[1][n] = MFMA16(wf, __builtin_bit_cast(bf16x8, of[k][1]), acc3[1][n]);
        SB0();
        if constexpr (k + OWIN < 16 && n == 3) { of[k + OWIN][1] = LD128(oo1 + (k + OWIN) * 2048); SB0(); }
        if constexpr (k == YP_ && n == 3) { constexpr int e_ = YP_ + OWIN < 16 ? YP_ : 15 - OWIN; if (has_idx) owner(IC(2 * (e_ + 1))); SB0(); }
      });
    });
    static_for<4>([&](auto nc) {
      constexpr int n = decltype(nc)::value;
      asm volatile("s_waitcnt lgkmcnt(%2)" : "+v"(wa[1][2 * n]), "+v"(wa[1][2 * n + 1]) : "n"(6 - 2 * n));
      SB0();
      acc3[1][n] = MFMA16(cat8(wa[1][2 * n], wa[1][2 * n + 1]), __builtin_bit_cast(bf16x8, of[15][1]), acc3[1][n]);
      SB0();
      if constexpr (n == 3) { pw[0] = acc3[1][0]; SB0(); }
    });
    static_for<4>([&](auto nc) {
      constexpr int n = decltype(nc)::value;
      acc3[0][n] = MFMA16(cat8(wa[1][2 * n], wa[1][2 * n + 1]), __builtin_bit_cast(bf16x8, of[15][0]), acc3[0][n]);
      SB0();
      if constexpr (n < 3) { pw[(n + 1) * 64] = acc3[1][n + 1]; SB0(); }
    });
  }
  {
    RAW_BARRIER();
    f32x4 pr[4];
    DS_B128(pr[0], prd, 0); DS_B128(pr[1], prd, 1024); DS_B128(pr[2], prd, 2048); DS_B128(pr[3], prd, 3072);
    SB0();
#pragma unroll
    for (int i = 0; i < 2; ++i)
#pragma unroll
      for (int e = 0; e < 4; ++e) {
        const f32x4 a = acc3[0][2 * i + (e >> 1)];
        xf[i][e] = __builtin_elementwise_fma(f32x2{a[2 * (e & 1)], a[2 * (e & 1) + 1]}, gf[i][e], xf[i][e]);
        asm volatile("" : "+v"(xf[i][e]));
      }
    SB0();
    u32x4 res[2];
#pragma unroll
    for (int i = 0; i < 2; ++i) {
      if (i == 0) asm volatile("s_waitcnt lgkmcnt(2)" : "+v"(pr[0]), "+v"(pr[1])); else asm volatile("s_waitcnt lgkmcnt(2)" : "+v"(pr[2]), "+v"(pr[3]));
      const f32x4 s0 = pr[2 * i], s1 = pr[2 * i + 1];
      const f32x2 o0 = __builtin_elementwise_fma(f32x2{s0[0], s0[1]}, gf[i][0], xf[i][0]), o1 = __builtin_elementwise_fma(f32x2{s0[2], s0[3]}, gf[i][1], xf[i][1]);
      const f32x2 o2 = __builtin_elementwise_fma(f32x2{s1[0], s1[1]}, gf[i][2], xf[i][2]), o3 = __builtin_elementwise_fma(f32x2{s1[2], s1[3]}, gf[i][3], xf[i][3]);
      res[i] = u32x4{pk2(o0[0], o0[1]), pk2(o1[0], o1[1]), pk2(o2[0], o2[1]), pk2(o3[0], o3[1])};
      asm volatile("ds_write_b64 %0, %2\n\tds_write_b64 %1, %3" :: "v"(i ? tw1 : tw0), "v"(i ? tw3 : tw2), "v"(u32x2{res[i][0], res[i][1]}), "v"(u32x2{res[i][2], res[i][3]}) : "memory");
    }
    asm volatile("ds_read_b128 %0, %2\n\tds_read_b128 %1, %2 offset:1024\n\ts_waitcnt lgkmcnt(1)\n\t"
                 "global_store_dwordx4 %3, %0, off sc1\n\ts_waitcnt lgkmcnt(0)\n\tglobal_store_dwordx4 %4, %1, off sc1\n\ts_nop 1"
                 : "=&v"(res[0]), "=&v"(res[1]) : "v"(trd), "v"(po), "v"(po2) : "memory");
  }
  asm volatile(".Lkf_pf_end:" ::: "memory");
}

extern "C" void kernel_launch(void* const* d_in, const int* in_sizes, int n_in,
                              void* d_out, int out_size, void* d_ws, size_t ws_size,
                              hipStream_t stream) {
  (void)hipFuncSetAttribute((const void*)kf_all, hipFuncAttributeMaxDynamicSharedMemorySize, LDS_TOTAL);
  kf_all<<<256, 512, LDS_TOTAL, stream>>>((const u16*)d_in[0], (const u16*)d_in[1], (const u16*)d_in[2], (const u16*)d_in[3],
                                          (const u16*)d_in[5], (const u16*)d_in[6], (const u16*)d_in[7], (const u16*)d_in[8],
                                          (char*)d_ws, (u16*)d_out);
}
```

```cpp
#define KP 9
#define KC 10
#define F1_S 13
#define PF2_KT 8
#define WOK 2
#define H1_DELAY 4
constexpr int PACE_T[16] = {0, 108, 72, 60}, PACE_3[16] = {0, 60, 50, 50};
constexpr int DVN[20] = {0, 0, 3, 2, 1, 0, 1, 0, 1};
constexpr int dv_first(int k) { int n = 0; for (int i = -2; i < k; ++i) n += DVN[i + 2]; return n; }
constexpr bool CSS(int S) { return S < 2 || S == 6 || S == 7; }
#define NREG 8
static_assert(NREG <= 10, "first run");
#define WH0 12
#define WPOST 4
#define YP_ 11
#define PRIO_T 2
#define PRIO_H 1
#include <hip/hip_runtime.h>
#include <hip/hip_bf16.h>
#include <stdint.h>
#include <utility>
#include <type_traits>

typedef __attribute__((ext_vector_type(8))) short bf16x8;
typedef __attribute__((ext_vector_type(4))) short s16x4;
typedef __attribute__((ext_vector_type(16))) float f32x16;
typedef __attribute__((ext_vector_type(4))) float f32x4;
typedef __attribute__((ext_vector_type(2))) float f32x2;
typedef __attribute__((ext_vector_type(4))) unsigned u32x4;
typedef __attribute__((ext_vector_type(2))) unsigned u32x2;
typedef __attribute__((ext_vector_type(2))) __bf16 bfx2;
typedef __attribute__((ext_vector_type(4))) short v4i16_t;
typedef unsigned long long u64;
typedef unsigned short u16;

#define DI __device__ __forceinline__
#define MFMA(a, b, c) __builtin_amdgcn_mfma_f32_32x32x16_bf16((a), (b), (c), 0, 0, 0)
#define MFMA16H(a, b, c) __builtin_amdgcn_mfma_f32_16x16x16bf16_1k((a), (b), (c), 0, 0, 0)
#define MFMA16(a, b, c) __builtin_amdgcn_mfma_f32_16x16x32_bf16((a), (b), (c), 0, 0, 0)

DI unsigned pk2(float a, float b) { f32x2 v = {a, b}; return __builtin_bit_cast(unsigned, __builtin_convertvector(v, bfx2)); }
DI float bflo(unsigned u) { return __uint_as_float(u << 16); }
DI float bfhi(unsigned u) { return __uint_as_float(u & 0xffff0000u); }
DI float rbf(float x) { return (float)(__bf16)x; }
DI s16x4 trread(const char* p) {
  return __builtin_bit_cast(s16x4, __builtin_amdgcn_ds_read_tr16_b64_v4i16((__attribute__((address_space(3))) v4i16_t*)p));
}
DI bf16x8 cat8(s16x4 lo, s16x4 hi) { return __builtin_shufflevector(lo, hi, 0, 1, 2, 3, 4, 5, 6, 7); }
#define WAIT_V(n) asm volatile("s_waitcnt vmcnt(%0)" ::"n"(n) : "memory")
#define RAW_BARRIER() do { asm volatile("s_waitcnt lgkmcnt(0)" ::: "memory"); __builtin_amdgcn_s_barrier(); asm volatile("" ::: "memory"); } while (0)
#define DS_B128(dst, addr, off) asm volatile("ds_read_b128 %0, %1 offset:%2" : "=v"(dst) : "v"(addr), "n"(off))
#define DS_TR(dst, addr, off) asm volatile("ds_read_b64_tr_b16 %0, %1 offset:%2" : "=v"(dst) : "v"(addr), "n"(off))
#define LDS_OFF(p) ((unsigned)(uintptr_t)(__attribute__((address_space(3))) const char*)(p))
template <class F, int... I> DI void static_for_impl(F&& f, std::integer_sequence<int, I...>) { (f(std::integral_constant<int, I>{}), ...); }
template <int N, class F> DI void static_for(F&& f) { static_for_impl(f, std::make_integer_sequence<int, N>{}); }
DI float divc(float a, float b, float rinv) { const float q = a * rinv; return __builtin_fmaf(__builtin_fmaf(-q, b, a), rinv, q); }
DI float rcp_nr(float d) { const float r = __builtin_amdgcn_rcpf(d); return __builtin_fmaf(__builtin_fmaf(-d, r, 1.0f), r, r); }
DI float own(float v) { asm volatile("" : "+v"(v)); return v; }
DI float silu_r(float x) { return x * __builtin_amdgcn_rcpf(1.0f + __expf(-x)); }
#define SB0() __builtin_amdgcn_sched_barrier(0)
#define GLDS_S(vo, sp, ld) asm volatile("s_mov_b32 m0, %2\n\ts_nop 0\n\tglobal_load_lds_dwordx4 %0, %1" :: "v"(vo), "s"(sp), "s"(ld) : "m0", "memory")
template <int IMM> DI void gl_m0(unsigned vo, const void* sp, unsigned ls) {
  asm volatile("s_add_u32 m0, %2, %3\n\ts_nop 0\n\tglobal_load_lds_dwordx4 %0, %1" :: "v"(vo), "s"(sp), "s"(ls), "n"(IMM) : "m0", "memory", "scc");
}
template <int IMM> DI void gl_nt(unsigned vo, const void* sp, unsigned ls) {
  asm volatile("s_add_u32 m0, %2, %3\n\ts_nop 0\n\tglobal_load_lds_dwordx4 %0, %1 nt" :: "v"(vo), "s"(sp), "s"(ls), "n"(IMM) : "m0", "memory", "scc");
}
template <int N> DI void gl_run(unsigned vo, const void* sp, unsigned ls) {
  unsigned lo = (unsigned)(uintptr_t)sp, hi = (unsigned)((uintptr_t)sp >> 32);
  static_for<N>([&](auto ic) {
    constexpr int i = decltype(ic)::value;
    gl_m0<i * 1024>(vo, (const void*)(((uintptr_t)hi << 32) | lo), ls);
    if constexpr (i + 1 < N) asm volatile("s_add_u32 %0, %0, 0x4000\n\ts_addc_u32 %1, %1, 0" : "+s"(lo), "+s"(hi) :: "scc");
  });
}
#define GLDS_SC1(vo, sp, ld) asm volatile("s_mov_b32 m0, %2\n\ts_nop 0\n\tglobal_load_lds_dwordx4 %0, %1 sc1" :: "v"(vo), "s"(sp), "s"(ld) : "m0", "memory")
DI float x32_add(float v) { auto r = __builtin_amdgcn_permlane32_swap(__float_as_uint(v), __float_as_uint(v), false, false); return __uint_as_float(r[0]) + __uint_as_float(r[1]); }
DI float x32_max(float v) { auto r = __builtin_amdgcn_permlane32_swap(__float_as_uint(v), __float_as_uint(v), false, false); float o; asm("v_max_f32 %0, %1, %2" : "=v"(o) : "v"(r[0]), "v"(r[1])); return o; }
DI float x32_min(float v) { auto r = __builtin_amdgcn_permlane32_swap(__float_as_uint(v), __float_as_uint(v), false, false); float o; asm("v_min_f32 %0, %1, %2" : "=v"(o) : "v"(r[0]), "v"(r[1])); return o; }
DI float x16_add(float v) { auto r = __builtin_amdgcn_permlane16_swap(__float_as_uint(v), __float_as_uint(v), false, false); return __uint_as_float(r[0]) + __uint_as_float(r[1]); }
template <int CTRL, int RMASK> DI float dpp_add(float v) {
  return v + __builtin_bit_cast(float, __builtin_amdgcn_update_dpp(0, __builtin_bit_cast(int, v), CTRL, RMASK, 0xf, true));
}

DI unsigned ln_pair(unsigned u, float m, float den, float rinv) {
  const f32x2 mm = {m, m}, dd = {den, den}, rr = {rinv, rinv};
  const f32x2 d0 = f32x2{bflo(u), bfhi(u)} - mm;
  const unsigned du = pk2(d0[0], d0[1]);
  const f32x2 d = {bflo(du), bfhi(du)};
  const f32x2 q = d * rr;
  const f32x2 r = __builtin_elementwise_fma(__builtin_elementwise_fma(-q, dd, d), rr, q);
  return pk2(r[0], r[1]);
}
DI float dot2(unsigned a, unsigned b, float c) { return __builtin_amdgcn_fdot2_f32_bf16(__builtin_bit_cast(bfx2, a), __builtin_bit_cast(bfx2, b), c, false); }
DI float half_total(float v) {
  v = dpp_add<0x121, 0xf>(v); v = dpp_add<0x122, 0xf>(v); v = dpp_add<0x124, 0xf>(v); v = dpp_add<0x128, 0xf>(v);
  return x16_add(v);
}
DI void ln_rows(const u32x4 (&xr)[4], const u32x4 (&wr)[2], int h, float& m, float& den, float& rinv, float& r) {
  const float c0 = __uint_as_float(__builtin_amdgcn_readlane(xr[0][0], 0) << 16), c1 = __uint_as_float(__builtin_amdgcn_readlane(xr[2][0], 0) << 16);
  float s1[2] = {0.f, 0.f}, sx[2] = {0.f, 0.f}, sw = 0.f;
  f32x2 q[2] = {{0.f, 0.f}, {0.f, 0.f}};
#pragma unroll
  for (int i = 0; i < 2; ++i)
#pragma unroll
    for (int e = 0; e < 4; ++e) {
      sw = dot2(wr[i][e], 0x3F803F80u, sw);
#pragma unroll
      for (int t = 0; t < 2; ++t) {
        const unsigned u = xr[2 * t + i][e]; const float c = t ? c1 : c0;
        s1[t] = dot2(u, 0x3F803F80u, s1[t]); sx[t] = dot2(u, wr[i][e], sx[t]);
        const f32x2 d = f32x2{bflo(u), bfhi(u)} - f32x2{c, c};
        q[t] = __builtin_elementwise_fma(d, d, q[t]);
      }
    }
  auto mg = [](float a, float b) { const auto x = __builtin_amdgcn_permlane32_swap(__float_as_uint(a), __float_as_uint(b), false, false); return __uint_as_float(x[0]) + __uint_as_float(x[1]); };
  const float mean = half_total(mg(s1[0], s1[1])) * (1.0f / 1024.0f), m2 = half_total(mg(q[0][0] + own(q[0][1]), q[1][0] + own(q[1][1]))) * (1.0f / 1024.0f);
  const float tx = half_total(mg(sx[0], sx[1])), tw = half_total(x32_add(sw));
  const float dm = mean - (h ? c1 : c0);
  const float var = fmaxf(__builtin_fmaf(-dm, dm, m2), 0.f);
  m = rbf(mean);
  den = rbf(__builtin_amdgcn_sqrtf(rbf(rbf(var) + 1e-5f)));
  rinv = rcp_nr(den);
  r = rbf(divc(__builtin_fmaf(-m, tw, tx), den, rinv));
}

template <class HK> DI void phi_fn(const bf16x8 (&wp)[2], const bf16x8 f0, const bf16x8 f1, f32x2 (&ep)[8], f32x2 (&en)[8], float si, float so, HK&& hk) {
  f32x16 p;
#pragma unroll
  for (int i = 0; i < 16; ++i) p[i] = 0.f;
  p = MFMA(wp[0], f0, p);
  p = MFMA(wp[1], f1, p);
  hk(std::integral_constant<int, 0>{});
  float mx = -3.0e38f, mn = 3.0e38f;
  f32x2 p2[8];
  static_for<8>([&](auto ic) {
    constexpr int i = decltype(ic)::value;
    p2[i] = f32x2{p[2 * i], p[2 * i + 1]};
    mx = fmaxf(fmaxf(mx, p2[i][0]), p2[i][1]); mn = fminf(fminf(mn, p2[i][0]), p2[i][1]);
    if constexpr (i & 1) hk(std::integral_constant<int, 1 + (i >> 1)>{});
  });
  mx = x32_max(mx); mn = x32_min(mn);
  hk(std::integral_constant<int, 5>{});
  const float L2E = 1.44269504088896340736f * si, mxl = -mx * L2E, mnl = mn * L2E;
  const f32x2 cp = {L2E, L2E}, cn = {-L2E, -L2E}, bp = {mxl, mxl}, bn = {mnl, mnl};
  f32x2 sp2, sn2;
  static_for<8>([&](auto ic) {
    constexpr int i = decltype(ic)::value;
    const f32x2 tp2 = __builtin_elementwise_fma(p2[i], cp, bp), tn2 = __builtin_elementwise_fma(p2[i], cn, bn);
    ep[i] = f32x2{__builtin_amdgcn_exp2f(tp2[0]), __builtin_amdgcn_exp2f(tp2[1])};
    en[i] = f32x2{__builtin_amdgcn_exp2f(tn2[0]), __builtin_amdgcn_exp2f(tn2[1])};
    if constexpr (i == 0) { sp2 = ep[0]; sn2 = en[0]; } else { sp2 += ep[i]; sn2 += en[i]; }
    hk(std::integral_constant<int, 6 + i>{});
  });
  const float sp = x32_add(sp2[0] + own(sp2[1])), sn = x32_add(sn2[0] + own(sn2[1]));
  hk(std::integral_constant<int, 14>{});
  const float rp = __builtin_amdgcn_rcpf(sp) * so, rn = __builtin_amdgcn_rcpf(sn) * so;
  const f32x2 rp2 = {rp, rp}, rn2 = {rn, rn};
  hk(std::integral_constant<int, 15>{});
#pragma unroll
  for (int i = 0; i < 8; ++i) { ep[i] = ep[i] * rp2; en[i] = en[i] * rn2; }
  hk(std::integral_constant<int, 16>{});
}

constexpr int DM = 1024, FF = 3584;
constexpr unsigned SENT = 0xFFFFFFFFu;
constexpr unsigned SPIN_CAP = 1u << 17;

constexpr unsigned WS_EPOCH = 0;
constexpr unsigned WS_F1 = 4096;
constexpr unsigned WS_F4 = 896u << 10;
constexpr unsigned WS_STAT = 20480;
constexpr unsigned WS_CS = 36864;
constexpr unsigned WS_GUP = WS_CS + 16 * 16 * 768;
constexpr unsigned WS_YP = WS_GUP + 256 * 512;
constexpr unsigned WS_KVF = WS_YP + 32 * 4096;
constexpr unsigned WS_ZP = WS_KVF + 256 * 640;
constexpr unsigned WS_XA = 800u << 10;
constexpr unsigned WS_RI = 840u << 10;
static_assert(WS_ZP + 131072 <= WS_XA && WS_XA + 32768 <= WS_RI, "ws map");
constexpr unsigned WS_KVP = 1u << 20;
constexpr unsigned WS_O = 8u << 20;
constexpr unsigned WS_END = 16u << 20;

constexpr int ABYTES = 128 * 128, WSLAB = 64 * 128, STAGE1 = ABYTES + 3 * WSLAB, NST1 = 3;
constexpr int XB = NST1 * STAGE1;
constexpr int X_WO = XB;
constexpr int X_CSP = XB + 8192;
constexpr int X_P = 131072 + 4096;
constexpr int X_XW = X_P;
constexpr int X_XA = X_XW + 6144;
constexpr int X_ST = X_XA + 2048;
constexpr int X_MD = X_ST + 1024;
constexpr int X_AM = X_MD + 1024;
constexpr int X_XS = X_AM + 64;
constexpr int X_YR = X_XS + 512;
constexpr int X_YS = X_YR + 2048;
constexpr int X_WP = X_YS + 2048;
constexpr int X_KB = X_WP + 4096;
constexpr int LDS_TOTAL = 163840;
static_assert(X_KB + 2 * 4224 <= LDS_TOTAL && 131072 + 6 * 2048 <= X_MD && X_YS + 64 + 4096 <= X_KB && X_YS % 128 == 64, "lds");
static_assert(LDS_TOTAL <= 163840, "lds");
constexpr int gu_at(int ks) { return STAGE1 + ks * 8192; }
constexpr int HANDOFF = 106496;

#define LD128(off) __builtin_amdgcn_raw_buffer_load_b128(rs, (int)(off), 0, 16)
#define ST128(v, off) __builtin_amdgcn_raw_buffer_store_b128((v), rs, (int)(off), 0, 16)
#define ST64(v, off) __builtin_amdgcn_raw_buffer_store_b64((v), rs, (int)(off), 0, 16)
DI u32x4 f2u_tag(f32x4 v, unsigned t8) { u32x4 u = __builtin_bit_cast(u32x4, v); return (u & 0xFFFFFF00u) | t8; }
DI unsigned or4(u32x4 v) { return (v[0] | v[1]) | (v[2] | v[3]); }
#define SPIN_NEXT(sp) { if (++(sp) > SPIN_CAP) break; __builtin_amdgcn_s_sleep(1); }
#define TAG_OF(en) ((en) * 0x9E3779B97F4A7C15ull)

__global__ __launch_bounds__(512, 2) void kf_all(const u16* __restrict__ x, const u16* __restrict__ wqkv, const u16* __restrict__ wphi,
                                              const u16* __restrict__ wo, const u16* __restrict__ w_r, const u16* __restrict__ w_gate,
                                              const u16* __restrict__ w_up, const u16* __restrict__ w_down, char* ws, u16* __restrict__ out) {
  extern __shared__ __attribute__((aligned(16))) char smem[];
  asm volatile(".Lkf_pf_start:");
  const int b = blockIdx.x;
  const int tid = threadIdx.x, w = __builtin_amdgcn_readfirstlane(tid >> 6);
  unsigned pfd;
  {
    u64 pc; unsigned len;
    asm volatile("s_getpc_b64 %0\n.Lkf_pf_here:\n\ts_mov_b32 %1, .Lkf_pf_end+1152-256-.Lkf_pf_here" : "=s"(pc), "=s"(len));
    const u64 pa = pc + min((unsigned)((b & ~7) + w) << 8, len);
    asm volatile("s_load_dword %0, %1, 0x0\n\ts_load_dword %0, %1, 0x40\n\ts_load_dword %0, %1, 0x80\n\ts_load_dword %0, %1, 0xc0" : "=&s"(pfd) : "s"(pa) : "memory");
  }
  SB0();
  const int xcd = b & 7, jj = b >> 3, l = tid & 63;
  const int panel = (xcd >> 2) * 8 + (jj & 7), hp = (xcd & 3) * 4 + (jj >> 3);
  const int id = panel * 16 + hp;
  asm volatile("" : "+s"(x), "+s"(wqkv), "+s"(wphi), "+s"(wo), "+s"(w_r), "+s"(w_gate), "+s"(w_up), "+s"(w_down), "+s"(ws), "+s"(out));
  SB0();
#define GLD(dst, p) asm volatile("global_load_dwordx4 %0, %1, off" : "=v"(dst) : "v"(p) : "memory")
  u32x4 xr4[4], wr4[2];
  if (w < 4) {
    const u16* xr = x + (size_t)(panel * 128 + hp * 8 + 2 * w) * DM; const unsigned vl = (unsigned)l * 16;
#define GLS(dst, p, o) asm volatile("global_load_dwordx4 %0, %1, %2 offset:" #o : "=v"(dst) : "v"(vl), "s"(p) : "memory")
    GLS(xr4[0], xr, 0); GLS(xr4[1], xr, 1024); GLS(xr4[2], xr, 2048); GLS(xr4[3], xr, 3072); GLS(wr4[0], w_r, 0); GLS(wr4[1], w_r, 1024);
  }
  SB0();
  const int cw = w & 3, wm = cw & 1, wn = cw >> 1;
  const int h = l >> 5, r31 = l & 31, blk = (l >> 4) & 1, tq = (l & 15) >> 2, tp = l & 3;
  const int hd = hp * 2 + wn;
  const unsigned lbase = LDS_OFF(smem);
  const __amdgpu_buffer_rsrc_t rs = __builtin_amdgcn_make_buffer_rsrc((void*)ws, 0, (int)WS_END, 0x00020000);
  bf16x8 fr[4];
  bf16x8 wp[2];
  f32x4 qa[4][2]; float rt;
  const int kh = w >> 2;
  const int wt = wm * 2 + kh;
  const unsigned img = (wn * 4 + wt) * 4096 + r31 * 128 + (((2 * h) ^ (r31 & 7)) * 16);
  auto wpload = [&]() {
    const char* p = smem + X_WP + wn * 2048 + (8 * h + tq) * 64 + (16 * blk + 4 * tp) * 2;
    wp[0] = cat8(trread(p), trread(p + 256)); wp[1] = cat8(trread(p + 1024), trread(p + 1280));
  };
  auto imgw = [&](int g, const f32x2 (&ep)[8], const f32x2 (&en)[8]) {
    const f32x2 (&e)[8] = g < 2 ? ep : en; const int q = 4 * (g & 1);
    *(u32x4*)(smem + (img ^ ((g >> 1) * 64 + (g & 1) * 16))) = u32x4{pk2(e[q][0], e[q][1]), pk2(e[q + 1][0], e[q + 1][1]), pk2(e[q + 2][0], e[q + 2][1]), pk2(e[q + 3][0], e[q + 3][1])};
  };
  auto pack_u = [&](const f32x4 x0, const f32x4 x1) {
    const auto s0 = __builtin_amdgcn_permlane16_swap(pk2(x0[0], x0[1]), pk2(x1[0], x1[1]), false, false);
    const auto s1 = __builtin_amdgcn_permlane16_swap(pk2(x0[2], x0[3]), pk2(x1[2], x1[3]), false, false);
    return u32x4{s0[0], s1[0], s0[1], s1[1]};
  };
  const int hr = hp ^ 8;
  const int p3 = ((xcd >> 1) * 4 + (jj & 3)) ^ 8, nb = (int)(((xcd & 1) ? 0xF7EDCA98u : 0xB3654210u) >> (jj & 28)) & 15;
  const int flip = jj & 1;
  const unsigned voO = (unsigned)((l >> 3) * DM + (((l & 7) ^ (((l >> 4) & 1) | ((l >> 5) << 2))) * 8)) * 2;
  const bool has_gu = (hp & 7) != 3;
  int gr = (xcd == 6 && jj >= 12) ? 8 * (24 + (jj & 3)) + 2 * ((jj >> 2) - 3) : b;
  asm volatile("" : "+s"(gr));
  gr = jj >= 28 ? 8 * (4 * (xcd >> 1) + jj - 16) + 6 : gr;
  asm volatile("" : "+s"(gr));
  gr = has_gu ? gr : 224 + (jj - 24) * 4 + (xcd >> 1);
  const int gkq = (gr / 56) & 3;
  u32x4 dv[8]; unsigned ogq; bf16x8 dn[4];
  u32x4 wr_[16];
    const int srow = l >> 3, sch = l & 7;
    unsigned voA[4], voW[6];
#pragma unroll
    for (int i = 0; i < 4; ++i) voA[i] = (unsigned)((panel * 128 + (4 * cw + i) * 8 + srow) * DM + ((sch ^ (4 * (i & 1) + (srow >> 1))) * 8)) * 2;
#pragma unroll
    for (int i = 0; i < 6; ++i) voW[i] = (unsigned)(((2 * cw + (i & 1)) * 8 + srow) * 3072 + (i >> 1) * 1024 + hp * 64 + ((sch ^ ((((srow >> 1) & 1) << 2) | ((i & 1) << 1))) * 8)) * 2;
    const unsigned lwa = lbase + cw * 4096, lww = lbase + ABYTES + cw * 2048;
    const int rot = 4 * ((panel & 7) >> 1) + 2 * (panel >> 3) + 4 * (xcd & 3);
    u64 k0_ = (panel & 1) ? 0xEA62FB7C84D30951ull : 0xFB73EA6D95C21840ull, k1_ = (panel & 8) ? 0xD95FB73EA62C1840ull : 0xFB73EA62D95C1840ull;
    asm volatile("" : "+s"(k0_), "+s"(k1_));
    const u64 kord = has_gu ? k1_ : k0_;
    auto stage = [&](int it, auto bufc) {
      constexpr int B = decltype(bufc)::value * STAGE1;
      const int kt = ((int)(kord >> (4 * it)) + rot) & 15;
      const u16* gx = x + kt * 64; const u16* gq_ = wqkv + (size_t)kt * 64 * 3072;
      static_for<10>([&](auto ic) {
        constexpr int i = decltype(ic)::value;
        if constexpr (i < 4) gl_m0<B + i * 1024>(voA[i], gx, lwa);
        else gl_m0<B + ((i - 4) >> 1) * WSLAB + (i & 1) * 1024>(voW[i - 4], gq_, lww);
      });
    };
  unsigned pa0, pvh, dst, dz, pgw, ogu, oh1, oh1z;
  auto kvsetup = [&]() {
    int l = tid & 63; asm volatile("" : "+v"(l));
    const int g = l >> 4, c15 = l & 15, tq = c15 >> 2, tp = l & 3;
    pa0 = lbase + kh * 16384 + (4 * g + tq) * 128 + (((2 * cw + (tp >> 1)) ^ ((4 * g + tq) & 7)) * 16) + 8 * (tp & 1);
    pvh = lbase + HANDOFF + kh * 12288 + l * 16;
    dst = WS_KVP + (unsigned)((hp * 16 + panel) * 16 + (w & 6) * 2 + (g & 2) + (w & 1)) * 512 + c15 * 32 + (g & 1) * 16;
    dz = WS_ZP + (unsigned)((hp * 16 + (w & 6) * 2 + (g & 2) + (w & 1)) * 16 + panel) * 32 + (g & 1) * 16;
    pgw = lbase + gu_at(0) + kh * 4096 + (8 * g + tq) * 128 + (((2 * cw + (tp >> 1)) ^ ((((tq >> 1) & 1) << 2) | ((g & 1) << 1))) * 16) + 8 * (tp & 1);
    ogu = WS_GUP + (unsigned)gr * 512 + w * 64 + g * 16;
    oh1 = WS_KVP + (unsigned)((hr * 16 + (l >> 3)) * 16 + panel) * 512 + cw * 128 + (l & 7) * 16;
    oh1z = WS_ZP + (unsigned)((hr * 16 + panel) * 16) * 32 + l * 4;
    asm volatile("" : "+v"(pa0), "+v"(pvh), "+v"(dst), "+v"(dz), "+v"(pgw), "+v"(ogu), "+v"(oh1), "+v"(oh1z));
  };
#define IC(n) std::integral_constant<int, (n)>{}
#define CODE_PF(tagn, FROM, d) do { u64 pc_; unsigned len_; int off_; \
    asm volatile("s_getpc_b64 %0\n.Lkf_pf_h" #tagn ":\n\ts_mov_b32 %1, .Lkf_pf_end+1152-" FROM "\n\ts_mov_b32 %2, " FROM "-.Lkf_pf_h" #tagn : "=s"(pc_), "=s"(len_), "=s"(off_)); \
    unsigned o_ = (unsigned)(jj * 4 + cw) * 256u; o_ = o_ + 256u <= len_ ? o_ : len_ - 256u; \
    const u64 pa_ = (pc_ + (long long)off_ + o_) & ~63ull; \
    asm volatile("s_load_dword %0, %1, 0x0\n\ts_load_dword %0, %1, 0x40\n\ts_load_dword %0, %1, 0x80\n\ts_load_dword %0, %1, 0xc0" : "=&s"(d) : "s"(pa_) : "memory"); } while (0)
#define PF_DONE(d) asm volatile("s_waitcnt lgkmcnt(0)" : "+s"(d) :: "memory")
  if (w < 4) {
    unsigned oc2;
    {
      const unsigned r0 = (unsigned)(panel * 128 + hp * 8);
      oc2 = l < 33 ? WS_KVF + (unsigned)(hr * 16 + panel) * 640 + l * 16 : l == 33 ? WS_F1 + (unsigned)id * 16 : l == 34 ? WS_STAT + r0 * 8 : l == 35 ? WS_RI + (unsigned)(panel * 1024 + hp * 32)
          : l < 40 ? WS_F4 + (unsigned)(((panel * 4 + (l - 36)) * 16 + hp) * 2) * 32 : l < 44 ? WS_GUP + (unsigned)gr * 512 + (l - 40) * 128 : l == 44 ? WS_RI + (unsigned)(panel * 1024 + 512 + hp * 16)
          : WS_ZP + (unsigned)((hp * 16 + (l & 15)) * 16 + panel) * 32;
    }
    u32x4 tm, k0, k1; u32x2 tk;
    const unsigned tmofs = lbase + X_CSP + (l < 47 ? l : 46) * 16, kofs = lbase + X_CSP + 768, tgofs = lbase + X_AM + 48;
    unsigned opay;
    { const unsigned r0 = (unsigned)(panel * 128 + hp * 8);
      opay = l < 32 ? WS_CS + (unsigned)(hp * 8 + (panel >> 1)) * 768 + (panel & 1) * 512 + l * 16 : l < 40 ? WS_XA + r0 * 16 + (l - 32) * 16 : l < 44 ? WS_STAT + r0 * 8 + (l - 40) * 16 : l < 46 ? WS_RI + (unsigned)(panel * 1024 + hp * 32) + (l - 44) * 16 : WS_RI + (unsigned)(panel * 1024 + 512 + hp * 16); }
    const unsigned oflag = WS_F1 + (unsigned)id * 16;
    unsigned mz; asm volatile("s_cmp_eq_u32 %1, 0\n\ts_cselect_b32 %0, -1, 0" : "=s"(mz) : "s"(cw) : "scc");
    const u64 mk_all = ((u64)mz << 32) | mz, mk_pay = ((u64)(mz & 0x7FFFu) << 32) | (mz & ((panel & 1) ? 0xFFFFu : 0xFFFFFFFFu)), mk_one = mz & 1u;
    asm volatile("" : "+v"(oc2), "+v"(opay));
#define MST(op, data, off, mask) asm volatile("s_mov_b64 exec, %3\n\t" op " %0, %1, %2, 0 offen sc1\n\ts_mov_b64 exec, -1" :: "v"(data), "v"(off), "s"(rs), "s"(mask) : "memory")
    f32x4 csa[2] = {{0.f, 0.f, 0.f, 0.f}, {0.f, 0.f, 0.f, 0.f}};
    const bf16x8 ones = {0x3F80, 0x3F80, 0x3F80, 0x3F80, 0x3F80, 0x3F80, 0x3F80, 0x3F80};
    s16x4 cl[2][2], ch[2][2];
    const unsigned csw = lbase + X_CSP + (wm * 64 + wn * 32 + r31) * 4;
    f32x4 acc[4][6];
#pragma unroll
    for (int t = 0; t < 4; ++t)
#pragma unroll
      for (int a = 0; a < 6; ++a) acc[t][a] = f32x4{0.f, 0.f, 0.f, 0.f};
    const int g4 = l >> 4, c15 = l & 15;
    unsigned asw[2][2], wofs[2][2], cofs[2];
#pragma unroll
    for (int e = 0; e < 2; ++e) {
      asw[0][e] = lbase + (wm * 64 + c15) * 128 + (((4 * e + g4) ^ ((c15 >> 1) & 7)) * 16);
      wofs[0][e] = lbase + ABYTES + (8 * g4 + tq) * 128 + (((wn * 4 + 2 * e + (tp >> 1)) ^ ((((tq >> 1) & 1) << 2) | ((g4 & 1) << 1))) * 16) + 8 * (tp & 1);
      asw[1][e] = asw[0][e] + 2 * STAGE1; wofs[1][e] = wofs[0][e] + 2 * STAGE1;
      cofs[e] = wofs[0][e] + ((panel & 1) ? 2 : wm) * WSLAB;
    }
    asm volatile("" : "+v"(asw[0][0]), "+v"(asw[0][1]), "+v"(asw[1][0]), "+v"(asw[1][1]), "+v"(wofs[0][0]), "+v"(wofs[0][1]), "+v"(wofs[1][0]), "+v"(wofs[1][1]), "+v"(cofs[0]), "+v"(cofs[1]));
    bf16x8 af[2][4]; s16x4 wl[2][6], wh_[2][6];
    s16x4 ea[4], ew[6];
    const unsigned eaofs = lbase + X_XA + (wm * 64 + c15) * 16;
    const unsigned ewofs = lbase + X_XW + g4 * 1536 + (wn * 32 + c15) * 4;
    const unsigned riofs = lbase + X_ST + (wm * 64 + r31) * 4;
    auto rd = [&](auto ic, auto stc) {
      constexpr int i = decltype(ic)::value, S = decltype(stc)::value, s = S & 1, e = S & 1, buf = (S >> 1) % NST1;
      constexpr int b2 = buf == 2, bo = buf == 1 ? STAGE1 : 0;
      (void)&cl; (void)&ch; (void)&cofs; (void)&af; (void)&wl; (void)&wh_; (void)&asw; (void)&wofs;
      if constexpr (i == 0) DS_B128(af[s][0], asw[b2][e], bo);
      else if constexpr (i == 1) DS_TR(wl[s][0], wofs[b2][0], bo + e * 4096);
      else if constexpr (i == 2) DS_TR(wh_[s][0], wofs[b2][0], bo + e * 4096 + 512);
      else if constexpr (i < 6) DS_B128(af[s][i - 2], asw[b2][e], bo + (i - 2) * 2048);
      else if constexpr (i < 16) {
        constexpr int j = (i - 4) >> 1, off = bo + e * 4096 + (j >> 1) * WSLAB;
        if constexpr (i & 1) DS_TR(wh_[s][j], wofs[b2][j & 1], off + 512); else DS_TR(wl[s][j], wofs[b2][j & 1], off);
      } else {
        constexpr int c = (i - 16) >> 1;
        if constexpr (i & 1) DS_TR(ch[s][c], cofs[c], bo + e * 4096 + 512); else DS_TR(cl[s][c], cofs[c], bo + e * 4096);
      }
    };
    {
      SB0();
      asm volatile("s_waitcnt vmcnt(0) lgkmcnt(0)" : "+s"(pfd), "+v"(xr4[0]), "+v"(xr4[1]), "+v"(xr4[2]), "+v"(xr4[3]), "+v"(wr4[0]), "+v"(wr4[1]) :: "memory");
      SB0();
      float m, den, rinv, r;
      ln_rows(xr4, wr4, h, m, den, rinv, r);
      const int rw = tid >> 5;
      const unsigned rb = __float_as_uint(r) >> 16, ord = rb ^ ((rb & 0x8000u) ? 0xFFFFu : 0x8000u), nm = pk2(-m, -m);
      if (r31 == 0) { *(u32x4*)(smem + X_CSP + 512 + rw * 16) = u32x4{nm, nm, nm, nm};
        *(f32x2*)(smem + X_CSP + 640 + rw * 8) = f32x2{m, den}; *(float*)(smem + X_CSP + 704 + rw * 4) = rinv; *(u16*)(smem + X_CSP + 736 + rw * 2) = (u16)(__float_as_uint(den) >> 16);
        *(unsigned*)(smem + X_CSP + 768 + rw * 4) = (ord << 11) | (unsigned)(2047 - (id * 8 + rw)); }
      SB0();
    }
    __builtin_amdgcn_s_barrier();
    static_for<20>([&](auto ic) { rd(ic, IC(0)); });
    static_for<30>([&](auto stc) {
      constexpr int S = decltype(stc)::value, s = S & 1;
      constexpr bool bnd = (S & 1) == 1;
      static_for<24>([&](auto mc) {
        constexpr int m = decltype(mc)::value, j = m >> 2, t = (j & 1) ? 3 - (m & 3) : m & 3;
        (void)&tm; (void)&k0; (void)&k1; (void)&tk; (void)&ea; (void)&ew; (void)&oc2; (void)&opay; (void)&oflag; (void)&mk_all; (void)&mk_pay; (void)&mk_one; (void)&rs;
        (void)&tmofs; (void)&kofs; (void)&tgofs; (void)&csw; (void)&csa; (void)&eaofs; (void)&ewofs; (void)&riofs;
        if constexpr (m == 0) asm volatile("s_waitcnt lgkmcnt(%0)" :: "n"(bnd ? 0 : CSS(S) ? 14 : 10));
        else if constexpr (!bnd && m == 4) asm volatile("s_waitcnt lgkmcnt(%0)" :: "n"(CSS(S) ? 7 : 3));
        else if constexpr (!bnd && m == 12 && CSS(S)) asm volatile("s_waitcnt lgkmcnt(11)");
        SB0();
        const bf16x8 wf = cat8(wl[s][j], wh_[s][j]);
        if constexpr (j < 4) acc[t][j] = MFMA16(wf, af[s][t], acc[t][j]);
        else acc[t][j] = MFMA16(af[s][t], wf, acc[t][j]);
        SB0();
        if constexpr (m == 1) {
          if constexpr (S == 1) { const u32x4 sv = {SENT, SENT, SENT, SENT}; MST("buffer_store_dwordx4", sv, oc2, mk_all); }
          if constexpr (S == 11) { asm volatile("" : "+v"(tm), "+v"(k0), "+v"(k1), "+v"(tk)); MST("buffer_store_dwordx4", tm, opay, mk_pay); }
          if constexpr (S == F1_S) { WAIT_V(0); MST("buffer_store_dwordx2", tk, oflag, mk_one); }
          if constexpr (S == 3) kvsetup();
          if constexpr (bnd) __builtin_amdgcn_s_barrier();
        }
        if constexpr (m >= 1 && m - 1 < (CSS(S + 1) ? 20 : 16)) rd(IC(m - 1), IC(S + 1));
        if constexpr (S == 8 && m == 18) {
          asm volatile("" : "+v"(csa[0]), "+v"(csa[1]));
          const float c = blk ? csa[1][0] : csa[0][0];
          const unsigned u = pk2(c, c - rbf(c));
          asm volatile("ds_write_b32 %0, %1" :: "v"(csw), "v"(u) : "memory");
        }
        if constexpr (S == 9 && m >= 18 && m < 22) {
          if constexpr (m == 18) DS_B128(tm, tmofs, 0);
          if constexpr (m == 19) DS_B128(k0, kofs, 0);
          if constexpr (m == 20) DS_B128(k1, kofs, 16);
          if constexpr (m == 21) asm volatile("ds_read_b64 %0, %1" : "=v"(tk) : "v"(tgofs));
        }
        if constexpr (S == 11 && m == 18) { k0 = __builtin_elementwise_max(k0, k1); asm volatile("" : "+v"(k0)); }
        if constexpr (S == 11 && m == 20) { tk[0] = (tk[0] & ~0x7FFFFFFu) | max(max(k0[0], k0[1]), max(k0[2], k0[3])); asm volatile("" : "+v"(tk)); }
#define DS_B64(dst, addr, off) asm volatile("ds_read_b64 %0, %1 offset:%2" : "=v"(dst) : "v"(addr), "n"(off))
#define DS_B32(dst, addr, off) asm volatile("ds_read_b32 %0, %1 offset:%2" : "=v"(dst) : "v"(addr), "n"(off))
#define DS_2(dst, addr, o0, o1) asm volatile("ds_read2_b32 %0, %1 offset0:%2 offset1:%3" : "=v"(dst) : "v"(addr), "n"(o0), "n"(o1))
        SB0();
      });
      if constexpr (CSS(S)) {
        SB0();
        csa[0] = MFMA16(ones, cat8(cl[s][0], ch[s][0]), csa[0]); csa[1] = MFMA16(ones, cat8(cl[s][1], ch[s][1]), csa[1]);
        SB0();
      }
    });
    static_for<16>([&](auto ic) {
      constexpr int i = decltype(ic)::value, j = 2 + (i >> 2), t = (j & 1) ? 3 - (i & 3) : i & 3;
      (void)&ea; (void)&ew; (void)&eaofs; (void)&ewofs;
      if constexpr (i == 0) asm volatile("s_waitcnt lgkmcnt(6)");
      else if constexpr (i == 4) asm volatile("s_waitcnt lgkmcnt(8)");
      SB0();
      const bf16x8 wf = cat8(wl[0][j], wh_[0][j]);
      if constexpr (j < 4) acc[t][j] = MFMA16(wf, af[0][t], acc[t][j]); else acc[t][j] = MFMA16(af[0][t], wf, acc[t][j]);
      SB0();
      if constexpr (i < 8) { rd(IC(2 * i), IC(31)); rd(IC(2 * i + 1), IC(31)); }
      else if constexpr (i < 12) DS_B64(ea[i - 8], eaofs, (i - 8) * 256);
      else if constexpr (i < 14) { const unsigned a_ = ewofs + 256; DS_2(ew[i - 10], a_, (i - 12) * 16, (i - 12) * 16 + 192); }
      SB0();
    });
    {
      __builtin_amdgcn_s_setprio(PRIO_T);
      asm volatile("s_waitcnt lgkmcnt(0)" : "+v"(af[1][0]), "+v"(af[1][1]), "+v"(af[1][2]), "+v"(af[1][3]), "+v"(wl[1][0]), "+v"(wh_[1][0]), "+v"(wl[1][1]), "+v"(wh_[1][1]),
                   "+v"(wl[1][2]), "+v"(wh_[1][2]), "+v"(wl[1][3]), "+v"(wh_[1][3]), "+v"(wl[1][4]), "+v"(wh_[1][4]), "+v"(wl[1][5]), "+v"(wh_[1][5]));
      asm volatile("" : "+v"(ea[0]), "+v"(ea[1]), "+v"(ea[2]), "+v"(ea[3]), "+v"(ew[2]), "+v"(ew[3]));
      SB0();
      auto mm = [&](int t, int j) { const bf16x8 wf = cat8(wl[1][j], wh_[1][j]); if (j < 4) acc[t][j] = MFMA16(wf, af[1][t], acc[t][j]); else acc[t][j] = MFMA16(af[1][t], wf, acc[t][j]); };
      auto ff = [&](int t, int j) { if (j < 4) acc[t][j] = MFMA16H(ew[j], ea[t], acc[t][j]); else acc[t][j] = MFMA16H(ea[t], ew[j], acc[t][j]); };
      auto dm = [&](int n) {
        SB0();
        const int t = n & 3, j = (n < 16 ? 4 : 0) + ((n >> 2) & 1);
        if (n & 8) ff(t, j); else mm(t, j);
        SB0();
      };
      const unsigned ew0 = ewofs, ew2 = ewofs + 512;
      mm(2, 2); SB0(); DS_B32(rt, riofs, 0); SB0();
      mm(3, 2); SB0();
      mm(2, 3); SB0(); DS_2(ew[4], ew2, 0, 192); DS_2(ew[5], ew2, 16, 208); SB0();
      mm(3, 3); SB0(); DS_2(ew[0], ew0, 0, 192); DS_2(ew[1], ew0, 16, 208); SB0();
      ff(2, 2); ff(3, 2); SB0(); wpload(); SB0();
      ff(2, 3); ff(3, 3); SB0(); SB0();
      mm(0, 2); mm(1, 2);
      SB0();
      SB0();
      char* ho = smem + HANDOFF + cw * 6144 + l * 16;
      *(u32x4*)(ho + 2048) = pack_u(acc[2][2], acc[3][2]);
      SB0(); mm(0, 3); mm(1, 3); SB0();
      *(u32x4*)(ho + 3072) = pack_u(acc[2][3], acc[3][3]);
      SB0(); ff(0, 2); ff(1, 2); ff(0, 3); ff(1, 3); SB0();
      RAW_BARRIER();
      asm volatile("" : "+v"(rt), "+v"(ew[0]), "+v"(ew[1]), "+v"(ew[4]), "+v"(ew[5]));
      dm(0); dm(1);
      fr[2] = __builtin_bit_cast(bf16x8, pack_u(acc[0][2], acc[1][2]));
      dm(2); dm(3);
      fr[3] = __builtin_bit_cast(bf16x8, pack_u(acc[0][3], acc[1][3]));
      dm(4);
      f32x2 ep[8], en[8];
      phi_fn(wp, fr[2], fr[3], ep, en, rt, rt, [&](auto kc) { constexpr int k = decltype(kc)::value; if constexpr (k == 0) { dm(5); dm(6); dm(7); } else if constexpr (7 + k < 16) dm(7 + k); });
    { const unsigned a_ = lbase + X_ST + 512 + (l >> 4) * 8;
      static_for<4>([&](auto tc) { constexpr int tb = decltype(tc)::value; (void)&dn; (void)&a_;
        asm volatile("ds_read2_b64 %0, %1 offset0:%2 offset1:%3" : "=v"(dn[tb]) : "v"(a_), "n"(tb * 8), "n"(tb * 8 + 4)); }); }
#pragma unroll
      for (int g = 0; g < 4; ++g) imgw(g, ep, en);
#pragma unroll
      for (int u = 0; u < 4; ++u) {
        const int t2 = u >> 1, db = u & 1;
        const f32x4 x0 = acc[2 * t2][4 + db], x1 = acc[2 * t2 + 1][4 + db];
        char* pv = t2 ? ho + 4096 : ho;
        *(u32x4*)(pv + db * 1024) = u32x4{pk2(x0[0], x0[1]), pk2(x0[2], x0[3]), pk2(x1[0], x1[1]), pk2(x1[2], x1[3])};
      }
#pragma unroll
      for (int t = 0; t < 4; ++t) { qa[t][0] = acc[t][0]; qa[t][1] = acc[t][1]; wr_[t] = __builtin_bit_cast(u32x4, af[1][t]); wr_[9 + t] = __builtin_bit_cast(u32x4, af[0][t]); }
      wr_[13] = __builtin_bit_cast(u32x4, cat8(wl[0][0], wh_[0][0])); wr_[14] = __builtin_bit_cast(u32x4, cat8(wl[0][1], wh_[0][1]));
      wr_[4] = __builtin_bit_cast(u32x4, cat8(wl[1][0], wh_[1][0])); wr_[5] = __builtin_bit_cast(u32x4, cat8(wl[1][1], wh_[1][1]));
      wr_[6] = __builtin_bit_cast(u32x4, cat8(ea[0], ea[1])); wr_[7] = __builtin_bit_cast(u32x4, cat8(ea[2], ea[3])); wr_[8] = __builtin_bit_cast(u32x4, cat8(ew[0], ew[1]));
    }
  } else {
    const int t = tid - 256;
    __builtin_amdgcn_s_setprio(PRIO_H);
    u64 epoch;
    asm volatile("s_load_dwordx2 %0, %1, 0x0" : "=&s"(epoch) : "s"(ws) : "memory");
    stage(0, IC(0));
    asm volatile("s_waitcnt lgkmcnt(0)" : "+s"(pfd), "+s"(epoch) :: "memory");
    const u64 enx = epoch + (((unsigned)epoch & 0xFEu) == 0xFEu ? 3u - ((unsigned)epoch & 1u) : 1u);
    const u64 tag = TAG_OF(enx);
    if (t == 0) { *(u32x2*)(smem + X_AM) = u32x2{0u, 0u};
      const unsigned e2 = ((unsigned)enx & 0x55u) | (((unsigned)enx & 0xAAu) << 15);
      *(u32x4*)(smem + X_AM + 32) = u32x4{e2, e2 >> 2, e2 >> 4, e2 >> 6} & 0x00010001u;
      *(u32x4*)(smem + X_AM + 48) = u32x4{(unsigned)tag, (unsigned)(tag >> 32), (unsigned)enx, (unsigned)(enx >> 32)}; }
    u32x2 fl, si; unsigned pfd2, tpace = 0, xs;
    unsigned ofl = WS_F1 + (unsigned)t * 16, orA = WS_CS + (unsigned)(hp * 6144 + cw * 1024 + l * 16);
    unsigned orB = (cw < 2 ? WS_CS + (unsigned)(hp * 6144 + 4096 + cw * 1024) : WS_XA + (unsigned)(panel * 2048 + (cw - 2) * 1024)) + l * 16;
    unsigned orC = ((cw & 1) ? WS_STAT + (unsigned)p3 * 1024 : WS_RI + (unsigned)panel * 1024) + l * 16;
    unsigned oxs = (unsigned)(gkq * 256 + cw * 64 + r31 * 2) * 2, akey = lbase + X_AM + 4, vol = (unsigned)l * 16;
    unsigned voG = (unsigned)((8 * cw + srow) * FF + ((sch ^ ((((srow >> 1) & 1) << 2) | ((cw & 1) << 1))) * 8)) * 2;
    const u16* gg_ = w_gate + (size_t)gkq * 256 * FF + ((gr + 1) % 56) * 64;
    const u16* gu_ = w_up + (size_t)gkq * 256 * FF + ((gr + 3) % 56) * 64;
    const u16* gwo = wo + (size_t)(((xcd >> 1) * 4 + (jj >> 3)) * 64 + 2 * cw * 8) * DM + ((xcd & 1) * 8 + (jj & 7)) * 64;
    const u16* gwp = wphi + hp * 2048 + cw * 512;
    const u16* gdw = w_down + (size_t)(((gr >> 3) & 31) * 128 + 32 * cw) * DM + ((gr + 1) & 7) * 128;
    unsigned odw = (unsigned)((l >> 4) * DM + (l & 15) * 8) * 2;
    const u16* gw2a = wo + (size_t)(((cw * 10) ^ (flip * 64)) * 8) * DM + nb * 64;
    asm volatile("" : "+s"(gw2a));
    asm volatile("" : "+s"(gg_), "+s"(gu_), "+s"(gwo), "+s"(gwp), "+s"(gdw));
    { const int f = (gr >> 3) * 128 + 32 * cw + 4 * (l & 7), fs = (f >> 6) - ((l & 8) ? 3 : 1);
      ogq = WS_GUP + (unsigned)((l >> 4) * 56 + (fs < 0 ? fs + 56 : fs)) * 512 + (f & 63) * 4 + (l & 8) * 32; }
    asm volatile("" : "+v"(voG), "+v"(odw), "+v"(ogq));
    asm volatile("s_waitcnt lgkmcnt(0)" : "+v"(ofl), "+v"(orA), "+v"(orB), "+v"(orC), "+v"(oxs), "+v"(akey), "+v"(vol) :: "memory");
    stage(1, IC(1));
    WAIT_V(10);
    __builtin_amdgcn_s_barrier();
    auto wopf = [&]() {
      GLDS_S(voO, gwo, lbase + X_WO + 2 * cw * 1024); GLDS_S(voO, gwo + 8 * DM, lbase + X_WO + (2 * cw + 1) * 1024);
      GLDS_S(vol, gwp, lbase + X_WP + cw * 1024);
    };
    auto dvl = [&](auto kc) {
      constexpr int k = decltype(kc)::value;
      static_for<DVN[k + 2]>([&](auto ic) {
        constexpr int i = dv_first(k) + decltype(ic)::value;
        u32x4& d_ = dv[i]; const unsigned o_ = odw; const u16* p_ = gdw + (size_t)(4 * i) * DM;
        asm volatile("global_load_dwordx4 %0, %1, %2 nt" : "=v"(d_) : "v"(o_), "s"(p_) : "memory");
      });
    };
    stage(2, IC(2));
    const unsigned lgu = lbase + cw * 1024;
    auto gul = [&](auto ksc) {
      constexpr int ks = decltype(ksc)::value;
      gl_nt<gu_at(ks)>(voG, gg_ + (size_t)ks * 32 * FF, lgu); gl_nt<gu_at(ks) + 4096>(voG, gu_ + (size_t)ks * 32 * FF, lgu);
    };
#define BLD64(dst, off) asm volatile("buffer_load_dwordx2 %0, %1, %2, 0 offen sc1" : "=v"(dst) : "v"(off), "s"(rs) : "memory")
    static_for<15>([&](auto ktc) {
      constexpr int kt = decltype(ktc)::value;
      constexpr auto exw = [](int k) { return DVN[k + 2]; };
      constexpr auto exo = [](int k) { return k == KP ? 1 : k == KC + 1 ? 3 : k == KC + 2 ? 2 : 0; };
      constexpr int exa = ((kt - 2 == WOK || kt - 1 == WOK) ? 3 : 0) + (kt - 2 > KC ? exo(kt - 2) : 0);
      if constexpr (exw(kt - 2) + exw(kt - 1) != 0) { if (has_gu) WAIT_V(10 + exa + exw(kt - 2) + exw(kt - 1) + exo(kt - 1)); else WAIT_V(10 + exa + exo(kt - 1)); }
      else if constexpr (kt < 14) WAIT_V(10 + exa + exo(kt - 1));
      else { if (has_gu) WAIT_V(10); else WAIT_V(0); }
      if constexpr (PACE_T[kt] != 0) { if (cw == 0) { while ((unsigned)__builtin_amdgcn_s_memrealtime() - tpace < (unsigned)(has_gu ? PACE_T[kt] : PACE_3[kt])) {} } }
      __builtin_amdgcn_s_barrier();
      if constexpr (PACE_T[kt + 1] != 0) { if (cw == 0) tpace = (unsigned)__builtin_amdgcn_s_memrealtime(); SB0(); }
#define UMAX_DPP(C) { const unsigned o = (unsigned)__builtin_amdgcn_update_dpp(0, (int)key, C, 0xf, 0xf, true); key = o > key ? o : key; asm volatile("" : "+v"(key)); }
      if constexpr (kt + NST1 < 16) { SB0(); stage(kt + NST1, IC(kt % NST1)); SB0(); }
      if constexpr (kt == WOK) wopf();
      if constexpr (kt == PF2_KT) CODE_PF(2, ".Lkf_pf_mid", pfd2);
      if constexpr (kt == PF2_KT + 2) PF_DONE(pfd2);
      if constexpr (kt == KP) BLD64(fl, ofl);
      if constexpr (kt == KC) {
        static_assert(DVN[KP + 2] == 0, "count");
        if constexpr (KC == KP + 1) asm volatile("s_waitcnt vmcnt(10)" : "+v"(fl) :: "memory"); else asm volatile("" : "+v"(fl) :: "memory");
#define FL_OK() __all((((fl[0] ^ (unsigned)tag) >> 27) | (fl[1] ^ (unsigned)(tag >> 32))) == 0u)
        if (__builtin_expect(!FL_OK(), 0)) {
          for (unsigned sp = 0;;) {
            asm volatile("buffer_load_dwordx2 %0, %1, %2, 0 offen sc1\n\ts_waitcnt vmcnt(0)" : "=v"(fl) : "v"(ofl), "s"(rs) : "memory");
            if (FL_OK()) break;
            SPIN_NEXT(sp);
          }
        }
        unsigned key = fl[0];
        UMAX_DPP(0x121) UMAX_DPP(0x122) UMAX_DPP(0x124) UMAX_DPP(0x128)
        asm volatile("s_mov_b64 exec, %2\n\tds_max_u32 %0, %1\n\ts_mov_b64 exec, -1\n\ts_waitcnt lgkmcnt(0)" :: "v"(akey), "v"(key), "s"(0x0001000100010001ull) : "memory");
      }
      if constexpr (kt == KC + 1) { GLDS_SC1(orA, ws, lbase + X_XW + cw * 1024); GLDS_SC1(orB, ws, lbase + X_XW + 4096 + cw * 1024); GLDS_SC1(orC, ws, lbase + X_ST + (cw & 1) * 1024); }
      if constexpr (kt == KC + 2) {
        const int bi = 2047 - (int)(__builtin_amdgcn_readfirstlane(*(const unsigned*)(smem + X_AM + 4)) & 2047u);
        asm volatile("global_load_dword %0, %1, %2" : "=v"(xs) : "v"(oxs), "s"(x + (size_t)bi * DM) : "memory");
        asm volatile("buffer_load_dwordx2 %0, off, %1, %2 sc1" : "=v"(si) : "s"(rs), "s"(WS_STAT + (unsigned)bi * 8) : "memory");
      }
      if constexpr (DVN[kt + 2] != 0) { if (has_gu) dvl(ktc); }
      if constexpr (kt == 0) kvsetup();
      if constexpr (kt == 13) { if (has_gu) { gul(IC(0)); gul(IC(1)); gul(IC(2)); gul(IC(3)); gul(IC(4)); } }
      if constexpr (kt == 14) asm volatile("" : "+v"(xs), "+v"(si) :: "memory");
      if constexpr (kt == 14) { if (has_gu) { static_for<1>([&](auto ic) { gul(IC(5 + decltype(ic)::value)); }); } }
    });
    auto wld = [&](auto ic) {
      constexpr int i = decltype(ic)::value;
      u32x4& d_ = wr_[i]; const unsigned o_ = voO; const u16* p_ = gw2a + (size_t)(i * 8) * DM;
      asm volatile("global_load_dwordx4 %0, %1, %2" : "=v"(d_) : "v"(o_), "s"(p_) : "memory");
    };
    wpload();
    rt = own(*(const float*)(smem + X_ST + (wt * 32 + r31) * 4));
    {
      const float m = __uint_as_float(si[0]), den = own(__uint_as_float(si[1])), rinv = rcp_nr(den);
      *(unsigned*)(smem + X_XS + cw * 128 + r31 * 4) = ln_pair(xs, m, den, rinv);
    }
    RAW_BARRIER();
    const char* ho = smem + HANDOFF + cw * 6144 + l * 16;
    fr[2] = *(const bf16x8*)(ho + 2048); fr[3] = *(const bf16x8*)(ho + 3072);
    f32x2 ep[8], en[8];
    phi_fn(wp, fr[2], fr[3], ep, en, rt, rt, [&](auto kc) {
      constexpr int k = decltype(kc)::value;
      if constexpr (k == 0) { SB0(); if (has_gu) gul(IC(6)); SB0(); }
      if constexpr (k == 5) { SB0(); if (has_gu) gul(IC(7)); SB0(); }
      static_for<NREG - WPOST>([&](auto ic) {
        constexpr int i = decltype(ic)::value;
        if constexpr (k == WH0 + (i * (17 - WH0)) / (NREG - WPOST)) { SB0(); wld(ic); SB0(); }
      });
    });
    { const unsigned a_ = lbase + X_ST + 512 + (l >> 4) * 8;
      static_for<4>([&](auto tc) { constexpr int tb = decltype(tc)::value; (void)&dn; (void)&a_;
        asm volatile("ds_read2_b64 %0, %1 offset0:%2 offset1:%3" : "=v"(dn[tb]) : "v"(a_), "n"(tb * 8), "n"(tb * 8 + 4)); }); }
#pragma unroll
    for (int g = 0; g < 4; ++g) imgw(g, ep, en);
    SB0();
    static_for<WPOST>([&](auto ic) { wld(IC(NREG - WPOST + decltype(ic)::value)); });
  }
  asm volatile(".Lkf_pf_mid:");
  f32x16 zero;
#pragma unroll
  for (int i = 0; i < 16; ++i) zero[i] = 0.f;
  u32x2 tg; unsigned t8, rkey; u32x4 t2;
  {
    __builtin_amdgcn_s_setprio(2);
    RAW_BARRIER();
    asm volatile("" : "+v"(dn[0]), "+v"(dn[1]), "+v"(dn[2]), "+v"(dn[3]));
    u32x4 am; unsigned rk;
    { const unsigned a_ = lbase + X_AM; DS_B128(am, a_, 48); DS_B128(t2, a_, 32); asm volatile("ds_read_b32 %0, %1 offset:4" : "=v"(rk) : "v"(a_)); }
    s16x4 al[4], ah[4]; bf16x8 v0[4], v1[4];
    DS_TR(al[0], pa0, 0); DS_TR(ah[0], pa0, 2048); DS_B128(v0[0], pvh, 0); DS_B128(v1[0], pvh, 1024);
    DS_TR(al[1], pa0, 4096); DS_TR(ah[1], pa0, 4096 + 2048); DS_B128(v0[1], pvh, 4096); DS_B128(v1[1], pvh, 4096 + 1024);
    DS_TR(al[2], pa0, 8192); DS_TR(ah[2], pa0, 8192 + 2048); DS_B128(v0[2], pvh, 6144); DS_B128(v1[2], pvh, 6144 + 1024);
    DS_TR(al[3], pa0, 12288); DS_TR(ah[3], pa0, 12288 + 2048); DS_B128(v0[3], pvh, 6144 + 4096); DS_B128(v1[3], pvh, 6144 + 4096 + 1024);
    f32x4 k0 = {0.f, 0.f, 0.f, 0.f}, k1 = k0, zz = k0;
    static_for<4>([&](auto tbc) {
      constexpr int tb = decltype(tbc)::value;
      asm volatile("s_waitcnt lgkmcnt(%4)" : "+v"(al[tb]), "+v"(ah[tb]), "+v"(v0[tb]), "+v"(v1[tb]) : "n"(12 - 4 * tb));
      const bf16x8 af = cat8(al[tb], ah[tb]);
      k0 = MFMA16(af, v0[tb], k0); k1 = MFMA16(af, v1[tb], k1); zz = MFMA16(af, dn[tb], zz);
      SB0();
    });
    asm volatile("" : "+v"(am), "+v"(rk), "+v"(t2));
    auto tagv = [&](f32x4 v) { const u32x4 u = __builtin_bit_cast(u32x4, v); return (u & 0xFFFFFF00u) | (am[2] & 0xFFu); };
    ST128((u32x4{pk2(k0[0], k0[1]), pk2(k0[2], k0[3]), pk2(k1[0], k1[1]), pk2(k1[2], k1[3])} & 0xFFFEFFFEu) | t2, dst);
    const u32x4 zt = tagv(zz);
    asm volatile("s_mov_b64 exec, %3\n\tbuffer_store_dwordx4 %0, %1, %2, 0 offen sc1\n\ts_mov_b64 exec, -1" :: "v"(zt), "v"(dz), "s"(rs), "s"(0x0001000100010001ull) : "memory");
    SB0();
    tg = u32x2{am[0], am[1]}; t8 = __builtin_amdgcn_readfirstlane(am[2]) & 0xFFu; rkey = __builtin_amdgcn_readfirstlane(rk);
  }
  const int idx = 2047 - (int)(rkey & 2047u);
  const int fb = kh ^ ((idx >> 4) & 1);
  const unsigned lxc = lbase + (cw < 2 ? 131072 : X_YS + 64 - 4096) + cw * 2048, lxv = w < 4 ? lxc : lbase + X_XW + cw * 2048;
  {
    bf16x8 xf[8];
    const unsigned px = lbase + X_XS + (l >> 4) * 16, hq = lbase + X_XW + cw * 2048 + l * 16;
    static_for<8>([&](auto kc) { constexpr int ks = decltype(kc)::value; (void)&xf; (void)&px; DS_B128(xf[ks], px, ks * 64); });
    if (w >= 4) {
      __builtin_amdgcn_s_setprio(0);
      WAIT_V(NREG + 2);
    } else {
      auto lo4 = [](u32x4 v) { return __builtin_bit_cast(s16x4, u32x2{v[0], v[1]}); };
      auto hi4 = [](u32x4 v) { return __builtin_bit_cast(s16x4, u32x2{v[2], v[3]}); };
#pragma unroll
      for (int tt = 0; tt < 4; ++tt) {
        const int t = tt ^ 2;
#pragma unroll
        for (int j = 0; j < 2; ++j) qa[t][j] = MFMA16(__builtin_bit_cast(bf16x8, wr_[13 + j]), __builtin_bit_cast(bf16x8, wr_[9 + t]), qa[t][j]);
#pragma unroll
        for (int j = 0; j < 2; ++j) qa[t][j] = MFMA16(__builtin_bit_cast(bf16x8, wr_[4 + j]), __builtin_bit_cast(bf16x8, wr_[t]), qa[t][j]);
        if (tt & 1) {
#pragma unroll
          for (int u = t - 1; u <= t; ++u)
#pragma unroll
            for (int j = 0; j < 2; ++j) qa[u][j] = MFMA16H(j ? hi4(wr_[8]) : lo4(wr_[8]), (u & 1) ? hi4(wr_[6 + (u >> 1)]) : lo4(wr_[6 + (u >> 1)]), qa[u][j]);
        }
      }
      const u32x4 q0 = pack_u(qa[2][0], qa[3][0]), q1 = pack_u(qa[2][1], qa[3][1]);
      asm volatile("ds_write_b128 %0, %1\n\tds_write_b128 %0, %2 offset:1024" :: "v"(hq), "v"(q0), "v"(q1) : "memory");
    }
    RAW_BARRIER();
    asm volatile("" : "+v"(xf[0]), "+v"(xf[1]), "+v"(xf[2]), "+v"(xf[3]), "+v"(xf[4]), "+v"(xf[5]), "+v"(xf[6]), "+v"(xf[7]));
    if (w >= 4) {
      { const u16* ga = wo + (size_t)(((cw * 10) ^ (flip * 64)) * 8) * DM + nb * 64; const u16* gb = wo + (size_t)(((104 + cw * 6) ^ (flip * 64)) * 8) * DM + nb * 64;
        static_for<16 - NREG>([&](auto ic) { constexpr int i = NREG + decltype(ic)::value;
          if constexpr (i < 10) gl_m0<i * 1024>(voO, ga + (size_t)(i * 8) * DM, lbase + cw * 10240); else gl_m0<(104 + i - 10) * 1024>(voO, gb + (size_t)((i - 10) * 8) * DM, lbase + cw * 6144); }); }
    }
    if (has_gu) {
      s16x4 gl[8], gh[8];
      static_for<8>([&](auto kc) { constexpr int ks = decltype(kc)::value; (void)&gl; (void)&gh; (void)&pgw;
        DS_TR(gl[ks], pgw, ks * 8192); DS_TR(gh[ks], pgw, ks * 8192 + 512); });
      f32x4 c0 = {0.f, 0.f, 0.f, 0.f};
      static_for<8>([&](auto kc) { constexpr int ks = decltype(kc)::value;
        asm volatile("s_waitcnt lgkmcnt(%2)" : "+v"(gl[ks]), "+v"(gh[ks]) : "n"(14 - 2 * ks));
        c0 = MFMA16(cat8(gl[ks], gh[ks]), xf[ks], c0);
        SB0(); });
      const u32x4 gt_ = f2u_tag(c0, t8);
      asm volatile("s_mov_b64 exec, %3\n\tbuffer_store_dwordx4 %0, %1, %2, 0 offen sc1\n\ts_mov_b64 exec, -1" :: "v"(gt_), "v"(ogu), "s"(rs), "s"(0x0001000100010001ull) : "memory");
    }
  }
  const int erow = cw * 32 + fb * 16 + (l & 15), ech = l >> 4;
  u32x4 xv[2];
  const bool sw = ech & 1;
  const bool has_idx = w < 4 && (idx >> 5) == p3 * 4 + w;
  unsigned xrt = 0;
  unsigned of4, op4, oo0, oo1, kba, kbz, odo, o2, oz2, wa_, wz_;
  auto later = [&]() {
    SB0();
    of4 = WS_F4 + (unsigned)(((panel * 4 + wt) * 16 + hp) * 2 + wn) * 32; op4 = WS_F4 + (unsigned)(((p3 * 4 + cw) * 16 + 8 * (kh ^ flip)) * 2 + (l & 15)) * 32;
    oo0 = WS_O + 512 + (unsigned)((((p3 * 4 + cw) * 2 + fb) * 32 + (kh ^ flip) * 16) * 128 + h * 64 + r31) * 16; oo1 = oo0 ^ 65536u;
    kba = lbase + X_KB + wn * 4224 + l * 16; kbz = lbase + X_KB + wn * 4224 + 4096 + h * 64;
    odo = WS_O + 512 + (unsigned)((((panel * 4 + wt) * 2 + blk) * 32 + hd) * 128 + h * 16 + (l & 15)) * 16;
    asm volatile("" : "+v"(kba), "+v"(kbz), "+v"(odo), "+v"(of4), "+v"(op4), "+v"(oo0), "+v"(oo1));
    SB0();
  };
  if (w < 4) {
    unsigned pfd3, od; u64 mkp;
    {
      u32x4 pa[2]; unsigned z0 = t8, z1 = t8;
#define H1LD(p) asm volatile("buffer_load_dwordx4 %0, %4, %6, 0 offen sc1\n\tbuffer_load_dwordx4 %1, %4, %6, %7 offen sc1\n\t" \
                   "s_cmp_lg_u32 %8, 3\n\ts_cbranch_scc1 1f\n\tbuffer_load_dword %2, %5, %6, 0 offen sc1\n\tbuffer_load_dword %3, %5, %6, 0 offen offset:256 sc1\n1:" \
                   : "=&v"(p[0]), "=&v"(p[1]), "+v"(z0), "+v"(z1) : "v"(oh1), "v"(oh1z), "s"(rs), "s"(65536), "s"(cw) : "memory", "scc")
      __builtin_amdgcn_s_sleep(H1_DELAY);
      H1LD(pa);
      SB0();
      CODE_PF(3, ".Lkf_pf_mid", pfd3);
      od = WS_KVF + (unsigned)(hr * 16 + panel) * 640 + ((l & 8) ? 512 + (l & 7) * 2 : cw * 128 + h * 64 + (l & 7) * 8 + blk * 4);
      mkp = cw == 3 ? 0x00FF00FF00FF55FFull : 0x00FF00FF00FF00FFull;
      asm volatile("" : "+s"(mkp));
      fr[0] = __builtin_bit_cast(bf16x8, pack_u(qa[0][0], qa[1][0])); fr[1] = __builtin_bit_cast(bf16x8, pack_u(qa[0][1], qa[1][1]));
      asm volatile("" : "+v"(od), "+v"(fr[0]), "+v"(fr[1]));
      asm volatile("global_load_ushort %0, %1, %2" : "=v"(xrt) : "v"((unsigned)(idx * DM + nb * 64 + 4 * (l & 15) + ech) * 2), "s"(x) : "memory");
      later();
      o2 = WS_KVF + (unsigned)(hp * 16 + wn * 8 + wm * 4 + h) * 640 + ((r31 & 12) * 2 + (r31 >> 4) * 4 + (r31 & 3)) * 16;
      oz2 = WS_KVF + (unsigned)(hp * 16 + wn * 8 + wm * 4 + (l & 3)) * 640 + 512;
      wa_ = kba + wm * 2048; wz_ = lbase + X_KB + wn * 4224 + 4096 + (l & 1) * 64 + (wm * 2 + ((l >> 1) & 1)) * 16;
      asm volatile("" : "+v"(o2), "+v"(oz2), "+v"(wa_), "+v"(wz_));
      unsigned hb = 0x8000u, s_lo = 0x05040100u, s_hi = 0x07060100u;
      asm volatile("" : "+s"(s_lo), "+s"(s_hi));
      asm volatile("s_waitcnt vmcnt(1)" : "+v"(pa[0]), "+v"(pa[1]), "+v"(z0), "+v"(z1), "+v"(hb) :: "memory");
      u64 b0, b1, b2; u32x4 v0, v1;
      auto miss = [&]() __attribute__((always_inline)) {
        v0 = pa[0] ^ t2; v1 = pa[1] ^ t2;
        b0 = __ballot((or4(v0) & 0x00010001u) != 0u); b1 = __ballot((or4(v1) & 0x00010001u) != 0u);
        b2 = __ballot((((z0 ^ t8) | (z1 ^ t8)) & 0xFFu) != 0u);
        return (b0 | b1 | b2) != 0;
      };
      if (miss()) {
        unsigned sp = 0;
        do {
        b0 |= (b0 >> 4) & 0x0F0F0F0F0F0F0F0Full; b0 |= (b0 << 4) & 0xF0F0F0F0F0F0F0F0ull; b1 |= (b1 >> 4) & 0x0F0F0F0F0F0F0F0Full; b1 |= (b1 << 4) & 0xF0F0F0F0F0F0F0F0ull;
        asm volatile("s_wqm_b64 exec, %8\n\ts_cbranch_execz 1f\n\tbuffer_load_dwordx4 %0, %4, %6, 0 offen sc1\n1:\n\ts_wqm_b64 exec, %9\n\ts_cbranch_execz 2f\n\tbuffer_load_dwordx4 %1, %4, %6, %7 offen sc1\n2:\n\t"
                     "s_mov_b64 exec, -1\n\ts_cmp_eq_u64 %10, 0\n\ts_cbranch_scc1 3f\n\tbuffer_load_dword %2, %5, %6, 0 offen sc1\n\tbuffer_load_dword %3, %5, %6, 0 offen offset:256 sc1\n3:\n\ts_waitcnt vmcnt(0)"
                     : "+v"(pa[0]), "+v"(pa[1]), "+v"(z0), "+v"(z1) : "v"(oh1), "v"(oh1z), "s"(rs), "s"(65536), "s"(b0), "s"(b1), "s"(b2) : "memory", "scc");
        } while (miss() && ++sp < SPIN_CAP);
      }
    auto sw32 = [](float a, float c) { const auto r = __builtin_amdgcn_permlane32_swap(__float_as_uint(a), __float_as_uint(c), false, false); return __uint_as_float(r[0]) + __uint_as_float(r[1]); };
    auto sw16 = [](float a, float c) { const auto r = __builtin_amdgcn_permlane16_swap(__float_as_uint(a), __float_as_uint(c), false, false); return __uint_as_float(r[0]) + __uint_as_float(r[1]); };
    float sl[4], sh[4];
#pragma unroll
    for (int m = 0; m < 4; ++m) {
      sl[m] = own(__uint_as_float(__builtin_amdgcn_perm(v0[m], hb, s_lo)) + __uint_as_float(__builtin_amdgcn_perm(v1[m], hb, s_lo)));
      sh[m] = own(__uint_as_float(__builtin_amdgcn_perm(v0[m], hb, s_hi)) + __uint_as_float(__builtin_amdgcn_perm(v1[m], hb, s_hi))); }
    const float e0 = dpp_add<0x128, 0xf>(sw16(sw32(sl[0], sl[2]), sw32(sl[1], sl[3]))), e1 = dpp_add<0x128, 0xf>(sw16(sw32(sh[0], sh[2]), sw32(sh[1], sh[3])));
    const float zs = x32_add(x16_add(dpp_add<0x128, 0xf>(__uint_as_float(z0 ^ t8) + __uint_as_float(z1 ^ t8))));
    const float zn = __builtin_bit_cast(float, __builtin_amdgcn_update_dpp(0, __builtin_bit_cast(int, zs), 0xB1, 0xf, 0xf, true));
    const unsigned kvb = min((l & 8) ? pk2(zs, zn) : pk2(e0, e1), SENT - 1u);
    asm volatile("s_mov_b64 exec, %3\n\tbuffer_store_dword %0, %1, %2, 0 offen sc1\n\ts_mov_b64 exec, -1" :: "v"(kvb), "v"(od), "s"(rs), "s"(mkp) : "memory");
    PF_DONE(pfd3);
    }
  } else {
    const char* hq = smem + X_XW + cw * 2048 + l * 16;
    fr[0] = *(const bf16x8*)hq; fr[1] = *(const bf16x8*)(hq + 1024);
  }
  bf16x8 pf[4];
  {
    f32x2 ep[8], en[8];
    phi_fn(wp, fr[0], fr[1], ep, en, rt, 1.0f, [](auto) {});
#pragma unroll
    for (int s = 0; s < 2; ++s) {
      pf[s] = __builtin_bit_cast(bf16x8, u32x4{pk2(ep[4 * s][0], ep[4 * s][1]), pk2(ep[4 * s + 1][0], ep[4 * s + 1][1]), pk2(ep[4 * s + 2][0], ep[4 * s + 2][1]), pk2(ep[4 * s + 3][0], ep[4 * s + 3][1])});
      pf[2 + s] = __builtin_bit_cast(bf16x8, u32x4{pk2(en[4 * s][0], en[4 * s][1]), pk2(en[4 * s + 1][0], en[4 * s + 1][1]), pk2(en[4 * s + 2][0], en[4 * s + 2][1]), pk2(en[4 * s + 3][0], en[4 * s + 3][1])});
    }
    asm volatile("" : "+v"(pf[0]), "+v"(pf[1]), "+v"(pf[2]), "+v"(pf[3]));
  }
  SB0();
  if (b == 0 && tid == 64) ST64(*(const u32x2*)(smem + X_AM + 56), WS_EPOCH);
  u32x4 gq; unsigned ayr; u32x4 mk4;
#define GQLD(tail) asm volatile("buffer_load_dwordx4 %0, %1, %2, 0 offen sc1" tail : "=v"(gq) : "v"(ogq), "s"(rs) : "memory")
  if (w >= 4) {
    {
      WAIT_V(18 - NREG);
    { const unsigned o_ = (unsigned)((p3 * 128 + cw * 32 + (l >> 3)) * DM + nb * 64 + ((l & 7) ^ (l >> 3)) * 8) * 2; const u16* xa = x + fb * 16 * DM; const u16* xb = x + (fb ^ 1) * 16 * DM;
      gl_m0<0>(o_, xa, lxv); gl_m0<1024>(o_, xa + 8 * DM, lxv); gl_m0<0>(o_, xb, lxc); gl_m0<1024>(o_, xb + 8 * DM, lxc); }
      SB0();
      char* pa_ = smem + cw * 10 * 1024 + l * 16;
#pragma unroll
      for (int i = 0; i < NREG; ++i) *(u32x4*)(pa_ + i * 1024) = wr_[i];
      SB0();
    }
    asm volatile("" : "+v"(dv[0]), "+v"(dv[1]), "+v"(dv[2]), "+v"(dv[3]), "+v"(dv[4]), "+v"(dv[5]), "+v"(dv[6]), "+v"(dv[7]) :: "memory");
    later();
    ayr = lbase + X_YR + (cw * 128 + (l & 15) * 8 + blk * 4) * 4;
    { const unsigned hm = (l & 1) ? 0xFFFF0000u : 0x0000FFFFu; const int q = (l & 15) >> 1;
      mk4 = u32x4{q == 0 ? hm : 0u, q == 1 ? hm : 0u, q == 2 ? hm : 0u, q == 3 ? hm : 0u}; }
    asm volatile("" : "+v"(ayr), "+v"(mk4));
    SB0();
  }
  auto downp = [&]() __attribute__((always_inline)) {
            auto red4 = [](float s0, float s1, float s2, float s3) {
              const auto r01 = __builtin_amdgcn_permlane16_swap(__float_as_uint(s0), __float_as_uint(s1), false, false);
              const auto r23 = __builtin_amdgcn_permlane16_swap(__float_as_uint(s2), __float_as_uint(s3), false, false);
              const float t01 = __uint_as_float(r01[0]) + __uint_as_float(r01[1]), t23 = __uint_as_float(r23[0]) + __uint_as_float(r23[1]);
              const auto r = __builtin_amdgcn_permlane32_swap(__float_as_uint(t01), __float_as_uint(t23), false, false);
              return __uint_as_float(r[0]) + __uint_as_float(r[1]);
            };
            const float gg = rbf(red4(__uint_as_float(gq[0]), __uint_as_float(gq[1]), __uint_as_float(gq[2]), __uint_as_float(gq[3])));
            const float uu = __builtin_bit_cast(float, __builtin_amdgcn_update_dpp(0, __builtin_bit_cast(int, gg), 0x128, 0xf, 0xf, true));
            const float hv0 = rbf(silu_r(gg)) * uu;
            const int hh = (int)pk2(hv0, hv0);
            f32x4 ya = {0.f, 0.f, 0.f, 0.f};
            static_for<8>([&](auto ic) {
              constexpr int i = decltype(ic)::value;
              u32x4 sel;
#pragma unroll
              for (int e = 0; e < 4; ++e) sel[e] = (unsigned)__builtin_amdgcn_update_dpp(0, hh, 0x150 + i, 0xf, 0xf, true) & mk4[e];
              ya = MFMA16(__builtin_bit_cast(bf16x8, sel), __builtin_bit_cast(bf16x8, dv[i]), ya);
            });
            asm volatile("" : "+v"(ya));
            if (l < 32) *(__attribute__((address_space(3))) f32x4*)(uintptr_t)ayr = ya;
  };
  {
    u32x4 kf[4], zf[4];
    if (w < 4) {
      u32x4 a, c, z; u64 m0 = ~0ull, m1 = ~0ull, m2 = ~0ull;
      unsigned sp = 0;
      do {
        asm volatile("s_mov_b64 exec, %8\n\ts_cbranch_execz 1f\n\tbuffer_load_dwordx4 %0, %3, %5, 0 offen sc1\n1:\n\ts_mov_b64 exec, %9\n\ts_cbranch_execz 2f\n\tbuffer_load_dwordx4 %1, %3, %5, 0 offen offset:1280 sc1\n2:\n\t"
                     "s_mov_b64 exec, %10\n\ts_cbranch_execz 3f\n\tbuffer_load_dwordx4 %2, %4, %5, 0 offen sc1\n3:\n\ts_mov_b64 exec, -1\n\ts_waitcnt vmcnt(0)\n\t"
                     "ds_write_b128 %6, %0\n\tds_write_b128 %6, %1 offset:1024\n\tds_write_b128 %7, %2"
                     : "+v"(a), "+v"(c), "+v"(z) : "v"(o2), "v"(oz2), "s"(rs), "v"(wa_), "v"(wz_), "s"(m0), "s"(m1), "s"(m2) : "memory");
        m0 = __ballot(max(max(max(a[0], a[1]), a[2]), a[3]) == SENT); m1 = __ballot(max(max(max(c[0], c[1]), c[2]), c[3]) == SENT); m2 = __ballot(max(max(max(z[0], z[1]), z[2]), z[3]) == SENT);
      } while ((m0 | m1 | m2) != 0 && ++sp < SPIN_CAP);
      asm volatile("" : "+v"(xrt));
    }
    RAW_BARRIER();
    DS_B128(kf[0], kba, 0); DS_B128(zf[0], kbz, 0); DS_B128(kf[1], kba, 1024); DS_B128(zf[1], kbz, 16);
    DS_B128(kf[2], kba, 2048); DS_B128(zf[2], kbz, 32); DS_B128(kf[3], kba, 3072); DS_B128(zf[3], kbz, 48);
    if (w >= 4 && has_gu) GQLD("");
    __builtin_amdgcn_s_setprio(0);
    f32x16 num = zero;
    float d0 = 0.f, d1 = 0.f, d2 = 0.f, d3 = 0.f;
    static_for<4>([&](auto cc) {
      constexpr int c = decltype(cc)::value;
      asm volatile("s_waitcnt lgkmcnt(%0)" :: "n"(6 - 2 * c));
      SB0();
      num = MFMA(__builtin_bit_cast(bf16x8, kf[c]), pf[c], num);
      SB0();
      const u32x4 pc = __builtin_bit_cast(u32x4, pf[c]);
      d0 = dot2(zf[c][0], pc[0], d0); d1 = dot2(zf[c][1], pc[1], d1); d2 = dot2(zf[c][2], pc[2], d2); d3 = dot2(zf[c][3], pc[3], d3);
      SB0();
    });
    const float rinv = __builtin_amdgcn_rcpf(x32_add(own(d0 + d1) + own(d2 + d3)) + 1e-6f);
    const f32x2 rr = {rinv, rinv};
#pragma unroll
    for (int q = 0; q < 2; ++q) {
      u32x4 u;
#pragma unroll
      for (int e = 0; e < 4; ++e) {
        const f32x2 r = f32x2{num[8 * q + 2 * e], num[8 * q + 2 * e + 1]} * rr;
        u[e] = pk2(r[0], r[1]);
      }
      ST128(u, odo + q * 1024);
      SB0();
    }
  }
  const int p0 = 8 * w + 40;
  const u16* gw2 = wo + (size_t)((p0 ^ (flip * 64)) * 8) * DM + nb * 64; unsigned lw2 = lbase + p0 * 1024;
  asm volatile("" : "+s"(gw2), "+s"(lw2));
  bool glate = false;
  if (w >= 4 && has_gu) {
    asm volatile("s_waitcnt vmcnt(2)" : "+v"(gq) :: "memory");
    gq ^= t8;
    glate = !__all((or4(gq) & 0xFFu) == 0u);
    if (!glate) downp();
  }
  asm volatile("s_waitcnt vmcnt(0)\n\ts_mov_b64 exec, 1\n\tbuffer_store_dwordx2 %0, %1, %2, 0 offen sc1\n\ts_mov_b64 exec, -1" :: "v"(tg), "v"(of4), "s"(rs) : "memory");
  if (__builtin_expect(glate, 0)) {
    for (unsigned sp = 0;;) {
      GQLD("\n\ts_waitcnt vmcnt(0)");
      gq ^= t8;
      if (__all((or4(gq) & 0xFFu) == 0u)) break;
      SPIN_NEXT(sp);
    }
    downp();
  }
  RAW_BARRIER();
  float rxn = 0.f, rsg = 0.f;
  u32x4 yq[7];
  unsigned oy = WS_YP + (unsigned)(l >> 4) * 4096 + nb * 256 + (l & 15) * 16;
#define YLD(tail) asm volatile("buffer_load_dwordx4 %0, %7, %8, 0 offen sc1\n\tbuffer_load_dwordx4 %1, %7, %8, %9 offen sc1\n\tbuffer_load_dwordx4 %2, %7, %8, %10 offen sc1\n\t" \
    "buffer_load_dwordx4 %3, %7, %8, %11 offen sc1\n\tbuffer_load_dwordx4 %4, %7, %8, %12 offen sc1\n\tbuffer_load_dwordx4 %5, %7, %8, %13 offen sc1\n\t" \
    "buffer_load_dwordx4 %6, %7, %8, %14 offen sc1" tail \
    : "=&v"(yq[0]), "=&v"(yq[1]), "=&v"(yq[2]), "=&v"(yq[3]), "=&v"(yq[4]), "=&v"(yq[5]), "=&v"(yq[6]) \
    : "v"(oy), "s"(rs), "s"(16384), "s"(2 * 16384), "s"(3 * 16384), "s"(4 * 16384), "s"(5 * 16384), "s"(6 * 16384) : "memory")
  f32x2 xf[2][4], gf[2][4];
  __attribute__((address_space(3))) f32x4* pw = (__attribute__((address_space(3))) f32x4*)(smem + 131072 + w * 4096) + l;
  unsigned prd = lbase + 131072 + (w ^ 4) * 4096 + l * 16;
  u16* po = out + (size_t)(p3 * 128 + cw * 32 + fb * 16 + (l >> 3)) * DM + nb * 64 + (l & 7) * 8; u16* po2 = po + 8 * DM;
  unsigned tw0 = lbase + 131072 + (w ^ 4) * 4096 + (l & 15) * 128 + ((ech ^ (l & 7)) * 16) + (sw ? 8 : 0), tw1 = tw0 ^ 64, tw2 = tw0 ^ 8, tw3 = tw0 ^ 72, trd = lbase + 131072 + (w ^ 4) * 4096 + (l >> 3) * 128 + (((l & 7) ^ (l >> 3)) * 16);
    unsigned bw[4];
#pragma unroll
    for (int n = 0; n < 4; ++n) bw[n] = lbase + kh * 65536 + (16 * h + 4 * blk + tq) * 128 + (((4 * (n >> 1) + tp) ^ ((tq >> 1) | (blk << 2))) * 16) + 8 * ((n ^ tp) & 1);
  {
  if (w == 1 && has_gu && l < 32) {
    const f32x4* y = (const f32x4*)(smem + X_YR) + l;
    ST128(f2u_tag((y[0] + y[32]) + (y[64] + y[96]), t8), WS_YP + (unsigned)(gr >> 3) * 4096 + ((gr + 1) & 7) * 512 + l * 16);
  }
    SB0();
    gl_run<8>(voO, gw2, lw2);
    u32x2 f0;
    asm volatile("buffer_load_dwordx2 %0, %1, %2, 0 offen sc1" : "=v"(f0) : "v"(op4), "s"(rs) : "memory");
    SB0();
    {
      const f32x2 md = *(const f32x2*)(smem + X_MD + erow * 8);
      { const unsigned ax = lxv + (l & 15) * 128 + ((ech ^ (l & 7)) * 16), a0 = ax + (sw ? 8 : 0), a1 = ax + (sw ? 0 : 8);
        u32x2 q0, q1, q2, q3;
        asm volatile("ds_read_b64 %0, %4\n\tds_read_b64 %1, %5\n\tds_read_b64 %2, %6\n\tds_read_b64 %3, %7\n\ts_waitcnt lgkmcnt(0)" : "=&v"(q0), "=&v"(q1), "=&v"(q2), "=&v"(q3) : "v"(a0), "v"(a1), "v"(a0 ^ 64), "v"(a1 ^ 64) : "memory");
        xv[0] = u32x4{q0[0], q0[1], q1[0], q1[1]}; xv[1] = u32x4{q2[0], q2[1], q3[0], q3[1]}; }
      const float rinv = rcp_nr(own(md[1])), mr = -md[0] * rinv, L2E = 1.44269504088896340736f, rl = -L2E * rinv, ml = -L2E * mr;
#pragma unroll
      for (int i = 0; i < 2; ++i)
#pragma unroll
        for (int e = 0; e < 4; ++e) {
          xf[i][e] = f32x2{bflo(xv[i][e]), bfhi(xv[i][e])};
          const f32x2 xn = __builtin_elementwise_fma(xf[i][e], f32x2{rinv, rinv}, f32x2{mr, mr}), tn = __builtin_elementwise_fma(xf[i][e], f32x2{rl, rl}, f32x2{ml, ml});
          const f32x2 dd = f32x2{__builtin_amdgcn_exp2f(tn[0]), __builtin_amdgcn_exp2f(tn[1])} + f32x2{1.0f, 1.0f};
          gf[i][e] = xn * f32x2{__builtin_amdgcn_rcpf(dd[0]), __builtin_amdgcn_rcpf(dd[1])};
        }
#pragma unroll
      for (int i = 0; i < 2; ++i) asm volatile("" : "+v"(xf[i][0]), "+v"(xf[i][1]), "+v"(xf[i][2]), "+v"(xf[i][3]), "+v"(gf[i][0]), "+v"(gf[i][1]), "+v"(gf[i][2]), "+v"(gf[i][3]));
      asm volatile("" : "+v"(pw), "+v"(prd), "+v"(po2), "+v"(tw0), "+v"(tw1), "+v"(tw2), "+v"(tw3), "+v"(trd));
      asm volatile("" : "+v"(oy), "+v"(po), "+v"(bw[0]), "+v"(bw[1]), "+v"(bw[2]), "+v"(bw[3]));
    }
    if (has_idx) {
      const unsigned ord = (rkey >> 11) & 0xFFFFu;
      rsg = rbf(1.0f / (1.0f + __expf(-__uint_as_float((ord ^ ((ord & 0x8000u) ? 0x8000u : 0xFFFFu)) << 16))));
      const f32x2 md = *(const f32x2*)(smem + X_MD + (idx & 127) * 8);
      const float dn = own(md[1]), rinv = rcp_nr(dn);
      rxn = bflo(ln_pair(xrt, md[0], dn, rinv));
      asm volatile("" : "+v"(rxn), "+v"(rsg));
      { u64 pc_; unsigned d_, o_;
        asm volatile("s_getpc_b64 %0\n.Lkf_own1:\n\ts_mov_b32 %1, .Lkf_own0-.Lkf_own1" : "=s"(pc_), "=s"(o_));
        pc_ += o_;
        asm volatile("s_load_dword %0, %1, 0x0\n\ts_load_dword %0, %1, 0x80\n\ts_load_dword %0, %1, 0x100\n\ts_load_dword %0, %1, 0x180\n\ts_load_dword %0, %1, 0x200\n\ts_load_dword %0, %1, 0x280\n\ts_waitcnt lgkmcnt(0)" : "=&s"(d_) : "s"(pc_) : "memory"); }
    }
    u64 ne;
    SB0();
    asm volatile("s_waitcnt vmcnt(0)\n\tv_cmp_ne_u64 %0, %1, %2" : "=s"(ne) : "v"(f0), "v"(tg) : "memory");
    for (unsigned sp = 0; ne != 0 && sp < SPIN_CAP; ++sp) {
      u32x2 f;
      asm volatile("buffer_load_dwordx2 %1, %2, %3, 0 offen sc1\n\ts_waitcnt vmcnt(0)\n\tv_cmp_ne_u64 %0, %1, %4" : "=s"(ne), "=&v"(f) : "v"(op4), "s"(rs), "v"(tg) : "memory");
    }
  }
  constexpr int OWIN = 4;
  u32x4 of[16][2];
  {
    SB0();
#pragma unroll
    for (int i = 0; i < OWIN; ++i) { of[i][0] = LD128(oo0 + i * 2048); of[i][1] = LD128(oo1 + i * 2048); }
    SB0();
    if (has_idx) YLD("");
    SB0();
  }
    auto owner = [&](auto nc) __attribute__((always_inline)) {
      asm volatile(".Lkf_own0:\n\ts_waitcnt vmcnt(%7)" : "+v"(yq[0]), "+v"(yq[1]), "+v"(yq[2]), "+v"(yq[3]), "+v"(yq[4]), "+v"(yq[5]), "+v"(yq[6]) : "n"(decltype(nc)::value) : "memory");
      for (unsigned sp = 0;; ++sp) {
        unsigned bad = 0;
#pragma unroll
        for (int i = 0; i < 7; ++i) { yq[i] ^= t8; bad |= or4(yq[i]); }
        if (__builtin_expect(__all((bad & 0xFFu) == 0u) || sp > SPIN_CAP, 1)) break;
        YLD("\n\ts_waitcnt vmcnt(0)");
      }
#define YF(i) __builtin_bit_cast(f32x4, yq[i])
      const f32x4 y4 = ((YF(0) + YF(1)) + (YF(2) + YF(3))) + ((YF(4) + YF(5)) + YF(6));
      const auto r01 = __builtin_amdgcn_permlane16_swap(__float_as_uint(y4[0]), __float_as_uint(own(y4[1])), false, false);
      const auto r23 = __builtin_amdgcn_permlane16_swap(__float_as_uint(y4[2]), __float_as_uint(own(y4[3])), false, false);
      const float t01 = __uint_as_float(r01[0]) + __uint_as_float(r01[1]), t23 = __uint_as_float(r23[0]) + __uint_as_float(r23[1]);
      const auto r = __builtin_amdgcn_permlane32_swap(__float_as_uint(t01), __float_as_uint(t23), false, false);
      const float y = __uint_as_float(r[0]) + __uint_as_float(r[1]);
      const float g1 = rbf(silu_r(rbf(rxn + rbf(rbf(y) * rsg))));
      (void)&gf; (void)&oy; (void)&rs; const u64 mk_ = 0x0001000100010001ull << (idx & 15);
      const unsigned gb = lbase + (131072 + w * 4096), aw_ = gb + (l & 15) * 16 + ech * 4, a0 = gb + ech * 32 + (ech & 1) * 16, a1 = a0 ^ 16;
      asm volatile("ds_write_b32 %8, %9\n\ts_mov_b64 exec, %12\n\t"
                   "ds_read_b64 %0, %10\n\tds_read_b64 %1, %10 offset:8\n\tds_read_b64 %2, %11\n\tds_read_b64 %3, %11 offset:8\n\t"
                   "ds_read_b64 %4, %10 offset:128\n\tds_read_b64 %5, %10 offset:136\n\tds_read_b64 %6, %11 offset:128\n\tds_read_b64 %7, %11 offset:136\n\t"
                   "s_mov_b64 exec, -1\n\ts_waitcnt lgkmcnt(0)"
                   : "+v"(gf[0][0]), "+v"(gf[0][1]), "+v"(gf[0][2]), "+v"(gf[0][3]), "+v"(gf[1][0]), "+v"(gf[1][1]), "+v"(gf[1][2]), "+v"(gf[1][3])
                   : "v"(aw_), "v"(g1), "v"(a0), "v"(a1), "s"(mk_) : "memory");
    };
  f32x4 acc3[2][4];
#pragma unroll
  for (int t = 0; t < 2; ++t)
#pragma unroll
    for (int n = 0; n < 4; ++n) acc3[t][n] = f32x4{0.f, 0.f, 0.f, 0.f};
  {
    s16x4 wa[2][8];
    auto rd3 = [&](auto kc, auto nc) {
      constexpr int k = decltype(kc)::value, n = decltype(nc)::value;
      (void)&wa; (void)&bw;
      DS_TR(wa[k & 1][2 * n], bw[n], k * 4096); DS_TR(wa[k & 1][2 * n + 1], bw[n], k * 4096 + 1024);
    };
    RAW_BARRIER();
    static_for<4>([&](auto nc) { rd3(IC(0), nc); });
    static_for<15>([&](auto kc) {
      constexpr int k = decltype(kc)::value, sx = k & 1;
      static_for<4>([&](auto nc) {
        constexpr int n = decltype(nc)::value;
        asm volatile("s_waitcnt lgkmcnt(%2)" : "+v"(wa[sx][2 * n]), "+v"(wa[sx][2 * n + 1]) : "n"(6));
        SB0();
        const bf16x8 wf = cat8(wa[sx][2 * n], wa[sx][2 * n + 1]);
        acc3[0][n] = MFMA16(wf, __builtin_bit_cast(bf16x8, of[k][0]), acc3[0][n]);
        SB0();
        if constexpr (n == 0 && (k & 3) == 0) __builtin_amdgcn_s_setprio(3 - (k >> 2));
        if constexpr (k + 1 < 16) rd3(IC(k + 1), nc);
        if constexpr (k + OWIN < 16 && n == 3) { of[k + OWIN][0] = LD128(oo0 + (k + OWIN) * 2048); }
        SB0();
        acc3[1][n] = MFMA16(wf, __builtin_bit_cast(bf16x8, of[k][1]), acc3[1][n]);
        SB0();
        if constexpr (k + OWIN < 16 && n == 3) { of[k + OWIN][1] = LD128(oo1 + (k + OWIN) * 2048); SB0(); }
        if constexpr (k == YP_ && n == 3) { constexpr int e_ = YP_ + OWIN < 16 ? YP_ : 15 - OWIN; if (has_idx) owner(IC(2 * (e_ + 1))); SB0(); }
      });
    });
    static_for<4>([&](auto nc) {
      constexpr int n = decltype(nc)::value;
      asm volatile("s_waitcnt lgkmcnt(%2)" : "+v"(wa[1][2 * n]), "+v"(wa[1][2 * n + 1]) : "n"(6 - 2 * n));
      SB0();
      acc3[1][n] = MFMA16(cat8(wa[1][2 * n], wa[1][2 * n + 1]), __builtin_bit_cast(bf16x8, of[15][1]), acc3[1][n]);
      SB0();
      if constexpr (n == 3) { pw[0] = acc3[1][0]; SB0(); }
    });
    static_for<4>([&](auto nc) {
      constexpr int n = decltype(nc)::value;
      acc3[0][n] = MFMA16(cat8(wa[1][2 * n], wa[1][2 * n + 1]), __builtin_bit_cast(bf16x8, of[15][0]), acc3[0][n]);
      SB0();
      if constexpr (n < 3) { pw[(n + 1) * 64] = acc3[1][n + 1]; SB0(); }
    });
  }
  {
    RAW_BARRIER();
    f32x4 pr[4];
    DS_B128(pr[0], prd, 0); DS_B128(pr[1], prd, 1024); DS_B128(pr[2], prd, 2048); DS_B128(pr[3], prd, 3072);
    SB0();
#pragma unroll
    for (int i = 0; i < 2; ++i)
#pragma unroll
      for (int e = 0; e < 4; ++e) {
        const f32x4 a = acc3[0][2 * i + (e >> 1)];
        xf[i][e] = __builtin_elementwise_fma(f32x2{a[2 * (e & 1)], a[2 * (e & 1) + 1]}, gf[i][e], xf[i][e]);
        asm volatile("" : "+v"(xf[i][e]));
      }
    SB0();
    u32x4 res[2];
#pragma unroll
    for (int i = 0; i < 2; ++i) {
      if (i == 0) asm volatile("s_waitcnt lgkmcnt(2)" : "+v"(pr[0]), "+v"(pr[1])); else asm volatile("s_waitcnt lgkmcnt(2)" : "+v"(pr[2]), "+v"(pr[3]));
      const f32x4 s0 = pr[2 * i], s1 = pr[2 * i + 1];
      const f32x2 o0 = __builtin_elementwise_fma(f32x2{s0[0], s0[1]}, gf[i][0], xf[i][0]), o1 = __builtin_elementwise_fma(f32x2{s0[2], s0[3]}, gf[i][1], xf[i][1]);
      const f32x2 o2 = __builtin_elementwise_fma(f32x2{s1[0], s1[1]}, gf[i][2], xf[i][2]), o3 = __builtin_elementwise_fma(f32x2{s1[2], s1[3]}, gf[i][3], xf[i][3]);
      res[i] = u32x4{pk2(o0[0], o0[1]), pk2(o1[0], o1[1]), pk2(o2[0], o2[1]), pk2(o3[0], o3[1])};
      asm volatile("ds_write_b64 %0, %2\n\tds_write_b64 %1, %3" :: "v"(i ? tw1 : tw0), "v"(i ? tw3 : tw2), "v"(u32x2{res[i][0], res[i][1]}), "v"(u32x2{res[i][2], res[i][3]}) : "memory");
    }
    asm volatile("ds_read_b128 %0, %2\n\tds_read_b128 %1, %2 offset:1024\n\ts_waitcnt lgkmcnt(1)\n\t"
                 "global_store_dwordx4 %3, %0, off sc1\n\ts_waitcnt lgkmcnt(0)\n\tglobal_store_dwordx4 %4, %1, off sc1\n\ts_nop 1"
                 : "=&v"(res[0]), "=&v"(res[1]) : "v"(trd), "v"(po), "v"(po2) : "memory");
  }
  asm volatile(".Lkf_pf_end:" ::: "memory");
}

extern "C" void kernel_launch(void* const* d_in, const int* in_sizes, int n_in,
                              void* d_out, int out_size, void* d_ws, size_t ws_size,
                              hipStream_t stream) {
  (void)hipFuncSetAttribute((const void*)kf_all, hipFuncAttributeMaxDynamicSharedMemorySize, LDS_TOTAL);
  kf_all<<<256, 512, LDS_TOTAL, stream>>>((const u16*)d_in[0], (const u16*)d_in[1], (const u16*)d_in[2], (const u16*)d_in[3],
                                          (const u16*)d_in[5], (const u16*)d_in[6], (const u16*)d_in[7], (const u16*)d_in[8],
                                          (char*)d_ws, (u16*)d_out);
}
```

```cpp
#define KP 9
#define KC 10
#define F1_S 13
#define PF2_KT 8
#define WOK 2
#define H1_DELAY 4
constexpr int PACE_T[16] = {0, 114, 84, 64}, PACE_3[16] = {0, 60, 52, 52};
constexpr int DVN[20] = {0, 0, 4, 2, 1, 0, 1};
constexpr int dv_first(int k) { int n = 0; for (int i = -2; i < k; ++i) n += DVN[i + 2]; return n; }
constexpr bool CSS(int S) { return S < 2 || S == 6 || S == 7; }
#define NREG 8
static_assert(NREG <= 10, "first run");
#define WH0 12
#define WPOST 4
#define YP_ 11
#define PRIO_T 2
#define PRIO_H 1
#include <hip/hip_runtime.h>
#include <hip/hip_bf16.h>
#include <stdint.h>
#include <utility>
#include <type_traits>

typedef __attribute__((ext_vector_type(8))) short bf16x8;
typedef __attribute__((ext_vector_type(4))) short s16x4;
typedef __attribute__((ext_vector_type(16))) float f32x16;
typedef __attribute__((ext_vector_type(4))) float f32x4;
typedef __attribute__((ext_vector_type(2))) float f32x2;
typedef __attribute__((ext_vector_type(4))) unsigned u32x4;
typedef __attribute__((ext_vector_type(2))) unsigned u32x2;
typedef __attribute__((ext_vector_type(2))) __bf16 bfx2;
typedef __attribute__((ext_vector_type(4))) short v4i16_t;
typedef unsigned long long u64;
typedef unsigned short u16;

#define DI __device__ __forceinline__
#define MFMA(a, b, c) __builtin_amdgcn_mfma_f32_32x32x16_bf16((a), (b), (c), 0, 0, 0)
#define MFMA16H(a, b, c) __builtin_amdgcn_mfma_f32_16x16x16bf16_1k((a), (b), (c), 0, 0, 0)
#define MFMA16(a, b, c) __builtin_amdgcn_mfma_f32_16x16x32_bf16((a), (b), (c), 0, 0, 0)

DI unsigned pk2(float a, float b) { f32x2 v = {a, b}; return __builtin_bit_cast(unsigned, __builtin_convertvector(v, bfx2)); }
DI float bflo(unsigned u) { return __uint_as_float(u << 16); }
DI float bfhi(unsigned u) { return __uint_as_float(u & 0xffff0000u); }
DI float rbf(float x) { return (float)(__bf16)x; }
DI s16x4 trread(const char* p) {
  return __builtin_bit_cast(s16x4, __builtin_amdgcn_ds_read_tr16_b64_v4i16((__attribute__((address_space(3))) v4i16_t*)p));
}
DI bf16x8 cat8(s16x4 lo, s16x4 hi) { return __builtin_shufflevector(lo, hi, 0, 1, 2, 3, 4, 5, 6, 7); }
#define WAIT_V(n) asm volatile("s_waitcnt vmcnt(%0)" ::"n"(n) : "memory")
#define RAW_BARRIER() do { asm volatile("s_waitcnt lgkmcnt(0)" ::: "memory"); __builtin_amdgcn_s_barrier(); asm volatile("" ::: "memory"); } while (0)
#define DS_B128(dst, addr, off) asm volatile("ds_read_b128 %0, %1 offset:%2" : "=v"(dst) : "v"(addr), "n"(off))
#define DS_TR(dst, addr, off) asm volatile("ds_read_b64_tr_b16 %0, %1 offset:%2" : "=v"(dst) : "v"(addr), "n"(off))
#define LDS_OFF(p) ((unsigned)(uintptr_t)(__attribute__((address_space(3))) const char*)(p))
template <class F, int... I> DI void static_for_impl(F&& f, std::integer_sequence<int, I...>) { (f(std::integral_constant<int, I>{}), ...); }
template <int N, class F> DI void static_for(F&& f) { static_for_impl(f, std::make_integer_sequence<int, N>{}); }
DI float divc(float a, float b, float rinv) { const float q = a * rinv; return __builtin_fmaf(__builtin_fmaf(-q, b, a), rinv, q); }
DI float rcp_nr(float d) { const float r = __builtin_amdgcn_rcpf(d); return __builtin_fmaf(__builtin_fmaf(-d, r, 1.0f), r, r); }
DI float own(float v) { asm volatile("" : "+v"(v)); return v; }
DI float silu_r(float x) { return x * __builtin_amdgcn_rcpf(1.0f + __expf(-x)); }
#define SB0() __builtin_amdgcn_sched_barrier(0)
#define GLDS_S(vo, sp, ld) asm volatile("s_mov_b32 m0, %2\n\ts_nop 0\n\tglobal_load_lds_dwordx4 %0, %1" :: "v"(vo), "s"(sp), "s"(ld) : "m0", "memory")
template <int IMM> DI void gl_m0(unsigned vo, const void* sp, unsigned ls) {
  asm volatile("s_add_u32 m0, %2, %3\n\ts_nop 0\n\tglobal_load_lds_dwordx4 %0, %1" :: "v"(vo), "s"(sp), "s"(ls), "n"(IMM) : "m0", "memory", "scc");
}
template <int IMM> DI void gl_nt(unsigned vo, const void* sp, unsigned ls) {
  asm volatile("s_add_u32 m0, %2, %3\n\ts_nop 0\n\tglobal_load_lds_dwordx4 %0, %1 nt" :: "v"(vo), "s"(sp), "s"(ls), "n"(IMM) : "m0", "memory", "scc");
}
template <int N> DI void gl_run(unsigned vo, const void* sp, unsigned ls) {
  unsigned lo = (unsigned)(uintptr_t)sp, hi = (unsigned)((uintptr_t)sp >> 32);
  static_for<N>([&](auto ic) {
    constexpr int i = decltype(ic)::value;
    gl_m0<i * 1024>(vo, (const void*)(((uintptr_t)hi << 32) | lo), ls);
    if constexpr (i + 1 < N) asm volatile("s_add_u32 %0, %0, 0x4000\n\ts_addc_u32 %1, %1, 0" : "+s"(lo), "+s"(hi) :: "scc");
  });
}
#define GLDS_SC1(vo, sp, ld) asm volatile("s_mov_b32 m0, %2\n\ts_nop 0\n\tglobal_load_lds_dwordx4 %0, %1 sc1" :: "v"(vo), "s"(sp), "s"(ld) : "m0", "memory")
DI float x32_add(float v) { auto r = __builtin_amdgcn_permlane32_swap(__float_as_uint(v), __float_as_uint(v), false, false); return __uint_as_float(r[0]) + __uint_as_float(r[1]); }
DI float x32_max(float v) { auto r = __builtin_amdgcn_permlane32_swap(__float_as_uint(v), __float_as_uint(v), false, false); float o; asm("v_max_f32 %0, %1, %2" : "=v"(o) : "v"(r[0]), "v"(r[1])); return o; }
DI float x32_min(float v) { auto r = __builtin_amdgcn_permlane32_swap(__float_as_uint(v), __float_as_uint(v), false, false); float o; asm("v_min_f32 %0, %1, %2" : "=v"(o) : "v"(r[0]), "v"(r[1])); return o; }
DI float x16_add(float v) { auto r = __builtin_amdgcn_permlane16_swap(__float_as_uint(v), __float_as_uint(v), false, false); return __uint_as_float(r[0]) + __uint_as_float(r[1]); }
template <int CTRL, int RMASK> DI float dpp_add(float v) {
  return v + __builtin_bit_cast(float, __builtin_amdgcn_update_dpp(0, __builtin_bit_cast(int, v), CTRL, RMASK, 0xf, true));
}

DI unsigned ln_pair(unsigned u, float m, float den, float rinv) {
  const f32x2 mm = {m, m}, dd = {den, den}, rr = {rinv, rinv};
  const f32x2 d0 = f32x2{bflo(u), bfhi(u)} - mm;
  const unsigned du = pk2(d0[0], d0[1]);
  const f32x2 d = {bflo(du), bfhi(du)};
  const f32x2 q = d * rr;
  const f32x2 r = __builtin_elementwise_fma(__builtin_elementwise_fma(-q, dd, d), rr, q);
  return pk2(r[0], r[1]);
}
DI float dot2(unsigned a, unsigned b, float c) { return __builtin_amdgcn_fdot2_f32_bf16(__builtin_bit_cast(bfx2, a), __builtin_bit_cast(bfx2, b), c, false); }
DI float half_total(float v) {
  v = dpp_add<0x121, 0xf>(v); v = dpp_add<0x122, 0xf>(v); v = dpp_add<0x124, 0xf>(v); v = dpp_add<0x128, 0xf>(v);
  return x16_add(v);
}
DI void ln_rows(const u32x4 (&xr)[4], const u32x4 (&wr)[2], int h, float& m, float& den, float& rinv, float& r) {
  const float c0 = __uint_as_float(__builtin_amdgcn_readlane(xr[0][0], 0) << 16), c1 = __uint_as_float(__builtin_amdgcn_readlane(xr[2][0], 0) << 16);
  float s1[2] = {0.f, 0.f}, sx[2] = {0.f, 0.f}, sw = 0.f;
  f32x2 q[2] = {{0.f, 0.f}, {0.f, 0.f}};
#pragma unroll
  for (int i = 0; i < 2; ++i)
#pragma unroll
    for (int e = 0; e < 4; ++e) {
      sw = dot2(wr[i][e], 0x3F803F80u, sw);
#pragma unroll
      for (int t = 0; t < 2; ++t) {
        const unsigned u = xr[2 * t + i][e]; const float c = t ? c1 : c0;
        s1[t] = dot2(u, 0x3F803F80u, s1[t]); sx[t] = dot2(u, wr[i][e], sx[t]);
        const f32x2 d = f32x2{bflo(u), bfhi(u)} - f32x2{c, c};
        q[t] = __builtin_elementwise_fma(d, d, q[t]);
      }
    }
  auto mg = [](float a, float b) { const auto x = __builtin_amdgcn_permlane32_swap(__float_as_uint(a), __float_as_uint(b), false, false); return __uint_as_float(x[0]) + __uint_as_float(x[1]); };
  const float mean = half_total(mg(s1[0], s1[1])) * (1.0f / 1024.0f), m2 = half_total(mg(q[0][0] + own(q[0][1]), q[1][0] + own(q[1][1]))) * (1.0f / 1024.0f);
  const float tx = half_total(mg(sx[0], sx[1])), tw = half_total(x32_add(sw));
  const float dm = mean - (h ? c1 : c0);
  const float var = fmaxf(__builtin_fmaf(-dm, dm, m2), 0.f);
  m = rbf(mean);
  den = rbf(__builtin_amdgcn_sqrtf(rbf(rbf(var) + 1e-5f)));
  rinv = rcp_nr(den);
  r = rbf(divc(__builtin_fmaf(-m, tw, tx), den, rinv));
}

template <class HK> DI void phi_fn(const bf16x8 (&wp)[2], const bf16x8 f0, const bf16x8 f1, f32x2 (&ep)[8], f32x2 (&en)[8], float si, float so, HK&& hk) {
  f32x16 p;
#pragma unroll
  for (int i = 0; i < 16; ++i) p[i] = 0.f;
  p = MFMA(wp[0], f0, p);
  p = MFMA(wp[1], f1, p);
  hk(std::integral_constant<int, 0>{});
  float mx = -3.0e38f, mn = 3.0e38f;
  f32x2 p2[8];
  static_for<8>([&](auto ic) {
    constexpr int i = decltype(ic)::value;
    p2[i] = f32x2{p[2 * i], p[2 * i + 1]};
    mx = fmaxf(fmaxf(mx, p2[i][0]), p2[i][1]); mn = fminf(fminf(mn, p2[i][0]), p2[i][1]);
    if constexpr (i & 1) hk(std::integral_constant<int, 1 + (i >> 1)>{});
  });
  mx = x32_max(mx); mn = x32_min(mn);
  hk(std::integral_constant<int, 5>{});
  const float L2E = 1.44269504088896340736f * si, mxl = -mx * L2E, mnl = mn * L2E;
  const f32x2 cp = {L2E, L2E}, cn = {-L2E, -L2E}, bp = {mxl, mxl}, bn = {mnl, mnl};
  f32x2 sp2, sn2;
  static_for<8>([&](auto ic) {
    constexpr int i = decltype(ic)::value;
    const f32x2 tp2 = __builtin_elementwise_fma(p2[i], cp, bp), tn2 = __builtin_elementwise_fma(p2[i], cn, bn);
    ep[i] = f32x2{__builtin_amdgcn_exp2f(tp2[0]), __builtin_amdgcn_exp2f(tp2[1])};
    en[i] = f32x2{__builtin_amdgcn_exp2f(tn2[0]), __builtin_amdgcn_exp2f(tn2[1])};
    if constexpr (i == 0) { sp2 = ep[0]; sn2 = en[0]; } else { sp2 += ep[i]; sn2 += en[i]; }
    hk(std::integral_constant<int, 6 + i>{});
  });
  const float sp = x32_add(sp2[0] + own(sp2[1])), sn = x32_add(sn2[0] + own(sn2[1]));
  hk(std::integral_constant<int, 14>{});
  const float rp = __builtin_amdgcn_rcpf(sp) * so, rn = __builtin_amdgcn_rcpf(sn) * so;
  const f32x2 rp2 = {rp, rp}, rn2 = {rn, rn};
  hk(std::integral_constant<int, 15>{});
#pragma unroll
  for (int i = 0; i < 8; ++i) { ep[i] = ep[i] * rp2; en[i] = en[i] * rn2; }
  hk(std::integral_constant<int, 16>{});
}

constexpr int DM = 1024, FF = 3584;
constexpr unsigned SENT = 0xFFFFFFFFu;
constexpr unsigned SPIN_CAP = 1u << 17;

constexpr unsigned WS_EPOCH = 0;
constexpr unsigned WS_F1 = 4096;
constexpr unsigned WS_F4 = 896u << 10;
constexpr unsigned WS_STAT = 20480;
constexpr unsigned WS_CS = 36864;
constexpr unsigned WS_GUP = WS_CS + 16 * 16 * 768;
constexpr unsigned WS_YP = WS_GUP + 256 * 512;
constexpr unsigned WS_KVF = WS_YP + 32 * 4096;
constexpr unsigned WS_ZP = WS_KVF + 256 * 640;
constexpr unsigned WS_XA = 800u << 10;
constexpr unsigned WS_RI = 840u << 10;
static_assert(WS_ZP + 131072 <= WS_XA && WS_XA + 32768 <= WS_RI, "ws map");
constexpr unsigned WS_KVP = 1u << 20;
constexpr unsigned WS_O = 8u << 20;
constexpr unsigned WS_END = 16u << 20;

constexpr int ABYTES = 128 * 128, WSLAB = 64 * 128, STAGE1 = ABYTES + 3 * WSLAB, NST1 = 3;
constexpr int XB = NST1 * STAGE1;
constexpr int X_WO = XB;
constexpr int X_CSP = XB + 8192;
constexpr int X_P = 131072 + 4096;
constexpr int X_XW = X_P;
constexpr int X_XA = X_XW + 6144;
constexpr int X_ST = X_XA + 2048;
constexpr int X_MD = X_ST + 1024;
constexpr int X_AM = X_MD + 1024;
constexpr int X_XS = X_AM + 64;
constexpr int X_YR = X_XS + 512;
constexpr int X_YS = X_YR + 2048;
constexpr int X_WP = X_YS + 2048;
constexpr int X_KB = X_WP + 4096;
constexpr int LDS_TOTAL = 163840;
static_assert(X_KB + 2 * 4224 <= LDS_TOTAL && 131072 + 6 * 2048 <= X_MD && X_YS + 64 + 4096 <= X_KB && X_YS % 128 == 64, "lds");
static_assert(LDS_TOTAL <= 163840, "lds");
constexpr int gu_at(int ks) { return STAGE1 + ks * 8192; }
constexpr int HANDOFF = 106496;

#define LD128(off) __builtin_amdgcn_raw_buffer_load_b128(rs, (int)(off), 0, 16)
#define ST128(v, off) __builtin_amdgcn_raw_buffer_store_b128((v), rs, (int)(off), 0, 16)
#define ST64(v, off) __builtin_amdgcn_raw_buffer_store_b64((v), rs, (int)(off), 0, 16)
DI u32x4 f2u_tag(f32x4 v, unsigned t8) { u32x4 u = __builtin_bit_cast(u32x4, v); return (u & 0xFFFFFF00u) | t8; }
DI unsigned or4(u32x4 v) { return (v[0] | v[1]) | (v[2] | v[3]); }
#define SPIN_NEXT(sp) { if (++(sp) > SPIN_CAP) break; __builtin_amdgcn_s_sleep(1); }
#define TAG_OF(en) ((en) * 0x9E3779B97F4A7C15ull)

__global__ __launch_bounds__(512, 2) void kf_all(const u16* __restrict__ x, const u16* __restrict__ wqkv, const u16* __restrict__ wphi,
                                              const u16* __restrict__ wo, const u16* __restrict__ w_r, const u16* __restrict__ w_gate,
                                              const u16* __restrict__ w_up, const u16* __restrict__ w_down, char* ws, u16* __restrict__ out) {
  extern __shared__ __attribute__((aligned(16))) char smem[];
  asm volatile(".Lkf_pf_start:");
  const int b = blockIdx.x;
  const int tid = threadIdx.x, w = __builtin_amdgcn_readfirstlane(tid >> 6);
  unsigned pfd;
  {
    u64 pc; unsigned len;
    asm volatile("s_getpc_b64 %0\n.Lkf_pf_here:\n\ts_mov_b32 %1, .Lkf_pf_end+1152-256-.Lkf_pf_here" : "=s"(pc), "=s"(len));
    const u64 pa = pc + min((unsigned)((b & ~7) + w) << 8, len);
    asm volatile("s_load_dword %0, %1, 0x0\n\ts_load_dword %0, %1, 0x40\n\ts_load_dword %0, %1, 0x80\n\ts_load_dword %0, %1, 0xc0" : "=&s"(pfd) : "s"(pa) : "memory");
  }
  SB0();
  const int xcd = b & 7, jj = b >> 3, l = tid & 63;
  const int panel = (xcd >> 2) * 8 + (jj & 7), hp = (xcd & 3) * 4 + (jj >> 3);
  const int id = panel * 16 + hp;
  asm volatile("" : "+s"(x), "+s"(wqkv), "+s"(wphi), "+s"(wo), "+s"(w_r), "+s"(w_gate), "+s"(w_up), "+s"(w_down), "+s"(ws), "+s"(out));
  SB0();
  u32x4 xr4[4], wr4[2];
  if (w < 4) {
    const u16* xr = x + (size_t)(panel * 128 + hp * 8 + 2 * w) * DM; const unsigned vl = (unsigned)l * 16;
#define GLS(dst, p, o) asm volatile("global_load_dwordx4 %0, %1, %2 offset:" #o : "=v"(dst) : "v"(vl), "s"(p) : "memory")
    GLS(xr4[0], xr, 0); GLS(xr4[1], xr, 1024); GLS(xr4[2], xr, 2048); GLS(xr4[3], xr, 3072); GLS(wr4[0], w_r, 0); GLS(wr4[1], w_r, 1024);
  }
  SB0();
  const int cw = w & 3, wm = cw & 1, wn = cw >> 1;
  const int h = l >> 5, r31 = l & 31, blk = (l >> 4) & 1, tq = (l & 15) >> 2, tp = l & 3;
  const int hd = hp * 2 + wn;
  const unsigned lbase = LDS_OFF(smem);
  const __amdgpu_buffer_rsrc_t rs = __builtin_amdgcn_make_buffer_rsrc((void*)ws, 0, (int)WS_END, 0x00020000);
  bf16x8 fr[4];
  bf16x8 wp[2];
  f32x4 qa[4][2]; float rt;
  const int kh = w >> 2;
  const int wt = wm * 2 + kh;
  const unsigned img = (wn * 4 + wt) * 4096 + r31 * 128 + (((2 * h) ^ (r31 & 7)) * 16);
  auto wpload = [&]() {
    const char* p = smem + X_WP + wn * 2048 + (8 * h + tq) * 64 + (16 * blk + 4 * tp) * 2;
    wp[0] = cat8(trread(p), trread(p + 256)); wp[1] = cat8(trread(p + 1024), trread(p + 1280));
  };
  auto imgw = [&](int g, const f32x2 (&ep)[8], const f32x2 (&en)[8]) {
    const f32x2 (&e)[8] = g < 2 ? ep : en; const int q = 4 * (g & 1);
    *(u32x4*)(smem + (img ^ ((g >> 1) * 64 + (g & 1) * 16))) = u32x4{pk2(e[q][0], e[q][1]), pk2(e[q + 1][0], e[q + 1][1]), pk2(e[q + 2][0], e[q + 2][1]), pk2(e[q + 3][0], e[q + 3][1])};
  };
  auto pack_u = [&](const f32x4 x0, const f32x4 x1) {
    const auto s0 = __builtin_amdgcn_permlane16_swap(pk2(x0[0], x0[1]), pk2(x1[0], x1[1]), false, false);
    const auto s1 = __builtin_amdgcn_permlane16_swap(pk2(x0[2], x0[3]), pk2(x1[2], x1[3]), false, false);
    return u32x4{s0[0], s1[0], s0[1], s1[1]};
  };
  const int hr = hp ^ 8;
  const int p3 = ((xcd >> 1) * 4 + (jj & 3)) ^ 8, nb = (int)(((xcd & 1) ? 0xF7EDCA98u : 0xB3654210u) >> (jj & 28)) & 15;
  const int flip = jj & 1;
  const unsigned voO = (unsigned)((l >> 3) * DM + (((l & 7) ^ (((l >> 4) & 1) | ((l >> 5) << 2))) * 8)) * 2;
  const bool has_gu = (hp & 7) != 3;
  int gr = (xcd == 6 && jj >= 12) ? 8 * (24 + (jj & 3)) + 2 * ((jj >> 2) - 3) : b;
  asm volatile("" : "+s"(gr));
  gr = jj >= 28 ? 8 * (4 * (xcd >> 1) + jj - 16) + 6 : gr;
  asm volatile("" : "+s"(gr));
  gr = has_gu ? gr : 224 + (jj - 24) * 4 + (xcd >> 1);
  const int gkq = (gr / 56) & 3;
  u32x4 dv[8]; unsigned ogq; bf16x8 dn[4];
  u32x4 wr_[16];
    const int srow = l >> 3, sch = l & 7;
    unsigned voA[4], voW[6];
#pragma unroll
    for (int i = 0; i < 4; ++i) voA[i] = (unsigned)((panel * 128 + (4 * cw + i) * 8 + srow) * DM + ((sch ^ (4 * (i & 1) + (srow >> 1))) * 8)) * 2;
#pragma unroll
    for (int i = 0; i < 6; ++i) voW[i] = (unsigned)(((2 * cw + (i & 1)) * 8 + srow) * 3072 + (i >> 1) * 1024 + hp * 64 + ((sch ^ ((((srow >> 1) & 1) << 2) | ((i & 1) << 1))) * 8)) * 2;
    const unsigned lwa = lbase + cw * 4096, lww = lbase + ABYTES + cw * 2048;
    const int rot = 4 * ((panel & 7) >> 1) + 2 * (panel >> 3) + 4 * (xcd & 3);
    u64 k0_ = (panel & 1) ? 0xEA62FB7C84D30951ull : 0xFB73EA6D95C21840ull, k1_ = (panel & 8) ? 0xD95FB73EA62C1840ull : 0xFB73EA62D95C1840ull;
    asm volatile("" : "+s"(k0_), "+s"(k1_));
    const u64 kord = has_gu ? k1_ : k0_;
    auto stage = [&](int it, auto bufc) {
      constexpr int B = decltype(bufc)::value * STAGE1;
      const int kt = ((int)(kord >> (4 * it)) + rot) & 15;
      const u16* gx = x + kt * 64; const u16* gq_ = wqkv + (size_t)kt * 64 * 3072;
      static_for<10>([&](auto ic) {
        constexpr int i = decltype(ic)::value;
        if constexpr (i < 4) gl_m0<B + i * 1024>(voA[i], gx, lwa);
        else gl_m0<B + ((i - 4) >> 1) * WSLAB + (i & 1) * 1024>(voW[i - 4], gq_, lww);
      });
    };
  unsigned pa0, pvh, dst, dz, pgw, ogu, oh1, oh1z;
  auto kvsetup = [&]() {
    int l = tid & 63; asm volatile("" : "+v"(l));
    const int g = l >> 4, c15 = l & 15, tq = c15 >> 2, tp = l & 3;
    pa0 = lbase + kh * 16384 + (4 * g + tq) * 128 + (((2 * cw + (tp >> 1)) ^ ((4 * g + tq) & 7)) * 16) + 8 * (tp & 1);
    pvh = lbase + HANDOFF + kh * 12288 + l * 16;
    dst = WS_KVP + (unsigned)((hp * 16 + panel) * 16 + (w & 6) * 2 + (g & 2) + (w & 1)) * 512 + c15 * 32 + (g & 1) * 16;
    dz = WS_ZP + (unsigned)((hp * 16 + (w & 6) * 2 + (g & 2) + (w & 1)) * 16 + panel) * 32 + (g & 1) * 16;
    pgw = lbase + gu_at(0) + kh * 4096 + (8 * g + tq) * 128 + (((2 * cw + (tp >> 1)) ^ ((((tq >> 1) & 1) << 2) | ((g & 1) << 1))) * 16) + 8 * (tp & 1);
    ogu = WS_GUP + (unsigned)gr * 512 + w * 64 + g * 16;
    oh1 = WS_KVP + (unsigned)((hr * 16 + (l >> 3)) * 16 + panel) * 512 + cw * 128 + (l & 7) * 16;
    oh1z = WS_ZP + (unsigned)((hr * 16 + panel) * 16) * 32 + l * 4;
    asm volatile("" : "+v"(pa0), "+v"(pvh), "+v"(dst), "+v"(dz), "+v"(pgw), "+v"(ogu), "+v"(oh1), "+v"(oh1z));
  };
#define STUB(n) asm volatile(".Lkf_st" #n ":\n\ts_cbranch_execz .Lkf_rt" #n)
#define VISIT(n, wv) asm volatile("s_cmp_lg_u32 %0, " #wv "\n\ts_cbranch_scc1 .Lkf_rt" #n "\n\ts_mov_b64 exec, 0\n\ts_branch .Lkf_st" #n "\n.Lkf_rt" #n ":\n\ts_mov_b64 exec, -1" :: "s"(cw) : "scc")
#define IC(n) std::integral_constant<int, (n)>{}
#define CODE_PF(tagn, FROM, d) do { u64 pc_; unsigned len_; int off_; \
    asm volatile("s_getpc_b64 %0\n.Lkf_pf_h" #tagn ":\n\ts_mov_b32 %1, .Lkf_pf_end+1152-" FROM "\n\ts_mov_b32 %2, " FROM "-.Lkf_pf_h" #tagn : "=s"(pc_), "=s"(len_), "=s"(off_)); \
    unsigned o_ = (unsigned)(jj * 4 + cw) * 256u; o_ = o_ + 256u <= len_ ? o_ : len_ - 256u; \
    const u64 pa_ = (pc_ + (long long)off_ + o_) & ~63ull; \
    asm volatile("s_load_dword %0, %1, 0x0\n\ts_load_dword %0, %1, 0x40\n\ts_load_dword %0, %1, 0x80\n\ts_load_dword %0, %1, 0xc0" : "=&s"(d) : "s"(pa_) : "memory"); } while (0)
#define PF_DONE(d) asm volatile("s_waitcnt lgkmcnt(0)" : "+s"(d) :: "memory")
  if (w < 4) {
    unsigned oc2;
    {
      const unsigned r0 = (unsigned)(panel * 128 + hp * 8);
      oc2 = l < 33 ? WS_KVF + (unsigned)(hr * 16 + panel) * 640 + l * 16 : l == 33 ? WS_F1 + (unsigned)id * 16 : l == 34 ? WS_STAT + r0 * 8 : l == 35 ? WS_RI + (unsigned)(panel * 1024 + hp * 32)
          : l < 40 ? WS_F4 + (unsigned)(((panel * 4 + (l - 36)) * 16 + hp) * 2) * 32 : l < 44 ? WS_GUP + (unsigned)gr * 512 + (l - 40) * 128 : l == 44 ? WS_RI + (unsigned)(panel * 1024 + 512 + hp * 16)
          : WS_ZP + (unsigned)((hp * 16 + (l & 15)) * 16 + panel) * 32;
    }
    u32x4 tm, k0, k1; u32x2 tk;
    const unsigned tmofs = lbase + X_CSP + (l < 47 ? l : 46) * 16, kofs = lbase + X_CSP + 768, tgofs = lbase + X_AM + 48;
    unsigned opay;
    { const unsigned r0 = (unsigned)(panel * 128 + hp * 8);
      opay = l < 32 ? WS_CS + (unsigned)(hp * 8 + (panel >> 1)) * 768 + (panel & 1) * 512 + l * 16 : l < 40 ? WS_XA + r0 * 16 + (l - 32) * 16 : l < 44 ? WS_STAT + r0 * 8 + (l - 40) * 16 : l < 46 ? WS_RI + (unsigned)(panel * 1024 + hp * 32) + (l - 44) * 16 : WS_RI + (unsigned)(panel * 1024 + 512 + hp * 16); }
    const unsigned oflag = WS_F1 + (unsigned)id * 16;
    unsigned mz; asm volatile("s_cmp_eq_u32 %1, 0\n\ts_cselect_b32 %0, -1, 0" : "=s"(mz) : "s"(cw) : "scc");
    const u64 mk_all = ((u64)mz << 32) | mz, mk_pay = ((u64)(mz & 0x7FFFu) << 32) | (mz & ((panel & 1) ? 0xFFFFu : 0xFFFFFFFFu)), mk_one = mz & 1u;
    asm volatile("" : "+v"(oc2), "+v"(opay));
#define MST(op, data, off, mask) asm volatile("s_mov_b64 exec, %3\n\t" op " %0, %1, %2, 0 offen sc1\n\ts_mov_b64 exec, -1" :: "v"(data), "v"(off), "s"(rs), "s"(mask) : "memory")
    f32x4 csa[2] = {{0.f, 0.f, 0.f, 0.f}, {0.f, 0.f, 0.f, 0.f}};
    const bf16x8 ones = {0x3F80, 0x3F80, 0x3F80, 0x3F80, 0x3F80, 0x3F80, 0x3F80, 0x3F80};
    s16x4 cl[2][2], ch[2][2];
    const unsigned csw = lbase + X_CSP + (wm * 64 + wn * 32 + r31) * 4;
    f32x4 acc[4][6];
#pragma unroll
    for (int t = 0; t < 4; ++t)
#pragma unroll
      for (int a = 0; a < 6; ++a) acc[t][a] = f32x4{0.f, 0.f, 0.f, 0.f};
    const int g4 = l >> 4, c15 = l & 15;
    unsigned asw[2][2], wofs[2][2], cofs[2];
#pragma unroll
    for (int e = 0; e < 2; ++e) {
      asw[0][e] = lbase + (wm * 64 + c15) * 128 + (((4 * e + g4) ^ ((c15 >> 1) & 7)) * 16);
      wofs[0][e] = lbase + ABYTES + (8 * g4 + tq) * 128 + (((wn * 4 + 2 * e + (tp >> 1)) ^ ((((tq >> 1) & 1) << 2) | ((g4 & 1) << 1))) * 16) + 8 * (tp & 1);
      asw[1][e] = asw[0][e] + 2 * STAGE1; wofs[1][e] = wofs[0][e] + 2 * STAGE1;
      cofs[e] = wofs[0][e] + ((panel & 1) ? 2 : wm) * WSLAB;
    }
    asm volatile("" : "+v"(asw[0][0]), "+v"(asw[0][1]), "+v"(asw[1][0]), "+v"(asw[1][1]), "+v"(wofs[0][0]), "+v"(wofs[0][1]), "+v"(wofs[1][0]), "+v"(wofs[1][1]), "+v"(cofs[0]), "+v"(cofs[1]));
    bf16x8 af[2][4]; s16x4 wl[2][6], wh_[2][6];
    s16x4 ea[4], ew[6];
    const unsigned eaofs = lbase + X_XA + (wm * 64 + c15) * 16;
    const unsigned ewofs = lbase + X_XW + g4 * 1536 + (wn * 32 + c15) * 4;
    const unsigned riofs = lbase + X_ST + (wm * 64 + r31) * 4;
    auto rd = [&](auto ic, auto stc) {
      constexpr int i = decltype(ic)::value, S = decltype(stc)::value, s = S & 1, e = S & 1, buf = (S >> 1) % NST1;
      constexpr int b2 = buf == 2, bo = buf == 1 ? STAGE1 : 0;
      (void)&cl; (void)&ch; (void)&cofs; (void)&af; (void)&wl; (void)&wh_; (void)&asw; (void)&wofs;
      if constexpr (i == 0) DS_B128(af[s][0], asw[b2][e], bo);
      else if constexpr (i == 1) DS_TR(wl[s][0], wofs[b2][0], bo + e * 4096);
      else if constexpr (i == 2) DS_TR(wh_[s][0], wofs[b2][0], bo + e * 4096 + 512);
      else if constexpr (i < 6) DS_B128(af[s][i - 2], asw[b2][e], bo + (i - 2) * 2048);
      else if constexpr (i < 16) {
        constexpr int j = (i - 4) >> 1, off = bo + e * 4096 + (j >> 1) * WSLAB;
        if constexpr (i & 1) DS_TR(wh_[s][j], wofs[b2][j & 1], off + 512); else DS_TR(wl[s][j], wofs[b2][j & 1], off);
      } else {
        constexpr int c = (i - 16) >> 1;
        if constexpr (i & 1) DS_TR(ch[s][c], cofs[c], bo + e * 4096 + 512); else DS_TR(cl[s][c], cofs[c], bo + e * 4096);
      }
    };
    {
      SB0();
      asm volatile("s_waitcnt vmcnt(0) lgkmcnt(0)" : "+s"(pfd), "+v"(xr4[0]), "+v"(xr4[1]), "+v"(xr4[2]), "+v"(xr4[3]), "+v"(wr4[0]), "+v"(wr4[1]) :: "memory");
      SB0();
      float m, den, rinv, r;
      ln_rows(xr4, wr4, h, m, den, rinv, r);
      const int rw = tid >> 5;
      const unsigned rb = __float_as_uint(r) >> 16, ord = rb ^ ((rb & 0x8000u) ? 0xFFFFu : 0x8000u), nm = pk2(-m, -m);
      if (r31 == 0) { *(u32x4*)(smem + X_CSP + 512 + rw * 16) = u32x4{nm, nm, nm, nm};
        *(f32x2*)(smem + X_CSP + 640 + rw * 8) = f32x2{m, den}; *(float*)(smem + X_CSP + 704 + rw * 4) = rinv; *(u16*)(smem + X_CSP + 736 + rw * 2) = (u16)(__float_as_uint(den) >> 16);
        *(unsigned*)(smem + X_CSP + 768 + rw * 4) = (ord << 11) | (unsigned)(2047 - (id * 8 + rw)); }
      SB0();
    }
    __builtin_amdgcn_s_barrier();
    static_for<20>([&](auto ic) { rd(ic, IC(0)); });
    static_for<30>([&](auto stc) {
      constexpr int S = decltype(stc)::value, s = S & 1;
      constexpr bool bnd = (S & 1) == 1;
      static_for<24>([&](auto mc) {
        constexpr int m = decltype(mc)::value, j = m >> 2, t = (j & 1) ? 3 - (m & 3) : m & 3;
        (void)&tm; (void)&k0; (void)&k1; (void)&tk; (void)&ea; (void)&ew; (void)&oc2; (void)&opay; (void)&oflag; (void)&mk_all; (void)&mk_pay; (void)&mk_one; (void)&rs;
        (void)&tmofs; (void)&kofs; (void)&tgofs; (void)&csw; (void)&csa; (void)&eaofs; (void)&ewofs; (void)&riofs;
        if constexpr (m == 0) asm volatile("s_waitcnt lgkmcnt(%0)" :: "n"(bnd ? 0 : CSS(S) ? 14 : 10));
        else if constexpr (!bnd && m == 4) asm volatile("s_waitcnt lgkmcnt(%0)" :: "n"(CSS(S) ? 7 : 3));
        else if constexpr (!bnd && m == 12 && CSS(S)) asm volatile("s_waitcnt lgkmcnt(11)");
        SB0();
        const bf16x8 wf = cat8(wl[s][j], wh_[s][j]);
        if constexpr (j < 4) acc[t][j] = MFMA16(wf, af[s][t], acc[t][j]);
        else acc[t][j] = MFMA16(af[s][t], wf, acc[t][j]);
        SB0();
        if constexpr (S * 24 + m == 351) STUB(6);
        if constexpr (S * 24 + m == 652) STUB(7);
        if constexpr (m == 1) {
          if constexpr (S == 1) { const u32x4 sv = {SENT, SENT, SENT, SENT}; MST("buffer_store_dwordx4", sv, oc2, mk_all); }
          if constexpr (S == 11) { asm volatile("" : "+v"(tm), "+v"(k0), "+v"(k1), "+v"(tk)); MST("buffer_store_dwordx4", tm, opay, mk_pay); }
          if constexpr (S == F1_S) { WAIT_V(0); MST("buffer_store_dwordx2", tk, oflag, mk_one); }
          if constexpr (S == 3) kvsetup();
          if constexpr (S == 5) { (void)&cw; VISIT(6, 1); VISIT(7, 2); VISIT(10, 3); VISIT(9, 0); }
          if constexpr (bnd) __builtin_amdgcn_s_barrier();
        }
        if constexpr (m >= 1 && m - 1 < (CSS(S + 1) ? 20 : 16)) rd(IC(m - 1), IC(S + 1));
        if constexpr (S == 8 && m == 18) {
          asm volatile("" : "+v"(csa[0]), "+v"(csa[1]));
          const float c = blk ? csa[1][0] : csa[0][0];
          const unsigned u = pk2(c, c - rbf(c));
          asm volatile("ds_write_b32 %0, %1" :: "v"(csw), "v"(u) : "memory");
        }
        if constexpr (S == 9 && m >= 18 && m < 22) {
          if constexpr (m == 18) DS_B128(tm, tmofs, 0);
          if constexpr (m == 19) DS_B128(k0, kofs, 0);
          if constexpr (m == 20) DS_B128(k1, kofs, 16);
          if constexpr (m == 21) asm volatile("ds_read_b64 %0, %1" : "=v"(tk) : "v"(tgofs));
        }
        if constexpr (S == 11 && m == 18) { k0 = __builtin_elementwise_max(k0, k1); asm volatile("" : "+v"(k0)); }
        if constexpr (S == 11 && m == 20) { tk[0] = (tk[0] & ~0x7FFFFFFu) | max(max(k0[0], k0[1]), max(k0[2], k0[3])); asm volatile("" : "+v"(tk)); }
#define DS_B64(dst, addr, off) asm volatile("ds_read_b64 %0, %1 offset:%2" : "=v"(dst) : "v"(addr), "n"(off))
#define DS_B32(dst, addr, off) asm volatile("ds_read_b32 %0, %1 offset:%2" : "=v"(dst) : "v"(addr), "n"(off))
#define DS_2(dst, addr, o0, o1) asm volatile("ds_read2_b32 %0, %1 offset0:%2 offset1:%3" : "=v"(dst) : "v"(addr), "n"(o0), "n"(o1))
        SB0();
      });
      if constexpr (CSS(S)) {
        SB0();
        csa[0] = MFMA16(ones, cat8(cl[s][0], ch[s][0]), csa[0]); csa[1] = MFMA16(ones, cat8(cl[s][1], ch[s][1]), csa[1]);
        SB0();
      }
    });
    static_for<16>([&](auto ic) {
      constexpr int i = decltype(ic)::value, j = 2 + (i >> 2), t = (j & 1) ? 3 - (i & 3) : i & 3;
      (void)&ea; (void)&ew; (void)&eaofs; (void)&ewofs;
      if constexpr (i == 0) asm volatile("s_waitcnt lgkmcnt(6)");
      else if constexpr (i == 4) asm volatile("s_waitcnt lgkmcnt(8)");
      SB0();
      const bf16x8 wf = cat8(wl[0][j], wh_[0][j]);
      if constexpr (j < 4) acc[t][j] = MFMA16(wf, af[0][t], acc[t][j]); else acc[t][j] = MFMA16(af[0][t], wf, acc[t][j]);
      SB0();
      if constexpr (i < 8) { rd(IC(2 * i), IC(31)); rd(IC(2 * i + 1), IC(31)); }
      else if constexpr (i < 12) DS_B64(ea[i - 8], eaofs, (i - 8) * 256);
      else if constexpr (i < 14) { const unsigned a_ = ewofs + 256; DS_2(ew[i - 10], a_, (i - 12) * 16, (i - 12) * 16 + 192); }
      SB0();
    });
    {
      __builtin_amdgcn_s_setprio(PRIO_T);
      asm volatile("s_waitcnt lgkmcnt(0)" : "+v"(af[1][0]), "+v"(af[1][1]), "+v"(af[1][2]), "+v"(af[1][3]), "+v"(wl[1][0]), "+v"(wh_[1][0]), "+v"(wl[1][1]), "+v"(wh_[1][1]),
                   "+v"(wl[1][2]), "+v"(wh_[1][2]), "+v"(wl[1][3]), "+v"(wh_[1][3]), "+v"(wl[1][4]), "+v"(wh_[1][4]), "+v"(wl[1][5]), "+v"(wh_[1][5]));
      asm volatile("" : "+v"(ea[0]), "+v"(ea[1]), "+v"(ea[2]), "+v"(ea[3]), "+v"(ew[2]), "+v"(ew[3]));
      SB0();
      auto mm = [&](int t, int j) { const bf16x8 wf = cat8(wl[1][j], wh_[1][j]); if (j < 4) acc[t][j] = MFMA16(wf, af[1][t], acc[t][j]); else acc[t][j] = MFMA16(af[1][t], wf, acc[t][j]); };
      auto ff = [&](int t, int j) { if (j < 4) acc[t][j] = MFMA16H(ew[j], ea[t], acc[t][j]); else acc[t][j] = MFMA16H(ea[t], ew[j], acc[t][j]); };
      auto dm = [&](int n) {
        SB0();
        const int t = n & 3, j = (n < 16 ? 4 : 0) + ((n >> 2) & 1);
        if (n & 8) ff(t, j); else mm(t, j);
        SB0();
      };
      const unsigned ew0 = ewofs, ew2 = ewofs + 512;
      mm(2, 2); SB0(); DS_B32(rt, riofs, 0); SB0();
      mm(3, 2); SB0();
      mm(2, 3); SB0(); DS_2(ew[4], ew2, 0, 192); DS_2(ew[5], ew2, 16, 208); SB0();
      mm(3, 3); SB0(); DS_2(ew[0], ew0, 0, 192); DS_2(ew[1], ew0, 16, 208); SB0();
      ff(2, 2); ff(3, 2); SB0(); wpload(); SB0();
      ff(2, 3); ff(3, 3); SB0(); SB0();
      mm(0, 2); mm(1, 2);
      SB0();
      SB0();
      char* ho = smem + HANDOFF + cw * 6144 + l * 16;
      *(u32x4*)(ho + 2048) = pack_u(acc[2][2], acc[3][2]);
      SB0(); mm(0, 3); mm(1, 3); SB0();
      *(u32x4*)(ho + 3072) = pack_u(acc[2][3], acc[3][3]);
      SB0(); ff(0, 2); ff(1, 2); ff(0, 3); ff(1, 3); SB0();
      RAW_BARRIER();
      asm volatile("" : "+v"(rt), "+v"(ew[0]), "+v"(ew[1]), "+v"(ew[4]), "+v"(ew[5]));
      dm(0); dm(1);
      fr[2] = __builtin_bit_cast(bf16x8, pack_u(acc[0][2], acc[1][2]));
      dm(2); dm(3);
      fr[3] = __builtin_bit_cast(bf16x8, pack_u(acc[0][3], acc[1][3]));
      dm(4);
      f32x2 ep[8], en[8];
      phi_fn(wp, fr[2], fr[3], ep, en, rt, rt, [&](auto kc) { constexpr int k = decltype(kc)::value; if constexpr (k == 0) { dm(5); dm(6); dm(7); } else if constexpr (7 + k < 16) dm(7 + k); });
    { const unsigned a_ = lbase + X_ST + 512 + (l >> 4) * 8;
      static_for<4>([&](auto tc) { constexpr int tb = decltype(tc)::value; (void)&dn; (void)&a_;
        asm volatile("ds_read2_b64 %0, %1 offset0:%2 offset1:%3" : "=v"(dn[tb]) : "v"(a_), "n"(tb * 8), "n"(tb * 8 + 4)); }); }
#pragma unroll
      for (int g = 0; g < 4; ++g) imgw(g, ep, en);
#pragma unroll
      for (int u = 0; u < 4; ++u) {
        const int t2 = u >> 1, db = u & 1;
        const f32x4 x0 = acc[2 * t2][4 + db], x1 = acc[2 * t2 + 1][4 + db];
        char* pv = t2 ? ho + 4096 : ho;
        *(u32x4*)(pv + db * 1024) = u32x4{pk2(x0[0], x0[1]), pk2(x0[2], x0[3]), pk2(x1[0], x1[1]), pk2(x1[2], x1[3])};
      }
#pragma unroll
      for (int t = 0; t < 4; ++t) { qa[t][0] = acc[t][0]; qa[t][1] = acc[t][1]; wr_[t] = __builtin_bit_cast(u32x4, af[1][t]); wr_[9 + t] = __builtin_bit_cast(u32x4, af[0][t]); }
      wr_[13] = __builtin_bit_cast(u32x4, cat8(wl[0][0], wh_[0][0])); wr_[14] = __builtin_bit_cast(u32x4, cat8(wl[0][1], wh_[0][1]));
      wr_[4] = __builtin_bit_cast(u32x4, cat8(wl[1][0], wh_[1][0])); wr_[5] = __builtin_bit_cast(u32x4, cat8(wl[1][1], wh_[1][1]));
      wr_[6] = __builtin_bit_cast(u32x4, cat8(ea[0], ea[1])); wr_[7] = __builtin_bit_cast(u32x4, cat8(ea[2], ea[3])); wr_[8] = __builtin_bit_cast(u32x4, cat8(ew[0], ew[1]));
    }
  } else {
    const int t = tid - 256;
    __builtin_amdgcn_s_setprio(PRIO_H);
    u64 epoch;
    asm volatile("s_load_dwordx2 %0, %1, 0x0" : "=&s"(epoch) : "s"(ws) : "memory");
    stage(0, IC(0));
    asm volatile("s_waitcnt lgkmcnt(0)" : "+s"(pfd), "+s"(epoch) :: "memory");
    const u64 enx = epoch + (((unsigned)epoch & 0xFEu) == 0xFEu ? 3u - ((unsigned)epoch & 1u) : 1u);
    const u64 tag = TAG_OF(enx);
    if (t == 0) { *(u32x2*)(smem + X_AM) = u32x2{0u, 0u};
      const unsigned e2 = ((unsigned)enx & 0x55u) | (((unsigned)enx & 0xAAu) << 15);
      *(u32x4*)(smem + X_AM + 32) = u32x4{e2, e2 >> 2, e2 >> 4, e2 >> 6} & 0x00010001u;
      *(u32x4*)(smem + X_AM + 48) = u32x4{(unsigned)tag, (unsigned)(tag >> 32), (unsigned)enx, (unsigned)(enx >> 32)}; }
    u32x2 fl, si; unsigned pfd2, tpace = 0, xs;
    unsigned ofl = WS_F1 + (unsigned)t * 16, orA = WS_CS + (unsigned)(hp * 6144 + cw * 1024 + l * 16);
    unsigned orB = (cw < 2 ? WS_CS + (unsigned)(hp * 6144 + 4096 + cw * 1024) : WS_XA + (unsigned)(panel * 2048 + (cw - 2) * 1024)) + l * 16;
    unsigned orC = ((cw & 1) ? WS_STAT + (unsigned)p3 * 1024 : WS_RI + (unsigned)panel * 1024) + l * 16;
    unsigned oxs = (unsigned)(gkq * 256 + cw * 64 + r31 * 2) * 2, akey = lbase + X_AM + 4, vol = (unsigned)l * 16;
    unsigned voG = (unsigned)((8 * cw + srow) * FF + ((sch ^ ((((srow >> 1) & 1) << 2) | ((cw & 1) << 1))) * 8)) * 2;
    const u16* gg_ = w_gate + (size_t)gkq * 256 * FF + ((gr + 1) % 56) * 64;
    const u16* gu_ = w_up + (size_t)gkq * 256 * FF + ((gr + 3) % 56) * 64;
    const u16* gwo = wo + (size_t)(((xcd >> 1) * 4 + (jj >> 3)) * 64 + 2 * cw * 8) * DM + ((xcd & 1) * 8 + (jj & 7)) * 64;
    const u16* gwp = wphi + hp * 2048 + cw * 512;
    const u16* gdw = w_down + (size_t)(((gr >> 3) & 31) * 128 + 32 * cw) * DM + ((gr + 1) & 7) * 128;
    unsigned odw = (unsigned)((l >> 4) * DM + (l & 15) * 8) * 2;
    const u16* gw2a = wo + (size_t)(((cw * 10) ^ (flip * 64)) * 8) * DM + nb * 64;
    asm volatile("" : "+s"(gw2a));
    asm volatile("" : "+s"(gg_), "+s"(gu_), "+s"(gwo), "+s"(gwp), "+s"(gdw));
    { const int f = (gr >> 3) * 128 + 32 * cw + 4 * (l & 7), fs = (f >> 6) - ((l & 8) ? 3 : 1);
      ogq = WS_GUP + (unsigned)((l >> 4) * 56 + (fs < 0 ? fs + 56 : fs)) * 512 + (f & 63) * 4 + (l & 8) * 32; }
    asm volatile("" : "+v"(voG), "+v"(odw), "+v"(ogq));
    asm volatile("s_waitcnt lgkmcnt(0)" : "+v"(ofl), "+v"(orA), "+v"(orB), "+v"(orC), "+v"(oxs), "+v"(akey), "+v"(vol) :: "memory");
    stage(1, IC(1));
    WAIT_V(10);
    __builtin_amdgcn_s_barrier();
    auto wopf = [&]() {
      GLDS_S(voO, gwo, lbase + X_WO + 2 * cw * 1024); GLDS_S(voO, gwo + 8 * DM, lbase + X_WO + (2 * cw + 1) * 1024);
      GLDS_S(vol, gwp, lbase + X_WP + cw * 1024);
    };
    auto dvl = [&](auto kc) {
      constexpr int k = decltype(kc)::value;
      static_for<DVN[k + 2]>([&](auto ic) {
        constexpr int i = dv_first(k) + decltype(ic)::value;
        u32x4& d_ = dv[i]; const unsigned o_ = odw; const u16* p_ = gdw + (size_t)(4 * i) * DM;
        asm volatile("global_load_dwordx4 %0, %1, %2 nt" : "=v"(d_) : "v"(o_), "s"(p_) : "memory");
      });
    };
    stage(2, IC(2));
    const unsigned lgu = lbase + cw * 1024;
    auto gul = [&](auto ksc) {
      constexpr int ks = decltype(ksc)::value;
      gl_nt<gu_at(ks)>(voG, gg_ + (size_t)ks * 32 * FF, lgu); gl_nt<gu_at(ks) + 4096>(voG, gu_ + (size_t)ks * 32 * FF, lgu);
    };
#define BLD64(dst, off) asm volatile("buffer_load_dwordx2 %0, %1, %2, 0 offen sc1" : "=v"(dst) : "v"(off), "s"(rs) : "memory")
    static_for<15>([&](auto ktc) {
      constexpr int kt = decltype(ktc)::value;
      constexpr auto exw = [](int k) { return DVN[k + 2]; };
      constexpr auto exo = [](int k) { return k == KP ? 1 : k == KC + 1 ? 3 : k == KC + 2 ? 2 : 0; };
      constexpr int exa = ((kt - 2 == WOK || kt - 1 == WOK) ? 3 : 0) + (kt - 2 > KC ? exo(kt - 2) : 0);
      if constexpr (exw(kt - 2) + exw(kt - 1) != 0) { if (has_gu) WAIT_V(10 + exa + exw(kt - 2) + exw(kt - 1) + exo(kt - 1)); else WAIT_V(10 + exa + exo(kt - 1)); }
      else if constexpr (kt < 14) WAIT_V(10 + exa + exo(kt - 1));
      else { if (has_gu) WAIT_V(10); else WAIT_V(0); }
      if constexpr (PACE_T[kt] != 0) { if (cw == 0) { while ((unsigned)__builtin_amdgcn_s_memrealtime() - tpace < (unsigned)(has_gu ? PACE_T[kt] : PACE_3[kt])) {} } }
      __builtin_amdgcn_s_barrier();
      if constexpr (PACE_T[kt + 1] != 0) { if (cw == 0) tpace = (unsigned)__builtin_amdgcn_s_memrealtime(); SB0(); }
#define UMAX_DPP(C) { const unsigned o = (unsigned)__builtin_amdgcn_update_dpp(0, (int)key, C, 0xf, 0xf, true); key = o > key ? o : key; asm volatile("" : "+v"(key)); }
      if constexpr (kt + NST1 < 16) { SB0(); stage(kt + NST1, IC(kt % NST1)); SB0(); }
      if constexpr (kt == WOK) wopf();
      if constexpr (kt == PF2_KT) CODE_PF(2, ".Lkf_pf_mid", pfd2);
      if constexpr (kt == PF2_KT + 2) PF_DONE(pfd2);
      if constexpr (kt == KP) BLD64(fl, ofl);
      if constexpr (kt == KC) {
        static_assert(DVN[KP + 2] == 0, "count");
        if constexpr (KC == KP + 1) asm volatile("s_waitcnt vmcnt(10)" : "+v"(fl) :: "memory"); else asm volatile("" : "+v"(fl) :: "memory");
#define FL_OK() __all((((fl[0] ^ (unsigned)tag) >> 27) | (fl[1] ^ (unsigned)(tag >> 32))) == 0u)
        if (__builtin_expect(!FL_OK(), 0)) {
          for (unsigned sp = 0;;) {
            asm volatile("buffer_load_dwordx2 %0, %1, %2, 0 offen sc1\n\ts_waitcnt vmcnt(0)" : "=v"(fl) : "v"(ofl), "s"(rs) : "memory");
            if (FL_OK()) break;
            SPIN_NEXT(sp);
          }
        }
        unsigned key = fl[0];
        UMAX_DPP(0x121) UMAX_DPP(0x122) UMAX_DPP(0x124) UMAX_DPP(0x128)
        asm volatile("s_mov_b64 exec, %2\n\tds_max_u32 %0, %1\n\ts_mov_b64 exec, -1\n\ts_waitcnt lgkmcnt(0)" :: "v"(akey), "v"(key), "s"(0x0001000100010001ull) : "memory");
      }
      if constexpr (kt == KC + 1) { GLDS_SC1(orA, ws, lbase + X_XW + cw * 1024); GLDS_SC1(orB, ws, lbase + X_XW + 4096 + cw * 1024); GLDS_SC1(orC, ws, lbase + X_ST + (cw & 1) * 1024); }
      if constexpr (kt == KC + 2) {
        const int bi = 2047 - (int)(__builtin_amdgcn_readfirstlane(*(const unsigned*)(smem + X_AM + 4)) & 2047u);
        asm volatile("global_load_dword %0, %1, %2" : "=v"(xs) : "v"(oxs), "s"(x + (size_t)bi * DM) : "memory");
        asm volatile("buffer_load_dwordx2 %0, off, %1, %2 sc1" : "=v"(si) : "s"(rs), "s"(WS_STAT + (unsigned)bi * 8) : "memory");
      }
      if constexpr (DVN[kt + 2] != 0) { if (has_gu) dvl(ktc); }
      if constexpr (kt == 0) kvsetup();
      if constexpr (kt == 13) { if (has_gu) { gul(IC(0)); gul(IC(1)); gul(IC(2)); gul(IC(3)); gul(IC(4)); } }
      if constexpr (kt == 14) asm volatile("" : "+v"(xs), "+v"(si) :: "memory");
      if constexpr (kt == 14) { if (has_gu) { static_for<1>([&](auto ic) { gul(IC(5 + decltype(ic)::value)); }); } }
    });
    auto wld = [&](auto ic) {
      constexpr int i = decltype(ic)::value;
      u32x4& d_ = wr_[i]; const unsigned o_ = voO; const u16* p_ = gw2a + (size_t)(i * 8) * DM;
      asm volatile("global_load_dwordx4 %0, %1, %2" : "=v"(d_) : "v"(o_), "s"(p_) : "memory");
    };
    wpload();
    rt = own(*(const float*)(smem + X_ST + (wt * 32 + r31) * 4));
    {
      const float m = __uint_as_float(si[0]), den = own(__uint_as_float(si[1])), rinv = rcp_nr(den);
      *(unsigned*)(smem + X_XS + cw * 128 + r31 * 4) = ln_pair(xs, m, den, rinv);
    }
    RAW_BARRIER();
    const char* ho = smem + HANDOFF + cw * 6144 + l * 16;
    fr[2] = *(const bf16x8*)(ho + 2048); fr[3] = *(const bf16x8*)(ho + 3072);
    f32x2 ep[8], en[8];
    phi_fn(wp, fr[2], fr[3], ep, en, rt, rt, [&](auto kc) {
      constexpr int k = decltype(kc)::value;
      if constexpr (k == 0) { SB0(); if (has_gu) gul(IC(6)); SB0(); }
      if constexpr (k == 5) { SB0(); if (has_gu) gul(IC(7)); SB0(); }
      static_for<NREG - WPOST>([&](auto ic) {
        constexpr int i = decltype(ic)::value;
        if constexpr (k == WH0 + (i * (17 - WH0)) / (NREG - WPOST)) { SB0(); wld(ic); SB0(); }
      });
    });
    { const unsigned a_ = lbase + X_ST + 512 + (l >> 4) * 8;
      static_for<4>([&](auto tc) { constexpr int tb = decltype(tc)::value; (void)&dn; (void)&a_;
        asm volatile("ds_read2_b64 %0, %1 offset0:%2 offset1:%3" : "=v"(dn[tb]) : "v"(a_), "n"(tb * 8), "n"(tb * 8 + 4)); }); }
#pragma unroll
    for (int g = 0; g < 4; ++g) imgw(g, ep, en);
    SB0();
    static_for<WPOST>([&](auto ic) { wld(IC(NREG - WPOST + decltype(ic)::value)); });
  }
  asm volatile(".Lkf_pf_mid:");
  f32x16 zero;
#pragma unroll
  for (int i = 0; i < 16; ++i) zero[i] = 0.f;
  u32x2 tg; unsigned t8, rkey; u32x4 t2;
  {
    __builtin_amdgcn_s_setprio(2);
    RAW_BARRIER();
    asm volatile("" : "+v"(dn[0]), "+v"(dn[1]), "+v"(dn[2]), "+v"(dn[3]));
    u32x4 am; unsigned rk;
    { const unsigned a_ = lbase + X_AM; DS_B128(am, a_, 48); DS_B128(t2, a_, 32); asm volatile("ds_read_b32 %0, %1 offset:4" : "=v"(rk) : "v"(a_)); }
    s16x4 al[4], ah[4]; bf16x8 v0[4], v1[4];
    DS_TR(al[0], pa0, 0); DS_TR(ah[0], pa0, 2048); DS_B128(v0[0], pvh, 0); DS_B128(v1[0], pvh, 1024);
    DS_TR(al[1], pa0, 4096); DS_TR(ah[1], pa0, 4096 + 2048); DS_B128(v0[1], pvh, 4096); DS_B128(v1[1], pvh, 4096 + 1024);
    DS_TR(al[2], pa0, 8192); DS_TR(ah[2], pa0, 8192 + 2048); DS_B128(v0[2], pvh, 6144); DS_B128(v1[2], pvh, 6144 + 1024);
    DS_TR(al[3], pa0, 12288); DS_TR(ah[3], pa0, 12288 + 2048); DS_B128(v0[3], pvh, 6144 + 4096); DS_B128(v1[3], pvh, 6144 + 4096 + 1024);
    f32x4 k0 = {0.f, 0.f, 0.f, 0.f}, k1 = k0, zz = k0;
    static_for<4>([&](auto tbc) {
      constexpr int tb = decltype(tbc)::value;
      asm volatile("s_waitcnt lgkmcnt(%4)" : "+v"(al[tb]), "+v"(ah[tb]), "+v"(v0[tb]), "+v"(v1[tb]) : "n"(12 - 4 * tb));
      const bf16x8 af = cat8(al[tb], ah[tb]);
      k0 = MFMA16(af, v0[tb], k0); k1 = MFMA16(af, v1[tb], k1); zz = MFMA16(af, dn[tb], zz);
      SB0();
    });
    asm volatile("" : "+v"(am), "+v"(rk), "+v"(t2));
    auto tagv = [&](f32x4 v) { const u32x4 u = __builtin_bit_cast(u32x4, v); return (u & 0xFFFFFF00u) | (am[2] & 0xFFu); };
    ST128((u32x4{pk2(k0[0], k0[1]), pk2(k0[2], k0[3]), pk2(k1[0], k1[1]), pk2(k1[2], k1[3])} & 0xFFFEFFFEu) | t2, dst);
    const u32x4 zt = tagv(zz);
    asm volatile("s_mov_b64 exec, %3\n\tbuffer_store_dwordx4 %0, %1, %2, 0 offen sc1\n\ts_mov_b64 exec, -1" :: "v"(zt), "v"(dz), "s"(rs), "s"(0x0001000100010001ull) : "memory");
    SB0();
    tg = u32x2{am[0], am[1]}; t8 = __builtin_amdgcn_readfirstlane(am[2]) & 0xFFu; rkey = __builtin_amdgcn_readfirstlane(rk);
  }
  const int idx = 2047 - (int)(rkey & 2047u);
  const int fb = kh ^ ((idx >> 4) & 1);
  const unsigned lxc = lbase + (cw < 2 ? 131072 : X_YS + 64 - 4096) + cw * 2048, lxv = w < 4 ? lxc : lbase + X_XW + cw * 2048;
  {
    bf16x8 xf[8];
    const unsigned px = lbase + X_XS + (l >> 4) * 16, hq = lbase + X_XW + cw * 2048 + l * 16;
    static_for<8>([&](auto kc) { constexpr int ks = decltype(kc)::value; (void)&xf; (void)&px; DS_B128(xf[ks], px, ks * 64); });
    if (w >= 4) {
      __builtin_amdgcn_s_setprio(0);
      WAIT_V(NREG + 2);
    } else {
      auto lo4 = [](u32x4 v) { return __builtin_bit_cast(s16x4, u32x2{v[0], v[1]}); };
      auto hi4 = [](u32x4 v) { return __builtin_bit_cast(s16x4, u32x2{v[2], v[3]}); };
#pragma unroll
      for (int tt = 0; tt < 4; ++tt) {
        const int t = tt ^ 2;
#pragma unroll
        for (int j = 0; j < 2; ++j) qa[t][j] = MFMA16(__builtin_bit_cast(bf16x8, wr_[13 + j]), __builtin_bit_cast(bf16x8, wr_[9 + t]), qa[t][j]);
#pragma unroll
        for (int j = 0; j < 2; ++j) qa[t][j] = MFMA16(__builtin_bit_cast(bf16x8, wr_[4 + j]), __builtin_bit_cast(bf16x8, wr_[t]), qa[t][j]);
        if (tt & 1) {
#pragma unroll
          for (int u = t - 1; u <= t; ++u)
#pragma unroll
            for (int j = 0; j < 2; ++j) qa[u][j] = MFMA16H(j ? hi4(wr_[8]) : lo4(wr_[8]), (u & 1) ? hi4(wr_[6 + (u >> 1)]) : lo4(wr_[6 + (u >> 1)]), qa[u][j]);
        }
      }
      const u32x4 q0 = pack_u(qa[2][0], qa[3][0]), q1 = pack_u(qa[2][1], qa[3][1]);
      asm volatile("ds_write_b128 %0, %1\n\tds_write_b128 %0, %2 offset:1024" :: "v"(hq), "v"(q0), "v"(q1) : "memory");
    }
    RAW_BARRIER();
    asm volatile("" : "+v"(xf[0]), "+v"(xf[1]), "+v"(xf[2]), "+v"(xf[3]), "+v"(xf[4]), "+v"(xf[5]), "+v"(xf[6]), "+v"(xf[7]));
    if (w >= 4) {
      { const u16* ga = wo + (size_t)(((cw * 10) ^ (flip * 64)) * 8) * DM + nb * 64; const u16* gb = wo + (size_t)(((104 + cw * 6) ^ (flip * 64)) * 8) * DM + nb * 64;
        static_for<16 - NREG>([&](auto ic) { constexpr int i = NREG + decltype(ic)::value;
          if constexpr (i < 10) gl_m0<i * 1024>(voO, ga + (size_t)(i * 8) * DM, lbase + cw * 10240); else gl_m0<(104 + i - 10) * 1024>(voO, gb + (size_t)((i - 10) * 8) * DM, lbase + cw * 6144); }); }
    }
    if (has_gu) {
      s16x4 gl[8], gh[8];
      static_for<8>([&](auto kc) { constexpr int ks = decltype(kc)::value; (void)&gl; (void)&gh; (void)&pgw;
        DS_TR(gl[ks], pgw, ks * 8192); DS_TR(gh[ks], pgw, ks * 8192 + 512); });
      f32x4 c0 = {0.f, 0.f, 0.f, 0.f};
      static_for<8>([&](auto kc) { constexpr int ks = decltype(kc)::value;
        asm volatile("s_waitcnt lgkmcnt(%2)" : "+v"(gl[ks]), "+v"(gh[ks]) : "n"(14 - 2 * ks));
        c0 = MFMA16(cat8(gl[ks], gh[ks]), xf[ks], c0);
        SB0(); });
      const u32x4 gt_ = f2u_tag(c0, t8);
      asm volatile("s_mov_b64 exec, %3\n\tbuffer_store_dwordx4 %0, %1, %2, 0 offen sc1\n\ts_mov_b64 exec, -1" :: "v"(gt_), "v"(ogu), "s"(rs), "s"(0x0001000100010001ull) : "memory");
    }
  }
  const int erow = cw * 32 + fb * 16 + (l & 15), ech = l >> 4;
  u32x4 xv[2];
  const bool sw = ech & 1;
  const bool has_idx = w < 4 && (idx >> 5) == p3 * 4 + w;
  unsigned xrt = 0;
  unsigned of4, op4, oo0, oo1, kba, kbz, odo, o2, oz2, wa_, wz_;
  auto later = [&]() {
    SB0();
    of4 = WS_F4 + (unsigned)(((panel * 4 + wt) * 16 + hp) * 2 + wn) * 32; op4 = WS_F4 + (unsigned)(((p3 * 4 + cw) * 16 + 8 * (kh ^ flip)) * 2 + (l & 15)) * 32;
    oo0 = WS_O + 512 + (unsigned)((((p3 * 4 + cw) * 2 + fb) * 32 + (kh ^ flip) * 16) * 128 + h * 64 + r31) * 16; oo1 = oo0 ^ 65536u;
    kba = lbase + X_KB + wn * 4224 + l * 16; kbz = lbase + X_KB + wn * 4224 + 4096 + h * 64;
    odo = WS_O + 512 + (unsigned)((((panel * 4 + wt) * 2 + blk) * 32 + hd) * 128 + h * 16 + (l & 15)) * 16;
    asm volatile("" : "+v"(kba), "+v"(kbz), "+v"(odo), "+v"(of4), "+v"(op4), "+v"(oo0), "+v"(oo1));
    SB0();
  };
  if (w < 4) {
    unsigned pfd3, od; u64 mkp;
    {
      u32x4 pa[2]; unsigned z0 = t8, z1 = t8;
#define H1LD(p) asm volatile("buffer_load_dwordx4 %0, %4, %6, 0 offen sc1\n\tbuffer_load_dwordx4 %1, %4, %6, %7 offen sc1\n\t" \
                   "s_cmp_lg_u32 %8, 3\n\ts_cbranch_scc1 1f\n\tbuffer_load_dword %2, %5, %6, 0 offen sc1\n\tbuffer_load_dword %3, %5, %6, 0 offen offset:256 sc1\n1:" \
                   : "=&v"(p[0]), "=&v"(p[1]), "+v"(z0), "+v"(z1) : "v"(oh1), "v"(oh1z), "s"(rs), "s"(65536), "s"(cw) : "memory", "scc")
      __builtin_amdgcn_s_sleep(H1_DELAY);
      H1LD(pa);
      SB0();
      CODE_PF(3, ".Lkf_pf_mid", pfd3);
      od = WS_KVF + (unsigned)(hr * 16 + panel) * 640 + ((l & 8) ? 512 + (l & 7) * 2 : cw * 128 + h * 64 + (l & 7) * 8 + blk * 4);
      mkp = cw == 3 ? 0x00FF00FF00FF55FFull : 0x00FF00FF00FF00FFull;
      asm volatile("" : "+s"(mkp));
      fr[0] = __builtin_bit_cast(bf16x8, pack_u(qa[0][0], qa[1][0])); fr[1] = __builtin_bit_cast(bf16x8, pack_u(qa[0][1], qa[1][1]));
      asm volatile("" : "+v"(od), "+v"(fr[0]), "+v"(fr[1]));
      asm volatile("global_load_ushort %0, %1, %2" : "=v"(xrt) : "v"((unsigned)(idx * DM + nb * 64 + 4 * (l & 15) + ech) * 2), "s"(x) : "memory");
      later();
      o2 = WS_KVF + (unsigned)(hp * 16 + wn * 8 + wm * 4 + h) * 640 + ((r31 & 12) * 2 + (r31 >> 4) * 4 + (r31 & 3)) * 16;
      oz2 = WS_KVF + (unsigned)(hp * 16 + wn * 8 + wm * 4 + (l & 3)) * 640 + 512;
      wa_ = kba + wm * 2048; wz_ = lbase + X_KB + wn * 4224 + 4096 + (l & 1) * 64 + (wm * 2 + ((l >> 1) & 1)) * 16;
      asm volatile("" : "+v"(o2), "+v"(oz2), "+v"(wa_), "+v"(wz_));
      unsigned hb = 0x8000u, s_lo = 0x05040100u, s_hi = 0x07060100u;
      asm volatile("" : "+s"(s_lo), "+s"(s_hi));
      asm volatile("s_waitcnt vmcnt(1)" : "+v"(pa[0]), "+v"(pa[1]), "+v"(z0), "+v"(z1), "+v"(hb) :: "memory");
      u64 b0, b1, b2; u32x4 v0, v1;
      auto miss = [&]() __attribute__((always_inline)) {
        v0 = pa[0] ^ t2; v1 = pa[1] ^ t2;
        b0 = __ballot((or4(v0) & 0x00010001u) != 0u); b1 = __ballot((or4(v1) & 0x00010001u) != 0u);
        b2 = __ballot((((z0 ^ t8) | (z1 ^ t8)) & 0xFFu) != 0u);
        return (b0 | b1 | b2) != 0;
      };
      if (miss()) {
        unsigned sp = 0;
        do {
        b0 |= (b0 >> 4) & 0x0F0F0F0F0F0F0F0Full; b0 |= (b0 << 4) & 0xF0F0F0F0F0F0F0F0ull; b1 |= (b1 >> 4) & 0x0F0F0F0F0F0F0F0Full; b1 |= (b1 << 4) & 0xF0F0F0F0F0F0F0F0ull;
        asm volatile("s_wqm_b64 exec, %8\n\ts_cbranch_execz 1f\n\tbuffer_load_dwordx4 %0, %4, %6, 0 offen sc1\n1:\n\ts_wqm_b64 exec, %9\n\ts_cbranch_execz 2f\n\tbuffer_load_dwordx4 %1, %4, %6, %7 offen sc1\n2:\n\t"
                     "s_mov_b64 exec, -1\n\ts_cmp_eq_u64 %10, 0\n\ts_cbranch_scc1 3f\n\tbuffer_load_dword %2, %5, %6, 0 offen sc1\n\tbuffer_load_dword %3, %5, %6, 0 offen offset:256 sc1\n3:\n\ts_waitcnt vmcnt(0)"
                     : "+v"(pa[0]), "+v"(pa[1]), "+v"(z0), "+v"(z1) : "v"(oh1), "v"(oh1z), "s"(rs), "s"(65536), "s"(b0), "s"(b1), "s"(b2) : "memory", "scc");
        } while (miss() && ++sp < SPIN_CAP);
      }
    auto sw32 = [](float a, float c) { const auto r = __builtin_amdgcn_permlane32_swap(__float_as_uint(a), __float_as_uint(c), false, false); return __uint_as_float(r[0]) + __uint_as_float(r[1]); };
    auto sw16 = [](float a, float c) { const auto r = __builtin_amdgcn_permlane16_swap(__float_as_uint(a), __float_as_uint(c), false, false); return __uint_as_float(r[0]) + __uint_as_float(r[1]); };
    float sl[4], sh[4];
#pragma unroll
    for (int m = 0; m < 4; ++m) {
      sl[m] = own(__uint_as_float(__builtin_amdgcn_perm(v0[m], hb, s_lo)) + __uint_as_float(__builtin_amdgcn_perm(v1[m], hb, s_lo)));
      sh[m] = own(__uint_as_float(__builtin_amdgcn_perm(v0[m], hb, s_hi)) + __uint_as_float(__builtin_amdgcn_perm(v1[m], hb, s_hi))); }
    const float e0 = dpp_add<0x128, 0xf>(sw16(sw32(sl[0], sl[2]), sw32(sl[1], sl[3]))), e1 = dpp_add<0x128, 0xf>(sw16(sw32(sh[0], sh[2]), sw32(sh[1], sh[3])));
    const float zs = x32_add(x16_add(dpp_add<0x128, 0xf>(__uint_as_float(z0 ^ t8) + __uint_as_float(z1 ^ t8))));
    const float zn = __builtin_bit_cast(float, __builtin_amdgcn_update_dpp(0, __builtin_bit_cast(int, zs), 0xB1, 0xf, 0xf, true));
    const unsigned kvb = min((l & 8) ? pk2(zs, zn) : pk2(e0, e1), SENT - 1u);
    asm volatile("s_mov_b64 exec, %3\n\tbuffer_store_dword %0, %1, %2, 0 offen sc1\n\ts_mov_b64 exec, -1" :: "v"(kvb), "v"(od), "s"(rs), "s"(mkp) : "memory");
    PF_DONE(pfd3);
    }
  } else {
    const char* hq = smem + X_XW + cw * 2048 + l * 16;
    fr[0] = *(const bf16x8*)hq; fr[1] = *(const bf16x8*)(hq + 1024);
  }
  bf16x8 pf[4];
  {
    f32x2 ep[8], en[8];
    phi_fn(wp, fr[0], fr[1], ep, en, rt, 1.0f, [](auto) {});
#pragma unroll
    for (int s = 0; s < 2; ++s) {
      pf[s] = __builtin_bit_cast(bf16x8, u32x4{pk2(ep[4 * s][0], ep[4 * s][1]), pk2(ep[4 * s + 1][0], ep[4 * s + 1][1]), pk2(ep[4 * s + 2][0], ep[4 * s + 2][1]), pk2(ep[4 * s + 3][0], ep[4 * s + 3][1])});
      pf[2 + s] = __builtin_bit_cast(bf16x8, u32x4{pk2(en[4 * s][0], en[4 * s][1]), pk2(en[4 * s + 1][0], en[4 * s + 1][1]), pk2(en[4 * s + 2][0], en[4 * s + 2][1]), pk2(en[4 * s + 3][0], en[4 * s + 3][1])});
    }
    asm volatile("" : "+v"(pf[0]), "+v"(pf[1]), "+v"(pf[2]), "+v"(pf[3]));
  }
  SB0();
  if (b == 0 && tid == 64) ST64(*(const u32x2*)(smem + X_AM + 56), WS_EPOCH);
  u32x4 gq; unsigned ayr; u32x4 mk4;
#define GQLD(tail) asm volatile("buffer_load_dwordx4 %0, %1, %2, 0 offen sc1" tail : "=v"(gq) : "v"(ogq), "s"(rs) : "memory")
  if (w >= 4) {
    {
      WAIT_V(18 - NREG);
    { const unsigned o_ = (unsigned)((p3 * 128 + cw * 32 + (l >> 3)) * DM + nb * 64 + ((l & 7) ^ (l >> 3)) * 8) * 2; const u16* xa = x + fb * 16 * DM; const u16* xb = x + (fb ^ 1) * 16 * DM;
      gl_m0<0>(o_, xa, lxv); gl_m0<1024>(o_, xa + 8 * DM, lxv); gl_m0<0>(o_, xb, lxc); gl_m0<1024>(o_, xb + 8 * DM, lxc); }
      SB0();
      char* pa_ = smem + cw * 10 * 1024 + l * 16;
#pragma unroll
      for (int i = 0; i < NREG; ++i) *(u32x4*)(pa_ + i * 1024) = wr_[i];
      SB0();
    }
    asm volatile("" : "+v"(dv[0]), "+v"(dv[1]), "+v"(dv[2]), "+v"(dv[3]), "+v"(dv[4]), "+v"(dv[5]), "+v"(dv[6]), "+v"(dv[7]) :: "memory");
    later();
    ayr = lbase + X_YR + (cw * 128 + (l & 15) * 8 + blk * 4) * 4;
    { const unsigned hm = (l & 1) ? 0xFFFF0000u : 0x0000FFFFu; const int q = (l & 15) >> 1;
      mk4 = u32x4{q == 0 ? hm : 0u, q == 1 ? hm : 0u, q == 2 ? hm : 0u, q == 3 ? hm : 0u}; }
    asm volatile("" : "+v"(ayr), "+v"(mk4));
    SB0();
  }
  auto downp = [&]() __attribute__((always_inline)) {
            auto red4 = [](float s0, float s1, float s2, float s3) {
              const auto r01 = __builtin_amdgcn_permlane16_swap(__float_as_uint(s0), __float_as_uint(s1), false, false);
              const auto r23 = __builtin_amdgcn_permlane16_swap(__float_as_uint(s2), __float_as_uint(s3), false, false);
              const float t01 = __uint_as_float(r01[0]) + __uint_as_float(r01[1]), t23 = __uint_as_float(r23[0]) + __uint_as_float(r23[1]);
              const auto r = __builtin_amdgcn_permlane32_swap(__float_as_uint(t01), __float_as_uint(t23), false, false);
              return __uint_as_float(r[0]) + __uint_as_float(r[1]);
            };
            const float gg = rbf(red4(__uint_as_float(gq[0]), __uint_as_float(gq[1]), __uint_as_float(gq[2]), __uint_as_float(gq[3])));
            const float uu = __builtin_bit_cast(float, __builtin_amdgcn_update_dpp(0, __builtin_bit_cast(int, gg), 0x128, 0xf, 0xf, true));
            const float hv0 = rbf(silu_r(gg)) * uu;
            const int hh = (int)pk2(hv0, hv0);
            f32x4 ya = {0.f, 0.f, 0.f, 0.f};
            static_for<8>([&](auto ic) {
              constexpr int i = decltype(ic)::value;
              u32x4 sel;
#pragma unroll
              for (int e = 0; e < 4; ++e) sel[e] = (unsigned)__builtin_amdgcn_update_dpp(0, hh, 0x150 + i, 0xf, 0xf, true) & mk4[e];
              ya = MFMA16(__builtin_bit_cast(bf16x8, sel), __builtin_bit_cast(bf16x8, dv[i]), ya);
            });
            asm volatile("" : "+v"(ya));
            if (l < 32) *(__attribute__((address_space(3))) f32x4*)(uintptr_t)ayr = ya;
  };
  {
    u32x4 kf[4], zf[4];
    if (w < 4) {
      u32x4 a, c, z; u64 m0 = ~0ull, m1 = ~0ull, m2 = ~0ull;
      unsigned sp = 0;
      do {
        asm volatile("s_mov_b64 exec, %8\n\ts_cbranch_execz 1f\n\tbuffer_load_dwordx4 %0, %3, %5, 0 offen sc1\n1:\n\ts_mov_b64 exec, %9\n\ts_cbranch_execz 2f\n\tbuffer_load_dwordx4 %1, %3, %5, 0 offen offset:1280 sc1\n2:\n\t"
                     "s_mov_b64 exec, %10\n\ts_cbranch_execz 3f\n\tbuffer_load_dwordx4 %2, %4, %5, 0 offen sc1\n3:\n\ts_mov_b64 exec, -1\n\ts_waitcnt vmcnt(0)\n\t"
                     "ds_write_b128 %6, %0\n\tds_write_b128 %6, %1 offset:1024\n\tds_write_b128 %7, %2"
                     : "+v"(a), "+v"(c), "+v"(z) : "v"(o2), "v"(oz2), "s"(rs), "v"(wa_), "v"(wz_), "s"(m0), "s"(m1), "s"(m2) : "memory");
        STUB(9);
        m0 = __ballot(max(max(max(a[0], a[1]), a[2]), a[3]) == SENT); m1 = __ballot(max(max(max(c[0], c[1]), c[2]), c[3]) == SENT); m2 = __ballot(max(max(max(z[0], z[1]), z[2]), z[3]) == SENT);
      } while ((m0 | m1 | m2) != 0 && ++sp < SPIN_CAP);
      asm volatile("" : "+v"(xrt));
    }
    RAW_BARRIER();
    DS_B128(kf[0], kba, 0); DS_B128(zf[0], kbz, 0); DS_B128(kf[1], kba, 1024); DS_B128(zf[1], kbz, 16);
    DS_B128(kf[2], kba, 2048); DS_B128(zf[2], kbz, 32); DS_B128(kf[3], kba, 3072); DS_B128(zf[3], kbz, 48);
    if (w >= 4 && has_gu) GQLD("");
    __builtin_amdgcn_s_setprio(0);
    f32x16 num = zero;
    float d0 = 0.f, d1 = 0.f, d2 = 0.f, d3 = 0.f;
    static_for<4>([&](auto cc) {
      constexpr int c = decltype(cc)::value;
      asm volatile("s_waitcnt lgkmcnt(%0)" :: "n"(6 - 2 * c));
      SB0();
      num = MFMA(__builtin_bit_cast(bf16x8, kf[c]), pf[c], num);
      SB0();
      const u32x4 pc = __builtin_bit_cast(u32x4, pf[c]);
      d0 = dot2(zf[c][0], pc[0], d0); d1 = dot2(zf[c][1], pc[1], d1); d2 = dot2(zf[c][2], pc[2], d2); d3 = dot2(zf[c][3], pc[3], d3);
      SB0();
    });
    const float rinv = __builtin_amdgcn_rcpf(x32_add(own(d0 + d1) + own(d2 + d3)) + 1e-6f);
    const f32x2 rr = {rinv, rinv};
#pragma unroll
    for (int q = 0; q < 2; ++q) {
      u32x4 u;
#pragma unroll
      for (int e = 0; e < 4; ++e) {
        const f32x2 r = f32x2{num[8 * q + 2 * e], num[8 * q + 2 * e + 1]} * rr;
        u[e] = pk2(r[0], r[1]);
      }
      ST128(u, odo + q * 1024);
      SB0();
    }
  }
  const int p0 = 8 * w + 40;
  const u16* gw2 = wo + (size_t)((p0 ^ (flip * 64)) * 8) * DM + nb * 64; unsigned lw2 = lbase + p0 * 1024;
  asm volatile("" : "+s"(gw2), "+s"(lw2));
  bool glate = false;
  if (w >= 4 && has_gu) {
    asm volatile("s_waitcnt vmcnt(2)" : "+v"(gq) :: "memory");
    gq ^= t8;
    glate = !__all((or4(gq) & 0xFFu) == 0u);
    if (!glate) downp();
  }
  asm volatile("s_waitcnt vmcnt(0)\n\ts_mov_b64 exec, 1\n\tbuffer_store_dwordx2 %0, %1, %2, 0 offen sc1\n\ts_mov_b64 exec, -1" :: "v"(tg), "v"(of4), "s"(rs) : "memory");
  if (__builtin_expect(glate, 0)) {
    for (unsigned sp = 0;;) {
      GQLD("\n\ts_waitcnt vmcnt(0)");
      gq ^= t8;
      if (__all((or4(gq) & 0xFFu) == 0u)) break;
      SPIN_NEXT(sp);
    }
    downp();
  }
  RAW_BARRIER();
  float rxn = 0.f, rsg = 0.f;
  u32x4 yq[7];
  unsigned oy = WS_YP + (unsigned)(l >> 4) * 4096 + nb * 256 + (l & 15) * 16;
#define YLD(tail) asm volatile("buffer_load_dwordx4 %0, %7, %8, 0 offen sc1\n\tbuffer_load_dwordx4 %1, %7, %8, %9 offen sc1\n\tbuffer_load_dwordx4 %2, %7, %8, %10 offen sc1\n\t" \
    "buffer_load_dwordx4 %3, %7, %8, %11 offen sc1\n\tbuffer_load_dwordx4 %4, %7, %8, %12 offen sc1\n\tbuffer_load_dwordx4 %5, %7, %8, %13 offen sc1\n\t" \
    "buffer_load_dwordx4 %6, %7, %8, %14 offen sc1" tail \
    : "=&v"(yq[0]), "=&v"(yq[1]), "=&v"(yq[2]), "=&v"(yq[3]), "=&v"(yq[4]), "=&v"(yq[5]), "=&v"(yq[6]) \
    : "v"(oy), "s"(rs), "s"(16384), "s"(2 * 16384), "s"(3 * 16384), "s"(4 * 16384), "s"(5 * 16384), "s"(6 * 16384) : "memory")
  f32x2 xf[2][4], gf[2][4];
  __attribute__((address_space(3))) f32x4* pw = (__attribute__((address_space(3))) f32x4*)(smem + 131072 + w * 4096) + l;
  unsigned prd = lbase + 131072 + (w ^ 4) * 4096 + l * 16;
  u16* po = out + (size_t)(p3 * 128 + cw * 32 + fb * 16 + (l >> 3)) * DM + nb * 64 + (l & 7) * 8; u16* po2 = po + 8 * DM;
  unsigned tw0 = lbase + 131072 + (w ^ 4) * 4096 + (l & 15) * 128 + ((ech ^ (l & 7)) * 16) + (sw ? 8 : 0), tw1 = tw0 ^ 64, tw2 = tw0 ^ 8, tw3 = tw0 ^ 72, trd = lbase + 131072 + (w ^ 4) * 4096 + (l >> 3) * 128 + (((l & 7) ^ (l >> 3)) * 16);
    unsigned bw[4];
#pragma unroll
    for (int n = 0; n < 4; ++n) bw[n] = lbase + kh * 65536 + (16 * h + 4 * blk + tq) * 128 + (((4 * (n >> 1) + tp) ^ ((tq >> 1) | (blk << 2))) * 16) + 8 * ((n ^ tp) & 1);
  {
  if (w == 1 && has_gu && l < 32) {
    const f32x4* y = (const f32x4*)(smem + X_YR) + l;
    ST128(f2u_tag((y[0] + y[32]) + (y[64] + y[96]), t8), WS_YP + (unsigned)(gr >> 3) * 4096 + ((gr + 1) & 7) * 512 + l * 16);
  }
    SB0();
    gl_run<8>(voO, gw2, lw2);
    u32x2 f0;
    asm volatile("buffer_load_dwordx2 %0, %1, %2, 0 offen sc1" : "=v"(f0) : "v"(op4), "s"(rs) : "memory");
    SB0();
    {
      const f32x2 md = *(const f32x2*)(smem + X_MD + erow * 8);
      { const unsigned ax = lxv + (l & 15) * 128 + ((ech ^ (l & 7)) * 16), a0 = ax + (sw ? 8 : 0), a1 = ax + (sw ? 0 : 8);
        u32x2 q0, q1, q2, q3;
        asm volatile("ds_read_b64 %0, %4\n\tds_read_b64 %1, %5\n\tds_read_b64 %2, %6\n\tds_read_b64 %3, %7\n\ts_waitcnt lgkmcnt(0)" : "=&v"(q0), "=&v"(q1), "=&v"(q2), "=&v"(q3) : "v"(a0), "v"(a1), "v"(a0 ^ 64), "v"(a1 ^ 64) : "memory");
        xv[0] = u32x4{q0[0], q0[1], q1[0], q1[1]}; xv[1] = u32x4{q2[0], q2[1], q3[0], q3[1]}; }
      const float rinv = rcp_nr(own(md[1])), mr = -md[0] * rinv, L2E = 1.44269504088896340736f, rl = -L2E * rinv, ml = -L2E * mr;
#pragma unroll
      for (int i = 0; i < 2; ++i)
#pragma unroll
        for (int e = 0; e < 4; ++e) {
          xf[i][e] = f32x2{bflo(xv[i][e]), bfhi(xv[i][e])};
          const f32x2 xn = __builtin_elementwise_fma(xf[i][e], f32x2{rinv, rinv}, f32x2{mr, mr}), tn = __builtin_elementwise_fma(xf[i][e], f32x2{rl, rl}, f32x2{ml, ml});
          const f32x2 dd = f32x2{__builtin_amdgcn_exp2f(tn[0]), __builtin_amdgcn_exp2f(tn[1])} + f32x2{1.0f, 1.0f};
          gf[i][e] = xn * f32x2{__builtin_amdgcn_rcpf(dd[0]), __builtin_amdgcn_rcpf(dd[1])};
        }
#pragma unroll
      for (int i = 0; i < 2; ++i) asm volatile("" : "+v"(xf[i][0]), "+v"(xf[i][1]), "+v"(xf[i][2]), "+v"(xf[i][3]), "+v"(gf[i][0]), "+v"(gf[i][1]), "+v"(gf[i][2]), "+v"(gf[i][3]));
      asm volatile("" : "+v"(pw), "+v"(prd), "+v"(po2), "+v"(tw0), "+v"(tw1), "+v"(tw2), "+v"(tw3), "+v"(trd));
      asm volatile("" : "+v"(oy), "+v"(po), "+v"(bw[0]), "+v"(bw[1]), "+v"(bw[2]), "+v"(bw[3]));
    }
    if (has_idx) {
      const unsigned ord = (rkey >> 11) & 0xFFFFu;
      rsg = rbf(1.0f / (1.0f + __expf(-__uint_as_float((ord ^ ((ord & 0x8000u) ? 0x8000u : 0xFFFFu)) << 16))));
      const f32x2 md = *(const f32x2*)(smem + X_MD + (idx & 127) * 8);
      const float dn = own(md[1]), rinv = rcp_nr(dn);
      rxn = bflo(ln_pair(xrt, md[0], dn, rinv));
      asm volatile("" : "+v"(rxn), "+v"(rsg));
      { u64 pc_; unsigned d_, o_;
        asm volatile("s_getpc_b64 %0\n.Lkf_own1:\n\ts_mov_b32 %1, .Lkf_own0-.Lkf_own1" : "=s"(pc_), "=s"(o_));
        pc_ += o_;
        asm volatile("s_load_dword %0, %1, 0x0\n\ts_load_dword %0, %1, 0x80\n\ts_load_dword %0, %1, 0x100\n\ts_load_dword %0, %1, 0x180\n\ts_load_dword %0, %1, 0x200\n\ts_load_dword %0, %1, 0x280\n\ts_waitcnt lgkmcnt(0)" : "=&s"(d_) : "s"(pc_) : "memory"); }
    }
    u64 ne;
    SB0();
    asm volatile("s_waitcnt vmcnt(0)\n\tv_cmp_ne_u64 %0, %1, %2" : "=s"(ne) : "v"(f0), "v"(tg) : "memory");
    for (unsigned sp = 0; ne != 0 && sp < SPIN_CAP; ++sp) {
      u32x2 f;
      asm volatile("buffer_load_dwordx2 %1, %2, %3, 0 offen sc1\n\ts_waitcnt vmcnt(0)\n\tv_cmp_ne_u64 %0, %1, %4" : "=s"(ne), "=&v"(f) : "v"(op4), "s"(rs), "v"(tg) : "memory");
    }
  }
  constexpr int OWIN = 4;
  u32x4 of[16][2];
  {
    SB0();
#pragma unroll
    for (int i = 0; i < OWIN; ++i) { of[i][0] = LD128(oo0 + i * 2048); of[i][1] = LD128(oo1 + i * 2048); }
    SB0();
    if (has_idx) YLD("");
    SB0();
  }
    auto owner = [&](auto nc) __attribute__((always_inline)) {
      asm volatile(".Lkf_own0:\n\ts_waitcnt vmcnt(%7)" : "+v"(yq[0]), "+v"(yq[1]), "+v"(yq[2]), "+v"(yq[3]), "+v"(yq[4]), "+v"(yq[5]), "+v"(yq[6]) : "n"(decltype(nc)::value) : "memory");
      for (unsigned sp = 0;; ++sp) {
        unsigned bad = 0;
#pragma unroll
        for (int i = 0; i < 7; ++i) { yq[i] ^= t8; bad |= or4(yq[i]); }
        if (__builtin_expect(__all((bad & 0xFFu) == 0u) || sp > SPIN_CAP, 1)) break;
        YLD("\n\ts_waitcnt vmcnt(0)");
      }
#define YF(i) __builtin_bit_cast(f32x4, yq[i])
      const f32x4 y4 = ((YF(0) + YF(1)) + (YF(2) + YF(3))) + ((YF(4) + YF(5)) + YF(6));
      const auto r01 = __builtin_amdgcn_permlane16_swap(__float_as_uint(y4[0]), __float_as_uint(own(y4[1])), false, false);
      const auto r23 = __builtin_amdgcn_permlane16_swap(__float_as_uint(y4[2]), __float_as_uint(own(y4[3])), false, false);
      const float t01 = __uint_as_float(r01[0]) + __uint_as_float(r01[1]), t23 = __uint_as_float(r23[0]) + __uint_as_float(r23[1]);
      const auto r = __builtin_amdgcn_permlane32_swap(__float_as_uint(t01), __float_as_uint(t23), false, false);
      const float y = __uint_as_float(r[0]) + __uint_as_float(r[1]);
      const float g1 = rbf(silu_r(rbf(rxn + rbf(rbf(y) * rsg))));
      (void)&gf; (void)&oy; (void)&rs; const u64 mk_ = 0x0001000100010001ull << (idx & 15);
      const unsigned gb = lbase + (131072 + w * 4096), aw_ = gb + (l & 15) * 16 + ech * 4, a0 = gb + ech * 32 + (ech & 1) * 16, a1 = a0 ^ 16;
      asm volatile("ds_write_b32 %8, %9\n\ts_mov_b64 exec, %12\n\t"
                   "ds_read_b64 %0, %10\n\tds_read_b64 %1, %10 offset:8\n\tds_read_b64 %2, %11\n\tds_read_b64 %3, %11 offset:8\n\t"
                   "ds_read_b64 %4, %10 offset:128\n\tds_read_b64 %5, %10 offset:136\n\tds_read_b64 %6, %11 offset:128\n\tds_read_b64 %7, %11 offset:136\n\t"
                   "s_mov_b64 exec, -1\n\ts_waitcnt lgkmcnt(0)"
                   : "+v"(gf[0][0]), "+v"(gf[0][1]), "+v"(gf[0][2]), "+v"(gf[0][3]), "+v"(gf[1][0]), "+v"(gf[1][1]), "+v"(gf[1][2]), "+v"(gf[1][3])
                   : "v"(aw_), "v"(g1), "v"(a0), "v"(a1), "s"(mk_) : "memory");
    };
  f32x4 acc3[2][4];
#pragma unroll
  for (int t = 0; t < 2; ++t)
#pragma unroll
    for (int n = 0; n < 4; ++n) acc3[t][n] = f32x4{0.f, 0.f, 0.f, 0.f};
  {
    s16x4 wa[2][8];
    auto rd3 = [&](auto kc, auto nc) {
      constexpr int k = decltype(kc)::value, n = decltype(nc)::value;
      (void)&wa; (void)&bw;
      DS_TR(wa[k & 1][2 * n], bw[n], k * 4096); DS_TR(wa[k & 1][2 * n + 1], bw[n], k * 4096 + 1024);
    };
    RAW_BARRIER();
    static_for<4>([&](auto nc) { rd3(IC(0), nc); });
    static_for<15>([&](auto kc) {
      constexpr int k = decltype(kc)::value, sx = k & 1;
      static_for<4>([&](auto nc) {
        constexpr int n = decltype(nc)::value;
        asm volatile("s_waitcnt lgkmcnt(%2)" : "+v"(wa[sx][2 * n]), "+v"(wa[sx][2 * n + 1]) : "n"(6));
        SB0();
        const bf16x8 wf = cat8(wa[sx][2 * n], wa[sx][2 * n + 1]);
        acc3[0][n] = MFMA16(wf, __builtin_bit_cast(bf16x8, of[k][0]), acc3[0][n]);
        SB0();
        if constexpr (k * 4 + n == 13) STUB(10);
        if constexpr (n == 0 && (k & 3) == 0) __builtin_amdgcn_s_setprio(3 - (k >> 2));
        if constexpr (k + 1 < 16) rd3(IC(k + 1), nc);
        if constexpr (k + OWIN < 16 && n == 3) { of[k + OWIN][0] = LD128(oo0 + (k + OWIN) * 2048); }
        SB0();
        acc3[1][n] = MFMA16(wf, __builtin_bit_cast(bf16x8, of[k][1]), acc3[1][n]);
        SB0();
        if constexpr (k + OWIN < 16 && n == 3) { of[k + OWIN][1] = LD128(oo1 + (k + OWIN) * 2048); SB0(); }
        if constexpr (k == YP_ && n == 3) { constexpr int e_ = YP_ + OWIN < 16 ? YP_ : 15 - OWIN; if (has_idx) owner(IC(2 * (e_ + 1))); SB0(); }
      });
    });
    static_for<4>([&](auto nc) {
      constexpr int n = decltype(nc)::value;
      asm volatile("s_waitcnt lgkmcnt(%2)" : "+v"(wa[1][2 * n]), "+v"(wa[1][2 * n + 1]) : "n"(6 - 2 * n));
      SB0();
      acc3[1][n] = MFMA16(cat8(wa[1][2 * n], wa[1][2 * n + 1]), __builtin_bit_cast(bf16x8, of[15][1]), acc3[1][n]);
      SB0();
      if constexpr (n == 3) { pw[0] = acc3[1][0]; SB0(); }
    });
    static_for<4>([&](auto nc) {
      constexpr int n = decltype(nc)::value;
      acc3[0][n] = MFMA16(cat8(wa[1][2 * n], wa[1][2 * n + 1]), __builtin_bit_cast(bf16x8, of[15][0]), acc3[0][n]);
      SB0();
      if constexpr (n < 3) { pw[(n + 1) * 64] = acc3[1][n + 1]; SB0(); }
    });
  }
  {
    RAW_BARRIER();
    f32x4 pr[4];
    DS_B128(pr[0], prd, 0); DS_B128(pr[1], prd, 1024); DS_B128(pr[2], prd, 2048); DS_B128(pr[3], prd, 3072);
    SB0();
#pragma unroll
    for (int i = 0; i < 2; ++i)
#pragma unroll
      for (int e = 0; e < 4; ++e) {
        const f32x4 a = acc3[0][2 * i + (e >> 1)];
        xf[i][e] = __builtin_elementwise_fma(f32x2{a[2 * (e & 1)], a[2 * (e & 1) + 1]}, gf[i][e], xf[i][e]);
        asm volatile("" : "+v"(xf[i][e]));
      }
    SB0();
    u32x4 res[2];
#pragma unroll
    for (int i = 0; i < 2; ++i) {
      if (i == 0) asm volatile("s_waitcnt lgkmcnt(2)" : "+v"(pr[0]), "+v"(pr[1])); else asm volatile("s_waitcnt lgkmcnt(2)" : "+v"(pr[2]), "+v"(pr[3]));
      const f32x4 s0 = pr[2 * i], s1 = pr[2 * i + 1];
      const f32x2 o0 = __builtin_elementwise_fma(f32x2{s0[0], s0[1]}, gf[i][0], xf[i][0]), o1 = __builtin_elementwise_fma(f32x2{s0[2], s0[3]}, gf[i][1], xf[i][1]);
      const f32x2 o2 = __builtin_elementwise_fma(f32x2{s1[0], s1[1]}, gf[i][2], xf[i][2]), o3 = __builtin_elementwise_fma(f32x2{s1[2], s1[3]}, gf[i][3], xf[i][3]);
      res[i] = u32x4{pk2(o0[0], o0[1]), pk2(o1[0], o1[1]), pk2(o2[0], o2[1]), pk2(o3[0], o3[1])};
      asm volatile("ds_write_b64 %0, %2\n\tds_write_b64 %1, %3" :: "v"(i ? tw1 : tw0), "v"(i ? tw3 : tw2), "v"(u32x2{res[i][0], res[i][1]}), "v"(u32x2{res[i][2], res[i][3]}) : "memory");
    }
    asm volatile("ds_read_b128 %0, %2\n\tds_read_b128 %1, %2 offset:1024\n\ts_waitcnt lgkmcnt(1)\n\t"
                 "global_store_dwordx4 %3, %0, off sc1\n\ts_waitcnt lgkmcnt(0)\n\tglobal_store_dwordx4 %4, %1, off sc1\n\ts_nop 1"
                 : "=&v"(res[0]), "=&v"(res[1]) : "v"(trd), "v"(po), "v"(po2) : "memory");
  }
  asm volatile(".Lkf_pf_end:" ::: "memory");
}

extern "C" void kernel_launch(void* const* d_in, const int* in_sizes, int n_in,
                              void* d_out, int out_size, void* d_ws, size_t ws_size,
                              hipStream_t stream) {
  (void)hipFuncSetAttribute((const void*)kf_all, hipFuncAttributeMaxDynamicSharedMemorySize, LDS_TOTAL);
  kf_all<<<256, 512, LDS_TOTAL, stream>>>((const u16*)d_in[0], (const u16*)d_in[1], (const u16*)d_in[2], (const u16*)d_in[3],
                                          (const u16*)d_in[5], (const u16*)d_in[6], (const u16*)d_in[7], (const u16*)d_in[8],
                                          (char*)d_ws, (u16*)d_out);
}
```

```cpp
#define KP 9
#define KC 10
#define F1_S 13
#define PF2_KT 8
#define WOK 2
#define H1_DELAY 4
constexpr int PACE_T[16] = {0, 114, 84, 64}, PACE_3[16] = {0, 70, 60, 56};
constexpr int DVN[20] = {0, 0, 4, 2, 1, 0, 1};
constexpr int dv_first(int k) { int n = 0; for (int i = -2; i < k; ++i) n += DVN[i + 2]; return n; }
constexpr bool CSS(int S) { return S < 2 || S == 6 || S == 7; }
#define NREG 8
static_assert(NREG <= 10, "first run");
#define WH0 12
#define WPOST 4
#define YP_ 11
#define PRIO_T 2
#define PRIO_H 1
#include <hip/hip_runtime.h>
#include <hip/hip_bf16.h>
#include <stdint.h>
#include <utility>
#include <type_traits>

typedef __attribute__((ext_vector_type(8))) short bf16x8;
typedef __attribute__((ext_vector_type(4))) short s16x4;
typedef __attribute__((ext_vector_type(16))) float f32x16;
typedef __attribute__((ext_vector_type(4))) float f32x4;
typedef __attribute__((ext_vector_type(2))) float f32x2;
typedef __attribute__((ext_vector_type(4))) unsigned u32x4;
typedef __attribute__((ext_vector_type(2))) unsigned u32x2;
typedef __attribute__((ext_vector_type(2))) __bf16 bfx2;
typedef __attribute__((ext_vector_type(4))) short v4i16_t;
typedef unsigned long long u64;
typedef unsigned short u16;

#define DI __device__ __forceinline__
#define MFMA(a, b, c) __builtin_amdgcn_mfma_f32_32x32x16_bf16((a), (b), (c), 0, 0, 0)
#define MFMA16H(a, b, c) __builtin_amdgcn_mfma_f32_16x16x16bf16_1k((a), (b), (c), 0, 0, 0)
#define MFMA16(a, b, c) __builtin_amdgcn_mfma_f32_16x16x32_bf16((a), (b), (c), 0, 0, 0)

DI unsigned pk2(float a, float b) { f32x2 v = {a, b}; return __builtin_bit_cast(unsigned, __builtin_convertvector(v, bfx2)); }
DI float bflo(unsigned u) { return __uint_as_float(u << 16); }
DI float bfhi(unsigned u) { return __uint_as_float(u & 0xffff0000u); }
DI float rbf(float x) { return (float)(__bf16)x; }
DI s16x4 trread(const char* p) {
  return __builtin_bit_cast(s16x4, __builtin_amdgcn_ds_read_tr16_b64_v4i16((__attribute__((address_space(3))) v4i16_t*)p));
}
DI bf16x8 cat8(s16x4 lo, s16x4 hi) { return __builtin_shufflevector(lo, hi, 0, 1, 2, 3, 4, 5, 6, 7); }
#define WAIT_V(n) asm volatile("s_waitcnt vmcnt(%0)" ::"n"(n) : "memory")
#define RAW_BARRIER() do { asm volatile("s_waitcnt lgkmcnt(0)" ::: "memory"); __builtin_amdgcn_s_barrier(); asm volatile("" ::: "memory"); } while (0)
#define DS_B128(dst, addr, off) asm volatile("ds_read_b128 %0, %1 offset:%2" : "=v"(dst) : "v"(addr), "n"(off))
#define DS_TR(dst, addr, off) asm volatile("ds_read_b64_tr_b16 %0, %1 offset:%2" : "=v"(dst) : "v"(addr), "n"(off))
#define LDS_OFF(p) ((unsigned)(uintptr_t)(__attribute__((address_space(3))) const char*)(p))
template <class F, int... I> DI void static_for_impl(F&& f, std::integer_sequence<int, I...>) { (f(std::integral_constant<int, I>{}), ...); }
template <int N, class F> DI void static_for(F&& f) { static_for_impl(f, std::make_integer_sequence<int, N>{}); }
DI float divc(float a, float b, float rinv) { const float q = a * rinv; return __builtin_fmaf(__builtin_fmaf(-q, b, a), rinv, q); }
DI float rcp_nr(float d) { const float r = __builtin_amdgcn_rcpf(d); return __builtin_fmaf(__builtin_fmaf(-d, r, 1.0f), r, r); }
DI float own(float v) { asm volatile("" : "+v"(v)); return v; }
DI float silu_r(float x) { return x * __builtin_amdgcn_rcpf(1.0f + __expf(-x)); }
#define SB0() __builtin_amdgcn_sched_barrier(0)
#define GLDS_S(vo, sp, ld) asm volatile("s_mov_b32 m0, %2\n\ts_nop 0\n\tglobal_load_lds_dwordx4 %0, %1" :: "v"(vo), "s"(sp), "s"(ld) : "m0", "memory")
template <int IMM> DI void gl_m0(unsigned vo, const void* sp, unsigned ls) {
  asm volatile("s_add_u32 m0, %2, %3\n\ts_nop 0\n\tglobal_load_lds_dwordx4 %0, %1" :: "v"(vo), "s"(sp), "s"(ls), "n"(IMM) : "m0", "memory", "scc");
}
template <int IMM> DI void gl_nt(unsigned vo, const void* sp, unsigned ls) {
  asm volatile("s_add_u32 m0, %2, %3\n\ts_nop 0\n\tglobal_load_lds_dwordx4 %0, %1 nt" :: "v"(vo), "s"(sp), "s"(ls), "n"(IMM) : "m0", "memory", "scc");
}
template <int N> DI void gl_run(unsigned vo, const void* sp, unsigned ls) {
  unsigned lo = (unsigned)(uintptr_t)sp, hi = (unsigned)((uintptr_t)sp >> 32);
  static_for<N>([&](auto ic) {
    constexpr int i = decltype(ic)::value;
    gl_m0<i * 1024>(vo, (const void*)(((uintptr_t)hi << 32) | lo), ls);
    if constexpr (i + 1 < N) asm volatile("s_add_u32 %0, %0, 0x4000\n\ts_addc_u32 %1, %1, 0" : "+s"(lo), "+s"(hi) :: "scc");
  });
}
#define GLDS_SC1(vo, sp, ld) asm volatile("s_mov_b32 m0, %2\n\ts_nop 0\n\tglobal_load_lds_dwordx4 %0, %1 sc1" :: "v"(vo), "s"(sp), "s"(ld) : "m0", "memory")
DI float x32_add(float v) { auto r = __builtin_amdgcn_permlane32_swap(__float_as_uint(v), __float_as_uint(v), false, false); return __uint_as_float(r[0]) + __uint_as_float(r[1]); }
DI float x32_max(float v) { auto r = __builtin_amdgcn_permlane32_swap(__float_as_uint(v), __float_as_uint(v), false, false); float o; asm("v_max_f32 %0, %1, %2" : "=v"(o) : "v"(r[0]), "v"(r[1])); return o; }
DI float x32_min(float v) { auto r = __builtin_amdgcn_permlane32_swap(__float_as_uint(v), __float_as_uint(v), false, false); float o; asm("v_min_f32 %0, %1, %2" : "=v"(o) : "v"(r[0]), "v"(r[1])); return o; }
DI float x16_add(float v) { auto r = __builtin_amdgcn_permlane16_swap(__float_as_uint(v), __float_as_uint(v), false, false); return __uint_as_float(r[0]) + __uint_as_float(r[1]); }
template <int CTRL, int RMASK> DI float dpp_add(float v) {
  return v + __builtin_bit_cast(float, __builtin_amdgcn_update_dpp(0, __builtin_bit_cast(int, v), CTRL, RMASK, 0xf, true));
}

DI unsigned ln_pair(unsigned u, float m, float den, float rinv) {
  const f32x2 mm = {m, m}, dd = {den, den}, rr = {rinv, rinv};
  const f32x2 d0 = f32x2{bflo(u), bfhi(u)} - mm;
  const unsigned du = pk2(d0[0], d0[1]);
  const f32x2 d = {bflo(du), bfhi(du)};
  const f32x2 q = d * rr;
  const f32x2 r = __builtin_elementwise_fma(__builtin_elementwise_fma(-q, dd, d), rr, q);
  return pk2(r[0], r[1]);
}
DI float dot2(unsigned a, unsigned b, float c) { return __builtin_amdgcn_fdot2_f32_bf16(__builtin_bit_cast(bfx2, a), __builtin_bit_cast(bfx2, b), c, false); }
DI float half_total(float v) {
  v = dpp_add<0x121, 0xf>(v); v = dpp_add<0x122, 0xf>(v); v = dpp_add<0x124, 0xf>(v); v = dpp_add<0x128, 0xf>(v);
  return x16_add(v);
}
DI void ln_rows(const u32x4 (&xr)[4], const u32x4 (&wr)[2], int h, float& m, float& den, float& rinv, float& r) {
  const float c0 = __uint_as_float(__builtin_amdgcn_readlane(xr[0][0], 0) << 16), c1 = __uint_as_float(__builtin_amdgcn_readlane(xr[2][0], 0) << 16);
  float s1[2] = {0.f, 0.f}, sx[2] = {0.f, 0.f}, sw = 0.f;
  f32x2 q[2] = {{0.f, 0.f}, {0.f, 0.f}};
#pragma unroll
  for (int i = 0; i < 2; ++i)
#pragma unroll
    for (int e = 0; e < 4; ++e) {
      sw = dot2(wr[i][e], 0x3F803F80u, sw);
#pragma unroll
      for (int t = 0; t < 2; ++t) {
        const unsigned u = xr[2 * t + i][e]; const float c = t ? c1 : c0;
        s1[t] = dot2(u, 0x3F803F80u, s1[t]); sx[t] = dot2(u, wr[i][e], sx[t]);
        const f32x2 d = f32x2{bflo(u), bfhi(u)} - f32x2{c, c};
        q[t] = __builtin_elementwise_fma(d, d, q[t]);
      }
    }
  auto mg = [](float a, float b) { const auto x = __builtin_amdgcn_permlane32_swap(__float_as_uint(a), __float_as_uint(b), false, false); return __uint_as_float(x[0]) + __uint_as_float(x[1]); };
  const float mean = half_total(mg(s1[0], s1[1])) * (1.0f / 1024.0f), m2 = half_total(mg(q[0][0] + own(q[0][1]), q[1][0] + own(q[1][1]))) * (1.0f / 1024.0f);
  const float tx = half_total(mg(sx[0], sx[1])), tw = half_total(x32_add(sw));
  const float dm = mean - (h ? c1 : c0);
  const float var = fmaxf(__builtin_fmaf(-dm, dm, m2), 0.f);
  m = rbf(mean);
  den = rbf(__builtin_amdgcn_sqrtf(rbf(rbf(var) + 1e-5f)));
  rinv = rcp_nr(den);
  r = rbf(divc(__builtin_fmaf(-m, tw, tx), den, rinv));
}

template <class HK> DI void phi_fn(const bf16x8 (&wp)[2], const bf16x8 f0, const bf16x8 f1, f32x2 (&ep)[8], f32x2 (&en)[8], float si, float so, HK&& hk) {
  f32x16 p;
#pragma unroll
  for (int i = 0; i < 16; ++i) p[i] = 0.f;
  p = MFMA(wp[0], f0, p);
  p = MFMA(wp[1], f1, p);
  hk(std::integral_constant<int, 0>{});
  float mx = -3.0e38f, mn = 3.0e38f;
  f32x2 p2[8];
  static_for<8>([&](auto ic) {
    constexpr int i = decltype(ic)::value;
    p2[i] = f32x2{p[2 * i], p[2 * i + 1]};
    mx = fmaxf(fmaxf(mx, p2[i][0]), p2[i][1]); mn = fminf(fminf(mn, p2[i][0]), p2[i][1]);
    if constexpr (i & 1) hk(std::integral_constant<int, 1 + (i >> 1)>{});
  });
  mx = x32_max(mx); mn = x32_min(mn);
  hk(std::integral_constant<int, 5>{});
  const float L2E = 1.44269504088896340736f * si, mxl = -mx * L2E, mnl = mn * L2E;
  const f32x2 cp = {L2E, L2E}, cn = {-L2E, -L2E}, bp = {mxl, mxl}, bn = {mnl, mnl};
  f32x2 sp2, sn2;
  static_for<8>([&](auto ic) {
    constexpr int i = decltype(ic)::value;
    const f32x2 tp2 = __builtin_elementwise_fma(p2[i], cp, bp), tn2 = __builtin_elementwise_fma(p2[i], cn, bn);
    ep[i] = f32x2{__builtin_amdgcn_exp2f(tp2[0]), __builtin_amdgcn_exp2f(tp2[1])};
    en[i] = f32x2{__builtin_amdgcn_exp2f(tn2[0]), __builtin_amdgcn_exp2f(tn2[1])};
    if constexpr (i == 0) { sp2 = ep[0]; sn2 = en[0]; } else { sp2 += ep[i]; sn2 += en[i]; }
    hk(std::integral_constant<int, 6 + i>{});
  });
  const float sp = x32_add(sp2[0] + own(sp2[1])), sn = x32_add(sn2[0] + own(sn2[1]));
  hk(std::integral_constant<int, 14>{});
  const float rp = __builtin_amdgcn_rcpf(sp) * so, rn = __builtin_amdgcn_rcpf(sn) * so;
  const f32x2 rp2 = {rp, rp}, rn2 = {rn, rn};
  hk(std::integral_constant<int, 15>{});
#pragma unroll
  for (int i = 0; i < 8; ++i) { ep[i] = ep[i] * rp2; en[i] = en[i] * rn2; }
  hk(std::integral_constant<int, 16>{});
}

constexpr int DM = 1024, FF = 3584;
constexpr unsigned SENT = 0xFFFFFFFFu;
constexpr unsigned SPIN_CAP = 1u << 17;

constexpr unsigned WS_EPOCH = 0;
constexpr unsigned WS_F1 = 4096;
constexpr unsigned WS_F4 = 896u << 10;
constexpr unsigned WS_STAT = 20480;
constexpr unsigned WS_CS = 36864;
constexpr unsigned WS_GUP = WS_CS + 16 * 16 * 768;
constexpr unsigned WS_YP = WS_GUP + 256 * 512;
constexpr unsigned WS_KVF = WS_YP + 32 * 4096;
constexpr unsigned WS_ZP = WS_KVF + 256 * 640;
constexpr unsigned WS_XA = 800u << 10;
constexpr unsigned WS_RI = 840u << 10;
static_assert(WS_ZP + 131072 <= WS_XA && WS_XA + 32768 <= WS_RI, "ws map");
constexpr unsigned WS_KVP = 1u << 20;
constexpr unsigned WS_O = 8u << 20;
constexpr unsigned WS_END = 16u << 20;

constexpr int ABYTES = 128 * 128, WSLAB = 64 * 128, STAGE1 = ABYTES + 3 * WSLAB, NST1 = 3;
constexpr int XB = NST1 * STAGE1;
constexpr int X_WO = XB;
constexpr int X_CSP = XB + 8192;
constexpr int X_P = 131072 + 4096;
constexpr int X_XW = X_P;
constexpr int X_XA = X_XW + 6144;
constexpr int X_ST = X_XA + 2048;
constexpr int X_MD = X_ST + 1024;
constexpr int X_AM = X_MD + 1024;
constexpr int X_XS = X_AM + 64;
constexpr int X_YR = X_XS + 512;
constexpr int X_YS = X_YR + 2048;
constexpr int X_WP = X_YS + 2048;
constexpr int X_KB = X_WP + 4096;
constexpr int LDS_TOTAL = 163840;
static_assert(X_KB + 2 * 4224 <= LDS_TOTAL && 131072 + 6 * 2048 <= X_MD && X_YS + 64 + 4096 <= X_KB && X_YS % 128 == 64, "lds");
static_assert(LDS_TOTAL <= 163840, "lds");
constexpr int gu_at(int ks) { return STAGE1 + ks * 8192; }
constexpr int HANDOFF = 106496;

#define LD128(off) __builtin_amdgcn_raw_buffer_load_b128(rs, (int)(off), 0, 16)
#define ST128(v, off) __builtin_amdgcn_raw_buffer_store_b128((v), rs, (int)(off), 0, 16)
#define ST64(v, off) __builtin_amdgcn_raw_buffer_store_b64((v), rs, (int)(off), 0, 16)
DI u32x4 f2u_tag(f32x4 v, unsigned t8) { u32x4 u = __builtin_bit_cast(u32x4, v); return (u & 0xFFFFFF00u) | t8; }
DI unsigned or4(u32x4 v) { return (v[0] | v[1]) | (v[2] | v[3]); }
#define SPIN_NEXT(sp) { if (++(sp) > SPIN_CAP) break; __builtin_amdgcn_s_sleep(1); }
#define TAG_OF(en) ((en) * 0x9E3779B97F4A7C15ull)

__global__ __launch_bounds__(512, 2) void kf_all(const u16* __restrict__ x, const u16* __restrict__ wqkv, const u16* __restrict__ wphi,
                                              const u16* __restrict__ wo, const u16* __restrict__ w_r, const u16* __restrict__ w_gate,
                                              const u16* __restrict__ w_up, const u16* __restrict__ w_down, char* ws, u16* __restrict__ out) {
  extern __shared__ __attribute__((aligned(16))) char smem[];
  asm volatile(".Lkf_pf_start:");
  const int b = blockIdx.x;
  const int tid = threadIdx.x, w = __builtin_amdgcn_readfirstlane(tid >> 6);
  unsigned pfd;
  {
    u64 pc; unsigned len;
    asm volatile("s_getpc_b64 %0\n.Lkf_pf_here:\n\ts_mov_b32 %1, .Lkf_pf_end+1152-256-.Lkf_pf_here" : "=s"(pc), "=s"(len));
    const u64 pa = pc + min((unsigned)((b & ~7) + w) << 8, len);
    asm volatile("s_load_dword %0, %1, 0x0\n\ts_load_dword %0, %1, 0x40\n\ts_load_dword %0, %1, 0x80\n\ts_load_dword %0, %1, 0xc0" : "=&s"(pfd) : "s"(pa) : "memory");
  }
  SB0();
  const int xcd = b & 7, jj = b >> 3, l = tid & 63;
  const int panel = (xcd >> 2) * 8 + (jj & 7), hp = (xcd & 3) * 4 + (jj >> 3);
  const int id = panel * 16 + hp;
  asm volatile("" : "+s"(x), "+s"(wqkv), "+s"(wphi), "+s"(wo), "+s"(w_r), "+s"(w_gate), "+s"(w_up), "+s"(w_down), "+s"(ws), "+s"(out));
  SB0();
  u32x4 xr4[4], wr4[2];
  if (w < 4) {
    const u16* xr = x + (size_t)(panel * 128 + hp * 8 + 2 * w) * DM; const unsigned vl = (unsigned)l * 16;
#define GLS(dst, p, o) asm volatile("global_load_dwordx4 %0, %1, %2 offset:" #o : "=v"(dst) : "v"(vl), "s"(p) : "memory")
    GLS(xr4[0], xr, 0); GLS(xr4[1], xr, 1024); GLS(xr4[2], xr, 2048); GLS(xr4[3], xr, 3072); GLS(wr4[0], w_r, 0); GLS(wr4[1], w_r, 1024);
  }
  SB0();
  const int cw = w & 3, wm = cw & 1, wn = cw >> 1;
  const int h = l >> 5, r31 = l & 31, blk = (l >> 4) & 1, tq = (l & 15) >> 2, tp = l & 3;
  const int hd = hp * 2 + wn;
  const unsigned lbase = LDS_OFF(smem);
  const __amdgpu_buffer_rsrc_t rs = __builtin_amdgcn_make_buffer_rsrc((void*)ws, 0, (int)WS_END, 0x00020000);
  bf16x8 fr[4];
  bf16x8 wp[2];
  f32x4 qa[4][2]; float rt;
  const int kh = w >> 2;
  const int wt = wm * 2 + kh;
  const unsigned img = (wn * 4 + wt) * 4096 + r31 * 128 + (((2 * h) ^ (r31 & 7)) * 16);
  auto wpload = [&]() {
    const char* p = smem + X_WP + wn * 2048 + (8 * h + tq) * 64 + (16 * blk + 4 * tp) * 2;
    wp[0] = cat8(trread(p), trread(p + 256)); wp[1] = cat8(trread(p + 1024), trread(p + 1280));
  };
  auto imgw = [&](int g, const f32x2 (&ep)[8], const f32x2 (&en)[8]) {
    const f32x2 (&e)[8] = g < 2 ? ep : en; const int q = 4 * (g & 1);
    *(u32x4*)(smem + (img ^ ((g >> 1) * 64 + (g & 1) * 16))) = u32x4{pk2(e[q][0], e[q][1]), pk2(e[q + 1][0], e[q + 1][1]), pk2(e[q + 2][0], e[q + 2][1]), pk2(e[q + 3][0], e[q + 3][1])};
  };
  auto pack_u = [&](const f32x4 x0, const f32x4 x1) {
    const auto s0 = __builtin_amdgcn_permlane16_swap(pk2(x0[0], x0[1]), pk2(x1[0], x1[1]), false, false);
    const auto s1 = __builtin_amdgcn_permlane16_swap(pk2(x0[2], x0[3]), pk2(x1[2], x1[3]), false, false);
    return u32x4{s0[0], s1[0], s0[1], s1[1]};
  };
  const int hr = hp ^ 8;
  const int p3 = ((xcd >> 1) * 4 + (jj & 3)) ^ 8, nb = (int)(((xcd & 1) ? 0xF7EDCA98u : 0xB3654210u) >> (jj & 28)) & 15;
  const int flip = jj & 1;
  const unsigned voO = (unsigned)((l >> 3) * DM + (((l & 7) ^ (((l >> 4) & 1) | ((l >> 5) << 2))) * 8)) * 2;
  const bool has_gu = (hp & 7) != 3;
  int gr = (xcd == 6 && jj >= 12) ? 8 * (24 + (jj & 3)) + 2 * ((jj >> 2) - 3) : b;
  asm volatile("" : "+s"(gr));
  gr = jj >= 28 ? 8 * (4 * (xcd >> 1) + jj - 16) + 6 : gr;
  asm volatile("" : "+s"(gr));
  gr = has_gu ? gr : 224 + (jj - 24) * 4 + (xcd >> 1);
  const int gkq = (gr / 56) & 3;
  u32x4 dv[8]; unsigned ogq; bf16x8 dn[4];
  u32x4 wr_[16];
    const int srow = l >> 3, sch = l & 7;
    unsigned voA[4], voW[6];
#pragma unroll
    for (int i = 0; i < 4; ++i) voA[i] = (unsigned)((panel * 128 + (4 * cw + i) * 8 + srow) * DM + ((sch ^ (4 * (i & 1) + (srow >> 1))) * 8)) * 2;
#pragma unroll
    for (int i = 0; i < 6; ++i) voW[i] = (unsigned)(((2 * cw + (i & 1)) * 8 + srow) * 3072 + (i >> 1) * 1024 + hp * 64 + ((sch ^ ((((srow >> 1) & 1) << 2) | ((i & 1) << 1))) * 8)) * 2;
    const unsigned lwa = lbase + cw * 4096, lww = lbase + ABYTES + cw * 2048;
    const int rot = 4 * ((panel & 7) >> 1) + 2 * (panel >> 3) + 4 * (xcd & 3);
    u64 k0_ = (panel & 8) ? ((panel & 1) ? 0xD95FB7EA62C30841ull : 0xD95FB73EA6C21840ull) : (panel & 1) ? 0xFB7EA62D95C30841ull : 0xFB73EA6D95C21840ull, k1_ = (panel & 8) ? 0xD95FB73EA62C1840ull : 0xFB73EA62D95C1840ull;
    asm volatile("" : "+s"(k0_), "+s"(k1_));
    const u64 kord = has_gu ? k1_ : k0_;
    auto stage = [&](int it, auto bufc) {
      constexpr int B = decltype(bufc)::value * STAGE1;
      const int kt = ((int)(kord >> (4 * it)) + rot) & 15;
      const u16* gx = x + kt * 64; const u16* gq_ = wqkv + (size_t)kt * 64 * 3072;
      static_for<10>([&](auto ic) {
        constexpr int i = decltype(ic)::value;
        if constexpr (i < 4) gl_m0<B + i * 1024>(voA[i], gx, lwa);
        else gl_m0<B + ((i - 4) >> 1) * WSLAB + (i & 1) * 1024>(voW[i - 4], gq_, lww);
      });
    };
  unsigned pa0, pvh, dst, dz, pgw, ogu, oh1, oh1z;
  auto kvsetup = [&]() {
    int l = tid & 63; asm volatile("" : "+v"(l));
    const int g = l >> 4, c15 = l & 15, tq = c15 >> 2, tp = l & 3;
    pa0 = lbase + kh * 16384 + (4 * g + tq) * 128 + (((2 * cw + (tp >> 1)) ^ ((4 * g + tq) & 7)) * 16) + 8 * (tp & 1);
    pvh = lbase + HANDOFF + kh * 12288 + l * 16;
    dst = WS_KVP + (unsigned)((hp * 16 + panel) * 16 + (w & 6) * 2 + (g & 2) + (w & 1)) * 512 + c15 * 32 + (g & 1) * 16;
    dz = WS_ZP + (unsigned)((hp * 16 + (w & 6) * 2 + (g & 2) + (w & 1)) * 16 + panel) * 32 + (g & 1) * 16;
    pgw = lbase + gu_at(0) + kh * 4096 + (8 * g + tq) * 128 + (((2 * cw + (tp >> 1)) ^ ((((tq >> 1) & 1) << 2) | ((g & 1) << 1))) * 16) + 8 * (tp & 1);
    ogu = WS_GUP + (unsigned)gr * 512 + w * 64 + g * 16;
    oh1 = WS_KVP + (unsigned)((hr * 16 + (l >> 3)) * 16 + panel) * 512 + cw * 128 + (l & 7) * 16;
    oh1z = WS_ZP + (unsigned)((hr * 16 + panel) * 16) * 32 + l * 4;
    asm volatile("" : "+v"(pa0), "+v"(pvh), "+v"(dst), "+v"(dz), "+v"(pgw), "+v"(ogu), "+v"(oh1), "+v"(oh1z));
  };
#define STUB(n) asm volatile(".Lkf_st" #n ":\n\ts_cbranch_execz .Lkf_rt" #n)
#define VISIT(n, wv) asm volatile("s_cmp_lg_u32 %0, " #wv "\n\ts_cbranch_scc1 .Lkf_rt" #n "\n\ts_mov_b64 exec, 0\n\ts_branch .Lkf_st" #n "\n.Lkf_rt" #n ":\n\ts_mov_b64 exec, -1" :: "s"(cw) : "scc")
#define IC(n) std::integral_constant<int, (n)>{}
#define CODE_PF(tagn, FROM, d) do { u64 pc_; unsigned len_; int off_; \
    asm volatile("s_getpc_b64 %0\n.Lkf_pf_h" #tagn ":\n\ts_mov_b32 %1, .Lkf_pf_end+1152-" FROM "\n\ts_mov_b32 %2, " FROM "-.Lkf_pf_h" #tagn : "=s"(pc_), "=s"(len_), "=s"(off_)); \
    unsigned o_ = (unsigned)(jj * 4 + cw) * 256u; o_ = o_ + 256u <= len_ ? o_ : len_ - 256u; \
    const u64 pa_ = (pc_ + (long long)off_ + o_) & ~63ull; \
    asm volatile("s_load_dword %0, %1, 0x0\n\ts_load_dword %0, %1, 0x40\n\ts_load_dword %0, %1, 0x80\n\ts_load_dword %0, %1, 0xc0" : "=&s"(d) : "s"(pa_) : "memory"); } while (0)
#define PF_DONE(d) asm volatile("s_waitcnt lgkmcnt(0)" : "+s"(d) :: "memory")
  if (w < 4) {
    unsigned oc2;
    {
      const unsigned r0 = (unsigned)(panel * 128 + hp * 8);
      oc2 = l < 33 ? WS_KVF + (unsigned)(hr * 16 + panel) * 640 + l * 16 : l == 33 ? WS_F1 + (unsigned)id * 16 : l == 34 ? WS_STAT + r0 * 8 : l == 35 ? WS_RI + (unsigned)(panel * 1024 + hp * 32)
          : l < 40 ? WS_F4 + (unsigned)(((panel * 4 + (l - 36)) * 16 + hp) * 2) * 32 : l < 44 ? WS_GUP + (unsigned)gr * 512 + (l - 40) * 128 : l == 44 ? WS_RI + (unsigned)(panel * 1024 + 512 + hp * 16)
          : WS_ZP + (unsigned)((hp * 16 + (l & 15)) * 16 + panel) * 32;
    }
    u32x4 tm, k0, k1; u32x2 tk;
    const unsigned tmofs = lbase + X_CSP + (l < 47 ? l : 46) * 16, kofs = lbase + X_CSP + 768, tgofs = lbase + X_AM + 48;
    unsigned opay;
    { const unsigned r0 = (unsigned)(panel * 128 + hp * 8);
      opay = l < 32 ? WS_CS + (unsigned)(hp * 8 + (panel >> 1)) * 768 + (panel & 1) * 512 + l * 16 : l < 40 ? WS_XA + r0 * 16 + (l - 32) * 16 : l < 44 ? WS_STAT + r0 * 8 + (l - 40) * 16 : l < 46 ? WS_RI + (unsigned)(panel * 1024 + hp * 32) + (l - 44) * 16 : WS_RI + (unsigned)(panel * 1024 + 512 + hp * 16); }
    const unsigned oflag = WS_F1 + (unsigned)id * 16;
    unsigned mz; asm volatile("s_cmp_eq_u32 %1, 0\n\ts_cselect_b32 %0, -1, 0" : "=s"(mz) : "s"(cw) : "scc");
    const u64 mk_all = ((u64)mz << 32) | mz, mk_pay = ((u64)(mz & 0x7FFFu) << 32) | (mz & ((panel & 1) ? 0xFFFFu : 0xFFFFFFFFu)), mk_one = mz & 1u;
    asm volatile("" : "+v"(oc2), "+v"(opay));
#define MST(op, data, off, mask) asm volatile("s_mov_b64 exec, %3\n\t" op " %0, %1, %2, 0 offen sc1\n\ts_mov_b64 exec, -1" :: "v"(data), "v"(off), "s"(rs), "s"(mask) : "memory")
    f32x4 csa[2] = {{0.f, 0.f, 0.f, 0.f}, {0.f, 0.f, 0.f, 0.f}};
    const bf16x8 ones = {0x3F80, 0x3F80, 0x3F80, 0x3F80, 0x3F80, 0x3F80, 0x3F80, 0x3F80};
    s16x4 cl[2][2], ch[2][2];
    const unsigned csw = lbase + X_CSP + (wm * 64 + wn * 32 + r31) * 4;
    f32x4 acc[4][6];
#pragma unroll
    for (int t = 0; t < 4; ++t)
#pragma unroll
      for (int a = 0; a < 6; ++a) acc[t][a] = f32x4{0.f, 0.f, 0.f, 0.f};
    const int g4 = l >> 4, c15 = l & 15;
    unsigned asw[2][2], wofs[2][2], cofs[2];
#pragma unroll
    for (int e = 0; e < 2; ++e) {
      asw[0][e] = lbase + (wm * 64 + c15) * 128 + (((4 * e + g4) ^ ((c15 >> 1) & 7)) * 16);
      wofs[0][e] = lbase + ABYTES + (8 * g4 + tq) * 128 + (((wn * 4 + 2 * e + (tp >> 1)) ^ ((((tq >> 1) & 1) << 2) | ((g4 & 1) << 1))) * 16) + 8 * (tp & 1);
      asw[1][e] = asw[0][e] + 2 * STAGE1; wofs[1][e] = wofs[0][e] + 2 * STAGE1;
      cofs[e] = wofs[0][e] + ((panel & 1) ? 2 : wm) * WSLAB;
    }
    asm volatile("" : "+v"(asw[0][0]), "+v"(asw[0][1]), "+v"(asw[1][0]), "+v"(asw[1][1]), "+v"(wofs[0][0]), "+v"(wofs[0][1]), "+v"(wofs[1][0]), "+v"(wofs[1][1]), "+v"(cofs[0]), "+v"(cofs[1]));
    bf16x8 af[2][4]; s16x4 wl[2][6], wh_[2][6];
    s16x4 ea[4], ew[6];
    const unsigned eaofs = lbase + X_XA + (wm * 64 + c15) * 16;
    const unsigned ewofs = lbase + X_XW + g4 * 1536 + (wn * 32 + c15) * 4;
    const unsigned riofs = lbase + X_ST + (wm * 64 + r31) * 4;
    auto rd = [&](auto ic, auto stc) {
      constexpr int i = decltype(ic)::value, S = decltype(stc)::value, s = S & 1, e = S & 1, buf = (S >> 1) % NST1;
      constexpr int b2 = buf == 2, bo = buf == 1 ? STAGE1 : 0;
      (void)&cl; (void)&ch; (void)&cofs; (void)&af; (void)&wl; (void)&wh_; (void)&asw; (void)&wofs;
      if constexpr (i == 0) DS_B128(af[s][0], asw[b2][e], bo);
      else if constexpr (i == 1) DS_TR(wl[s][0], wofs[b2][0], bo + e * 4096);
      else if constexpr (i == 2) DS_TR(wh_[s][0], wofs[b2][0], bo + e * 4096 + 512);
      else if constexpr (i < 6) DS_B128(af[s][i - 2], asw[b2][e], bo + (i - 2) * 2048);
      else if constexpr (i < 16) {
        constexpr int j = (i - 4) >> 1, off = bo + e * 4096 + (j >> 1) * WSLAB;
        if constexpr (i & 1) DS_TR(wh_[s][j], wofs[b2][j & 1], off + 512); else DS_TR(wl[s][j], wofs[b2][j & 1], off);
      } else {
        constexpr int c = (i - 16) >> 1;
        if constexpr (i & 1) DS_TR(ch[s][c], cofs[c], bo + e * 4096 + 512); else DS_TR(cl[s][c], cofs[c], bo + e * 4096);
      }
    };
    {
      SB0();
      asm volatile("s_waitcnt vmcnt(0) lgkmcnt(0)" : "+s"(pfd), "+v"(xr4[0]), "+v"(xr4[1]), "+v"(xr4[2]), "+v"(xr4[3]), "+v"(wr4[0]), "+v"(wr4[1]) :: "memory");
      SB0();
      float m, den, rinv, r;
      ln_rows(xr4, wr4, h, m, den, rinv, r);
      const int rw = tid >> 5;
      const unsigned rb = __float_as_uint(r) >> 16, ord = rb ^ ((rb & 0x8000u) ? 0xFFFFu : 0x8000u), nm = pk2(-m, -m);
      if (r31 == 0) { *(u32x4*)(smem + X_CSP + 512 + rw * 16) = u32x4{nm, nm, nm, nm};
        *(f32x2*)(smem + X_CSP + 640 + rw * 8) = f32x2{m, den}; *(float*)(smem + X_CSP + 704 + rw * 4) = rinv; *(u16*)(smem + X_CSP + 736 + rw * 2) = (u16)(__float_as_uint(den) >> 16);
        *(unsigned*)(smem + X_CSP + 768 + rw * 4) = (ord << 11) | (unsigned)(2047 - (id * 8 + rw)); }
      SB0();
    }
    __builtin_amdgcn_s_barrier();
    static_for<20>([&](auto ic) { rd(ic, IC(0)); });
    static_for<30>([&](auto stc) {
      constexpr int S = decltype(stc)::value, s = S & 1;
      constexpr bool bnd = (S & 1) == 1;
      static_for<24>([&](auto mc) {
        constexpr int m = decltype(mc)::value, j = m >> 2, t = (j & 1) ? 3 - (m & 3) : m & 3;
        (void)&tm; (void)&k0; (void)&k1; (void)&tk; (void)&ea; (void)&ew; (void)&oc2; (void)&opay; (void)&oflag; (void)&mk_all; (void)&mk_pay; (void)&mk_one; (void)&rs;
        (void)&tmofs; (void)&kofs; (void)&tgofs; (void)&csw; (void)&csa; (void)&eaofs; (void)&ewofs; (void)&riofs;
        if constexpr (m == 0) asm volatile("s_waitcnt lgkmcnt(%0)" :: "n"(bnd ? 0 : CSS(S) ? 14 : 10));
        else if constexpr (!bnd && m == 4) asm volatile("s_waitcnt lgkmcnt(%0)" :: "n"(CSS(S) ? 7 : 3));
        else if constexpr (!bnd && m == 12 && CSS(S)) asm volatile("s_waitcnt lgkmcnt(11)");
        SB0();
        const bf16x8 wf = cat8(wl[s][j], wh_[s][j]);
        if constexpr (j < 4) acc[t][j] = MFMA16(wf, af[s][t], acc[t][j]);
        else acc[t][j] = MFMA16(af[s][t], wf, acc[t][j]);
        SB0();
        if constexpr (S * 24 + m == 348) STUB(6);
        if constexpr (S * 24 + m == 649) STUB(7);
        if constexpr (m == 1) {
          if constexpr (S == 1) { const u32x4 sv = {SENT, SENT, SENT, SENT}; MST("buffer_store_dwordx4", sv, oc2, mk_all); }
          if constexpr (S == 11) { asm volatile("" : "+v"(tm), "+v"(k0), "+v"(k1), "+v"(tk)); MST("buffer_store_dwordx4", tm, opay, mk_pay); }
          if constexpr (S == F1_S) { WAIT_V(0); MST("buffer_store_dwordx2", tk, oflag, mk_one); }
          if constexpr (S == 3) kvsetup();
          if constexpr (S == 5) { (void)&cw; VISIT(6, 1); VISIT(7, 2); VISIT(10, 3); VISIT(9, 0); }
          if constexpr (bnd) __builtin_amdgcn_s_barrier();
        }
        if constexpr (m >= 1 && m - 1 < (CSS(S + 1) ? 20 : 16)) rd(IC(m - 1), IC(S + 1));
        if constexpr (S == 8 && m == 18) {
          asm volatile("" : "+v"(csa[0]), "+v"(csa[1]));
          const float c = blk ? csa[1][0] : csa[0][0];
          const unsigned u = pk2(c, c - rbf(c));
          asm volatile("ds_write_b32 %0, %1" :: "v"(csw), "v"(u) : "memory");
        }
        if constexpr (S == 9 && m >= 18 && m < 22) {
          if constexpr (m == 18) DS_B128(tm, tmofs, 0);
          if constexpr (m == 19) DS_B128(k0, kofs, 0);
          if constexpr (m == 20) DS_B128(k1, kofs, 16);
          if constexpr (m == 21) asm volatile("ds_read_b64 %0, %1" : "=v"(tk) : "v"(tgofs));
        }
        if constexpr (S == 11 && m == 18) { k0 = __builtin_elementwise_max(k0, k1); asm volatile("" : "+v"(k0)); }
        if constexpr (S == 11 && m == 20) { tk[0] = (tk[0] & ~0x7FFFFFFu) | max(max(k0[0], k0[1]), max(k0[2], k0[3])); asm volatile("" : "+v"(tk)); }
#define DS_B64(dst, addr, off) asm volatile("ds_read_b64 %0, %1 offset:%2" : "=v"(dst) : "v"(addr), "n"(off))
#define DS_B32(dst, addr, off) asm volatile("ds_read_b32 %0, %1 offset:%2" : "=v"(dst) : "v"(addr), "n"(off))
#define DS_2(dst, addr, o0, o1) asm volatile("ds_read2_b32 %0, %1 offset0:%2 offset1:%3" : "=v"(dst) : "v"(addr), "n"(o0), "n"(o1))
        SB0();
      });
      if constexpr (CSS(S)) {
        SB0();
        csa[0] = MFMA16(ones, cat8(cl[s][0], ch[s][0]), csa[0]); csa[1] = MFMA16(ones, cat8(cl[s][1], ch[s][1]), csa[1]);
        SB0();
      }
    });
    static_for<16>([&](auto ic) {
      constexpr int i = decltype(ic)::value, j = 2 + (i >> 2), t = (j & 1) ? 3 - (i & 3) : i & 3;
      (void)&ea; (void)&ew; (void)&eaofs; (void)&ewofs;
      if constexpr (i == 0) asm volatile("s_waitcnt lgkmcnt(6)");
      else if constexpr (i == 4) asm volatile("s_waitcnt lgkmcnt(8)");
      SB0();
      const bf16x8 wf = cat8(wl[0][j], wh_[0][j]);
      if constexpr (j < 4) acc[t][j] = MFMA16(wf, af[0][t], acc[t][j]); else acc[t][j] = MFMA16(af[0][t], wf, acc[t][j]);
      SB0();
      if constexpr (i < 8) { rd(IC(2 * i), IC(31)); rd(IC(2 * i + 1), IC(31)); }
      else if constexpr (i < 12) DS_B64(ea[i - 8], eaofs, (i - 8) * 256);
      else if constexpr (i < 14) { const unsigned a_ = ewofs + 256; DS_2(ew[i - 10], a_, (i - 12) * 16, (i - 12) * 16 + 192); }
      SB0();
    });
    {
      __builtin_amdgcn_s_setprio(PRIO_T);
      asm volatile("s_waitcnt lgkmcnt(0)" : "+v"(af[1][0]), "+v"(af[1][1]), "+v"(af[1][2]), "+v"(af[1][3]), "+v"(wl[1][0]), "+v"(wh_[1][0]), "+v"(wl[1][1]), "+v"(wh_[1][1]),
                   "+v"(wl[1][2]), "+v"(wh_[1][2]), "+v"(wl[1][3]), "+v"(wh_[1][3]), "+v"(wl[1][4]), "+v"(wh_[1][4]), "+v"(wl[1][5]), "+v"(wh_[1][5]));
      asm volatile("" : "+v"(ea[0]), "+v"(ea[1]), "+v"(ea[2]), "+v"(ea[3]), "+v"(ew[2]), "+v"(ew[3]));
      SB0();
      auto mm = [&](int t, int j) { const bf16x8 wf = cat8(wl[1][j], wh_[1][j]); if (j < 4) acc[t][j] = MFMA16(wf, af[1][t], acc[t][j]); else acc[t][j] = MFMA16(af[1][t], wf, acc[t][j]); };
      auto ff = [&](int t, int j) { if (j < 4) acc[t][j] = MFMA16H(ew[j], ea[t], acc[t][j]); else acc[t][j] = MFMA16H(ea[t], ew[j], acc[t][j]); };
      auto dm = [&](int n) {
        SB0();
        const int t = n & 3, j = (n < 16 ? 4 : 0) + ((n >> 2) & 1);
        if (n & 8) ff(t, j); else mm(t, j);
        SB0();
      };
      const unsigned ew0 = ewofs, ew2 = ewofs + 512;
      mm(2, 2); SB0(); DS_B32(rt, riofs, 0); SB0();
      mm(3, 2); SB0();
      mm(2, 3); SB0(); DS_2(ew[4], ew2, 0, 192); DS_2(ew[5], ew2, 16, 208); SB0();
      mm(3, 3); SB0(); DS_2(ew[0], ew0, 0, 192); DS_2(ew[1], ew0, 16, 208); SB0();
      ff(2, 2); ff(3, 2); SB0(); wpload(); SB0();
      ff(2, 3); ff(3, 3); SB0(); SB0();
      mm(0, 2); mm(1, 2);
      SB0();
      SB0();
      char* ho = smem + HANDOFF + cw * 6144 + l * 16;
      *(u32x4*)(ho + 2048) = pack_u(acc[2][2], acc[3][2]);
      SB0(); mm(0, 3); mm(1, 3); SB0();
      *(u32x4*)(ho + 3072) = pack_u(acc[2][3], acc[3][3]);
      SB0(); ff(0, 2); ff(1, 2); ff(0, 3); ff(1, 3); SB0();
      RAW_BARRIER();
      asm volatile("" : "+v"(rt), "+v"(ew[0]), "+v"(ew[1]), "+v"(ew[4]), "+v"(ew[5]));
      dm(0); dm(1);
      fr[2] = __builtin_bit_cast(bf16x8, pack_u(acc[0][2], acc[1][2]));
      dm(2); dm(3);
      fr[3] = __builtin_bit_cast(bf16x8, pack_u(acc[0][3], acc[1][3]));
      dm(4);
      f32x2 ep[8], en[8];
      phi_fn(wp, fr[2], fr[3], ep, en, rt, rt, [&](auto kc) { constexpr int k = decltype(kc)::value; if constexpr (k == 0) { dm(5); dm(6); dm(7); } else if constexpr (7 + k < 16) dm(7 + k); });
    { const unsigned a_ = lbase + X_ST + 512 + (l >> 4) * 8;
      static_for<4>([&](auto tc) { constexpr int tb = decltype(tc)::value; (void)&dn; (void)&a_;
        asm volatile("ds_read2_b64 %0, %1 offset0:%2 offset1:%3" : "=v"(dn[tb]) : "v"(a_), "n"(tb * 8), "n"(tb * 8 + 4)); }); }
#pragma unroll
      for (int g = 0; g < 4; ++g) imgw(g, ep, en);
#pragma unroll
      for (int u = 0; u < 4; ++u) {
        const int t2 = u >> 1, db = u & 1;
        const f32x4 x0 = acc[2 * t2][4 + db], x1 = acc[2 * t2 + 1][4 + db];
        char* pv = t2 ? ho + 4096 : ho;
        *(u32x4*)(pv + db * 1024) = u32x4{pk2(x0[0], x0[1]), pk2(x0[2], x0[3]), pk2(x1[0], x1[1]), pk2(x1[2], x1[3])};
      }
#pragma unroll
      for (int t = 0; t < 4; ++t) { qa[t][0] = acc[t][0]; qa[t][1] = acc[t][1]; wr_[t] = __builtin_bit_cast(u32x4, af[1][t]); wr_[9 + t] = __builtin_bit_cast(u32x4, af[0][t]); }
      wr_[13] = __builtin_bit_cast(u32x4, cat8(wl[0][0], wh_[0][0])); wr_[14] = __builtin_bit_cast(u32x4, cat8(wl[0][1], wh_[0][1]));
      wr_[4] = __builtin_bit_cast(u32x4, cat8(wl[1][0], wh_[1][0])); wr_[5] = __builtin_bit_cast(u32x4, cat8(wl[1][1], wh_[1][1]));
      wr_[6] = __builtin_bit_cast(u32x4, cat8(ea[0], ea[1])); wr_[7] = __builtin_bit_cast(u32x4, cat8(ea[2], ea[3])); wr_[8] = __builtin_bit_cast(u32x4, cat8(ew[0], ew[1]));
    }
  } else {
    const int t = tid - 256;
    __builtin_amdgcn_s_setprio(PRIO_H);
    u64 epoch;
    asm volatile("s_load_dwordx2 %0, %1, 0x0" : "=&s"(epoch) : "s"(ws) : "memory");
    stage(0, IC(0));
    asm volatile("s_waitcnt lgkmcnt(0)" : "+s"(pfd), "+s"(epoch) :: "memory");
    const u64 enx = epoch + (((unsigned)epoch & 0xFEu) == 0xFEu ? 3u - ((unsigned)epoch & 1u) : 1u);
    const u64 tag = TAG_OF(enx);
    if (t == 0) { *(u32x2*)(smem + X_AM) = u32x2{0u, 0u};
      const unsigned e2 = ((unsigned)enx & 0x55u) | (((unsigned)enx & 0xAAu) << 15);
      *(u32x4*)(smem + X_AM + 32) = u32x4{e2, e2 >> 2, e2 >> 4, e2 >> 6} & 0x00010001u;
      *(u32x4*)(smem + X_AM + 48) = u32x4{(unsigned)tag, (unsigned)(tag >> 32), (unsigned)enx, (unsigned)(enx >> 32)}; }
    u32x2 fl, si; unsigned pfd2, tpace = 0, xs;
    unsigned ofl = WS_F1 + (unsigned)t * 16, orA = WS_CS + (unsigned)(hp * 6144 + cw * 1024 + l * 16);
    unsigned orB = (cw < 2 ? WS_CS + (unsigned)(hp * 6144 + 4096 + cw * 1024) : WS_XA + (unsigned)(panel * 2048 + (cw - 2) * 1024)) + l * 16;
    unsigned orC = ((cw & 1) ? WS_STAT + (unsigned)p3 * 1024 : WS_RI + (unsigned)panel * 1024) + l * 16;
    unsigned oxs = (unsigned)(gkq * 256 + cw * 64 + r31 * 2) * 2, akey = lbase + X_AM + 4, vol = (unsigned)l * 16;
    unsigned voG = (unsigned)((8 * cw + srow) * FF + ((sch ^ ((((srow >> 1) & 1) << 2) | ((cw & 1) << 1))) * 8)) * 2;
    const u16* gg_ = w_gate + (size_t)gkq * 256 * FF + ((gr + 1) % 56) * 64;
    const u16* gu_ = w_up + (size_t)gkq * 256 * FF + ((gr + 3) % 56) * 64;
    const u16* gwo = wo + (size_t)(((xcd >> 1) * 4 + (jj >> 3)) * 64 + 2 * cw * 8) * DM + ((xcd & 1) * 8 + (jj & 7)) * 64;
    const u16* gwp = wphi + hp * 2048 + cw * 512;
    const u16* gdw = w_down + (size_t)(((gr >> 3) & 31) * 128 + 32 * cw) * DM + ((gr + 1) & 7) * 128;
    unsigned odw = (unsigned)((l >> 4) * DM + (l & 15) * 8) * 2;
    const u16* gw2a = wo + (size_t)(((cw * 10) ^ (flip * 64)) * 8) * DM + nb * 64;
    asm volatile("" : "+s"(gw2a));
    asm volatile("" : "+s"(gg_), "+s"(gu_), "+s"(gwo), "+s"(gwp), "+s"(gdw));
    { const int f = (gr >> 3) * 128 + 32 * cw + 4 * (l & 7), fs = (f >> 6) - ((l & 8) ? 3 : 1);
      ogq = WS_GUP + (unsigned)((l >> 4) * 56 + (fs < 0 ? fs + 56 : fs)) * 512 + (f & 63) * 4 + (l & 8) * 32; }
    asm volatile("" : "+v"(voG), "+v"(odw), "+v"(ogq));
    asm volatile("s_waitcnt lgkmcnt(0)" : "+v"(ofl), "+v"(orA), "+v"(orB), "+v"(orC), "+v"(oxs), "+v"(akey), "+v"(vol) :: "memory");
    stage(1, IC(1));
    WAIT_V(10);
    __builtin_amdgcn_s_barrier();
    auto wopf = [&]() {
      GLDS_S(voO, gwo, lbase + X_WO + 2 * cw * 1024); GLDS_S(voO, gwo + 8 * DM, lbase + X_WO + (2 * cw + 1) * 1024);
      GLDS_S(vol, gwp, lbase + X_WP + cw * 1024);
    };
    auto dvl = [&](auto kc) {
      constexpr int k = decltype(kc)::value;
      static_for<DVN[k + 2]>([&](auto ic) {
        constexpr int i = dv_first(k) + decltype(ic)::value;
        u32x4& d_ = dv[i]; const unsigned o_ = odw; const u16* p_ = gdw + (size_t)(4 * i) * DM;
        asm volatile("global_load_dwordx4 %0, %1, %2 nt" : "=v"(d_) : "v"(o_), "s"(p_) : "memory");
      });
    };
    stage(2, IC(2));
    const unsigned lgu = lbase + cw * 1024;
    auto gul = [&](auto ksc) {
      constexpr int ks = decltype(ksc)::value;
      gl_nt<gu_at(ks)>(voG, gg_ + (size_t)ks * 32 * FF, lgu); gl_nt<gu_at(ks) + 4096>(voG, gu_ + (size_t)ks * 32 * FF, lgu);
    };
#define BLD64(dst, off) asm volatile("buffer_load_dwordx2 %0, %1, %2, 0 offen sc1" : "=v"(dst) : "v"(off), "s"(rs) : "memory")
    static_for<15>([&](auto ktc) {
      constexpr int kt = decltype(ktc)::value;
      constexpr auto exw = [](int k) { return DVN[k + 2]; };
      constexpr auto exo = [](int k) { return k == KP ? 1 : k == KC + 1 ? 3 : k == KC + 2 ? 2 : 0; };
      constexpr int exa = ((kt - 2 == WOK || kt - 1 == WOK) ? 3 : 0) + (kt - 2 > KC ? exo(kt - 2) : 0);
      if constexpr (exw(kt - 2) + exw(kt - 1) != 0) { if (has_gu) WAIT_V(10 + exa + exw(kt - 2) + exw(kt - 1) + exo(kt - 1)); else WAIT_V(10 + exa + exo(kt - 1)); }
      else if constexpr (kt < 14) WAIT_V(10 + exa + exo(kt - 1));
      else { if (has_gu) WAIT_V(10); else WAIT_V(0); }
      if constexpr (PACE_T[kt] != 0) { if (cw == 0) { while ((unsigned)__builtin_amdgcn_s_memrealtime() - tpace < (unsigned)(has_gu ? PACE_T[kt] : PACE_3[kt])) {} } }
      __builtin_amdgcn_s_barrier();
      if constexpr (PACE_T[kt + 1] != 0) { if (cw == 0) tpace = (unsigned)__builtin_amdgcn_s_memrealtime(); SB0(); }
#define UMAX_DPP(C) { const unsigned o = (unsigned)__builtin_amdgcn_update_dpp(0, (int)key, C, 0xf, 0xf, true); key = o > key ? o : key; asm volatile("" : "+v"(key)); }
      if constexpr (kt + NST1 < 16) { SB0(); stage(kt + NST1, IC(kt % NST1)); SB0(); }
      if constexpr (kt == WOK) wopf();
      if constexpr (kt == PF2_KT) CODE_PF(2, ".Lkf_pf_mid", pfd2);
      if constexpr (kt == PF2_KT + 2) PF_DONE(pfd2);
      if constexpr (kt == KP) BLD64(fl, ofl);
      if constexpr (kt == KC) {
        static_assert(DVN[KP + 2] == 0, "count");
        if constexpr (KC == KP + 1) asm volatile("s_waitcnt vmcnt(10)" : "+v"(fl) :: "memory"); else asm volatile("" : "+v"(fl) :: "memory");
#define FL_OK() __all((((fl[0] ^ (unsigned)tag) >> 27) | (fl[1] ^ (unsigned)(tag >> 32))) == 0u)
        if (__builtin_expect(!FL_OK(), 0)) {
          for (unsigned sp = 0;;) {
            asm volatile("buffer_load_dwordx2 %0, %1, %2, 0 offen sc1\n\ts_waitcnt vmcnt(0)" : "=v"(fl) : "v"(ofl), "s"(rs) : "memory");
            if (FL_OK()) break;
            SPIN_NEXT(sp);
          }
        }
        unsigned key = fl[0];
        UMAX_DPP(0x121) UMAX_DPP(0x122) UMAX_DPP(0x124) UMAX_DPP(0x128)
        asm volatile("s_mov_b64 exec, %2\n\tds_max_u32 %0, %1\n\ts_mov_b64 exec, -1\n\ts_waitcnt lgkmcnt(0)" :: "v"(akey), "v"(key), "s"(0x0001000100010001ull) : "memory");
      }
      if constexpr (kt == KC + 1) { GLDS_SC1(orA, ws, lbase + X_XW + cw * 1024); GLDS_SC1(orB, ws, lbase + X_XW + 4096 + cw * 1024); GLDS_SC1(orC, ws, lbase + X_ST + (cw & 1) * 1024); }
      if constexpr (kt == KC + 2) {
        const int bi = 2047 - (int)(__builtin_amdgcn_readfirstlane(*(const unsigned*)(smem + X_AM + 4)) & 2047u);
        asm volatile("global_load_dword %0, %1, %2" : "=v"(xs) : "v"(oxs), "s"(x + (size_t)bi * DM) : "memory");
        asm volatile("buffer_load_dwordx2 %0, off, %1, %2 sc1" : "=v"(si) : "s"(rs), "s"(WS_STAT + (unsigned)bi * 8) : "memory");
      }
      if constexpr (DVN[kt + 2] != 0) { if (has_gu) dvl(ktc); }
      if constexpr (kt == 0) kvsetup();
      if constexpr (kt == 13) { if (has_gu) { gul(IC(0)); gul(IC(1)); gul(IC(2)); gul(IC(3)); gul(IC(4)); } }
      if constexpr (kt == 14) asm volatile("" : "+v"(xs), "+v"(si) :: "memory");
      if constexpr (kt == 14) { if (has_gu) { static_for<1>([&](auto ic) { gul(IC(5 + decltype(ic)::value)); }); } }
    });
    auto wld = [&](auto ic) {
      constexpr int i = decltype(ic)::value;
      u32x4& d_ = wr_[i]; const unsigned o_ = voO; const u16* p_ = gw2a + (size_t)(i * 8) * DM;
      asm volatile("global_load_dwordx4 %0, %1, %2" : "=v"(d_) : "v"(o_), "s"(p_) : "memory");
    };
    wpload();
    rt = own(*(const float*)(smem + X_ST + (wt * 32 + r31) * 4));
    {
      const float m = __uint_as_float(si[0]), den = own(__uint_as_float(si[1])), rinv = rcp_nr(den);
      *(unsigned*)(smem + X_XS + cw * 128 + r31 * 4) = ln_pair(xs, m, den, rinv);
    }
    RAW_BARRIER();
    const char* ho = smem + HANDOFF + cw * 6144 + l * 16;
    fr[2] = *(const bf16x8*)(ho + 2048); fr[3] = *(const bf16x8*)(ho + 3072);
    f32x2 ep[8], en[8];
    phi_fn(wp, fr[2], fr[3], ep, en, rt, rt, [&](auto kc) {
      constexpr int k = decltype(kc)::value;
      if constexpr (k == 0) { SB0(); if (has_gu) gul(IC(6)); SB0(); }
      if constexpr (k == 5) { SB0(); if (has_gu) gul(IC(7)); SB0(); }
      static_for<NREG - WPOST>([&](auto ic) {
        constexpr int i = decltype(ic)::value;
        if constexpr (k == WH0 + (i * (17 - WH0)) / (NREG - WPOST)) { SB0(); wld(ic); SB0(); }
      });
    });
    { const unsigned a_ = lbase + X_ST + 512 + (l >> 4) * 8;
      static_for<4>([&](auto tc) { constexpr int tb = decltype(tc)::value; (void)&dn; (void)&a_;
        asm volatile("ds_read2_b64 %0, %1 offset0:%2 offset1:%3" : "=v"(dn[tb]) : "v"(a_), "n"(tb * 8), "n"(tb * 8 + 4)); }); }
#pragma unroll
    for (int g = 0; g < 4; ++g) imgw(g, ep, en);
    SB0();
    static_for<WPOST>([&](auto ic) { wld(IC(NREG - WPOST + decltype(ic)::value)); });
  }
  asm volatile(".Lkf_pf_mid:");
  f32x16 zero;
#pragma unroll
  for (int i = 0; i < 16; ++i) zero[i] = 0.f;
  u32x2 tg; unsigned t8, rkey; u32x4 t2;
  {
    __builtin_amdgcn_s_setprio(2);
    RAW_BARRIER();
    asm volatile("" : "+v"(dn[0]), "+v"(dn[1]), "+v"(dn[2]), "+v"(dn[3]));
    u32x4 am; unsigned rk;
    { const unsigned a_ = lbase + X_AM; DS_B128(am, a_, 48); DS_B128(t2, a_, 32); asm volatile("ds_read_b32 %0, %1 offset:4" : "=v"(rk) : "v"(a_)); }
    s16x4 al[4], ah[4]; bf16x8 v0[4], v1[4];
    DS_TR(al[0], pa0, 0); DS_TR(ah[0], pa0, 2048); DS_B128(v0[0], pvh, 0); DS_B128(v1[0], pvh, 1024);
    DS_TR(al[1], pa0, 4096); DS_TR(ah[1], pa0, 4096 + 2048); DS_B128(v0[1], pvh, 4096); DS_B128(v1[1], pvh, 4096 + 1024);
    DS_TR(al[2], pa0, 8192); DS_TR(ah[2], pa0, 8192 + 2048); DS_B128(v0[2], pvh, 6144); DS_B128(v1[2], pvh, 6144 + 1024);
    DS_TR(al[3], pa0, 12288); DS_TR(ah[3], pa0, 12288 + 2048); DS_B128(v0[3], pvh, 6144 + 4096); DS_B128(v1[3], pvh, 6144 + 4096 + 1024);
    f32x4 k0 = {0.f, 0.f, 0.f, 0.f}, k1 = k0, zz = k0;
    static_for<4>([&](auto tbc) {
      constexpr int tb = decltype(tbc)::value;
      asm volatile("s_waitcnt lgkmcnt(%4)" : "+v"(al[tb]), "+v"(ah[tb]), "+v"(v0[tb]), "+v"(v1[tb]) : "n"(12 - 4 * tb));
      const bf16x8 af = cat8(al[tb], ah[tb]);
      k0 = MFMA16(af, v0[tb], k0); k1 = MFMA16(af, v1[tb], k1); zz = MFMA16(af, dn[tb], zz);
      SB0();
    });
    asm volatile("" : "+v"(am), "+v"(rk), "+v"(t2));
    auto tagv = [&](f32x4 v) { const u32x4 u = __builtin_bit_cast(u32x4, v); return (u & 0xFFFFFF00u) | (am[2] & 0xFFu); };
    ST128((u32x4{pk2(k0[0], k0[1]), pk2(k0[2], k0[3]), pk2(k1[0], k1[1]), pk2(k1[2], k1[3])} & 0xFFFEFFFEu) | t2, dst);
    const u32x4 zt = tagv(zz);
    asm volatile("s_mov_b64 exec, %3\n\tbuffer_store_dwordx4 %0, %1, %2, 0 offen sc1\n\ts_mov_b64 exec, -1" :: "v"(zt), "v"(dz), "s"(rs), "s"(0x0001000100010001ull) : "memory");
    SB0();
    tg = u32x2{am[0], am[1]}; t8 = __builtin_amdgcn_readfirstlane(am[2]) & 0xFFu; rkey = __builtin_amdgcn_readfirstlane(rk);
  }
  const int idx = 2047 - (int)(rkey & 2047u);
  const int fb = kh ^ ((idx >> 4) & 1);
  const unsigned lxc = lbase + (cw < 2 ? 131072 : X_YS + 64 - 4096) + cw * 2048, lxv = w < 4 ? lxc : lbase + X_XW + cw * 2048;
  {
    bf16x8 xf[8];
    const unsigned px = lbase + X_XS + (l >> 4) * 16, hq = lbase + X_XW + cw * 2048 + l * 16;
    static_for<8>([&](auto kc) { constexpr int ks = decltype(kc)::value; (void)&xf; (void)&px; DS_B128(xf[ks], px, ks * 64); });
    if (w >= 4) {
      __builtin_amdgcn_s_setprio(0);
      WAIT_V(NREG + 2);
    } else {
      auto lo4 = [](u32x4 v) { return __builtin_bit_cast(s16x4, u32x2{v[0], v[1]}); };
      auto hi4 = [](u32x4 v) { return __builtin_bit_cast(s16x4, u32x2{v[2], v[3]}); };
#pragma unroll
      for (int tt = 0; tt < 4; ++tt) {
        const int t = tt ^ 2;
#pragma unroll
        for (int j = 0; j < 2; ++j) qa[t][j] = MFMA16(__builtin_bit_cast(bf16x8, wr_[13 + j]), __builtin_bit_cast(bf16x8, wr_[9 + t]), qa[t][j]);
#pragma unroll
        for (int j = 0; j < 2; ++j) qa[t][j] = MFMA16(__builtin_bit_cast(bf16x8, wr_[4 + j]), __builtin_bit_cast(bf16x8, wr_[t]), qa[t][j]);
        if (tt & 1) {
#pragma unroll
          for (int u = t - 1; u <= t; ++u)
#pragma unroll
            for (int j = 0; j < 2; ++j) qa[u][j] = MFMA16H(j ? hi4(wr_[8]) : lo4(wr_[8]), (u & 1) ? hi4(wr_[6 + (u >> 1)]) : lo4(wr_[6 + (u >> 1)]), qa[u][j]);
        }
      }
      const u32x4 q0 = pack_u(qa[2][0], qa[3][0]), q1 = pack_u(qa[2][1], qa[3][1]);
      asm volatile("ds_write_b128 %0, %1\n\tds_write_b128 %0, %2 offset:1024" :: "v"(hq), "v"(q0), "v"(q1) : "memory");
    }
    RAW_BARRIER();
    asm volatile("" : "+v"(xf[0]), "+v"(xf[1]), "+v"(xf[2]), "+v"(xf[3]), "+v"(xf[4]), "+v"(xf[5]), "+v"(xf[6]), "+v"(xf[7]));
    if (w >= 4) {
      { const u16* ga = wo + (size_t)(((cw * 10) ^ (flip * 64)) * 8) * DM + nb * 64; const u16* gb = wo + (size_t)(((104 + cw * 6) ^ (flip * 64)) * 8) * DM + nb * 64;
        static_for<16 - NREG>([&](auto ic) { constexpr int i = NREG + decltype(ic)::value;
          if constexpr (i < 10) gl_m0<i * 1024>(voO, ga + (size_t)(i * 8) * DM, lbase + cw * 10240); else gl_m0<(104 + i - 10) * 1024>(voO, gb + (size_t)((i - 10) * 8) * DM, lbase + cw * 6144); }); }
    }
    if (has_gu) {
      s16x4 gl[8], gh[8];
      static_for<8>([&](auto kc) { constexpr int ks = decltype(kc)::value; (void)&gl; (void)&gh; (void)&pgw;
        DS_TR(gl[ks], pgw, ks * 8192); DS_TR(gh[ks], pgw, ks * 8192 + 512); });
      f32x4 c0 = {0.f, 0.f, 0.f, 0.f};
      static_for<8>([&](auto kc) { constexpr int ks = decltype(kc)::value;
        asm volatile("s_waitcnt lgkmcnt(%2)" : "+v"(gl[ks]), "+v"(gh[ks]) : "n"(14 - 2 * ks));
        c0 = MFMA16(cat8(gl[ks], gh[ks]), xf[ks], c0);
        SB0(); });
      const u32x4 gt_ = f2u_tag(c0, t8);
      asm volatile("s_mov_b64 exec, %3\n\tbuffer_store_dwordx4 %0, %1, %2, 0 offen sc1\n\ts_mov_b64 exec, -1" :: "v"(gt_), "v"(ogu), "s"(rs), "s"(0x0001000100010001ull) : "memory");
    }
  }
  const int erow = cw * 32 + fb * 16 + (l & 15), ech = l >> 4;
  u32x4 xv[2];
  const bool sw = ech & 1;
  const bool has_idx = w < 4 && (idx >> 5) == p3 * 4 + w;
  unsigned xrt = 0;
  unsigned of4, op4, oo0, oo1, kba, kbz, odo, o2, oz2, wa_, wz_;
  auto later = [&]() {
    SB0();
    of4 = WS_F4 + (unsigned)(((panel * 4 + wt) * 16 + hp) * 2 + wn) * 32; op4 = WS_F4 + (unsigned)(((p3 * 4 + cw) * 16 + 8 * (kh ^ flip)) * 2 + (l & 15)) * 32;
    oo0 = WS_O + 512 + (unsigned)((((p3 * 4 + cw) * 2 + fb) * 32 + (kh ^ flip) * 16) * 128 + h * 64 + r31) * 16; oo1 = oo0 ^ 65536u;
    kba = lbase + X_KB + wn * 4224 + l * 16; kbz = lbase + X_KB + wn * 4224 + 4096 + h * 64;
    odo = WS_O + 512 + (unsigned)((((panel * 4 + wt) * 2 + blk) * 32 + hd) * 128 + h * 16 + (l & 15)) * 16;
    asm volatile("" : "+v"(kba), "+v"(kbz), "+v"(odo), "+v"(of4), "+v"(op4), "+v"(oo0), "+v"(oo1));
    SB0();
  };
  if (w < 4) {
    unsigned pfd3, od; u64 mkp;
    {
      u32x4 pa[2]; unsigned z0 = t8, z1 = t8;
#define H1LD(p) asm volatile("buffer_load_dwordx4 %0, %4, %6, 0 offen sc1\n\tbuffer_load_dwordx4 %1, %4, %6, %7 offen sc1\n\t" \
                   "s_cmp_lg_u32 %8, 3\n\ts_cbranch_scc1 1f\n\tbuffer_load_dword %2, %5, %6, 0 offen sc1\n\tbuffer_load_dword %3, %5, %6, 0 offen offset:256 sc1\n1:" \
                   : "=&v"(p[0]), "=&v"(p[1]), "+v"(z0), "+v"(z1) : "v"(oh1), "v"(oh1z), "s"(rs), "s"(65536), "s"(cw) : "memory", "scc")
      __builtin_amdgcn_s_sleep(H1_DELAY);
      H1LD(pa);
      SB0();
      CODE_PF(3, ".Lkf_pf_mid", pfd3);
      od = WS_KVF + (unsigned)(hr * 16 + panel) * 640 + ((l & 8) ? 512 + (l & 7) * 2 : cw * 128 + h * 64 + (l & 7) * 8 + blk * 4);
      mkp = cw == 3 ? 0x00FF00FF00FF55FFull : 0x00FF00FF00FF00FFull;
      asm volatile("" : "+s"(mkp));
      fr[0] = __builtin_bit_cast(bf16x8, pack_u(qa[0][0], qa[1][0])); fr[1] = __builtin_bit_cast(bf16x8, pack_u(qa[0][1], qa[1][1]));
      asm volatile("" : "+v"(od), "+v"(fr[0]), "+v"(fr[1]));
      asm volatile("global_load_ushort %0, %1, %2" : "=v"(xrt) : "v"((unsigned)(idx * DM + nb * 64 + 4 * (l & 15) + ech) * 2), "s"(x) : "memory");
      later();
      o2 = WS_KVF + (unsigned)(hp * 16 + wn * 8 + wm * 4 + h) * 640 + ((r31 & 12) * 2 + (r31 >> 4) * 4 + (r31 & 3)) * 16;
      oz2 = WS_KVF + (unsigned)(hp * 16 + wn * 8 + wm * 4 + (l & 3)) * 640 + 512;
      wa_ = kba + wm * 2048; wz_ = lbase + X_KB + wn * 4224 + 4096 + (l & 1) * 64 + (wm * 2 + ((l >> 1) & 1)) * 16;
      asm volatile("" : "+v"(o2), "+v"(oz2), "+v"(wa_), "+v"(wz_));
      unsigned hb = 0x8000u, s_lo = 0x05040100u, s_hi = 0x07060100u;
      asm volatile("" : "+s"(s_lo), "+s"(s_hi));
      asm volatile("s_waitcnt vmcnt(1)" : "+v"(pa[0]), "+v"(pa[1]), "+v"(z0), "+v"(z1), "+v"(hb) :: "memory");
      u64 b0, b1, b2; u32x4 v0, v1;
      auto miss = [&]() __attribute__((always_inline)) {
        v0 = pa[0] ^ t2; v1 = pa[1] ^ t2;
        b0 = __ballot((or4(v0) & 0x00010001u) != 0u); b1 = __ballot((or4(v1) & 0x00010001u) != 0u);
        b2 = __ballot((((z0 ^ t8) | (z1 ^ t8)) & 0xFFu) != 0u);
        return (b0 | b1 | b2) != 0;
      };
      if (miss()) {
        unsigned sp = 0;
        do {
        b0 |= (b0 >> 4) & 0x0F0F0F0F0F0F0F0Full; b0 |= (b0 << 4) & 0xF0F0F0F0F0F0F0F0ull; b1 |= (b1 >> 4) & 0x0F0F0F0F0F0F0F0Full; b1 |= (b1 << 4) & 0xF0F0F0F0F0F0F0F0ull;
        asm volatile("s_wqm_b64 exec, %8\n\ts_cbranch_execz 1f\n\tbuffer_load_dwordx4 %0, %4, %6, 0 offen sc1\n1:\n\ts_wqm_b64 exec, %9\n\ts_cbranch_execz 2f\n\tbuffer_load_dwordx4 %1, %4, %6, %7 offen sc1\n2:\n\t"
                     "s_mov_b64 exec, -1\n\ts_cmp_eq_u64 %10, 0\n\ts_cbranch_scc1 3f\n\tbuffer_load_dword %2, %5, %6, 0 offen sc1\n\tbuffer_load_dword %3, %5, %6, 0 offen offset:256 sc1\n3:\n\ts_waitcnt vmcnt(0)"
                     : "+v"(pa[0]), "+v"(pa[1]), "+v"(z0), "+v"(z1) : "v"(oh1), "v"(oh1z), "s"(rs), "s"(65536), "s"(b0), "s"(b1), "s"(b2) : "memory", "scc");
        } while (miss() && ++sp < SPIN_CAP);
      }
    auto sw32 = [](float a, float c) { const auto r = __builtin_amdgcn_permlane32_swap(__float_as_uint(a), __float_as_uint(c), false, false); return __uint_as_float(r[0]) + __uint_as_float(r[1]); };
    auto sw16 = [](float a, float c) { const auto r = __builtin_amdgcn_permlane16_swap(__float_as_uint(a), __float_as_uint(c), false, false); return __uint_as_float(r[0]) + __uint_as_float(r[1]); };
    float sl[4], sh[4];
#pragma unroll
    for (int m = 0; m < 4; ++m) {
      sl[m] = own(__uint_as_float(__builtin_amdgcn_perm(v0[m], hb, s_lo)) + __uint_as_float(__builtin_amdgcn_perm(v1[m], hb, s_lo)));
      sh[m] = own(__uint_as_float(__builtin_amdgcn_perm(v0[m], hb, s_hi)) + __uint_as_float(__builtin_amdgcn_perm(v1[m], hb, s_hi))); }
    const float e0 = dpp_add<0x128, 0xf>(sw16(sw32(sl[0], sl[2]), sw32(sl[1], sl[3]))), e1 = dpp_add<0x128, 0xf>(sw16(sw32(sh[0], sh[2]), sw32(sh[1], sh[3])));
    const float zs = x32_add(x16_add(dpp_add<0x128, 0xf>(__uint_as_float(z0 ^ t8) + __uint_as_float(z1 ^ t8))));
    const float zn = __builtin_bit_cast(float, __builtin_amdgcn_update_dpp(0, __builtin_bit_cast(int, zs), 0xB1, 0xf, 0xf, true));
    const unsigned kvb = min((l & 8) ? pk2(zs, zn) : pk2(e0, e1), SENT - 1u);
    asm volatile("s_mov_b64 exec, %3\n\tbuffer_store_dword %0, %1, %2, 0 offen sc1\n\ts_mov_b64 exec, -1" :: "v"(kvb), "v"(od), "s"(rs), "s"(mkp) : "memory");
    PF_DONE(pfd3);
    }
  } else {
    const char* hq = smem + X_XW + cw * 2048 + l * 16;
    fr[0] = *(const bf16x8*)hq; fr[1] = *(const bf16x8*)(hq + 1024);
  }
  bf16x8 pf[4];
  {
    f32x2 ep[8], en[8];
    phi_fn(wp, fr[0], fr[1], ep, en, rt, 1.0f, [](auto) {});
#pragma unroll
    for (int s = 0; s < 2; ++s) {
      pf[s] = __builtin_bit_cast(bf16x8, u32x4{pk2(ep[4 * s][0], ep[4 * s][1]), pk2(ep[4 * s + 1][0], ep[4 * s + 1][1]), pk2(ep[4 * s + 2][0], ep[4 * s + 2][1]), pk2(ep[4 * s + 3][0], ep[4 * s + 3][1])});
      pf[2 + s] = __builtin_bit_cast(bf16x8, u32x4{pk2(en[4 * s][0], en[4 * s][1]), pk2(en[4 * s + 1][0], en[4 * s + 1][1]), pk2(en[4 * s + 2][0], en[4 * s + 2][1]), pk2(en[4 * s + 3][0], en[4 * s + 3][1])});
    }
    asm volatile("" : "+v"(pf[0]), "+v"(pf[1]), "+v"(pf[2]), "+v"(pf[3]));
  }
  SB0();
  if (b == 0 && tid == 64) ST64(*(const u32x2*)(smem + X_AM + 56), WS_EPOCH);
  u32x4 gq; unsigned ayr; u32x4 mk4;
#define GQLD(tail) asm volatile("buffer_load_dwordx4 %0, %1, %2, 0 offen sc1" tail : "=v"(gq) : "v"(ogq), "s"(rs) : "memory")
  if (w >= 4) {
    {
      WAIT_V(18 - NREG);
    { const unsigned o_ = (unsigned)((p3 * 128 + cw * 32 + (l >> 3)) * DM + nb * 64 + ((l & 7) ^ (l >> 3)) * 8) * 2; const u16* xa = x + fb * 16 * DM; const u16* xb = x + (fb ^ 1) * 16 * DM;
      gl_m0<0>(o_, xa, lxv); gl_m0<1024>(o_, xa + 8 * DM, lxv); gl_m0<0>(o_, xb, lxc); gl_m0<1024>(o_, xb + 8 * DM, lxc); }
      SB0();
      char* pa_ = smem + cw * 10 * 1024 + l * 16;
#pragma unroll
      for (int i = 0; i < NREG; ++i) *(u32x4*)(pa_ + i * 1024) = wr_[i];
      SB0();
    }
    asm volatile("" : "+v"(dv[0]), "+v"(dv[1]), "+v"(dv[2]), "+v"(dv[3]), "+v"(dv[4]), "+v"(dv[5]), "+v"(dv[6]), "+v"(dv[7]) :: "memory");
    later();
    ayr = lbase + X_YR + (cw * 128 + (l & 15) * 8 + blk * 4) * 4;
    { const unsigned hm = (l & 1) ? 0xFFFF0000u : 0x0000FFFFu; const int q = (l & 15) >> 1;
      mk4 = u32x4{q == 0 ? hm : 0u, q == 1 ? hm : 0u, q == 2 ? hm : 0u, q == 3 ? hm : 0u}; }
    asm volatile("" : "+v"(ayr), "+v"(mk4));
    SB0();
  }
  auto downp = [&]() __attribute__((always_inline)) {
            auto red4 = [](float s0, float s1, float s2, float s3) {
              const auto r01 = __builtin_amdgcn_permlane16_swap(__float_as_uint(s0), __float_as_uint(s1), false, false);
              const auto r23 = __builtin_amdgcn_permlane16_swap(__float_as_uint(s2), __float_as_uint(s3), false, false);
              const float t01 = __uint_as_float(r01[0]) + __uint_as_float(r01[1]), t23 = __uint_as_float(r23[0]) + __uint_as_float(r23[1]);
              const auto r = __builtin_amdgcn_permlane32_swap(__float_as_uint(t01), __float_as_uint(t23), false, false);
              return __uint_as_float(r[0]) + __uint_as_float(r[1]);
            };
            const float gg = rbf(red4(__uint_as_float(gq[0]), __uint_as_float(gq[1]), __uint_as_float(gq[2]), __uint_as_float(gq[3])));
            const float uu = __builtin_bit_cast(float, __builtin_amdgcn_update_dpp(0, __builtin_bit_cast(int, gg), 0x128, 0xf, 0xf, true));
            const float hv0 = rbf(silu_r(gg)) * uu;
            const int hh = (int)pk2(hv0, hv0);
            f32x4 ya = {0.f, 0.f, 0.f, 0.f};
            static_for<8>([&](auto ic) {
              constexpr int i = decltype(ic)::value;
              u32x4 sel;
#pragma unroll
              for (int e = 0; e < 4; ++e) sel[e] = (unsigned)__builtin_amdgcn_update_dpp(0, hh, 0x150 + i, 0xf, 0xf, true) & mk4[e];
              ya = MFMA16(__builtin_bit_cast(bf16x8, sel), __builtin_bit_cast(bf16x8, dv[i]), ya);
            });
            asm volatile("" : "+v"(ya));
            if (l < 32) *(__attribute__((address_space(3))) f32x4*)(uintptr_t)ayr = ya;
  };
  {
    u32x4 kf[4], zf[4];
    if (w < 4) {
      u32x4 a, c, z; u64 m0 = ~0ull, m1 = ~0ull, m2 = ~0ull;
      unsigned sp = 0;
      do {
        asm volatile("s_mov_b64 exec, %8\n\ts_cbranch_execz 1f\n\tbuffer_load_dwordx4 %0, %3, %5, 0 offen sc1\n1:\n\ts_mov_b64 exec, %9\n\ts_cbranch_execz 2f\n\tbuffer_load_dwordx4 %1, %3, %5, 0 offen offset:1280 sc1\n2:\n\t"
                     "s_mov_b64 exec, %10\n\ts_cbranch_execz 3f\n\tbuffer_load_dwordx4 %2, %4, %5, 0 offen sc1\n3:\n\ts_mov_b64 exec, -1\n\ts_waitcnt vmcnt(0)\n\t"
                     "ds_write_b128 %6, %0\n\tds_write_b128 %6, %1 offset:1024\n\tds_write_b128 %7, %2"
                     : "+v"(a), "+v"(c), "+v"(z) : "v"(o2), "v"(oz2), "s"(rs), "v"(wa_), "v"(wz_), "s"(m0), "s"(m1), "s"(m2) : "memory");
        STUB(9);
        m0 = __ballot(max(max(max(a[0], a[1]), a[2]), a[3]) == SENT); m1 = __ballot(max(max(max(c[0], c[1]), c[2]), c[3]) == SENT); m2 = __ballot(max(max(max(z[0], z[1]), z[2]), z[3]) == SENT);
      } while ((m0 | m1 | m2) != 0 && ++sp < SPIN_CAP);
      asm volatile("" : "+v"(xrt));
    }
    RAW_BARRIER();
    DS_B128(kf[0], kba, 0); DS_B128(zf[0], kbz, 0); DS_B128(kf[1], kba, 1024); DS_B128(zf[1], kbz, 16);
    DS_B128(kf[2], kba, 2048); DS_B128(zf[2], kbz, 32); DS_B128(kf[3], kba, 3072); DS_B128(zf[3], kbz, 48);
    if (w >= 4 && has_gu) GQLD("");
    __builtin_amdgcn_s_setprio(0);
    f32x16 num = zero;
    float d0 = 0.f, d1 = 0.f, d2 = 0.f, d3 = 0.f;
    static_for<4>([&](auto cc) {
      constexpr int c = decltype(cc)::value;
      asm volatile("s_waitcnt lgkmcnt(%0)" :: "n"(6 - 2 * c));
      SB0();
      num = MFMA(__builtin_bit_cast(bf16x8, kf[c]), pf[c], num);
      SB0();
      const u32x4 pc = __builtin_bit_cast(u32x4, pf[c]);
      d0 = dot2(zf[c][0], pc[0], d0); d1 = dot2(zf[c][1], pc[1], d1); d2 = dot2(zf[c][2], pc[2], d2); d3 = dot2(zf[c][3], pc[3], d3);
      SB0();
    });
    const float rinv = __builtin_amdgcn_rcpf(x32_add(own(d0 + d1) + own(d2 + d3)) + 1e-6f);
    const f32x2 rr = {rinv, rinv};
#pragma unroll
    for (int q = 0; q < 2; ++q) {
      u32x4 u;
#pragma unroll
      for (int e = 0; e < 4; ++e) {
        const f32x2 r = f32x2{num[8 * q + 2 * e], num[8 * q + 2 * e + 1]} * rr;
        u[e] = pk2(r[0], r[1]);
      }
      ST128(u, odo + q * 1024);
      SB0();
    }
  }
  const int p0 = 8 * w + 40;
  const u16* gw2 = wo + (size_t)((p0 ^ (flip * 64)) * 8) * DM + nb * 64; unsigned lw2 = lbase + p0 * 1024;
  asm volatile("" : "+s"(gw2), "+s"(lw2));
  bool glate = false;
  if (w >= 4 && has_gu) {
    asm volatile("s_waitcnt vmcnt(2)" : "+v"(gq) :: "memory");
    gq ^= t8;
    glate = !__all((or4(gq) & 0xFFu) == 0u);
    if (!glate) downp();
  }
  asm volatile("s_waitcnt vmcnt(0)\n\ts_mov_b64 exec, 1\n\tbuffer_store_dwordx2 %0, %1, %2, 0 offen sc1\n\ts_mov_b64 exec, -1" :: "v"(tg), "v"(of4), "s"(rs) : "memory");
  if (__builtin_expect(glate, 0)) {
    for (unsigned sp = 0;;) {
      GQLD("\n\ts_waitcnt vmcnt(0)");
      gq ^= t8;
      if (__all((or4(gq) & 0xFFu) == 0u)) break;
      SPIN_NEXT(sp);
    }
    downp();
  }
  RAW_BARRIER();
  float rxn = 0.f, rsg = 0.f;
  u32x4 yq[7];
  unsigned oy = WS_YP + (unsigned)(l >> 4) * 4096 + nb * 256 + (l & 15) * 16;
#define YLD(tail) asm volatile("buffer_load_dwordx4 %0, %7, %8, 0 offen sc1\n\tbuffer_load_dwordx4 %1, %7, %8, %9 offen sc1\n\tbuffer_load_dwordx4 %2, %7, %8, %10 offen sc1\n\t" \
    "buffer_load_dwordx4 %3, %7, %8, %11 offen sc1\n\tbuffer_load_dwordx4 %4, %7, %8, %12 offen sc1\n\tbuffer_load_dwordx4 %5, %7, %8, %13 offen sc1\n\t" \
    "buffer_load_dwordx4 %6, %7, %8, %14 offen sc1" tail \
    : "=&v"(yq[0]), "=&v"(yq[1]), "=&v"(yq[2]), "=&v"(yq[3]), "=&v"(yq[4]), "=&v"(yq[5]), "=&v"(yq[6]) \
    : "v"(oy), "s"(rs), "s"(16384), "s"(2 * 16384), "s"(3 * 16384), "s"(4 * 16384), "s"(5 * 16384), "s"(6 * 16384) : "memory")
  f32x2 xf[2][4], gf[2][4];
  __attribute__((address_space(3))) f32x4* pw = (__attribute__((address_space(3))) f32x4*)(smem + 131072 + w * 4096) + l;
  unsigned prd = lbase + 131072 + (w ^ 4) * 4096 + l * 16;
  u16* po = out + (size_t)(p3 * 128 + cw * 32 + fb * 16 + (l >> 3)) * DM + nb * 64 + (l & 7) * 8; u16* po2 = po + 8 * DM;
  unsigned tw0 = lbase + 131072 + (w ^ 4) * 4096 + (l & 15) * 128 + ((ech ^ (l & 7)) * 16) + (sw ? 8 : 0), tw1 = tw0 ^ 64, tw2 = tw0 ^ 8, tw3 = tw0 ^ 72, trd = lbase + 131072 + (w ^ 4) * 4096 + (l >> 3) * 128 + (((l & 7) ^ (l >> 3)) * 16);
    unsigned bw[4];
#pragma unroll
    for (int n = 0; n < 4; ++n) bw[n] = lbase + kh * 65536 + (16 * h + 4 * blk + tq) * 128 + (((4 * (n >> 1) + tp) ^ ((tq >> 1) | (blk << 2))) * 16) + 8 * ((n ^ tp) & 1);
  {
  if (w == 1 && has_gu && l < 32) {
    const f32x4* y = (const f32x4*)(smem + X_YR) + l;
    ST128(f2u_tag((y[0] + y[32]) + (y[64] + y[96]), t8), WS_YP + (unsigned)(gr >> 3) * 4096 + ((gr + 1) & 7) * 512 + l * 16);
  }
    SB0();
    gl_run<8>(voO, gw2, lw2);
    u32x2 f0;
    asm volatile("buffer_load_dwordx2 %0, %1, %2, 0 offen sc1" : "=v"(f0) : "v"(op4), "s"(rs) : "memory");
    SB0();
    {
      const f32x2 md = *(const f32x2*)(smem + X_MD + erow * 8);
      { const unsigned ax = lxv + (l & 15) * 128 + ((ech ^ (l & 7)) * 16), a0 = ax + (sw ? 8 : 0), a1 = ax + (sw ? 0 : 8);
        u32x2 q0, q1, q2, q3;
        asm volatile("ds_read_b64 %0, %4\n\tds_read_b64 %1, %5\n\tds_read_b64 %2, %6\n\tds_read_b64 %3, %7\n\ts_waitcnt lgkmcnt(0)" : "=&v"(q0), "=&v"(q1), "=&v"(q2), "=&v"(q3) : "v"(a0), "v"(a1), "v"(a0 ^ 64), "v"(a1 ^ 64) : "memory");
        xv[0] = u32x4{q0[0], q0[1], q1[0], q1[1]}; xv[1] = u32x4{q2[0], q2[1], q3[0], q3[1]}; }
      const float rinv = rcp_nr(own(md[1])), mr = -md[0] * rinv, L2E = 1.44269504088896340736f, rl = -L2E * rinv, ml = -L2E * mr;
#pragma unroll
      for (int i = 0; i < 2; ++i)
#pragma unroll
        for (int e = 0; e < 4; ++e) {
          xf[i][e] = f32x2{bflo(xv[i][e]), bfhi(xv[i][e])};
          const f32x2 xn = __builtin_elementwise_fma(xf[i][e], f32x2{rinv, rinv}, f32x2{mr, mr}), tn = __builtin_elementwise_fma(xf[i][e], f32x2{rl, rl}, f32x2{ml, ml});
          const f32x2 dd = f32x2{__builtin_amdgcn_exp2f(tn[0]), __builtin_amdgcn_exp2f(tn[1])} + f32x2{1.0f, 1.0f};
          gf[i][e] = xn * f32x2{__builtin_amdgcn_rcpf(dd[0]), __builtin_amdgcn_rcpf(dd[1])};
        }
#pragma unroll
      for (int i = 0; i < 2; ++i) asm volatile("" : "+v"(xf[i][0]), "+v"(xf[i][1]), "+v"(xf[i][2]), "+v"(xf[i][3]), "+v"(gf[i][0]), "+v"(gf[i][1]), "+v"(gf[i][2]), "+v"(gf[i][3]));
      asm volatile("" : "+v"(pw), "+v"(prd), "+v"(po2), "+v"(tw0), "+v"(tw1), "+v"(tw2), "+v"(tw3), "+v"(trd));
      asm volatile("" : "+v"(oy), "+v"(po), "+v"(bw[0]), "+v"(bw[1]), "+v"(bw[2]), "+v"(bw[3]));
    }
    if (has_idx) {
      const unsigned ord = (rkey >> 11) & 0xFFFFu;
      rsg = rbf(1.0f / (1.0f + __expf(-__uint_as_float((ord ^ ((ord & 0x8000u) ? 0x8000u : 0xFFFFu)) << 16))));
      const f32x2 md = *(const f32x2*)(smem + X_MD + (idx & 127) * 8);
      const float dn = own(md[1]), rinv = rcp_nr(dn);
      rxn = bflo(ln_pair(xrt, md[0], dn, rinv));
      asm volatile("" : "+v"(rxn), "+v"(rsg));
      { u64 pc_; unsigned d_, o_;
        asm volatile("s_getpc_b64 %0\n.Lkf_own1:\n\ts_mov_b32 %1, .Lkf_own0-.Lkf_own1" : "=s"(pc_), "=s"(o_));
        pc_ += o_;
        asm volatile("s_load_dword %0, %1, 0x0\n\ts_load_dword %0, %1, 0x80\n\ts_load_dword %0, %1, 0x100\n\ts_load_dword %0, %1, 0x180\n\ts_load_dword %0, %1, 0x200\n\ts_load_dword %0, %1, 0x280\n\ts_waitcnt lgkmcnt(0)" : "=&s"(d_) : "s"(pc_) : "memory"); }
    }
    u64 ne;
    SB0();
    asm volatile("s_waitcnt vmcnt(0)\n\tv_cmp_ne_u64 %0, %1, %2" : "=s"(ne) : "v"(f0), "v"(tg) : "memory");
    for (unsigned sp = 0; ne != 0 && sp < SPIN_CAP; ++sp) {
      u32x2 f;
      asm volatile("buffer_load_dwordx2 %1, %2, %3, 0 offen sc1\n\ts_waitcnt vmcnt(0)\n\tv_cmp_ne_u64 %0, %1, %4" : "=s"(ne), "=&v"(f) : "v"(op4), "s"(rs), "v"(tg) : "memory");
    }
  }
  constexpr int OWIN = 4;
  u32x4 of[16][2];
  {
    SB0();
#pragma unroll
    for (int i = 0; i < OWIN; ++i) { of[i][0] = LD128(oo0 + i * 2048); of[i][1] = LD128(oo1 + i * 2048); }
    SB0();
    if (has_idx) YLD("");
    SB0();
  }
    auto owner = [&](auto nc) __attribute__((always_inline)) {
      asm volatile(".Lkf_own0:\n\ts_waitcnt vmcnt(%7)" : "+v"(yq[0]), "+v"(yq[1]), "+v"(yq[2]), "+v"(yq[3]), "+v"(yq[4]), "+v"(yq[5]), "+v"(yq[6]) : "n"(decltype(nc)::value) : "memory");
      for (unsigned sp = 0;; ++sp) {
        unsigned bad = 0;
#pragma unroll
        for (int i = 0; i < 7; ++i) { yq[i] ^= t8; bad |= or4(yq[i]); }
        if (__builtin_expect(__all((bad & 0xFFu) == 0u) || sp > SPIN_CAP, 1)) break;
        YLD("\n\ts_waitcnt vmcnt(0)");
      }
#define YF(i) __builtin_bit_cast(f32x4, yq[i])
      const f32x4 y4 = ((YF(0) + YF(1)) + (YF(2) + YF(3))) + ((YF(4) + YF(5)) + YF(6));
      const auto r01 = __builtin_amdgcn_permlane16_swap(__float_as_uint(y4[0]), __float_as_uint(own(y4[1])), false, false);
      const auto r23 = __builtin_amdgcn_permlane16_swap(__float_as_uint(y4[2]), __float_as_uint(own(y4[3])), false, false);
      const float t01 = __uint_as_float(r01[0]) + __uint_as_float(r01[1]), t23 = __uint_as_float(r23[0]) + __uint_as_float(r23[1]);
      const auto r = __builtin_amdgcn_permlane32_swap(__float_as_uint(t01), __float_as_uint(t23), false, false);
      const float y = __uint_as_float(r[0]) + __uint_as_float(r[1]);
      const float g1 = rbf(silu_r(rbf(rxn + rbf(rbf(y) * rsg))));
      (void)&gf; (void)&oy; (void)&rs; const u64 mk_ = 0x0001000100010001ull << (idx & 15);
      const unsigned gb = lbase + (131072 + w * 4096), aw_ = gb + (l & 15) * 16 + ech * 4, a0 = gb + ech * 32 + (ech & 1) * 16, a1 = a0 ^ 16;
      asm volatile("ds_write_b32 %8, %9\n\ts_mov_b64 exec, %12\n\t"
                   "ds_read_b64 %0, %10\n\tds_read_b64 %1, %10 offset:8\n\tds_read_b64 %2, %11\n\tds_read_b64 %3, %11 offset:8\n\t"
                   "ds_read_b64 %4, %10 offset:128\n\tds_read_b64 %5, %10 offset:136\n\tds_read_b64 %6, %11 offset:128\n\tds_read_b64 %7, %11 offset:136\n\t"
                   "s_mov_b64 exec, -1\n\ts_waitcnt lgkmcnt(0)"
                   : "+v"(gf[0][0]), "+v"(gf[0][1]), "+v"(gf[0][2]), "+v"(gf[0][3]), "+v"(gf[1][0]), "+v"(gf[1][1]), "+v"(gf[1][2]), "+v"(gf[1][3])
                   : "v"(aw_), "v"(g1), "v"(a0), "v"(a1), "s"(mk_) : "memory");
    };
  f32x4 acc3[2][4];
#pragma unroll
  for (int t = 0; t < 2; ++t)
#pragma unroll
    for (int n = 0; n < 4; ++n) acc3[t][n] = f32x4{0.f, 0.f, 0.f, 0.f};
  {
    s16x4 wa[2][8];
    auto rd3 = [&](auto kc, auto nc) {
      constexpr int k = decltype(kc)::value, n = decltype(nc)::value;
      (void)&wa; (void)&bw;
      DS_TR(wa[k & 1][2 * n], bw[n], k * 4096); DS_TR(wa[k & 1][2 * n + 1], bw[n], k * 4096 + 1024);
    };
    RAW_BARRIER();
    static_for<4>([&](auto nc) { rd3(IC(0), nc); });
    static_for<15>([&](auto kc) {
      constexpr int k = decltype(kc)::value, sx = k & 1;
      static_for<4>([&](auto nc) {
        constexpr int n = decltype(nc)::value;
        asm volatile("s_waitcnt lgkmcnt(%2)" : "+v"(wa[sx][2 * n]), "+v"(wa[sx][2 * n + 1]) : "n"(6));
        SB0();
        const bf16x8 wf = cat8(wa[sx][2 * n], wa[sx][2 * n + 1]);
        acc3[0][n] = MFMA16(wf, __builtin_bit_cast(bf16x8, of[k][0]), acc3[0][n]);
        SB0();
        if constexpr (k * 4 + n == 12) STUB(10);
        if constexpr (n == 0 && (k & 3) == 0) __builtin_amdgcn_s_setprio(3 - (k >> 2));
        if constexpr (k + 1 < 16) rd3(IC(k + 1), nc);
        if constexpr (k + OWIN < 16 && n == 3) { of[k + OWIN][0] = LD128(oo0 + (k + OWIN) * 2048); }
        SB0();
        acc3[1][n] = MFMA16(wf, __builtin_bit_cast(bf16x8, of[k][1]), acc3[1][n]);
        SB0();
        if constexpr (k + OWIN < 16 && n == 3) { of[k + OWIN][1] = LD128(oo1 + (k + OWIN) * 2048); SB0(); }
        if constexpr (k == YP_ && n == 3) { constexpr int e_ = YP_ + OWIN < 16 ? YP_ : 15 - OWIN; if (has_idx) owner(IC(2 * (e_ + 1))); SB0(); }
      });
    });
    static_for<4>([&](auto nc) {
      constexpr int n = decltype(nc)::value;
      asm volatile("s_waitcnt lgkmcnt(%2)" : "+v"(wa[1][2 * n]), "+v"(wa[1][2 * n + 1]) : "n"(6 - 2 * n));
      SB0();
      acc3[1][n] = MFMA16(cat8(wa[1][2 * n], wa[1][2 * n + 1]), __builtin_bit_cast(bf16x8, of[15][1]), acc3[1][n]);
      SB0();
      if constexpr (n == 3) { pw[0] = acc3[1][0]; SB0(); }
    });
    static_for<4>([&](auto nc) {
      constexpr int n = decltype(nc)::value;
      acc3[0][n] = MFMA16(cat8(wa[1][2 * n], wa[1][2 * n + 1]), __builtin_bit_cast(bf16x8, of[15][0]), acc3[0][n]);
      SB0();
      if constexpr (n < 3) { pw[(n + 1) * 64] = acc3[1][n + 1]; SB0(); }
    });
  }
  {
    RAW_BARRIER();
    f32x4 pr[4];
    DS_B128(pr[0], prd, 0); DS_B128(pr[1], prd, 1024); DS_B128(pr[2], prd, 2048); DS_B128(pr[3], prd, 3072);
    SB0();
#pragma unroll
    for (int i = 0; i < 2; ++i)
#pragma unroll
      for (int e = 0; e < 4; ++e) {
        const f32x4 a = acc3[0][2 * i + (e >> 1)];
        xf[i][e] = __builtin_elementwise_fma(f32x2{a[2 * (e & 1)], a[2 * (e & 1) + 1]}, gf[i][e], xf[i][e]);
        asm volatile("" : "+v"(xf[i][e]));
      }
    SB0();
    u32x4 res[2];
#pragma unroll
    for (int i = 0; i < 2; ++i) {
      if (i == 0) asm volatile("s_waitcnt lgkmcnt(2)" : "+v"(pr[0]), "+v"(pr[1])); else asm volatile("s_waitcnt lgkmcnt(2)" : "+v"(pr[2]), "+v"(pr[3]));
      const f32x4 s0 = pr[2 * i], s1 = pr[2 * i + 1];
      const f32x2 o0 = __builtin_elementwise_fma(f32x2{s0[0], s0[1]}, gf[i][0], xf[i][0]), o1 = __builtin_elementwise_fma(f32x2{s0[2], s0[3]}, gf[i][1], xf[i][1]);
      const f32x2 o2 = __builtin_elementwise_fma(f32x2{s1[0], s1[1]}, gf[i][2], xf[i][2]), o3 = __builtin_elementwise_fma(f32x2{s1[2], s1[3]}, gf[i][3], xf[i][3]);
      res[i] = u32x4{pk2(o0[0], o0[1]), pk2(o1[0], o1[1]), pk2(o2[0], o2[1]), pk2(o3[0], o3[1])};
      asm volatile("ds_write_b64 %0, %2\n\tds_write_b64 %1, %3" :: "v"(i ? tw1 : tw0), "v"(i ? tw3 : tw2), "v"(u32x2{res[i][0], res[i][1]}), "v"(u32x2{res[i][2], res[i][3]}) : "memory");
    }
    asm volatile("ds_read_b128 %0, %2\n\tds_read_b128 %1, %2 offset:1024\n\ts_waitcnt lgkmcnt(1)\n\t"
                 "global_store_dwordx4 %3, %0, off sc1\n\ts_waitcnt lgkmcnt(0)\n\tglobal_store_dwordx4 %4, %1, off sc1\n\ts_nop 1"
                 : "=&v"(res[0]), "=&v"(res[1]) : "v"(trd), "v"(po), "v"(po2) : "memory");
  }
  asm volatile(".Lkf_pf_end:" ::: "memory");
}

extern "C" void kernel_launch(void* const* d_in, const int* in_sizes, int n_in,
                              void* d_out, int out_size, void* d_ws, size_t ws_size,
                              hipStream_t stream) {
  (void)hipFuncSetAttribute((const void*)kf_all, hipFuncAttributeMaxDynamicSharedMemorySize, LDS_TOTAL);
  kf_all<<<256, 512, LDS_TOTAL, stream>>>((const u16*)d_in[0], (const u16*)d_in[1], (const u16*)d_in[2], (const u16*)d_in[3],
                                          (const u16*)d_in[5], (const u16*)d_in[6], (const u16*)d_in[7], (const u16*)d_in[8],
                                          (char*)d_ws, (u16*)d_out);
}
```

```cpp
#define KP 9
#define KC 10
#define F1_S 13
#define PF2_KT 8
#define WOK 2
#define H1_DELAY 4
constexpr int PACE_T[16] = {0, 114, 84, 64}, PACE_3[16] = {0, 70, 60, 56};
constexpr int DVN[20] = {0, 0, 4, 2, 1, 0, 1};
constexpr int dv_first(int k) { int n = 0; for (int i = -2; i < k; ++i) n += DVN[i + 2]; return n; }
constexpr bool CSS(int S) { return S < 2 || S == 6 || S == 7; }
#define NREG 8
static_assert(NREG <= 10, "first run");
#define WH0 12
#define WPOST 4
#define YP_ 11
#define PRIO_T 2
#include <hip/hip_runtime.h>
#include <hip/hip_bf16.h>
#include <stdint.h>
#include <utility>
#include <type_traits>

typedef __attribute__((ext_vector_type(8))) short bf16x8;
typedef __attribute__((ext_vector_type(4))) short s16x4;
typedef __attribute__((ext_vector_type(16))) float f32x16;
typedef __attribute__((ext_vector_type(4))) float f32x4;
typedef __attribute__((ext_vector_type(2))) float f32x2;
typedef __attribute__((ext_vector_type(4))) unsigned u32x4;
typedef __attribute__((ext_vector_type(2))) unsigned u32x2;
typedef __attribute__((ext_vector_type(2))) __bf16 bfx2;
typedef __attribute__((ext_vector_type(4))) short v4i16_t;
typedef unsigned long long u64;
typedef unsigned short u16;

#define DI __device__ __forceinline__
#define MFMA(a, b, c) __builtin_amdgcn_mfma_f32_32x32x16_bf16((a), (b), (c), 0, 0, 0)
#define MFMA16H(a, b, c) __builtin_amdgcn_mfma_f32_16x16x16bf16_1k((a), (b), (c), 0, 0, 0)
#define MFMA16(a, b, c) __builtin_amdgcn_mfma_f32_16x16x32_bf16((a), (b), (c), 0, 0, 0)

DI unsigned pk2(float a, float b) { f32x2 v = {a, b}; return __builtin_bit_cast(unsigned, __builtin_convertvector(v, bfx2)); }
DI float bflo(unsigned u) { return __uint_as_float(u << 16); }
DI float bfhi(unsigned u) { return __uint_as_float(u & 0xffff0000u); }
DI float rbf(float x) { return (float)(__bf16)x; }
DI s16x4 trread(const char* p) {
  return __builtin_bit_cast(s16x4, __builtin_amdgcn_ds_read_tr16_b64_v4i16((__attribute__((address_space(3))) v4i16_t*)p));
}
DI bf16x8 cat8(s16x4 lo, s16x4 hi) { return __builtin_shufflevector(lo, hi, 0, 1, 2, 3, 4, 5, 6, 7); }
#define WAIT_V(n) asm volatile("s_waitcnt vmcnt(%0)" ::"n"(n) : "memory")
#define RAW_BARRIER() do { asm volatile("s_waitcnt lgkmcnt(0)" ::: "memory"); __builtin_amdgcn_s_barrier(); asm volatile("" ::: "memory"); } while (0)
#define DS_B128(dst, addr, off) asm volatile("ds_read_b128 %0, %1 offset:%2" : "=v"(dst) : "v"(addr), "n"(off))
#define DS_TR(dst, addr, off) asm volatile("ds_read_b64_tr_b16 %0, %1 offset:%2" : "=v"(dst) : "v"(addr), "n"(off))
#define LDS_OFF(p) ((unsigned)(uintptr_t)(__attribute__((address_space(3))) const char*)(p))
template <class F, int... I> DI void static_for_impl(F&& f, std::integer_sequence<int, I...>) { (f(std::integral_constant<int, I>{}), ...); }
template <int N, class F> DI void static_for(F&& f) { static_for_impl(f, std::make_integer_sequence<int, N>{}); }
DI float divc(float a, float b, float rinv) { const float q = a * rinv; return __builtin_fmaf(__builtin_fmaf(-q, b, a), rinv, q); }
DI float rcp_nr(float d) { const float r = __builtin_amdgcn_rcpf(d); return __builtin_fmaf(__builtin_fmaf(-d, r, 1.0f), r, r); }
DI float own(float v) { asm volatile("" : "+v"(v)); return v; }
DI float silu_r(float x) { return x * __builtin_amdgcn_rcpf(1.0f + __expf(-x)); }
#define SB0() __builtin_amdgcn_sched_barrier(0)
#define GLDS_S(vo, sp, ld) asm volatile("s_mov_b32 m0, %2\n\ts_nop 0\n\tglobal_load_lds_dwordx4 %0, %1" :: "v"(vo), "s"(sp), "s"(ld) : "m0", "memory")
template <int IMM> DI void gl_m0(unsigned vo, const void* sp, unsigned ls) {
  asm volatile("s_add_u32 m0, %2, %3\n\ts_nop 0\n\tglobal_load_lds_dwordx4 %0, %1" :: "v"(vo), "s"(sp), "s"(ls), "n"(IMM) : "m0", "memory", "scc");
}
template <int IMM> DI void gl_nt(unsigned vo, const void* sp, unsigned ls) {
  asm volatile("s_add_u32 m0, %2, %3\n\ts_nop 0\n\tglobal_load_lds_dwordx4 %0, %1 nt" :: "v"(vo), "s"(sp), "s"(ls), "n"(IMM) : "m0", "memory", "scc");
}
template <int N> DI void gl_run(unsigned vo, const void* sp, unsigned ls) {
  unsigned lo = (unsigned)(uintptr_t)sp, hi = (unsigned)((uintptr_t)sp >> 32);
  static_for<N>([&](auto ic) {
    constexpr int i = decltype(ic)::value;
    gl_m0<i * 1024>(vo, (const void*)(((uintptr_t)hi << 32) | lo), ls);
    if constexpr (i + 1 < N) asm volatile("s_add_u32 %0, %0, 0x4000\n\ts_addc_u32 %1, %1, 0" : "+s"(lo), "+s"(hi) :: "scc");
  });
}
#define GLDS_SC1(vo, sp, ld) asm volatile("s_mov_b32 m0, %2\n\ts_nop 0\n\tglobal_load_lds_dwordx4 %0, %1 sc1" :: "v"(vo), "s"(sp), "s"(ld) : "m0", "memory")
DI float x32_add(float v) { auto r = __builtin_amdgcn_permlane32_swap(__float_as_uint(v), __float_as_uint(v), false, false); return __uint_as_float(r[0]) + __uint_as_float(r[1]); }
DI float x32_max(float v) { auto r = __builtin_amdgcn_permlane32_swap(__float_as_uint(v), __float_as_uint(v), false, false); float o; asm("v_max_f32 %0, %1, %2" : "=v"(o) : "v"(r[0]), "v"(r[1])); return o; }
DI float x32_min(float v) { auto r = __builtin_amdgcn_permlane32_swap(__float_as_uint(v), __float_as_uint(v), false, false); float o; asm("v_min_f32 %0, %1, %2" : "=v"(o) : "v"(r[0]), "v"(r[1])); return o; }
DI float x16_add(float v) { auto r = __builtin_amdgcn_permlane16_swap(__float_as_uint(v), __float_as_uint(v), false, false); return __uint_as_float(r[0]) + __uint_as_float(r[1]); }
template <int CTRL, int RMASK> DI float dpp_add(float v) {
  return v + __builtin_bit_cast(float, __builtin_amdgcn_update_dpp(0, __builtin_bit_cast(int, v), CTRL, RMASK, 0xf, true));
}

DI unsigned ln_pair(unsigned u, float m, float den, float rinv) {
  const f32x2 mm = {m, m}, dd = {den, den}, rr = {rinv, rinv};
  const f32x2 d0 = f32x2{bflo(u), bfhi(u)} - mm;
  const unsigned du = pk2(d0[0], d0[1]);
  const f32x2 d = {bflo(du), bfhi(du)};
  const f32x2 q = d * rr;
  const f32x2 r = __builtin_elementwise_fma(__builtin_elementwise_fma(-q, dd, d), rr, q);
  return pk2(r[0], r[1]);
}
DI float dot2(unsigned a, unsigned b, float c) { return __builtin_amdgcn_fdot2_f32_bf16(__builtin_bit_cast(bfx2, a), __builtin_bit_cast(bfx2, b), c, false); }
DI float half_total(float v) {
  v = dpp_add<0x121, 0xf>(v); v = dpp_add<0x122, 0xf>(v); v = dpp_add<0x124, 0xf>(v); v = dpp_add<0x128, 0xf>(v);
  return x16_add(v);
}
DI void ln_rows(const u32x4 (&xr)[4], const u32x4 (&wr)[2], int h, float& m, float& den, float& rinv, float& r) {
  const float c0 = __uint_as_float(__builtin_amdgcn_readlane(xr[0][0], 0) << 16), c1 = __uint_as_float(__builtin_amdgcn_readlane(xr[2][0], 0) << 16);
  float s1[2] = {0.f, 0.f}, sx[2] = {0.f, 0.f}, sw = 0.f;
  f32x2 q[2] = {{0.f, 0.f}, {0.f, 0.f}};
#pragma unroll
  for (int i = 0; i < 2; ++i)
#pragma unroll
    for (int e = 0; e < 4; ++e) {
      sw = dot2(wr[i][e], 0x3F803F80u, sw);
#pragma unroll
      for (int t = 0; t < 2; ++t) {
        const unsigned u = xr[2 * t + i][e]; const float c = t ? c1 : c0;
        s1[t] = dot2(u, 0x3F803F80u, s1[t]); sx[t] = dot2(u, wr[i][e], sx[t]);
        const f32x2 d = f32x2{bflo(u), bfhi(u)} - f32x2{c, c};
        q[t] = __builtin_elementwise_fma(d, d, q[t]);
      }
    }
  auto mg = [](float a, float b) { const auto x = __builtin_amdgcn_permlane32_swap(__float_as_uint(a), __float_as_uint(b), false, false); return __uint_as_float(x[0]) + __uint_as_float(x[1]); };
  const float mean = half_total(mg(s1[0], s1[1])) * (1.0f / 1024.0f), m2 = half_total(mg(q[0][0] + own(q[0][1]), q[1][0] + own(q[1][1]))) * (1.0f / 1024.0f);
  const float tx = half_total(mg(sx[0], sx[1])), tw = half_total(x32_add(sw));
  const float dm = mean - (h ? c1 : c0);
  const float var = fmaxf(__builtin_fmaf(-dm, dm, m2), 0.f);
  m = rbf(mean);
  den = rbf(__builtin_amdgcn_sqrtf(rbf(rbf(var) + 1e-5f)));
  rinv = rcp_nr(den);
  r = rbf(divc(__builtin_fmaf(-m, tw, tx), den, rinv));
}

template <class HK> DI void phi_fn(const bf16x8 (&wp)[2], const bf16x8 f0, const bf16x8 f1, f32x2 (&ep)[8], f32x2 (&en)[8], float si, float so, HK&& hk) {
  f32x16 p;
#pragma unroll
  for (int i = 0; i < 16; ++i) p[i] = 0.f;
  p = MFMA(wp[0], f0, p);
  p = MFMA(wp[1], f1, p);
  hk(std::integral_constant<int, 0>{});
  float mx = -3.0e38f, mn = 3.0e38f;
  f32x2 p2[8];
  static_for<8>([&](auto ic) {
    constexpr int i = decltype(ic)::value;
    p2[i] = f32x2{p[2 * i], p[2 * i + 1]};
    mx = fmaxf(fmaxf(mx, p2[i][0]), p2[i][1]); mn = fminf(fminf(mn, p2[i][0]), p2[i][1]);
    if constexpr (i & 1) hk(std::integral_constant<int, 1 + (i >> 1)>{});
  });
  mx = x32_max(mx); mn = x32_min(mn);
  hk(std::integral_constant<int, 5>{});
  const float L2E = 1.44269504088896340736f * si, mxl = -mx * L2E, mnl = mn * L2E;
  const f32x2 cp = {L2E, L2E}, cn = {-L2E, -L2E}, bp = {mxl, mxl}, bn = {mnl, mnl};
  f32x2 sp2, sn2;
  static_for<8>([&](auto ic) {
    constexpr int i = decltype(ic)::value;
    const f32x2 tp2 = __builtin_elementwise_fma(p2[i], cp, bp), tn2 = __builtin_elementwise_fma(p2[i], cn, bn);
    ep[i] = f32x2{__builtin_amdgcn_exp2f(tp2[0]), __builtin_amdgcn_exp2f(tp2[1])};
    en[i] = f32x2{__builtin_amdgcn_exp2f(tn2[0]), __builtin_amdgcn_exp2f(tn2[1])};
    if constexpr (i == 0) { sp2 = ep[0]; sn2 = en[0]; } else { sp2 += ep[i]; sn2 += en[i]; }
    hk(std::integral_constant<int, 6 + i>{});
  });
  const float sp = x32_add(sp2[0] + own(sp2[1])), sn = x32_add(sn2[0] + own(sn2[1]));
  hk(std::integral_constant<int, 14>{});
  const float rp = __builtin_amdgcn_rcpf(sp) * so, rn = __builtin_amdgcn_rcpf(sn) * so;
  const f32x2 rp2 = {rp, rp}, rn2 = {rn, rn};
  hk(std::integral_constant<int, 15>{});
#pragma unroll
  for (int i = 0; i < 8; ++i) { ep[i] = ep[i] * rp2; en[i] = en[i] * rn2; }
  hk(std::integral_constant<int, 16>{});
}

constexpr int DM = 1024, FF = 3584;
constexpr unsigned SENT = 0xFFFFFFFFu;
constexpr unsigned SPIN_CAP = 1u << 17;

constexpr unsigned WS_EPOCH = 0;
constexpr unsigned WS_F1 = 4096;
constexpr unsigned WS_F4 = 896u << 10;
constexpr unsigned WS_STAT = 20480;
constexpr unsigned WS_CS = 36864;
constexpr unsigned WS_GUP = WS_CS + 16 * 16 * 768;
constexpr unsigned WS_YP = WS_GUP + 256 * 512;
constexpr unsigned WS_KVF = WS_YP + 32 * 4096;
constexpr unsigned WS_ZP = WS_KVF + 256 * 640;
constexpr unsigned WS_XA = 800u << 10;
constexpr unsigned WS_RI = 840u << 10;
static_assert(WS_ZP + 131072 <= WS_XA && WS_XA + 32768 <= WS_RI, "ws map");
constexpr unsigned WS_KVP = 1u << 20;
constexpr unsigned WS_O = 8u << 20;
constexpr unsigned WS_END = 16u << 20;

constexpr int ABYTES = 128 * 128, WSLAB = 64 * 128, STAGE1 = ABYTES + 3 * WSLAB, NST1 = 3;
constexpr int XB = NST1 * STAGE1;
constexpr int X_WO = XB;
constexpr int X_CSP = XB + 8192;
constexpr int X_P = 131072 + 4096;
constexpr int X_XW = X_P;
constexpr int X_XA = X_XW + 6144;
constexpr int X_ST = X_XA + 2048;
constexpr int X_MD = X_ST + 1024;
constexpr int X_AM = X_MD + 1024;
constexpr int X_XS = X_AM + 64;
constexpr int X_YR = X_XS + 512;
constexpr int X_YS = X_YR + 2048;
constexpr int X_WP = X_YS + 2048;
constexpr int X_KB = X_WP + 4096;
constexpr int LDS_TOTAL = 163840;
static_assert(X_KB + 2 * 4224 <= LDS_TOTAL && 131072 + 6 * 2048 <= X_MD && X_YS + 64 + 4096 <= X_KB && X_YS % 128 == 64, "lds");
static_assert(LDS_TOTAL <= 163840, "lds");
constexpr int gu_at(int ks) { return STAGE1 + ks * 8192; }
constexpr int HANDOFF = 106496;

#define LD128(off) __builtin_amdgcn_raw_buffer_load_b128(rs, (int)(off), 0, 16)
#define ST128(v, off) __builtin_amdgcn_raw_buffer_store_b128((v), rs, (int)(off), 0, 16)
#define ST64(v, off) __builtin_amdgcn_raw_buffer_store_b64((v), rs, (int)(off), 0, 16)
DI u32x4 f2u_tag(f32x4 v, unsigned t8) { u32x4 u = __builtin_bit_cast(u32x4, v); return (u & 0xFFFFFF00u) | t8; }
DI unsigned or4(u32x4 v) { return (v[0] | v[1]) | (v[2] | v[3]); }
#define SPIN_NEXT(sp) { if (++(sp) > SPIN_CAP) break; __builtin_amdgcn_s_sleep(1); }
#define TAG_OF(en) ((en) * 0x9E3779B97F4A7C15ull)

__global__ __launch_bounds__(512, 2) void kf_all(const u16* __restrict__ x, const u16* __restrict__ wqkv, const u16* __restrict__ wphi,
                                              const u16* __restrict__ wo, const u16* __restrict__ w_r, const u16* __restrict__ w_gate,
                                              const u16* __restrict__ w_up, const u16* __restrict__ w_down, char* ws, u16* __restrict__ out) {
  extern __shared__ __attribute__((aligned(16))) char smem[];
  asm volatile(".Lkf_pf_start:");
  const int b = blockIdx.x;
  const int tid = threadIdx.x, w = __builtin_amdgcn_readfirstlane(tid >> 6);
  unsigned pfd;
  {
    u64 pc; unsigned len;
    asm volatile("s_getpc_b64 %0\n.Lkf_pf_here:\n\ts_mov_b32 %1, .Lkf_pf_end+1152-256-.Lkf_pf_here" : "=s"(pc), "=s"(len));
    const u64 pa = pc + min((unsigned)((b & ~7) + w) << 8, len);
    asm volatile("s_load_dword %0, %1, 0x0\n\ts_load_dword %0, %1, 0x40\n\ts_load_dword %0, %1, 0x80\n\ts_load_dword %0, %1, 0xc0" : "=&s"(pfd) : "s"(pa) : "memory");
  }
  SB0();
  const int xcd = b & 7, jj = b >> 3, l = tid & 63;
  const int panel = (xcd >> 2) * 8 + (jj & 7), hp = (xcd & 3) * 4 + (jj >> 3);
  const int id = panel * 16 + hp;
  asm volatile("" : "+s"(x), "+s"(wqkv), "+s"(wphi), "+s"(wo), "+s"(w_r), "+s"(w_gate), "+s"(w_up), "+s"(w_down), "+s"(ws), "+s"(out));
  SB0();
  u32x4 xr4[4], wr4[2];
  if (w < 4) {
    const u16* xr = x + (size_t)(panel * 128 + hp * 8 + 2 * w) * DM; const unsigned vl = (unsigned)l * 16;
#define GLS(dst, p, o) asm volatile("global_load_dwordx4 %0, %1, %2 offset:" #o : "=v"(dst) : "v"(vl), "s"(p) : "memory")
    GLS(xr4[0], xr, 0); GLS(xr4[1], xr, 1024); GLS(xr4[2], xr, 2048); GLS(xr4[3], xr, 3072); GLS(wr4[0], w_r, 0); GLS(wr4[1], w_r, 1024);
  }
  SB0();
  const int cw = w & 3, wm = cw & 1, wn = cw >> 1;
  const int h = l >> 5, r31 = l & 31, blk = (l >> 4) & 1, tq = (l & 15) >> 2, tp = l & 3;
  const int hd = hp * 2 + wn;
  const unsigned lbase = LDS_OFF(smem);
  const __amdgpu_buffer_rsrc_t rs = __builtin_amdgcn_make_buffer_rsrc((void*)ws, 0, (int)WS_END, 0x00020000);
  bf16x8 fr[4];
  bf16x8 wp[2];
  f32x4 qa[4][2]; float rt;
  const int kh = w >> 2;
  const int wt = wm * 2 + kh;
  const unsigned img = (wn * 4 + wt) * 4096 + r31 * 128 + (((2 * h) ^ (r31 & 7)) * 16);
  auto wpload = [&]() {
    const char* p = smem + X_WP + wn * 2048 + (8 * h + tq) * 64 + (16 * blk + 4 * tp) * 2;
    wp[0] = cat8(trread(p), trread(p + 256)); wp[1] = cat8(trread(p + 1024), trread(p + 1280));
  };
  auto imgw = [&](int g, const f32x2 (&ep)[8], const f32x2 (&en)[8]) {
    const f32x2 (&e)[8] = g < 2 ? ep : en; const int q = 4 * (g & 1);
    *(u32x4*)(smem + (img ^ ((g >> 1) * 64 + (g & 1) * 16))) = u32x4{pk2(e[q][0], e[q][1]), pk2(e[q + 1][0], e[q + 1][1]), pk2(e[q + 2][0], e[q + 2][1]), pk2(e[q + 3][0], e[q + 3][1])};
  };
  auto pack_u = [&](const f32x4 x0, const f32x4 x1) {
    const auto s0 = __builtin_amdgcn_permlane16_swap(pk2(x0[0], x0[1]), pk2(x1[0], x1[1]), false, false);
    const auto s1 = __builtin_amdgcn_permlane16_swap(pk2(x0[2], x0[3]), pk2(x1[2], x1[3]), false, false);
    return u32x4{s0[0], s1[0], s0[1], s1[1]};
  };
  const int hr = hp ^ 8;
  const int p3 = ((xcd >> 1) * 4 + (jj & 3)) ^ 8, nb = (int)(((xcd & 1) ? 0xF7EDCA98u : 0xB3654210u) >> (jj & 28)) & 15;
  const int flip = jj & 1;
  const unsigned voO = (unsigned)((l >> 3) * DM + (((l & 7) ^ (((l >> 4) & 1) | ((l >> 5) << 2))) * 8)) * 2;
  const bool has_gu = (hp & 7) != 3;
  int gr = (xcd == 6 && jj >= 12) ? 8 * (24 + (jj & 3)) + 2 * ((jj >> 2) - 3) : b;
  asm volatile("" : "+s"(gr));
  gr = jj >= 28 ? 8 * (4 * (xcd >> 1) + jj - 16) + 6 : gr;
  asm volatile("" : "+s"(gr));
  gr = has_gu ? gr : 224 + (jj - 24) * 4 + (xcd >> 1);
  const int gkq = (gr / 56) & 3;
  u32x4 dv[8]; unsigned ogq; bf16x8 dn[4];
  u32x4 wr_[16];
    const int srow = l >> 3, sch = l & 7;
    unsigned voA[4], voW[6];
#pragma unroll
    for (int i = 0; i < 4; ++i) voA[i] = (unsigned)((panel * 128 + (4 * cw + i) * 8 + srow) * DM + ((sch ^ (4 * (i & 1) + (srow >> 1))) * 8)) * 2;
#pragma unroll
    for (int i = 0; i < 6; ++i) voW[i] = (unsigned)(((2 * cw + (i & 1)) * 8 + srow) * 3072 + (i >> 1) * 1024 + hp * 64 + ((sch ^ ((((srow >> 1) & 1) << 2) | ((i & 1) << 1))) * 8)) * 2;
    const unsigned lwa = lbase + cw * 4096, lww = lbase + ABYTES + cw * 2048;
    const int rot = 4 * ((panel & 7) >> 1) + 2 * (panel >> 3) + 4 * (xcd & 3);
    u64 k0_ = (panel & 8) ? ((panel & 1) ? 0xD95FB7EA62C30841ull : 0xD95FB73EA6C21840ull) : (panel & 1) ? 0xFB7EA62D95C30841ull : 0xFB73EA6D95C21840ull, k1_ = (panel & 8) ? 0xD95FB73EA62C1840ull : 0xFB73EA62D95C1840ull;
    asm volatile("" : "+s"(k0_), "+s"(k1_));
    const u64 kord = has_gu ? k1_ : k0_;
    auto stage = [&](int it, auto bufc) {
      constexpr int B = decltype(bufc)::value * STAGE1;
      const int kt = ((int)(kord >> (4 * it)) + rot) & 15;
      const u16* gx = x + kt * 64; const u16* gq_ = wqkv + (size_t)kt * 64 * 3072;
      static_for<10>([&](auto ic) {
        constexpr int i = decltype(ic)::value;
        if constexpr (i < 4) gl_m0<B + i * 1024>(voA[i], gx, lwa);
        else gl_m0<B + ((i - 4) >> 1) * WSLAB + (i & 1) * 1024>(voW[i - 4], gq_, lww);
      });
    };
  unsigned pa0, pvh, dst, dz, pgw, ogu, oh1, oh1z;
  auto kvsetup = [&]() {
    int l = tid & 63; asm volatile("" : "+v"(l));
    const int g = l >> 4, c15 = l & 15, tq = c15 >> 2, tp = l & 3;
    pa0 = lbase + kh * 16384 + (4 * g + tq) * 128 + (((2 * cw + (tp >> 1)) ^ ((4 * g + tq) & 7)) * 16) + 8 * (tp & 1);
    pvh = lbase + HANDOFF + kh * 12288 + l * 16;
    dst = WS_KVP + (unsigned)((hp * 16 + panel) * 16 + (w & 6) * 2 + (g & 2) + (w & 1)) * 512 + c15 * 32 + (g & 1) * 16;
    dz = WS_ZP + (unsigned)((hp * 16 + (w & 6) * 2 + (g & 2) + (w & 1)) * 16 + panel) * 32 + (g & 1) * 16;
    pgw = lbase + gu_at(0) + kh * 4096 + (8 * g + tq) * 128 + (((2 * cw + (tp >> 1)) ^ ((((tq >> 1) & 1) << 2) | ((g & 1) << 1))) * 16) + 8 * (tp & 1);
    ogu = WS_GUP + (unsigned)gr * 512 + w * 64 + g * 16;
    oh1 = WS_KVP + (unsigned)((hr * 16 + (l >> 3)) * 16 + panel) * 512 + cw * 128 + (l & 7) * 16;
    oh1z = WS_ZP + (unsigned)((hr * 16 + panel) * 16) * 32 + l * 4;
    asm volatile("" : "+v"(pa0), "+v"(pvh), "+v"(dst), "+v"(dz), "+v"(pgw), "+v"(ogu), "+v"(oh1), "+v"(oh1z));
  };
#define STUB(n) asm volatile(".Lkf_st" #n ":\n\ts_cbranch_execz .Lkf_rt" #n)
#define VISIT(n, wv) asm volatile("s_cmp_lg_u32 %0, " #wv "\n\ts_cbranch_scc1 .Lkf_rt" #n "\n\ts_mov_b64 exec, 0\n\ts_branch .Lkf_st" #n "\n.Lkf_rt" #n ":\n\ts_mov_b64 exec, -1" :: "s"(cw) : "scc")
#define IC(n) std::integral_constant<int, (n)>{}
#define CODE_PF(tagn, FROM, d) do { u64 pc_; unsigned len_; int off_; \
    asm volatile("s_getpc_b64 %0\n.Lkf_pf_h" #tagn ":\n\ts_mov_b32 %1, .Lkf_pf_end+1152-" FROM "\n\ts_mov_b32 %2, " FROM "-.Lkf_pf_h" #tagn : "=s"(pc_), "=s"(len_), "=s"(off_)); \
    unsigned o_ = (unsigned)(jj * 4 + cw) * 256u; o_ = o_ + 256u <= len_ ? o_ : len_ - 256u; \
    const u64 pa_ = (pc_ + (long long)off_ + o_) & ~63ull; \
    asm volatile("s_load_dword %0, %1, 0x0\n\ts_load_dword %0, %1, 0x40\n\ts_load_dword %0, %1, 0x80\n\ts_load_dword %0, %1, 0xc0" : "=&s"(d) : "s"(pa_) : "memory"); } while (0)
#define PF_DONE(d) asm volatile("s_waitcnt lgkmcnt(0)" : "+s"(d) :: "memory")
  if (w < 4) {
    unsigned oc2;
    {
      const unsigned r0 = (unsigned)(panel * 128 + hp * 8);
      oc2 = l < 33 ? WS_KVF + (unsigned)(hr * 16 + panel) * 640 + l * 16 : l == 33 ? WS_F1 + (unsigned)id * 16 : l == 34 ? WS_STAT + r0 * 8 : l == 35 ? WS_RI + (unsigned)(panel * 1024 + hp * 32)
          : l < 40 ? WS_F4 + (unsigned)(((panel * 4 + (l - 36)) * 16 + hp) * 2) * 32 : l < 44 ? WS_ZP + (unsigned)(hr * 16 + panel) * 512 + (l - 40) * 128 : l < 48 ? WS_GUP + (unsigned)gr * 512 + (l & 3) * 128 : l == 48 ? WS_RI + (unsigned)(panel * 1024 + 512 + hp * 16) : WS_END;
    }
    u32x4 tm, k0, k1; u32x2 tk;
    const unsigned tmofs = lbase + X_CSP + (l < 47 ? l : 46) * 16, kofs = lbase + X_CSP + 768, tgofs = lbase + X_AM + 48;
    unsigned opay;
    { const unsigned r0 = (unsigned)(panel * 128 + hp * 8);
      opay = l < 32 ? WS_CS + (unsigned)(hp * 8 + (panel >> 1)) * 768 + (panel & 1) * 512 + l * 16 : l < 40 ? WS_XA + r0 * 16 + (l - 32) * 16 : l < 44 ? WS_STAT + r0 * 8 + (l - 40) * 16 : l < 46 ? WS_RI + (unsigned)(panel * 1024 + hp * 32) + (l - 44) * 16 : WS_RI + (unsigned)(panel * 1024 + 512 + hp * 16); }
    const unsigned oflag = WS_F1 + (unsigned)id * 16;
    unsigned mz; asm volatile("s_cmp_eq_u32 %1, 0\n\ts_cselect_b32 %0, -1, 0" : "=s"(mz) : "s"(cw) : "scc");
    const u64 mk_all = ((u64)mz << 32) | mz, mk_pay = ((u64)(mz & 0x7FFFu) << 32) | (mz & ((panel & 1) ? 0xFFFFu : 0xFFFFFFFFu)), mk_one = mz & 1u;
    asm volatile("" : "+v"(oc2), "+v"(opay));
#define MST(op, data, off, mask) asm volatile("s_mov_b64 exec, %3\n\t" op " %0, %1, %2, 0 offen sc1\n\ts_mov_b64 exec, -1" :: "v"(data), "v"(off), "s"(rs), "s"(mask) : "memory")
    f32x4 csa[2] = {{0.f, 0.f, 0.f, 0.f}, {0.f, 0.f, 0.f, 0.f}};
    const bf16x8 ones = {0x3F80, 0x3F80, 0x3F80, 0x3F80, 0x3F80, 0x3F80, 0x3F80, 0x3F80};
    s16x4 cl[2][2], ch[2][2];
    const unsigned csw = lbase + X_CSP + (wm * 64 + wn * 32 + r31) * 4;
    f32x4 acc[4][6];
#pragma unroll
    for (int t = 0; t < 4; ++t)
#pragma unroll
      for (int a = 0; a < 6; ++a) acc[t][a] = f32x4{0.f, 0.f, 0.f, 0.f};
    const int g4 = l >> 4, c15 = l & 15;
    unsigned asw[2][2], wofs[2][2], cofs[2];
#pragma unroll
    for (int e = 0; e < 2; ++e) {
      asw[0][e] = lbase + (wm * 64 + c15) * 128 + (((4 * e + g4) ^ ((c15 >> 1) & 7)) * 16);
      wofs[0][e] = lbase + ABYTES + (8 * g4 + tq) * 128 + (((wn * 4 + 2 * e + (tp >> 1)) ^ ((((tq >> 1) & 1) << 2) | ((g4 & 1) << 1))) * 16) + 8 * (tp & 1);
      asw[1][e] = asw[0][e] + 2 * STAGE1; wofs[1][e] = wofs[0][e] + 2 * STAGE1;
      cofs[e] = wofs[0][e] + ((panel & 1) ? 2 : wm) * WSLAB;
    }
    asm volatile("" : "+v"(asw[0][0]), "+v"(asw[0][1]), "+v"(asw[1][0]), "+v"(asw[1][1]), "+v"(wofs[0][0]), "+v"(wofs[0][1]), "+v"(wofs[1][0]), "+v"(wofs[1][1]), "+v"(cofs[0]), "+v"(cofs[1]));
    bf16x8 af[2][4]; s16x4 wl[2][6], wh_[2][6];
    s16x4 ea[4], ew[6];
    const unsigned eaofs = lbase + X_XA + (wm * 64 + c15) * 16;
    const unsigned ewofs = lbase + X_XW + g4 * 1536 + (wn * 32 + c15) * 4;
    const unsigned riofs = lbase + X_ST + (wm * 64 + r31) * 4;
    auto rd = [&](auto ic, auto stc) {
      constexpr int i = decltype(ic)::value, S = decltype(stc)::value, s = S & 1, e = S & 1, buf = (S >> 1) % NST1;
      constexpr int b2 = buf == 2, bo = buf == 1 ? STAGE1 : 0;
      (void)&cl; (void)&ch; (void)&cofs; (void)&af; (void)&wl; (void)&wh_; (void)&asw; (void)&wofs;
      if constexpr (i == 0) DS_B128(af[s][0], asw[b2][e], bo);
      else if constexpr (i == 1) DS_TR(wl[s][0], wofs[b2][0], bo + e * 4096);
      else if constexpr (i == 2) DS_TR(wh_[s][0], wofs[b2][0], bo + e * 4096 + 512);
      else if constexpr (i < 6) DS_B128(af[s][i - 2], asw[b2][e], bo + (i - 2) * 2048);
      else if constexpr (i < 16) {
        constexpr int j = (i - 4) >> 1, off = bo + e * 4096 + (j >> 1) * WSLAB;
        if constexpr (i & 1) DS_TR(wh_[s][j], wofs[b2][j & 1], off + 512); else DS_TR(wl[s][j], wofs[b2][j & 1], off);
      } else {
        constexpr int c = (i - 16) >> 1;
        if constexpr (i & 1) DS_TR(ch[s][c], cofs[c], bo + e * 4096 + 512); else DS_TR(cl[s][c], cofs[c], bo + e * 4096);
      }
    };
    {
      SB0();
      asm volatile("s_waitcnt vmcnt(0) lgkmcnt(0)" : "+s"(pfd), "+v"(xr4[0]), "+v"(xr4[1]), "+v"(xr4[2]), "+v"(xr4[3]), "+v"(wr4[0]), "+v"(wr4[1]) :: "memory");
      SB0();
      float m, den, rinv, r;
      ln_rows(xr4, wr4, h, m, den, rinv, r);
      const int rw = tid >> 5;
      const unsigned rb = __float_as_uint(r) >> 16, ord = rb ^ ((rb & 0x8000u) ? 0xFFFFu : 0x8000u), nm = pk2(-m, -m);
      if (r31 == 0) { *(u32x4*)(smem + X_CSP + 512 + rw * 16) = u32x4{nm, nm, nm, nm};
        *(f32x2*)(smem + X_CSP + 640 + rw * 8) = f32x2{m, den}; *(float*)(smem + X_CSP + 704 + rw * 4) = rinv; *(u16*)(smem + X_CSP + 736 + rw * 2) = (u16)(__float_as_uint(den) >> 16);
        *(unsigned*)(smem + X_CSP + 768 + rw * 4) = (ord << 11) | (unsigned)(2047 - (id * 8 + rw)); }
      SB0();
    }
    __builtin_amdgcn_s_barrier();
    static_for<20>([&](auto ic) { rd(ic, IC(0)); });
    static_for<30>([&](auto stc) {
      constexpr int S = decltype(stc)::value, s = S & 1;
      constexpr bool bnd = (S & 1) == 1;
      static_for<24>([&](auto mc) {
        constexpr int m = decltype(mc)::value, j = m >> 2, t = (j & 1) ? 3 - (m & 3) : m & 3;
        (void)&tm; (void)&k0; (void)&k1; (void)&tk; (void)&ea; (void)&ew; (void)&oc2; (void)&opay; (void)&oflag; (void)&mk_all; (void)&mk_pay; (void)&mk_one; (void)&rs;
        (void)&tmofs; (void)&kofs; (void)&tgofs; (void)&csw; (void)&csa; (void)&eaofs; (void)&ewofs; (void)&riofs;
        if constexpr (m == 0) asm volatile("s_waitcnt lgkmcnt(%0)" :: "n"(bnd ? 0 : CSS(S) ? 14 : 10));
        else if constexpr (!bnd && m == 4) asm volatile("s_waitcnt lgkmcnt(%0)" :: "n"(CSS(S) ? 7 : 3));
        else if constexpr (!bnd && m == 12 && CSS(S)) asm volatile("s_waitcnt lgkmcnt(11)");
        SB0();
        const bf16x8 wf = cat8(wl[s][j], wh_[s][j]);
        if constexpr (j < 4) acc[t][j] = MFMA16(wf, af[s][t], acc[t][j]);
        else acc[t][j] = MFMA16(af[s][t], wf, acc[t][j]);
        SB0();
        if constexpr (S * 24 + m == 349) STUB(6);
        if constexpr (S * 24 + m == 650) STUB(7);
        if constexpr (m == 1) {
          if constexpr (S == 3) { const u32x4 sv = {SENT, SENT, SENT, SENT}; MST("buffer_store_dwordx4", sv, oc2, mk_all); }
          if constexpr (S == 11) { asm volatile("" : "+v"(tm), "+v"(k0), "+v"(k1), "+v"(tk)); MST("buffer_store_dwordx4", tm, opay, mk_pay); }
          if constexpr (S == F1_S) { WAIT_V(0); MST("buffer_store_dwordx2", tk, oflag, mk_one); }
          if constexpr (S == 3) kvsetup();
          if constexpr (S == 5) { (void)&cw; VISIT(6, 1); VISIT(7, 2); VISIT(10, 3); }
          if constexpr (bnd) __builtin_amdgcn_s_barrier();
        }
        if constexpr (m >= 1 && m - 1 < (CSS(S + 1) ? 20 : 16)) rd(IC(m - 1), IC(S + 1));
        if constexpr (S == 8 && m == 18) {
          asm volatile("" : "+v"(csa[0]), "+v"(csa[1]));
          const float c = blk ? csa[1][0] : csa[0][0];
          const unsigned u = pk2(c, c - rbf(c));
          asm volatile("ds_write_b32 %0, %1" :: "v"(csw), "v"(u) : "memory");
        }
        if constexpr (S == 9 && m >= 18 && m < 22) {
          if constexpr (m == 18) DS_B128(tm, tmofs, 0);
          if constexpr (m == 19) DS_B128(k0, kofs, 0);
          if constexpr (m == 20) DS_B128(k1, kofs, 16);
          if constexpr (m == 21) asm volatile("ds_read_b64 %0, %1" : "=v"(tk) : "v"(tgofs));
        }
        if constexpr (S == 11 && m == 18) { k0 = __builtin_elementwise_max(k0, k1); asm volatile("" : "+v"(k0)); }
        if constexpr (S == 11 && m == 20) { tk[0] = (tk[0] & ~0x7FFFFFFu) | max(max(k0[0], k0[1]), max(k0[2], k0[3])); asm volatile("" : "+v"(tk)); }
#define DS_B64(dst, addr, off) asm volatile("ds_read_b64 %0, %1 offset:%2" : "=v"(dst) : "v"(addr), "n"(off))
#define DS_B32(dst, addr, off) asm volatile("ds_read_b32 %0, %1 offset:%2" : "=v"(dst) : "v"(addr), "n"(off))
#define DS_2(dst, addr, o0, o1) asm volatile("ds_read2_b32 %0, %1 offset0:%2 offset1:%3" : "=v"(dst) : "v"(addr), "n"(o0), "n"(o1))
        SB0();
      });
      if constexpr (CSS(S)) {
        SB0();
        csa[0] = MFMA16(ones, cat8(cl[s][0], ch[s][0]), csa[0]); csa[1] = MFMA16(ones, cat8(cl[s][1], ch[s][1]), csa[1]);
        SB0();
      }
    });
    static_for<16>([&](auto ic) {
      constexpr int i = decltype(ic)::value, j = 2 + (i >> 2), t = (j & 1) ? 3 - (i & 3) : i & 3;
      (void)&ea; (void)&ew; (void)&eaofs; (void)&ewofs;
      if constexpr (i == 0) asm volatile("s_waitcnt lgkmcnt(6)");
      else if constexpr (i == 4) asm volatile("s_waitcnt lgkmcnt(8)");
      SB0();
      const bf16x8 wf = cat8(wl[0][j], wh_[0][j]);
      if constexpr (j < 4) acc[t][j] = MFMA16(wf, af[0][t], acc[t][j]); else acc[t][j] = MFMA16(af[0][t], wf, acc[t][j]);
      SB0();
      if constexpr (i < 8) { rd(IC(2 * i), IC(31)); rd(IC(2 * i + 1), IC(31)); }
      else if constexpr (i < 12) DS_B64(ea[i - 8], eaofs, (i - 8) * 256);
      else if constexpr (i < 14) { const unsigned a_ = ewofs + 256; DS_2(ew[i - 10], a_, (i - 12) * 16, (i - 12) * 16 + 192); }
      SB0();
    });
    {
      __builtin_amdgcn_s_setprio(PRIO_T);
      asm volatile("s_waitcnt lgkmcnt(0)" : "+v"(af[1][0]), "+v"(af[1][1]), "+v"(af[1][2]), "+v"(af[1][3]), "+v"(wl[1][0]), "+v"(wh_[1][0]), "+v"(wl[1][1]), "+v"(wh_[1][1]),
                   "+v"(wl[1][2]), "+v"(wh_[1][2]), "+v"(wl[1][3]), "+v"(wh_[1][3]), "+v"(wl[1][4]), "+v"(wh_[1][4]), "+v"(wl[1][5]), "+v"(wh_[1][5]));
      asm volatile("" : "+v"(ea[0]), "+v"(ea[1]), "+v"(ea[2]), "+v"(ea[3]), "+v"(ew[2]), "+v"(ew[3]));
      SB0();
      auto mm = [&](int t, int j) { const bf16x8 wf = cat8(wl[1][j], wh_[1][j]); if (j < 4) acc[t][j] = MFMA16(wf, af[1][t], acc[t][j]); else acc[t][j] = MFMA16(af[1][t], wf, acc[t][j]); };
      auto ff = [&](int t, int j) { if (j < 4) acc[t][j] = MFMA16H(ew[j], ea[t], acc[t][j]); else acc[t][j] = MFMA16H(ea[t], ew[j], acc[t][j]); };
      auto dm = [&](int n) {
        SB0();
        const int t = n & 3, j = (n < 16 ? 4 : 0) + ((n >> 2) & 1);
        if (n & 8) ff(t, j); else mm(t, j);
        SB0();
      };
      const unsigned ew0 = ewofs, ew2 = ewofs + 512;
      mm(2, 2); SB0(); DS_B32(rt, riofs, 0); SB0();
      mm(3, 2); SB0();
      mm(2, 3); SB0(); DS_2(ew[4], ew2, 0, 192); DS_2(ew[5], ew2, 16, 208); SB0();
      mm(3, 3); SB0(); DS_2(ew[0], ew0, 0, 192); DS_2(ew[1], ew0, 16, 208); SB0();
      ff(2, 2); ff(3, 2); SB0(); wpload(); SB0();
      ff(2, 3); ff(3, 3); SB0(); SB0();
      mm(0, 2); mm(1, 2);
      SB0();
      SB0();
      char* ho = smem + HANDOFF + cw * 6144 + l * 16;
      *(u32x4*)(ho + 2048) = pack_u(acc[2][2], acc[3][2]);
      SB0(); mm(0, 3); mm(1, 3); SB0();
      *(u32x4*)(ho + 3072) = pack_u(acc[2][3], acc[3][3]);
      SB0(); ff(0, 2); ff(1, 2); ff(0, 3); ff(1, 3); SB0();
      RAW_BARRIER();
      asm volatile("" : "+v"(rt), "+v"(ew[0]), "+v"(ew[1]), "+v"(ew[4]), "+v"(ew[5]));
      dm(0); dm(1);
      fr[2] = __builtin_bit_cast(bf16x8, pack_u(acc[0][2], acc[1][2]));
      dm(2); dm(3);
      fr[3] = __builtin_bit_cast(bf16x8, pack_u(acc[0][3], acc[1][3]));
      dm(4);
      f32x2 ep[8], en[8];
      phi_fn(wp, fr[2], fr[3], ep, en, rt, rt, [&](auto kc) { constexpr int k = decltype(kc)::value; if constexpr (k == 0) { dm(5); dm(6); dm(7); } else if constexpr (7 + k < 16) dm(7 + k); });
    { const unsigned a_ = lbase + X_ST + 512 + (l >> 4) * 8;
      static_for<4>([&](auto tc) { constexpr int tb = decltype(tc)::value; (void)&dn; (void)&a_;
        asm volatile("ds_read2_b64 %0, %1 offset0:%2 offset1:%3" : "=v"(dn[tb]) : "v"(a_), "n"(tb * 8), "n"(tb * 8 + 4)); }); }
#pragma unroll
      for (int g = 0; g < 4; ++g) imgw(g, ep, en);
#pragma unroll
      for (int u = 0; u < 4; ++u) {
        const int t2 = u >> 1, db = u & 1;
        const f32x4 x0 = acc[2 * t2][4 + db], x1 = acc[2 * t2 + 1][4 + db];
        char* pv = t2 ? ho + 4096 : ho;
        *(u32x4*)(pv + db * 1024) = u32x4{pk2(x0[0], x0[1]), pk2(x0[2], x0[3]), pk2(x1[0], x1[1]), pk2(x1[2], x1[3])};
      }
#pragma unroll
      for (int t = 0; t < 4; ++t) { qa[t][0] = acc[t][0]; qa[t][1] = acc[t][1]; wr_[t] = __builtin_bit_cast(u32x4, af[1][t]); wr_[9 + t] = __builtin_bit_cast(u32x4, af[0][t]); }
      wr_[13] = __builtin_bit_cast(u32x4, cat8(wl[0][0], wh_[0][0])); wr_[14] = __builtin_bit_cast(u32x4, cat8(wl[0][1], wh_[0][1]));
      wr_[4] = __builtin_bit_cast(u32x4, cat8(wl[1][0], wh_[1][0])); wr_[5] = __builtin_bit_cast(u32x4, cat8(wl[1][1], wh_[1][1]));
      wr_[6] = __builtin_bit_cast(u32x4, cat8(ea[0], ea[1])); wr_[7] = __builtin_bit_cast(u32x4, cat8(ea[2], ea[3])); wr_[8] = __builtin_bit_cast(u32x4, cat8(ew[0], ew[1]));
    }
  } else {
    const int t = tid - 256;
    u64 epoch;
    asm volatile("s_load_dwordx2 %0, %1, 0x0" : "=&s"(epoch) : "s"(ws) : "memory");
    stage(0, IC(0));
    asm volatile("s_waitcnt lgkmcnt(0)" : "+s"(pfd), "+s"(epoch) :: "memory");
    const u64 enx = epoch + (((unsigned)epoch & 0xFEu) == 0xFEu ? 3u - ((unsigned)epoch & 1u) : 1u);
    const u64 tag = TAG_OF(enx);
    if (t == 0) { *(u32x2*)(smem + X_AM) = u32x2{0u, 0u};
      const unsigned e2 = ((unsigned)enx & 0x55u) | (((unsigned)enx & 0xAAu) << 15);
      *(u32x4*)(smem + X_AM + 32) = u32x4{e2, e2 >> 2, e2 >> 4, e2 >> 6} & 0x00010001u;
      *(u32x4*)(smem + X_AM + 48) = u32x4{(unsigned)tag, (unsigned)(tag >> 32), (unsigned)enx, (unsigned)(enx >> 32)}; }
    u32x2 fl, si; unsigned pfd2, tpace = 0, xs;
    unsigned ofl = WS_F1 + (unsigned)t * 16, orA = WS_CS + (unsigned)(hp * 6144 + cw * 1024 + l * 16);
    unsigned orB = (cw < 2 ? WS_CS + (unsigned)(hp * 6144 + 4096 + cw * 1024) : WS_XA + (unsigned)(panel * 2048 + (cw - 2) * 1024)) + l * 16;
    unsigned orC = ((cw & 1) ? WS_STAT + (unsigned)p3 * 1024 : WS_RI + (unsigned)panel * 1024) + l * 16;
    unsigned oxs = (unsigned)(gkq * 256 + cw * 64 + r31 * 2) * 2, akey = lbase + X_AM + 4, vol = (unsigned)l * 16;
    unsigned voG = (unsigned)((8 * cw + srow) * FF + ((sch ^ ((((srow >> 1) & 1) << 2) | ((cw & 1) << 1))) * 8)) * 2;
    const u16* gg_ = w_gate + (size_t)gkq * 256 * FF + ((gr + 1) % 56) * 64;
    const u16* gu_ = w_up + (size_t)gkq * 256 * FF + ((gr + 3) % 56) * 64;
    const u16* gwo = wo + (size_t)(((xcd >> 1) * 4 + (jj >> 3)) * 64 + 2 * cw * 8) * DM + ((xcd & 1) * 8 + (jj & 7)) * 64;
    const u16* gwp = wphi + hp * 2048 + cw * 512;
    const u16* gdw = w_down + (size_t)(((gr >> 3) & 31) * 128 + 32 * cw) * DM + ((gr + 1) & 7) * 128;
    unsigned odw = (unsigned)((l >> 4) * DM + (l & 15) * 8) * 2;
    const u16* gw2a = wo + (size_t)(((cw * 10) ^ (flip * 64)) * 8) * DM + nb * 64;
    asm volatile("" : "+s"(gw2a));
    asm volatile("" : "+s"(gg_), "+s"(gu_), "+s"(gwo), "+s"(gwp), "+s"(gdw));
    { const int f = (gr >> 3) * 128 + 32 * cw + 4 * (l & 7), fs = (f >> 6) - ((l & 8) ? 3 : 1);
      ogq = WS_GUP + (unsigned)((l >> 4) * 56 + (fs < 0 ? fs + 56 : fs)) * 512 + (f & 63) * 4 + (l & 8) * 32; }
    asm volatile("" : "+v"(voG), "+v"(odw), "+v"(ogq));
    asm volatile("s_waitcnt lgkmcnt(0)" : "+v"(ofl), "+v"(orA), "+v"(orB), "+v"(orC), "+v"(oxs), "+v"(akey), "+v"(vol) :: "memory");
    stage(1, IC(1));
    WAIT_V(10);
    __builtin_amdgcn_s_barrier();
    auto wopf = [&]() {
      GLDS_S(voO, gwo, lbase + X_WO + 2 * cw * 1024); GLDS_S(voO, gwo + 8 * DM, lbase + X_WO + (2 * cw + 1) * 1024);
      GLDS_S(vol, gwp, lbase + X_WP + cw * 1024);
    };
    auto dvl = [&](auto kc) {
      constexpr int k = decltype(kc)::value;
      static_for<DVN[k + 2]>([&](auto ic) {
        constexpr int i = dv_first(k) + decltype(ic)::value;
        u32x4& d_ = dv[i]; const unsigned o_ = odw; const u16* p_ = gdw + (size_t)(4 * i) * DM;
        asm volatile("global_load_dwordx4 %0, %1, %2 nt" : "=v"(d_) : "v"(o_), "s"(p_) : "memory");
      });
    };
    stage(2, IC(2));
    const unsigned lgu = lbase + cw * 1024;
    auto gul = [&](auto ksc) {
      constexpr int ks = decltype(ksc)::value;
      gl_nt<gu_at(ks)>(voG, gg_ + (size_t)ks * 32 * FF, lgu); gl_nt<gu_at(ks) + 4096>(voG, gu_ + (size_t)ks * 32 * FF, lgu);
    };
#define BLD64(dst, off) asm volatile("buffer_load_dwordx2 %0, %1, %2, 0 offen sc1" : "=v"(dst) : "v"(off), "s"(rs) : "memory")
    static_for<15>([&](auto ktc) {
      constexpr int kt = decltype(ktc)::value;
      constexpr auto exw = [](int k) { return DVN[k + 2]; };
      constexpr auto exo = [](int k) { return k == KP ? 1 : k == KC + 1 ? 3 : k == KC + 2 ? 2 : 0; };
      constexpr int exa = ((kt - 2 == WOK || kt - 1 == WOK) ? 3 : 0) + (kt - 2 > KC ? exo(kt - 2) : 0);
      if constexpr (exw(kt - 2) + exw(kt - 1) != 0) { if (has_gu) WAIT_V(10 + exa + exw(kt - 2) + exw(kt - 1) + exo(kt - 1)); else WAIT_V(10 + exa + exo(kt - 1)); }
      else if constexpr (kt < 14) WAIT_V(10 + exa + exo(kt - 1));
      else { if (has_gu) WAIT_V(10); else WAIT_V(0); }
      if constexpr (PACE_T[kt] != 0) { if (cw == 0) { while ((unsigned)__builtin_amdgcn_s_memrealtime() - tpace < (unsigned)(has_gu ? PACE_T[kt] : PACE_3[kt])) {} } }
      __builtin_amdgcn_s_barrier();
      if constexpr (PACE_T[kt + 1] != 0) { if (cw == 0) tpace = (unsigned)__builtin_amdgcn_s_memrealtime(); SB0(); }
#define UMAX_DPP(C) { const unsigned o = (unsigned)__builtin_amdgcn_update_dpp(0, (int)key, C, 0xf, 0xf, true); key = o > key ? o : key; asm volatile("" : "+v"(key)); }
      if constexpr (kt + NST1 < 16) { SB0(); stage(kt + NST1, IC(kt % NST1)); SB0(); }
      if constexpr (kt == WOK) wopf();
      if constexpr (kt == PF2_KT) CODE_PF(2, ".Lkf_pf_mid", pfd2);
      if constexpr (kt == PF2_KT + 2) PF_DONE(pfd2);
      if constexpr (kt == KP) BLD64(fl, ofl);
      if constexpr (kt == KC) {
        static_assert(DVN[KP + 2] == 0, "count");
        if constexpr (KC == KP + 1) asm volatile("s_waitcnt vmcnt(10)" : "+v"(fl) :: "memory"); else asm volatile("" : "+v"(fl) :: "memory");
#define FL_OK() __all((((fl[0] ^ (unsigned)tag) >> 27) | (fl[1] ^ (unsigned)(tag >> 32))) == 0u)
        if (__builtin_expect(!FL_OK(), 0)) {
          for (unsigned sp = 0;;) {
            asm volatile("buffer_load_dwordx2 %0, %1, %2, 0 offen sc1\n\ts_waitcnt vmcnt(0)" : "=v"(fl) : "v"(ofl), "s"(rs) : "memory");
            if (FL_OK()) break;
            SPIN_NEXT(sp);
          }
        }
        unsigned key = fl[0];
        UMAX_DPP(0x121) UMAX_DPP(0x122) UMAX_DPP(0x124) UMAX_DPP(0x128)
        asm volatile("s_mov_b64 exec, %2\n\tds_max_u32 %0, %1\n\ts_mov_b64 exec, -1\n\ts_waitcnt lgkmcnt(0)" :: "v"(akey), "v"(key), "s"(0x0001000100010001ull) : "memory");
      }
      if constexpr (kt == KC + 1) { GLDS_SC1(orA, ws, lbase + X_XW + cw * 1024); GLDS_SC1(orB, ws, lbase + X_XW + 4096 + cw * 1024); GLDS_SC1(orC, ws, lbase + X_ST + (cw & 1) * 1024); }
      if constexpr (kt == KC + 2) {
        const int bi = 2047 - (int)(__builtin_amdgcn_readfirstlane(*(const unsigned*)(smem + X_AM + 4)) & 2047u);
        asm volatile("global_load_dword %0, %1, %2" : "=v"(xs) : "v"(oxs), "s"(x + (size_t)bi * DM) : "memory");
        asm volatile("buffer_load_dwordx2 %0, off, %1, %2 sc1" : "=v"(si) : "s"(rs), "s"(WS_STAT + (unsigned)bi * 8) : "memory");
      }
      if constexpr (DVN[kt + 2] != 0) { if (has_gu) dvl(ktc); }
      if constexpr (kt == 0) kvsetup();
      if constexpr (kt == 13) { if (has_gu) { gul(IC(0)); gul(IC(1)); gul(IC(2)); gul(IC(3)); gul(IC(4)); } }
      if constexpr (kt == 14) asm volatile("" : "+v"(xs), "+v"(si) :: "memory");
      if constexpr (kt == 14) { if (has_gu) { static_for<1>([&](auto ic) { gul(IC(5 + decltype(ic)::value)); }); } }
    });
    auto wld = [&](auto ic) {
      constexpr int i = decltype(ic)::value;
      u32x4& d_ = wr_[i]; const unsigned o_ = voO; const u16* p_ = gw2a + (size_t)(i * 8) * DM;
      asm volatile("global_load_dwordx4 %0, %1, %2" : "=v"(d_) : "v"(o_), "s"(p_) : "memory");
    };
    wpload();
    rt = own(*(const float*)(smem + X_ST + (wt * 32 + r31) * 4));
    {
      const float m = __uint_as_float(si[0]), den = own(__uint_as_float(si[1])), rinv = rcp_nr(den);
      *(unsigned*)(smem + X_XS + cw * 128 + r31 * 4) = ln_pair(xs, m, den, rinv);
    }
    RAW_BARRIER();
    const char* ho = smem + HANDOFF + cw * 6144 + l * 16;
    fr[2] = *(const bf16x8*)(ho + 2048); fr[3] = *(const bf16x8*)(ho + 3072);
    f32x2 ep[8], en[8];
    phi_fn(wp, fr[2], fr[3], ep, en, rt, rt, [&](auto kc) {
      constexpr int k = decltype(kc)::value;
      if constexpr (k == 0) { SB0(); if (has_gu) gul(IC(6)); SB0(); }
      if constexpr (k == 5) { SB0(); if (has_gu) gul(IC(7)); SB0(); }
      static_for<NREG - WPOST>([&](auto ic) {
        constexpr int i = decltype(ic)::value;
        if constexpr (k == WH0 + (i * (17 - WH0)) / (NREG - WPOST)) { SB0(); wld(ic); SB0(); }
      });
    });
    { const unsigned a_ = lbase + X_ST + 512 + (l >> 4) * 8;
      static_for<4>([&](auto tc) { constexpr int tb = decltype(tc)::value; (void)&dn; (void)&a_;
        asm volatile("ds_read2_b64 %0, %1 offset0:%2 offset1:%3" : "=v"(dn[tb]) : "v"(a_), "n"(tb * 8), "n"(tb * 8 + 4)); }); }
#pragma unroll
    for (int g = 0; g < 4; ++g) imgw(g, ep, en);
    SB0();
    static_for<WPOST>([&](auto ic) { wld(IC(NREG - WPOST + decltype(ic)::value)); });
  }
  asm volatile(".Lkf_pf_mid:");
  f32x16 zero;
#pragma unroll
  for (int i = 0; i < 16; ++i) zero[i] = 0.f;
  u32x2 tg; unsigned t8, rkey; u32x4 t2;
  {
    __builtin_amdgcn_s_setprio(2);
    RAW_BARRIER();
    asm volatile("" : "+v"(dn[0]), "+v"(dn[1]), "+v"(dn[2]), "+v"(dn[3]));
    u32x4 am; unsigned rk;
    { const unsigned a_ = lbase + X_AM; DS_B128(am, a_, 48); DS_B128(t2, a_, 32); asm volatile("ds_read_b32 %0, %1 offset:4" : "=v"(rk) : "v"(a_)); }
    s16x4 al[4], ah[4]; bf16x8 v0[4], v1[4];
    DS_TR(al[0], pa0, 0); DS_TR(ah[0], pa0, 2048); DS_B128(v0[0], pvh, 0); DS_B128(v1[0], pvh, 1024);
    DS_TR(al[1], pa0, 4096); DS_TR(ah[1], pa0, 4096 + 2048); DS_B128(v0[1], pvh, 4096); DS_B128(v1[1], pvh, 4096 + 1024);
    DS_TR(al[2], pa0, 8192); DS_TR(ah[2], pa0, 8192 + 2048); DS_B128(v0[2], pvh, 6144); DS_B128(v1[2], pvh, 6144 + 1024);
    DS_TR(al[3], pa0, 12288); DS_TR(ah[3], pa0, 12288 + 2048); DS_B128(v0[3], pvh, 6144 + 4096); DS_B128(v1[3], pvh, 6144 + 4096 + 1024);
    f32x4 k0 = {0.f, 0.f, 0.f, 0.f}, k1 = k0, zz = k0;
    static_for<4>([&](auto tbc) {
      constexpr int tb = decltype(tbc)::value;
      asm volatile("s_waitcnt lgkmcnt(%4)" : "+v"(al[tb]), "+v"(ah[tb]), "+v"(v0[tb]), "+v"(v1[tb]) : "n"(12 - 4 * tb));
      const bf16x8 af = cat8(al[tb], ah[tb]);
      k0 = MFMA16(af, v0[tb], k0); k1 = MFMA16(af, v1[tb], k1); zz = MFMA16(af, dn[tb], zz);
      SB0();
    });
    asm volatile("" : "+v"(am), "+v"(rk), "+v"(t2));
    auto tagv = [&](f32x4 v) { const u32x4 u = __builtin_bit_cast(u32x4, v); return (u & 0xFFFFFF00u) | (am[2] & 0xFFu); };
    ST128((u32x4{pk2(k0[0], k0[1]), pk2(k0[2], k0[3]), pk2(k1[0], k1[1]), pk2(k1[2], k1[3])} & 0xFFFEFFFEu) | t2, dst);
    const u32x4 zt = tagv(zz);
    asm volatile("s_mov_b64 exec, %3\n\tbuffer_store_dwordx4 %0, %1, %2, 0 offen sc1\n\ts_mov_b64 exec, -1" :: "v"(zt), "v"(dz), "s"(rs), "s"(0x0001000100010001ull) : "memory");
    SB0();
    tg = u32x2{am[0], am[1]}; t8 = __builtin_amdgcn_readfirstlane(am[2]) & 0xFFu; rkey = __builtin_amdgcn_readfirstlane(rk);
  }
  const int idx = 2047 - (int)(rkey & 2047u);
  const int fb = kh ^ ((idx >> 4) & 1);
  const unsigned lxc = lbase + (cw < 2 ? 131072 : X_YS + 64 - 4096) + cw * 2048, lxv = w < 4 ? lxc : lbase + X_XW + cw * 2048;
  {
    bf16x8 xf[8];
    const unsigned px = lbase + X_XS + (l >> 4) * 16, hq = lbase + X_XW + cw * 2048 + l * 16;
    static_for<8>([&](auto kc) { constexpr int ks = decltype(kc)::value; (void)&xf; (void)&px; DS_B128(xf[ks], px, ks * 64); });
    if (w >= 4) {
      __builtin_amdgcn_s_setprio(0);
      WAIT_V(NREG + 2);
    } else {
      auto lo4 = [](u32x4 v) { return __builtin_bit_cast(s16x4, u32x2{v[0], v[1]}); };
      auto hi4 = [](u32x4 v) { return __builtin_bit_cast(s16x4, u32x2{v[2], v[3]}); };
#pragma unroll
      for (int tt = 0; tt < 4; ++tt) {
        const int t = tt ^ 2;
#pragma unroll
        for (int j = 0; j < 2; ++j) qa[t][j] = MFMA16(__builtin_bit_cast(bf16x8, wr_[13 + j]), __builtin_bit_cast(bf16x8, wr_[9 + t]), qa[t][j]);
#pragma unroll
        for (int j = 0; j < 2; ++j) qa[t][j] = MFMA16(__builtin_bit_cast(bf16x8, wr_[4 + j]), __builtin_bit_cast(bf16x8, wr_[t]), qa[t][j]);
        if (tt & 1) {
#pragma unroll
          for (int u = t - 1; u <= t; ++u)
#pragma unroll
            for (int j = 0; j < 2; ++j) qa[u][j] = MFMA16H(j ? hi4(wr_[8]) : lo4(wr_[8]), (u & 1) ? hi4(wr_[6 + (u >> 1)]) : lo4(wr_[6 + (u >> 1)]), qa[u][j]);
        }
      }
      const u32x4 q0 = pack_u(qa[2][0], qa[3][0]), q1 = pack_u(qa[2][1], qa[3][1]);
      asm volatile("ds_write_b128 %0, %1\n\tds_write_b128 %0, %2 offset:1024" :: "v"(hq), "v"(q0), "v"(q1) : "memory");
    }
    RAW_BARRIER();
    asm volatile("" : "+v"(xf[0]), "+v"(xf[1]), "+v"(xf[2]), "+v"(xf[3]), "+v"(xf[4]), "+v"(xf[5]), "+v"(xf[6]), "+v"(xf[7]));
    if (w >= 4) {
      { const u16* ga = wo + (size_t)(((cw * 10) ^ (flip * 64)) * 8) * DM + nb * 64; const u16* gb = wo + (size_t)(((104 + cw * 6) ^ (flip * 64)) * 8) * DM + nb * 64;
        static_for<16 - NREG>([&](auto ic) { constexpr int i = NREG + decltype(ic)::value;
          if constexpr (i < 10) gl_m0<i * 1024>(voO, ga + (size_t)(i * 8) * DM, lbase + cw * 10240); else gl_m0<(104 + i - 10) * 1024>(voO, gb + (size_t)((i - 10) * 8) * DM, lbase + cw * 6144); }); }
    }
    if (has_gu) {
      s16x4 gl[8], gh[8];
      static_for<8>([&](auto kc) { constexpr int ks = decltype(kc)::value; (void)&gl; (void)&gh; (void)&pgw;
        DS_TR(gl[ks], pgw, ks * 8192); DS_TR(gh[ks], pgw, ks * 8192 + 512); });
      f32x4 c0 = {0.f, 0.f, 0.f, 0.f};
      static_for<8>([&](auto kc) { constexpr int ks = decltype(kc)::value;
        asm volatile("s_waitcnt lgkmcnt(%2)" : "+v"(gl[ks]), "+v"(gh[ks]) : "n"(14 - 2 * ks));
        c0 = MFMA16(cat8(gl[ks], gh[ks]), xf[ks], c0);
        SB0(); });
      const u32x4 gt_ = f2u_tag(c0, t8);
      asm volatile("s_mov_b64 exec, %3\n\tbuffer_store_dwordx4 %0, %1, %2, 0 offen sc1\n\ts_mov_b64 exec, -1" :: "v"(gt_), "v"(ogu), "s"(rs), "s"(0x0001000100010001ull) : "memory");
    }
  }
  const int erow = cw * 32 + fb * 16 + (l & 15), ech = l >> 4;
  u32x4 xv[2];
  const bool sw = ech & 1;
  const bool has_idx = w < 4 && (idx >> 5) == p3 * 4 + w;
  unsigned xrt = 0;
  unsigned of4, op4, oo0, oo1, kba, kbz, odo, o2, oz2, wa_, wz_;
  auto later = [&]() {
    SB0();
    of4 = WS_F4 + (unsigned)(((panel * 4 + wt) * 16 + hp) * 2 + wn) * 32; op4 = WS_F4 + (unsigned)(((p3 * 4 + cw) * 16 + 8 * (kh ^ flip)) * 2 + (l & 15)) * 32;
    oo0 = WS_O + 512 + (unsigned)((((p3 * 4 + cw) * 2 + fb) * 32 + (kh ^ flip) * 16) * 128 + h * 64 + r31) * 16; oo1 = oo0 ^ 65536u;
    kba = lbase + X_KB + wn * 4224 + l * 16; kbz = lbase + X_KB + wn * 4224 + 4096 + h * 64;
    odo = WS_O + 512 + (unsigned)((((panel * 4 + wt) * 2 + blk) * 32 + hd) * 128 + h * 16 + (l & 15)) * 16;
    asm volatile("" : "+v"(kba), "+v"(kbz), "+v"(odo), "+v"(of4), "+v"(op4), "+v"(oo0), "+v"(oo1));
    SB0();
  };
  if (w < 4) {
    unsigned od; u64 mkp;
    {
      u32x4 pa[2]; unsigned z0 = t8, z1 = t8;
#define H1LD(p) asm volatile("buffer_load_dwordx4 %0, %4, %6, 0 offen sc1\n\tbuffer_load_dwordx4 %1, %4, %6, %7 offen sc1\n\t" \
                   "s_cmp_lg_u32 %8, 3\n\ts_cbranch_scc1 1f\n\tbuffer_load_dword %2, %5, %6, 0 offen sc1\n\tbuffer_load_dword %3, %5, %6, 0 offen offset:256 sc1\n1:" \
                   : "=&v"(p[0]), "=&v"(p[1]), "+v"(z0), "+v"(z1) : "v"(oh1), "v"(oh1z), "s"(rs), "s"(65536), "s"(cw) : "memory", "scc")
      __builtin_amdgcn_s_sleep(H1_DELAY);
      H1LD(pa);
      SB0();
      od = WS_KVF + (unsigned)(hr * 16 + panel) * 640 + ((l & 8) ? 512 + (l & 7) * 2 : cw * 128 + h * 64 + (l & 7) * 8 + blk * 4);
      mkp = cw == 3 ? 0x00FF00FF00FF55FFull : 0x00FF00FF00FF00FFull;
      asm volatile("" : "+s"(mkp));
      fr[0] = __builtin_bit_cast(bf16x8, pack_u(qa[0][0], qa[1][0])); fr[1] = __builtin_bit_cast(bf16x8, pack_u(qa[0][1], qa[1][1]));
      asm volatile("" : "+v"(od), "+v"(fr[0]), "+v"(fr[1]));
      asm volatile("global_load_ushort %0, %1, %2" : "=v"(xrt) : "v"((unsigned)(idx * DM + nb * 64 + 4 * (l & 15) + ech) * 2), "s"(x) : "memory");
      later();
      o2 = WS_KVF + (unsigned)(hp * 16 + wn * 8 + wm * 4 + h) * 640 + ((r31 & 12) * 2 + (r31 >> 4) * 4 + (r31 & 3)) * 16;
      oz2 = WS_KVF + (unsigned)(hp * 16 + wn * 8 + wm * 4 + (l & 3)) * 640 + 512;
      wa_ = kba + wm * 2048; wz_ = lbase + X_KB + wn * 4224 + 4096 + (l & 1) * 64 + (wm * 2 + ((l >> 1) & 1)) * 16;
      asm volatile("" : "+v"(o2), "+v"(oz2), "+v"(wa_), "+v"(wz_));
      unsigned hb = 0x8000u, s_lo = 0x05040100u, s_hi = 0x07060100u;
      asm volatile("" : "+s"(s_lo), "+s"(s_hi));
      asm volatile("s_waitcnt vmcnt(1)" : "+v"(pa[0]), "+v"(pa[1]), "+v"(z0), "+v"(z1), "+v"(hb) :: "memory");
      u64 b0, b1, b2; u32x4 v0, v1;
      auto miss = [&]() __attribute__((always_inline)) {
        v0 = pa[0] ^ t2; v1 = pa[1] ^ t2;
        b0 = __ballot((or4(v0) & 0x00010001u) != 0u); b1 = __ballot((or4(v1) & 0x00010001u) != 0u);
        b2 = __ballot((((z0 ^ t8) | (z1 ^ t8)) & 0xFFu) != 0u);
        return (b0 | b1 | b2) != 0;
      };
      if (miss()) {
        unsigned sp = 0;
        do {
        b0 |= (b0 >> 4) & 0x0F0F0F0F0F0F0F0Full; b0 |= (b0 << 4) & 0xF0F0F0F0F0F0F0F0ull; b1 |= (b1 >> 4) & 0x0F0F0F0F0F0F0F0Full; b1 |= (b1 << 4) & 0xF0F0F0F0F0F0F0F0ull;
        asm volatile("s_wqm_b64 exec, %8\n\ts_cbranch_execz 1f\n\tbuffer_load_dwordx4 %0, %4, %6, 0 offen sc1\n1:\n\ts_wqm_b64 exec, %9\n\ts_cbranch_execz 2f\n\tbuffer_load_dwordx4 %1, %4, %6, %7 offen sc1\n2:\n\t"
                     "s_mov_b64 exec, -1\n\ts_cmp_eq_u64 %10, 0\n\ts_cbranch_scc1 3f\n\tbuffer_load_dword %2, %5, %6, 0 offen sc1\n\tbuffer_load_dword %3, %5, %6, 0 offen offset:256 sc1\n3:\n\ts_waitcnt vmcnt(0)"
                     : "+v"(pa[0]), "+v"(pa[1]), "+v"(z0), "+v"(z1) : "v"(oh1), "v"(oh1z), "s"(rs), "s"(65536), "s"(b0), "s"(b1), "s"(b2) : "memory", "scc");
        } while (miss() && ++sp < SPIN_CAP);
      }
    auto sw32 = [](float a, float c) { const auto r = __builtin_amdgcn_permlane32_swap(__float_as_uint(a), __float_as_uint(c), false, false); return __uint_as_float(r[0]) + __uint_as_float(r[1]); };
    auto sw16 = [](float a, float c) { const auto r = __builtin_amdgcn_permlane16_swap(__float_as_uint(a), __float_as_uint(c), false, false); return __uint_as_float(r[0]) + __uint_as_float(r[1]); };
    float sl[4], sh[4];
#pragma unroll
    for (int m = 0; m < 4; ++m) {
      sl[m] = own(__uint_as_float(__builtin_amdgcn_perm(v0[m], hb, s_lo)) + __uint_as_float(__builtin_amdgcn_perm(v1[m], hb, s_lo)));
      sh[m] = own(__uint_as_float(__builtin_amdgcn_perm(v0[m], hb, s_hi)) + __uint_as_float(__builtin_amdgcn_perm(v1[m], hb, s_hi))); }
    const float e0 = dpp_add<0x128, 0xf>(sw16(sw32(sl[0], sl[2]), sw32(sl[1], sl[3]))), e1 = dpp_add<0x128, 0xf>(sw16(sw32(sh[0], sh[2]), sw32(sh[1], sh[3])));
    const float zs = x32_add(x16_add(dpp_add<0x128, 0xf>(__uint_as_float(z0 ^ t8) + __uint_as_float(z1 ^ t8))));
    const float zn = __builtin_bit_cast(float, __builtin_amdgcn_update_dpp(0, __builtin_bit_cast(int, zs), 0xB1, 0xf, 0xf, true));
    const unsigned kvb = min((l & 8) ? pk2(zs, zn) : pk2(e0, e1), SENT - 1u);
    asm volatile("s_mov_b64 exec, %3\n\tbuffer_store_dword %0, %1, %2, 0 offen sc1\n\ts_mov_b64 exec, -1" :: "v"(kvb), "v"(od), "s"(rs), "s"(mkp) : "memory");
    }
  } else {
    const char* hq = smem + X_XW + cw * 2048 + l * 16;
    fr[0] = *(const bf16x8*)hq; fr[1] = *(const bf16x8*)(hq + 1024);
  }
  bf16x8 pf[4];
  {
    f32x2 ep[8], en[8];
    phi_fn(wp, fr[0], fr[1], ep, en, rt, 1.0f, [](auto) {});
#pragma unroll
    for (int s = 0; s < 2; ++s) {
      pf[s] = __builtin_bit_cast(bf16x8, u32x4{pk2(ep[4 * s][0], ep[4 * s][1]), pk2(ep[4 * s + 1][0], ep[4 * s + 1][1]), pk2(ep[4 * s + 2][0], ep[4 * s + 2][1]), pk2(ep[4 * s + 3][0], ep[4 * s + 3][1])});
      pf[2 + s] = __builtin_bit_cast(bf16x8, u32x4{pk2(en[4 * s][0], en[4 * s][1]), pk2(en[4 * s + 1][0], en[4 * s + 1][1]), pk2(en[4 * s + 2][0], en[4 * s + 2][1]), pk2(en[4 * s + 3][0], en[4 * s + 3][1])});
    }
    asm volatile("" : "+v"(pf[0]), "+v"(pf[1]), "+v"(pf[2]), "+v"(pf[3]));
  }
  SB0();
  if (b == 0 && tid == 64) ST64(*(const u32x2*)(smem + X_AM + 56), WS_EPOCH);
  u32x4 gq; unsigned ayr; u32x4 mk4;
#define GQLD(tail) asm volatile("buffer_load_dwordx4 %0, %1, %2, 0 offen sc1" tail : "=v"(gq) : "v"(ogq), "s"(rs) : "memory")
  if (w >= 4) {
    {
      WAIT_V(18 - NREG);
    { const unsigned o_ = (unsigned)((p3 * 128 + cw * 32 + (l >> 3)) * DM + nb * 64 + ((l & 7) ^ (l >> 3)) * 8) * 2; const u16* xa = x + fb * 16 * DM; const u16* xb = x + (fb ^ 1) * 16 * DM;
      gl_m0<0>(o_, xa, lxv); gl_m0<1024>(o_, xa + 8 * DM, lxv); gl_m0<0>(o_, xb, lxc); gl_m0<1024>(o_, xb + 8 * DM, lxc); }
      SB0();
      char* pa_ = smem + cw * 10 * 1024 + l * 16;
#pragma unroll
      for (int i = 0; i < NREG; ++i) *(u32x4*)(pa_ + i * 1024) = wr_[i];
      SB0();
    }
    asm volatile("" : "+v"(dv[0]), "+v"(dv[1]), "+v"(dv[2]), "+v"(dv[3]), "+v"(dv[4]), "+v"(dv[5]), "+v"(dv[6]), "+v"(dv[7]) :: "memory");
    later();
    ayr = lbase + X_YR + (cw * 128 + (l & 15) * 8 + blk * 4) * 4;
    { const unsigned hm = (l & 1) ? 0xFFFF0000u : 0x0000FFFFu; const int q = (l & 15) >> 1;
      mk4 = u32x4{q == 0 ? hm : 0u, q == 1 ? hm : 0u, q == 2 ? hm : 0u, q == 3 ? hm : 0u}; }
    asm volatile("" : "+v"(ayr), "+v"(mk4));
    SB0();
  }
  auto downp = [&]() __attribute__((always_inline)) {
            auto red4 = [](float s0, float s1, float s2, float s3) {
              const auto r01 = __builtin_amdgcn_permlane16_swap(__float_as_uint(s0), __float_as_uint(s1), false, false);
              const auto r23 = __builtin_amdgcn_permlane16_swap(__float_as_uint(s2), __float_as_uint(s3), false, false);
              const float t01 = __uint_as_float(r01[0]) + __uint_as_float(r01[1]), t23 = __uint_as_float(r23[0]) + __uint_as_float(r23[1]);
              const auto r = __builtin_amdgcn_permlane32_swap(__float_as_uint(t01), __float_as_uint(t23), false, false);
              return __uint_as_float(r[0]) + __uint_as_float(r[1]);
            };
            const float gg = rbf(red4(__uint_as_float(gq[0]), __uint_as_float(gq[1]), __uint_as_float(gq[2]), __uint_as_float(gq[3])));
            const float uu = __builtin_bit_cast(float, __builtin_amdgcn_update_dpp(0, __builtin_bit_cast(int, gg), 0x128, 0xf, 0xf, true));
            const float hv0 = rbf(silu_r(gg)) * uu;
            const int hh = (int)pk2(hv0, hv0);
            f32x4 ya = {0.f, 0.f, 0.f, 0.f};
            static_for<8>([&](auto ic) {
              constexpr int i = decltype(ic)::value;
              u32x4 sel;
#pragma unroll
              for (int e = 0; e < 4; ++e) sel[e] = (unsigned)__builtin_amdgcn_update_dpp(0, hh, 0x150 + i, 0xf, 0xf, true) & mk4[e];
              ya = MFMA16(__builtin_bit_cast(bf16x8, sel), __builtin_bit_cast(bf16x8, dv[i]), ya);
            });
            asm volatile("" : "+v"(ya));
            if (l < 32) *(__attribute__((address_space(3))) f32x4*)(uintptr_t)ayr = ya;
  };
  {
    u32x4 kf[4], zf[4];
    if (w < 4) {
      u32x4 a, c, z; u64 m0 = ~0ull, m1 = ~0ull, m2 = ~0ull;
      unsigned sp = 0;
      do {
        asm volatile("s_mov_b64 exec, %8\n\ts_cbranch_execz 1f\n\tbuffer_load_dwordx4 %0, %3, %5, 0 offen sc1\n1:\n\ts_mov_b64 exec, %9\n\ts_cbranch_execz 2f\n\tbuffer_load_dwordx4 %1, %3, %5, 0 offen offset:1280 sc1\n2:\n\t"
                     "s_mov_b64 exec, %10\n\ts_cbranch_execz 3f\n\tbuffer_load_dwordx4 %2, %4, %5, 0 offen sc1\n3:\n\ts_mov_b64 exec, -1\n\ts_waitcnt vmcnt(0)\n\t"
                     "ds_write_b128 %6, %0\n\tds_write_b128 %6, %1 offset:1024\n\tds_write_b128 %7, %2"
                     : "+v"(a), "+v"(c), "+v"(z) : "v"(o2), "v"(oz2), "s"(rs), "v"(wa_), "v"(wz_), "s"(m0), "s"(m1), "s"(m2) : "memory");
        m0 = __ballot(max(max(max(a[0], a[1]), a[2]), a[3]) == SENT); m1 = __ballot(max(max(max(c[0], c[1]), c[2]), c[3]) == SENT); m2 = __ballot(max(max(max(z[0], z[1]), z[2]), z[3]) == SENT);
      } while ((m0 | m1 | m2) != 0 && ++sp < SPIN_CAP);
      asm volatile("" : "+v"(xrt));
    }
    RAW_BARRIER();
    DS_B128(kf[0], kba, 0); DS_B128(zf[0], kbz, 0); DS_B128(kf[1], kba, 1024); DS_B128(zf[1], kbz, 16);
    DS_B128(kf[2], kba, 2048); DS_B128(zf[2], kbz, 32); DS_B128(kf[3], kba, 3072); DS_B128(zf[3], kbz, 48);
    if (w >= 4 && has_gu) GQLD("");
    __builtin_amdgcn_s_setprio(0);
    f32x16 num = zero;
    float d0 = 0.f, d1 = 0.f, d2 = 0.f, d3 = 0.f;
    static_for<4>([&](auto cc) {
      constexpr int c = decltype(cc)::value;
      asm volatile("s_waitcnt lgkmcnt(%0)" :: "n"(6 - 2 * c));
      SB0();
      num = MFMA(__builtin_bit_cast(bf16x8, kf[c]), pf[c], num);
      SB0();
      const u32x4 pc = __builtin_bit_cast(u32x4, pf[c]);
      d0 = dot2(zf[c][0], pc[0], d0); d1 = dot2(zf[c][1], pc[1], d1); d2 = dot2(zf[c][2], pc[2], d2); d3 = dot2(zf[c][3], pc[3], d3);
      SB0();
    });
    const float rinv = __builtin_amdgcn_rcpf(x32_add(own(d0 + d1) + own(d2 + d3)) + 1e-6f);
    const f32x2 rr = {rinv, rinv};
#pragma unroll
    for (int q = 0; q < 2; ++q) {
      u32x4 u;
#pragma unroll
      for (int e = 0; e < 4; ++e) {
        const f32x2 r = f32x2{num[8 * q + 2 * e], num[8 * q + 2 * e + 1]} * rr;
        u[e] = pk2(r[0], r[1]);
      }
      ST128(u, odo + q * 1024);
      SB0();
    }
  }
  const int p0 = 8 * w + 40;
  const u16* gw2 = wo + (size_t)((p0 ^ (flip * 64)) * 8) * DM + nb * 64; unsigned lw2 = lbase + p0 * 1024;
  asm volatile("" : "+s"(gw2), "+s"(lw2));
  bool glate = false;
  if (w >= 4 && has_gu) {
    asm volatile("s_waitcnt vmcnt(2)" : "+v"(gq) :: "memory");
    gq ^= t8;
    glate = !__all((or4(gq) & 0xFFu) == 0u);
    if (!glate) downp();
  }
  asm volatile("s_waitcnt vmcnt(0)\n\ts_mov_b64 exec, 1\n\tbuffer_store_dwordx2 %0, %1, %2, 0 offen sc1\n\ts_mov_b64 exec, -1" :: "v"(tg), "v"(of4), "s"(rs) : "memory");
  if (__builtin_expect(glate, 0)) {
    for (unsigned sp = 0;;) {
      GQLD("\n\ts_waitcnt vmcnt(0)");
      gq ^= t8;
      if (__all((or4(gq) & 0xFFu) == 0u)) break;
      SPIN_NEXT(sp);
    }
    downp();
  }
  RAW_BARRIER();
  float rxn = 0.f, rsg = 0.f;
  u32x4 yq[7];
  unsigned oy = WS_YP + (unsigned)(l >> 4) * 4096 + nb * 256 + (l & 15) * 16;
#define YLD(tail) asm volatile("buffer_load_dwordx4 %0, %7, %8, 0 offen sc1\n\tbuffer_load_dwordx4 %1, %7, %8, %9 offen sc1\n\tbuffer_load_dwordx4 %2, %7, %8, %10 offen sc1\n\t" \
    "buffer_load_dwordx4 %3, %7, %8, %11 offen sc1\n\tbuffer_load_dwordx4 %4, %7, %8, %12 offen sc1\n\tbuffer_load_dwordx4 %5, %7, %8, %13 offen sc1\n\t" \
    "buffer_load_dwordx4 %6, %7, %8, %14 offen sc1" tail \
    : "=&v"(yq[0]), "=&v"(yq[1]), "=&v"(yq[2]), "=&v"(yq[3]), "=&v"(yq[4]), "=&v"(yq[5]), "=&v"(yq[6]) \
    : "v"(oy), "s"(rs), "s"(16384), "s"(2 * 16384), "s"(3 * 16384), "s"(4 * 16384), "s"(5 * 16384), "s"(6 * 16384) : "memory")
  f32x2 xf[2][4], gf[2][4];
  __attribute__((address_space(3))) f32x4* pw = (__attribute__((address_space(3))) f32x4*)(smem + 131072 + w * 4096) + l;
  unsigned prd = lbase + 131072 + (w ^ 4) * 4096 + l * 16;
  u16* po = out + (size_t)(p3 * 128 + cw * 32 + fb * 16 + (l >> 3)) * DM + nb * 64 + (l & 7) * 8; u16* po2 = po + 8 * DM;
  unsigned tw0 = lbase + 131072 + (w ^ 4) * 4096 + (l & 15) * 128 + ((ech ^ (l & 7)) * 16) + (sw ? 8 : 0), tw1 = tw0 ^ 64, tw2 = tw0 ^ 8, tw3 = tw0 ^ 72, trd = lbase + 131072 + (w ^ 4) * 4096 + (l >> 3) * 128 + (((l & 7) ^ (l >> 3)) * 16);
    unsigned bw[4];
#pragma unroll
    for (int n = 0; n < 4; ++n) bw[n] = lbase + kh * 65536 + (16 * h + 4 * blk + tq) * 128 + (((4 * (n >> 1) + tp) ^ ((tq >> 1) | (blk << 2))) * 16) + 8 * ((n ^ tp) & 1);
  {
  if (w == 1 && has_gu && l < 32) {
    const f32x4* y = (const f32x4*)(smem + X_YR) + l;
    ST128(f2u_tag((y[0] + y[32]) + (y[64] + y[96]), t8), WS_YP + (unsigned)(gr >> 3) * 4096 + ((gr + 1) & 7) * 512 + l * 16);
  }
    SB0();
    gl_run<8>(voO, gw2, lw2);
    u32x2 f0;
    asm volatile("buffer_load_dwordx2 %0, %1, %2, 0 offen sc1" : "=v"(f0) : "v"(op4), "s"(rs) : "memory");
    SB0();
    {
      const f32x2 md = *(const f32x2*)(smem + X_MD + erow * 8);
      { const unsigned ax = lxv + (l & 15) * 128 + ((ech ^ (l & 7)) * 16), a0 = ax + (sw ? 8 : 0), a1 = ax + (sw ? 0 : 8);
        u32x2 q0, q1, q2, q3;
        asm volatile("ds_read_b64 %0, %4\n\tds_read_b64 %1, %5\n\tds_read_b64 %2, %6\n\tds_read_b64 %3, %7\n\ts_waitcnt lgkmcnt(0)" : "=&v"(q0), "=&v"(q1), "=&v"(q2), "=&v"(q3) : "v"(a0), "v"(a1), "v"(a0 ^ 64), "v"(a1 ^ 64) : "memory");
        xv[0] = u32x4{q0[0], q0[1], q1[0], q1[1]}; xv[1] = u32x4{q2[0], q2[1], q3[0], q3[1]}; }
      const float rinv = rcp_nr(own(md[1])), mr = -md[0] * rinv, L2E = 1.44269504088896340736f, rl = -L2E * rinv, ml = -L2E * mr;
#pragma unroll
      for (int i = 0; i < 2; ++i)
#pragma unroll
        for (int e = 0; e < 4; ++e) {
          xf[i][e] = f32x2{bflo(xv[i][e]), bfhi(xv[i][e])};
          const f32x2 xn = __builtin_elementwise_fma(xf[i][e], f32x2{rinv, rinv}, f32x2{mr, mr}), tn = __builtin_elementwise_fma(xf[i][e], f32x2{rl, rl}, f32x2{ml, ml});
          const f32x2 dd = f32x2{__builtin_amdgcn_exp2f(tn[0]), __builtin_amdgcn_exp2f(tn[1])} + f32x2{1.0f, 1.0f};
          gf[i][e] = xn * f32x2{__builtin_amdgcn_rcpf(dd[0]), __builtin_amdgcn_rcpf(dd[1])};
        }
#pragma unroll
      for (int i = 0; i < 2; ++i) asm volatile("" : "+v"(xf[i][0]), "+v"(xf[i][1]), "+v"(xf[i][2]), "+v"(xf[i][3]), "+v"(gf[i][0]), "+v"(gf[i][1]), "+v"(gf[i][2]), "+v"(gf[i][3]));
      asm volatile("" : "+v"(pw), "+v"(prd), "+v"(po2), "+v"(tw0), "+v"(tw1), "+v"(tw2), "+v"(tw3), "+v"(trd));
      asm volatile("" : "+v"(oy), "+v"(po), "+v"(bw[0]), "+v"(bw[1]), "+v"(bw[2]), "+v"(bw[3]));
    }
    if (has_idx) {
      const unsigned ord = (rkey >> 11) & 0xFFFFu;
      rsg = rbf(1.0f / (1.0f + __expf(-__uint_as_float((ord ^ ((ord & 0x8000u) ? 0x8000u : 0xFFFFu)) << 16))));
      const f32x2 md = *(const f32x2*)(smem + X_MD + (idx & 127) * 8);
      const float dn = own(md[1]), rinv = rcp_nr(dn);
      rxn = bflo(ln_pair(xrt, md[0], dn, rinv));
      asm volatile("" : "+v"(rxn), "+v"(rsg));
    }
    u64 ne;
    SB0();
    asm volatile("s_waitcnt vmcnt(0)\n\tv_cmp_ne_u64 %0, %1, %2" : "=s"(ne) : "v"(f0), "v"(tg) : "memory");
    for (unsigned sp = 0; ne != 0 && sp < SPIN_CAP; ++sp) {
      u32x2 f;
      asm volatile("buffer_load_dwordx2 %1, %2, %3, 0 offen sc1\n\ts_waitcnt vmcnt(0)\n\tv_cmp_ne_u64 %0, %1, %4" : "=s"(ne), "=&v"(f) : "v"(op4), "s"(rs), "v"(tg) : "memory");
    }
  }
  constexpr int OWIN = 4;
  u32x4 of[16][2];
  {
    SB0();
#pragma unroll
    for (int i = 0; i < OWIN; ++i) { of[i][0] = LD128(oo0 + i * 2048); of[i][1] = LD128(oo1 + i * 2048); }
    SB0();
    if (has_idx) YLD("");
    SB0();
  }
    auto owner = [&](auto nc) __attribute__((always_inline)) {
      asm volatile("s_waitcnt vmcnt(%7)" : "+v"(yq[0]), "+v"(yq[1]), "+v"(yq[2]), "+v"(yq[3]), "+v"(yq[4]), "+v"(yq[5]), "+v"(yq[6]) : "n"(decltype(nc)::value) : "memory");
      for (unsigned sp = 0;; ++sp) {
        unsigned bad = 0;
#pragma unroll
        for (int i = 0; i < 7; ++i) { yq[i] ^= t8; bad |= or4(yq[i]); }
        if (__builtin_expect(__all((bad & 0xFFu) == 0u) || sp > SPIN_CAP, 1)) break;
        YLD("\n\ts_waitcnt vmcnt(0)");
      }
#define YF(i) __builtin_bit_cast(f32x4, yq[i])
      const f32x4 y4 = ((YF(0) + YF(1)) + (YF(2) + YF(3))) + ((YF(4) + YF(5)) + YF(6));
      const auto r01 = __builtin_amdgcn_permlane16_swap(__float_as_uint(y4[0]), __float_as_uint(own(y4[1])), false, false);
      const auto r23 = __builtin_amdgcn_permlane16_swap(__float_as_uint(y4[2]), __float_as_uint(own(y4[3])), false, false);
      const float t01 = __uint_as_float(r01[0]) + __uint_as_float(r01[1]), t23 = __uint_as_float(r23[0]) + __uint_as_float(r23[1]);
      const auto r = __builtin_amdgcn_permlane32_swap(__float_as_uint(t01), __float_as_uint(t23), false, false);
      const float y = __uint_as_float(r[0]) + __uint_as_float(r[1]);
      const float g1 = rbf(silu_r(rbf(rxn + rbf(rbf(y) * rsg))));
      (void)&gf; (void)&oy; (void)&rs; const u64 mk_ = 0x0001000100010001ull << (idx & 15);
      const unsigned gb = lbase + (131072 + w * 4096), aw_ = gb + (l & 15) * 16 + ech * 4, a0 = gb + ech * 32 + (ech & 1) * 16, a1 = a0 ^ 16;
      asm volatile("ds_write_b32 %8, %9\n\ts_mov_b64 exec, %12\n\t"
                   "ds_read_b64 %0, %10\n\tds_read_b64 %1, %10 offset:8\n\tds_read_b64 %2, %11\n\tds_read_b64 %3, %11 offset:8\n\t"
                   "ds_read_b64 %4, %10 offset:128\n\tds_read_b64 %5, %10 offset:136\n\tds_read_b64 %6, %11 offset:128\n\tds_read_b64 %7, %11 offset:136\n\t"
                   "s_mov_b64 exec, -1\n\ts_waitcnt lgkmcnt(0)"
                   : "+v"(gf[0][0]), "+v"(gf[0][1]), "+v"(gf[0][2]), "+v"(gf[0][3]), "+v"(gf[1][0]), "+v"(gf[1][1]), "+v"(gf[1][2]), "+v"(gf[1][3])
                   : "v"(aw_), "v"(g1), "v"(a0), "v"(a1), "s"(mk_) : "memory");
    };
  f32x4 acc3[2][4];
#pragma unroll
  for (int t = 0; t < 2; ++t)
#pragma unroll
    for (int n = 0; n < 4; ++n) acc3[t][n] = f32x4{0.f, 0.f, 0.f, 0.f};
  {
    s16x4 wa[2][8];
    auto rd3 = [&](auto kc, auto nc) {
      constexpr int k = decltype(kc)::value, n = decltype(nc)::value;
      (void)&wa; (void)&bw;
      DS_TR(wa[k & 1][2 * n], bw[n], k * 4096); DS_TR(wa[k & 1][2 * n + 1], bw[n], k * 4096 + 1024);
    };
    RAW_BARRIER();
    static_for<4>([&](auto nc) { rd3(IC(0), nc); });
    static_for<15>([&](auto kc) {
      constexpr int k = decltype(kc)::value, sx = k & 1;
      static_for<4>([&](auto nc) {
        constexpr int n = decltype(nc)::value;
        asm volatile("s_waitcnt lgkmcnt(%2)" : "+v"(wa[sx][2 * n]), "+v"(wa[sx][2 * n + 1]) : "n"(6));
        SB0();
        const bf16x8 wf = cat8(wa[sx][2 * n], wa[sx][2 * n + 1]);
        acc3[0][n] = MFMA16(wf, __builtin_bit_cast(bf16x8, of[k][0]), acc3[0][n]);
        SB0();
        if constexpr (k * 4 + n == 17) STUB(10);
        if constexpr (n == 0 && (k & 3) == 0) __builtin_amdgcn_s_setprio(3 - (k >> 2));
        if constexpr (k + 1 < 16) rd3(IC(k + 1), nc);
        if constexpr (k + OWIN < 16 && n == 3) { of[k + OWIN][0] = LD128(oo0 + (k + OWIN) * 2048); }
        SB0();
        acc3[1][n] = MFMA16(wf, __builtin_bit_cast(bf16x8, of[k][1]), acc3[1][n]);
        SB0();
        if constexpr (k + OWIN < 16 && n == 3) { of[k + OWIN][1] = LD128(oo1 + (k + OWIN) * 2048); SB0(); }
        if constexpr (k == YP_ && n == 3) { constexpr int e_ = YP_ + OWIN < 16 ? YP_ : 15 - OWIN; if (has_idx) owner(IC(2 * (e_ + 1))); SB0(); }
      });
    });
    static_for<4>([&](auto nc) {
      constexpr int n = decltype(nc)::value;
      asm volatile("s_waitcnt lgkmcnt(%2)" : "+v"(wa[1][2 * n]), "+v"(wa[1][2 * n + 1]) : "n"(6 - 2 * n));
      SB0();
      acc3[1][n] = MFMA16(cat8(wa[1][2 * n], wa[1][2 * n + 1]), __builtin_bit_cast(bf16x8, of[15][1]), acc3[1][n]);
      SB0();
      if constexpr (n == 3) { pw[0] = acc3[1][0]; SB0(); }
    });
    static_for<4>([&](auto nc) {
      constexpr int n = decltype(nc)::value;
      acc3[0][n] = MFMA16(cat8(wa[1][2 * n], wa[1][2 * n + 1]), __builtin_bit_cast(bf16x8, of[15][0]), acc3[0][n]);
      SB0();
      if constexpr (n < 3) { pw[(n + 1) * 64] = acc3[1][n + 1]; SB0(); }
    });
  }
  {
    RAW_BARRIER();
    f32x4 pr[4];
    DS_B128(pr[0], prd, 0); DS_B128(pr[1], prd, 1024); DS_B128(pr[2], prd, 2048); DS_B128(pr[3], prd, 3072);
    SB0();
#pragma unroll
    for (int i = 0; i < 2; ++i)
#pragma unroll
      for (int e = 0; e < 4; ++e) {
        const f32x4 a = acc3[0][2 * i + (e >> 1)];
        xf[i][e] = __builtin_elementwise_fma(f32x2{a[2 * (e & 1)], a[2 * (e & 1) + 1]}, gf[i][e], xf[i][e]);
        asm volatile("" : "+v"(xf[i][e]));
      }
    SB0();
    u32x4 res[2];
#pragma unroll
    for (int i = 0; i < 2; ++i) {
      if (i == 0) asm volatile("s_waitcnt lgkmcnt(2)" : "+v"(pr[0]), "+v"(pr[1])); else asm volatile("s_waitcnt lgkmcnt(2)" : "+v"(pr[2]), "+v"(pr[3]));
      const f32x4 s0 = pr[2 * i], s1 = pr[2 * i + 1];
      const f32x2 o0 = __builtin_elementwise_fma(f32x2{s0[0], s0[1]}, gf[i][0], xf[i][0]), o1 = __builtin_elementwise_fma(f32x2{s0[2], s0[3]}, gf[i][1], xf[i][1]);
      const f32x2 o2 = __builtin_elementwise_fma(f32x2{s1[0], s1[1]}, gf[i][2], xf[i][2]), o3 = __builtin_elementwise_fma(f32x2{s1[2], s1[3]}, gf[i][3], xf[i][3]);
      res[i] = u32x4{pk2(o0[0], o0[1]), pk2(o1[0], o1[1]), pk2(o2[0], o2[1]), pk2(o3[0], o3[1])};
      asm volatile("ds_write_b64 %0, %2\n\tds_write_b64 %1, %3" :: "v"(i ? tw1 : tw0), "v"(i ? tw3 : tw2), "v"(u32x2{res[i][0], res[i][1]}), "v"(u32x2{res[i][2], res[i][3]}) : "memory");
    }
    asm volatile("ds_read_b128 %0, %2\n\tds_read_b128 %1, %2 offset:1024\n\ts_waitcnt lgkmcnt(1)\n\t"
                 "global_store_dwordx4 %3, %0, off sc1\n\ts_waitcnt lgkmcnt(0)\n\tglobal_store_dwordx4 %4, %1, off sc1\n\ts_nop 1"
                 : "=&v"(res[0]), "=&v"(res[1]) : "v"(trd), "v"(po), "v"(po2) : "memory");
  }
  asm volatile(".Lkf_pf_end:" ::: "memory");
}

extern "C" void kernel_launch(void* const* d_in, const int* in_sizes, int n_in,
                              void* d_out, int out_size, void* d_ws, size_t ws_size,
                              hipStream_t stream) {
  (void)hipFuncSetAttribute((const void*)kf_all, hipFuncAttributeMaxDynamicSharedMemorySize, LDS_TOTAL);
  kf_all<<<256, 512, LDS_TOTAL, stream>>>((const u16*)d_in[0], (const u16*)d_in[1], (const u16*)d_in[2], (const u16*)d_in[3],
                                          (const u16*)d_in[5], (const u16*)d_in[6], (const u16*)d_in[7], (const u16*)d_in[8],
                                          (char*)d_ws, (u16*)d_out);
}
```

```cpp
#define KP 9
#define KC 10
#define F1_S 13
#define PF2_KT 8
#define WOK 2
#define H1_DELAY 4
constexpr int PACE_T[16] = {0, 114, 84, 64}, PACE_3[16] = {0, 70, 60, 56};
constexpr int DVN[20] = {0, 0, 4, 2, 1, 0, 1};
constexpr int dv_first(int k) { int n = 0; for (int i = -2; i < k; ++i) n += DVN[i + 2]; return n; }
constexpr bool CSS(int S) { return S < 2 || S == 6 || S == 7; }
#define NREG 8
static_assert(NREG <= 10, "first run");
#define WH0 12
#define WPOST 4
#define YP_ 11
#define PRIO_T 2
#include <hip/hip_runtime.h>
#include <hip/hip_bf16.h>
#include <stdint.h>
#include <utility>
#include <type_traits>

typedef __attribute__((ext_vector_type(8))) short bf16x8;
typedef __attribute__((ext_vector_type(4))) short s16x4;
typedef __attribute__((ext_vector_type(16))) float f32x16;
typedef __attribute__((ext_vector_type(4))) float f32x4;
typedef __attribute__((ext_vector_type(2))) float f32x2;
typedef __attribute__((ext_vector_type(4))) unsigned u32x4;
typedef __attribute__((ext_vector_type(2))) unsigned u32x2;
typedef __attribute__((ext_vector_type(2))) __bf16 bfx2;
typedef __attribute__((ext_vector_type(4))) short v4i16_t;
typedef unsigned long long u64;
typedef unsigned short u16;

#define DI __device__ __forceinline__
#define MFMA(a, b, c) __builtin_amdgcn_mfma_f32_32x32x16_bf16((a), (b), (c), 0, 0, 0)
#define MFMA16H(a, b, c) __builtin_amdgcn_mfma_f32_16x16x16bf16_1k((a), (b), (c), 0, 0, 0)
#define MFMA16(a, b, c) __builtin_amdgcn_mfma_f32_16x16x32_bf16((a), (b), (c), 0, 0, 0)

DI unsigned pk2(float a, float b) { f32x2 v = {a, b}; return __builtin_bit_cast(unsigned, __builtin_convertvector(v, bfx2)); }
DI float bflo(unsigned u) { return __uint_as_float(u << 16); }
DI float bfhi(unsigned u) { return __uint_as_float(u & 0xffff0000u); }
DI float rbf(float x) { return (float)(__bf16)x; }
DI s16x4 trread(const char* p) {
  return __builtin_bit_cast(s16x4, __builtin_amdgcn_ds_read_tr16_b64_v4i16((__attribute__((address_space(3))) v4i16_t*)p));
}
DI bf16x8 cat8(s16x4 lo, s16x4 hi) { return __builtin_shufflevector(lo, hi, 0, 1, 2, 3, 4, 5, 6, 7); }
#define WAIT_V(n) asm volatile("s_waitcnt vmcnt(%0)" ::"n"(n) : "memory")
#define RAW_BARRIER() do { asm volatile("s_waitcnt lgkmcnt(0)" ::: "memory"); __builtin_amdgcn_s_barrier(); asm volatile("" ::: "memory"); } while (0)
#define DS_B128(dst, addr, off) asm volatile("ds_read_b128 %0, %1 offset:%2" : "=v"(dst) : "v"(addr), "n"(off))
#define DS_TR(dst, addr, off) asm volatile("ds_read_b64_tr_b16 %0, %1 offset:%2" : "=v"(dst) : "v"(addr), "n"(off))
#define LDS_OFF(p) ((unsigned)(uintptr_t)(__attribute__((address_space(3))) const char*)(p))
template <class F, int... I> DI void static_for_impl(F&& f, std::integer_sequence<int, I...>) { (f(std::integral_constant<int, I>{}), ...); }
template <int N, class F> DI void static_for(F&& f) { static_for_impl(f, std::make_integer_sequence<int, N>{}); }
DI float divc(float a, float b, float rinv) { const float q = a * rinv; return __builtin_fmaf(__builtin_fmaf(-q, b, a), rinv, q); }
DI float rcp_nr(float d) { const float r = __builtin_amdgcn_rcpf(d); return __builtin_fmaf(__builtin_fmaf(-d, r, 1.0f), r, r); }
DI float own(float v) { asm volatile("" : "+v"(v)); return v; }
DI float silu_r(float x) { return x * __builtin_amdgcn_rcpf(1.0f + __expf(-x)); }
#define SB0() __builtin_amdgcn_sched_barrier(0)
#define GLDS_S(vo, sp, ld) asm volatile("s_mov_b32 m0, %2\n\ts_nop 0\n\tglobal_load_lds_dwordx4 %0, %1" :: "v"(vo), "s"(sp), "s"(ld) : "m0", "memory")
template <int IMM> DI void gl_m0(unsigned vo, const void* sp, unsigned ls) {
  asm volatile("s_add_u32 m0, %2, %3\n\ts_nop 0\n\tglobal_load_lds_dwordx4 %0, %1" :: "v"(vo), "s"(sp), "s"(ls), "n"(IMM) : "m0", "memory", "scc");
}
template <int IMM> DI void gl_nt(unsigned vo, const void* sp, unsigned ls) {
  asm volatile("s_add_u32 m0, %2, %3\n\ts_nop 0\n\tglobal_load_lds_dwordx4 %0, %1 nt" :: "v"(vo), "s"(sp), "s"(ls), "n"(IMM) : "m0", "memory", "scc");
}
template <int N> DI void gl_run(unsigned vo, const void* sp, unsigned ls) {
  unsigned lo = (unsigned)(uintptr_t)sp, hi = (unsigned)((uintptr_t)sp >> 32);
  static_for<N>([&](auto ic) {
    constexpr int i = decltype(ic)::value;
    gl_m0<i * 1024>(vo, (const void*)(((uintptr_t)hi << 32) | lo), ls);
    if constexpr (i + 1 < N) asm volatile("s_add_u32 %0, %0, 0x4000\n\ts_addc_u32 %1, %1, 0" : "+s"(lo), "+s"(hi) :: "scc");
  });
}
#define GLDS_SC1(vo, sp, ld) asm volatile("s_mov_b32 m0, %2\n\ts_nop 0\n\tglobal_load_lds_dwordx4 %0, %1 sc1" :: "v"(vo), "s"(sp), "s"(ld) : "m0", "memory")
DI float x32_add(float v) { auto r = __builtin_amdgcn_permlane32_swap(__float_as_uint(v), __float_as_uint(v), false, false); return __uint_as_float(r[0]) + __uint_as_float(r[1]); }
DI float x32_max(float v) { auto r = __builtin_amdgcn_permlane32_swap(__float_as_uint(v), __float_as_uint(v), false, false); float o; asm("v_max_f32 %0, %1, %2" : "=v"(o) : "v"(r[0]), "v"(r[1])); return o; }
DI float x32_min(float v) { auto r = __builtin_amdgcn_permlane32_swap(__float_as_uint(v), __float_as_uint(v), false, false); float o; asm("v_min_f32 %0, %1, %2" : "=v"(o) : "v"(r[0]), "v"(r[1])); return o; }
DI float x16_add(float v) { auto r = __builtin_amdgcn_permlane16_swap(__float_as_uint(v), __float_as_uint(v), false, false); return __uint_as_float(r[0]) + __uint_as_float(r[1]); }
template <int CTRL, int RMASK> DI float dpp_add(float v) {
  return v + __builtin_bit_cast(float, __builtin_amdgcn_update_dpp(0, __builtin_bit_cast(int, v), CTRL, RMASK, 0xf, true));
}

DI unsigned ln_pair(unsigned u, float m, float den, float rinv) {
  const f32x2 mm = {m, m}, dd = {den, den}, rr = {rinv, rinv};
  const f32x2 d0 = f32x2{bflo(u), bfhi(u)} - mm;
  const unsigned du = pk2(d0[0], d0[1]);
  const f32x2 d = {bflo(du), bfhi(du)};
  const f32x2 q = d * rr;
  const f32x2 r = __builtin_elementwise_fma(__builtin_elementwise_fma(-q, dd, d), rr, q);
  return pk2(r[0], r[1]);
}
DI float dot2(unsigned a, unsigned b, float c) { return __builtin_amdgcn_fdot2_f32_bf16(__builtin_bit_cast(bfx2, a), __builtin_bit_cast(bfx2, b), c, false); }
DI float half_total(float v) {
  v = dpp_add<0x121, 0xf>(v); v = dpp_add<0x122, 0xf>(v); v = dpp_add<0x124, 0xf>(v); v = dpp_add<0x128, 0xf>(v);
  return x16_add(v);
}
DI void ln_rows(const u32x4 (&xr)[4], const u32x4 (&wr)[2], int h, float& m, float& den, float& rinv, float& r) {
  const float c0 = __uint_as_float(__builtin_amdgcn_readlane(xr[0][0], 0) << 16), c1 = __uint_as_float(__builtin_amdgcn_readlane(xr[2][0], 0) << 16);
  float s1[2] = {0.f, 0.f}, sx[2] = {0.f, 0.f}, sw = 0.f;
  f32x2 q[2] = {{0.f, 0.f}, {0.f, 0.f}};
#pragma unroll
  for (int i = 0; i < 2; ++i)
#pragma unroll
    for (int e = 0; e < 4; ++e) {
      sw = dot2(wr[i][e], 0x3F803F80u, sw);
#pragma unroll
      for (int t = 0; t < 2; ++t) {
        const unsigned u = xr[2 * t + i][e]; const float c = t ? c1 : c0;
        s1[t] = dot2(u, 0x3F803F80u, s1[t]); sx[t] = dot2(u, wr[i][e], sx[t]);
        const f32x2 d = f32x2{bflo(u), bfhi(u)} - f32x2{c, c};
        q[t] = __builtin_elementwise_fma(d, d, q[t]);
      }
    }
  auto mg = [](float a, float b) { const auto x = __builtin_amdgcn_permlane32_swap(__float_as_uint(a), __float_as_uint(b), false, false); return __uint_as_float(x[0]) + __uint_as_float(x[1]); };
  const float mean = half_total(mg(s1[0], s1[1])) * (1.0f / 1024.0f), m2 = half_total(mg(q[0][0] + own(q[0][1]), q[1][0] + own(q[1][1]))) * (1.0f / 1024.0f);
  const float tx = half_total(mg(sx[0], sx[1])), tw = half_total(x32_add(sw));
  const float dm = mean - (h ? c1 : c0);
  const float var = fmaxf(__builtin_fmaf(-dm, dm, m2), 0.f);
  m = rbf(mean);
  den = rbf(__builtin_amdgcn_sqrtf(rbf(rbf(var) + 1e-5f)));
  rinv = rcp_nr(den);
  r = rbf(divc(__builtin_fmaf(-m, tw, tx), den, rinv));
}

template <class HK> DI void phi_fn(const bf16x8 (&wp)[2], const bf16x8 f0, const bf16x8 f1, f32x2 (&ep)[8], f32x2 (&en)[8], float si, float so, HK&& hk) {
  f32x16 p;
#pragma unroll
  for (int i = 0; i < 16; ++i) p[i] = 0.f;
  p = MFMA(wp[0], f0, p);
  p = MFMA(wp[1], f1, p);
  hk(std::integral_constant<int, 0>{});
  float mx = -3.0e38f, mn = 3.0e38f;
  f32x2 p2[8];
  static_for<8>([&](auto ic) {
    constexpr int i = decltype(ic)::value;
    p2[i] = f32x2{p[2 * i], p[2 * i + 1]};
    mx = fmaxf(fmaxf(mx, p2[i][0]), p2[i][1]); mn = fminf(fminf(mn, p2[i][0]), p2[i][1]);
    if constexpr (i & 1) hk(std::integral_constant<int, 1 + (i >> 1)>{});
  });
  mx = x32_max(mx); mn = x32_min(mn);
  hk(std::integral_constant<int, 5>{});
  const float L2E = 1.44269504088896340736f * si, mxl = -mx * L2E, mnl = mn * L2E;
  const f32x2 cp = {L2E, L2E}, cn = {-L2E, -L2E}, bp = {mxl, mxl}, bn = {mnl, mnl};
  f32x2 sp2, sn2;
  static_for<8>([&](auto ic) {
    constexpr int i = decltype(ic)::value;
    const f32x2 tp2 = __builtin_elementwise_fma(p2[i], cp, bp), tn2 = __builtin_elementwise_fma(p2[i], cn, bn);
    ep[i] = f32x2{__builtin_amdgcn_exp2f(tp2[0]), __builtin_amdgcn_exp2f(tp2[1])};
    en[i] = f32x2{__builtin_amdgcn_exp2f(tn2[0]), __builtin_amdgcn_exp2f(tn2[1])};
    if constexpr (i == 0) { sp2 = ep[0]; sn2 = en[0]; } else { sp2 += ep[i]; sn2 += en[i]; }
    hk(std::integral_constant<int, 6 + i>{});
  });
  const float sp = x32_add(sp2[0] + own(sp2[1])), sn = x32_add(sn2[0] + own(sn2[1]));
  hk(std::integral_constant<int, 14>{});
  const float rp = __builtin_amdgcn_rcpf(sp) * so, rn = __builtin_amdgcn_rcpf(sn) * so;
  const f32x2 rp2 = {rp, rp}, rn2 = {rn, rn};
  hk(std::integral_constant<int, 15>{});
#pragma unroll
  for (int i = 0; i < 8; ++i) { ep[i] = ep[i] * rp2; en[i] = en[i] * rn2; }
  hk(std::integral_constant<int, 16>{});
}

constexpr int DM = 1024, FF = 3584;
constexpr unsigned SENT = 0xFFFFFFFFu;
constexpr unsigned SPIN_CAP = 1u << 17;

constexpr unsigned WS_EPOCH = 0;
constexpr unsigned WS_F1 = 4096;
constexpr unsigned WS_F4 = 896u << 10;
constexpr unsigned WS_STAT = 20480;
constexpr unsigned WS_CS = 36864;
constexpr unsigned WS_GUP = WS_CS + 16 * 16 * 768;
constexpr unsigned WS_YP = WS_GUP + 256 * 512;
constexpr unsigned WS_KVF = WS_YP + 32 * 4096;
constexpr unsigned WS_ZP = WS_KVF + 256 * 640;
constexpr unsigned WS_XA = 800u << 10;
constexpr unsigned WS_RI = 840u << 10;
static_assert(WS_ZP + 131072 <= WS_XA && WS_XA + 32768 <= WS_RI, "ws map");
constexpr unsigned WS_KVP = 1u << 20;
constexpr unsigned WS_O = 8u << 20;
constexpr unsigned WS_END = 16u << 20;

constexpr int ABYTES = 128 * 128, WSLAB = 64 * 128, STAGE1 = ABYTES + 3 * WSLAB, NST1 = 3;
constexpr int XB = NST1 * STAGE1;
constexpr int X_WO = XB;
constexpr int X_CSP = XB + 8192;
constexpr int X_P = 131072 + 4096;
constexpr int X_XW = X_P;
constexpr int X_XA = X_XW + 6144;
constexpr int X_ST = X_XA + 2048;
constexpr int X_MD = X_ST + 1024;
constexpr int X_AM = X_MD + 1024;
constexpr int X_XS = X_AM + 64;
constexpr int X_YR = X_XS + 512;
constexpr int X_YS = X_YR + 2048;
constexpr int X_WP = X_YS + 2048;
constexpr int X_KB = X_WP + 4096;
constexpr int LDS_TOTAL = 163840;
static_assert(X_KB + 2 * 4224 <= LDS_TOTAL && 131072 + 6 * 2048 <= X_MD && X_YS + 64 + 4096 <= X_KB && X_YS % 128 == 64, "lds");
static_assert(LDS_TOTAL <= 163840, "lds");
constexpr int gu_at(int ks) { return STAGE1 + ks * 8192; }
constexpr int HANDOFF = 106496;

#define LD128(off) __builtin_amdgcn_raw_buffer_load_b128(rs, (int)(off), 0, 16)
#define ST128(v, off) __builtin_amdgcn_raw_buffer_store_b128((v), rs, (int)(off), 0, 16)
#define ST64(v, off) __builtin_amdgcn_raw_buffer_store_b64((v), rs, (int)(off), 0, 16)
DI u32x4 f2u_tag(f32x4 v, unsigned t8) { u32x4 u = __builtin_bit_cast(u32x4, v); return (u & 0xFFFFFF00u) | t8; }
DI unsigned or4(u32x4 v) { return (v[0] | v[1]) | (v[2] | v[3]); }
#define SPIN_NEXT(sp) { if (++(sp) > SPIN_CAP) break; __builtin_amdgcn_s_sleep(1); }
#define TAG_OF(en) ((en) * 0x9E3779B97F4A7C15ull)

__global__ __launch_bounds__(512, 2) void kf_all(const u16* __restrict__ x, const u16* __restrict__ wqkv, const u16* __restrict__ wphi,
                                              const u16* __restrict__ wo, const u16* __restrict__ w_r, const u16* __restrict__ w_gate,
                                              const u16* __restrict__ w_up, const u16* __restrict__ w_down, char* ws, u16* __restrict__ out) {
  extern __shared__ __attribute__((aligned(16))) char smem[];
  asm volatile(".Lkf_pf_start:");
  const int b = blockIdx.x;
  const int tid = threadIdx.x, w = __builtin_amdgcn_readfirstlane(tid >> 6);
  unsigned pfd;
  {
    u64 pc; unsigned len;
    asm volatile("s_getpc_b64 %0\n.Lkf_pf_here:\n\ts_mov_b32 %1, .Lkf_pf_end+1152-256-.Lkf_pf_here" : "=s"(pc), "=s"(len));
    const u64 pa = pc + min((unsigned)((b & ~7) + w) << 8, len);
    asm volatile("s_load_dword %0, %1, 0x0\n\ts_load_dword %0, %1, 0x40\n\ts_load_dword %0, %1, 0x80\n\ts_load_dword %0, %1, 0xc0" : "=&s"(pfd) : "s"(pa) : "memory");
  }
  SB0();
  const int xcd = b & 7, jj = b >> 3, l = tid & 63;
  const int panel = (xcd >> 2) * 8 + (jj & 7), hp = (xcd & 3) * 4 + (jj >> 3);
  const int id = panel * 16 + hp;
  asm volatile("" : "+s"(x), "+s"(wqkv), "+s"(wphi), "+s"(wo), "+s"(w_r), "+s"(w_gate), "+s"(w_up), "+s"(w_down), "+s"(ws), "+s"(out));
  SB0();
  u32x4 xr4[4], wr4[2];
  if (w < 4) {
    const u16* xr = x + (size_t)(panel * 128 + hp * 8 + 2 * w) * DM; const unsigned vl = (unsigned)l * 16;
#define GLS(dst, p, o) asm volatile("global_load_dwordx4 %0, %1, %2 offset:" #o : "=v"(dst) : "v"(vl), "s"(p) : "memory")
    GLS(xr4[0], xr, 0); GLS(xr4[1], xr, 1024); GLS(xr4[2], xr, 2048); GLS(xr4[3], xr, 3072); GLS(wr4[0], w_r, 0); GLS(wr4[1], w_r, 1024);
  }
  SB0();
  const int cw = w & 3, wm = cw & 1, wn = cw >> 1;
  const int h = l >> 5, r31 = l & 31, blk = (l >> 4) & 1, tq = (l & 15) >> 2, tp = l & 3;
  const int hd = hp * 2 + wn;
  const unsigned lbase = LDS_OFF(smem);
  const __amdgpu_buffer_rsrc_t rs = __builtin_amdgcn_make_buffer_rsrc((void*)ws, 0, (int)WS_END, 0x00020000);
  bf16x8 fr[4];
  bf16x8 wp[2];
  f32x4 qa[4][2]; float rt;
  const int kh = w >> 2;
  const int wt = wm * 2 + kh;
  const unsigned img = (wn * 4 + wt) * 4096 + r31 * 128 + (((2 * h) ^ (r31 & 7)) * 16);
  auto wpload = [&]() {
    const char* p = smem + X_WP + wn * 2048 + (8 * h + tq) * 64 + (16 * blk + 4 * tp) * 2;
    wp[0] = cat8(trread(p), trread(p + 256)); wp[1] = cat8(trread(p + 1024), trread(p + 1280));
  };
  auto imgw = [&](int g, const f32x2 (&ep)[8], const f32x2 (&en)[8]) {
    const f32x2 (&e)[8] = g < 2 ? ep : en; const int q = 4 * (g & 1);
    *(u32x4*)(smem + (img ^ ((g >> 1) * 64 + (g & 1) * 16))) = u32x4{pk2(e[q][0], e[q][1]), pk2(e[q + 1][0], e[q + 1][1]), pk2(e[q + 2][0], e[q + 2][1]), pk2(e[q + 3][0], e[q + 3][1])};
  };
  auto pack_u = [&](const f32x4 x0, const f32x4 x1) {
    const auto s0 = __builtin_amdgcn_permlane16_swap(pk2(x0[0], x0[1]), pk2(x1[0], x1[1]), false, false);
    const auto s1 = __builtin_amdgcn_permlane16_swap(pk2(x0[2], x0[3]), pk2(x1[2], x1[3]), false, false);
    return u32x4{s0[0], s1[0], s0[1], s1[1]};
  };
  const int hr = hp ^ 8;
  const int p3 = ((xcd >> 1) * 4 + (jj & 3)) ^ 8, nb = (int)(((xcd & 1) ? 0xF7EDCA98u : 0xB3654210u) >> (jj & 28)) & 15;
  const int flip = jj & 1;
  const unsigned voO = (unsigned)((l >> 3) * DM + (((l & 7) ^ (((l >> 4) & 1) | ((l >> 5) << 2))) * 8)) * 2;
  const bool has_gu = (hp & 7) != 3;
  int gr = (xcd == 6 && jj >= 12) ? 8 * (24 + (jj & 3)) + 2 * ((jj >> 2) - 3) : b;
  asm volatile("" : "+s"(gr));
  gr = jj >= 28 ? 8 * (4 * (xcd >> 1) + jj - 16) + 6 : gr;
  asm volatile("" : "+s"(gr));
  gr = has_gu ? gr : 224 + (jj - 24) * 4 + (xcd >> 1);
  const int gkq = (gr / 56) & 3;
  u32x4 dv[8]; unsigned ogq; bf16x8 dn[4];
  u32x4 wr_[16];
    const int srow = l >> 3, sch = l & 7;
    unsigned voA[4], voW[6];
#pragma unroll
    for (int i = 0; i < 4; ++i) voA[i] = (unsigned)((panel * 128 + (4 * cw + i) * 8 + srow) * DM + ((sch ^ (4 * (i & 1) + (srow >> 1))) * 8)) * 2;
#pragma unroll
    for (int i = 0; i < 6; ++i) voW[i] = (unsigned)(((2 * cw + (i & 1)) * 8 + srow) * 3072 + (i >> 1) * 1024 + hp * 64 + ((sch ^ ((((srow >> 1) & 1) << 2) | ((i & 1) << 1))) * 8)) * 2;
    const unsigned lwa = lbase + cw * 4096, lww = lbase + ABYTES + cw * 2048;
    const int rot = 4 * ((panel & 7) >> 1) + 2 * (panel >> 3) + 4 * (xcd & 3);
    u64 k0_ = (panel & 8) ? ((panel & 1) ? 0xD95FB7EA62C30841ull : 0xD95FB73EA6C21840ull) : (panel & 1) ? 0xFB7EA62D95C30841ull : 0xFB73EA6D95C21840ull, k1_ = (panel & 8) ? 0xD95FB73EA62C1840ull : 0xFB73EA62D95C1840ull;
    asm volatile("" : "+s"(k0_), "+s"(k1_));
    const u64 kord = has_gu ? k1_ : k0_;
    auto stage = [&](int it, auto bufc) {
      constexpr int B = decltype(bufc)::value * STAGE1;
      const int kt = ((int)(kord >> (4 * it)) + rot) & 15;
      const u16* gx = x + kt * 64; const u16* gq_ = wqkv + (size_t)kt * 64 * 3072;
      static_for<10>([&](auto ic) {
        constexpr int i = decltype(ic)::value;
        if constexpr (i < 4) gl_m0<B + i * 1024>(voA[i], gx, lwa);
        else gl_m0<B + ((i - 4) >> 1) * WSLAB + (i & 1) * 1024>(voW[i - 4], gq_, lww);
      });
    };
  unsigned pa0, pvh, dst, dz, pgw, ogu, oh1, oh1z;
  auto kvsetup = [&]() {
    int l = tid & 63; asm volatile("" : "+v"(l));
    const int g = l >> 4, c15 = l & 15, tq = c15 >> 2, tp = l & 3;
    pa0 = lbase + kh * 16384 + (4 * g + tq) * 128 + (((2 * cw + (tp >> 1)) ^ ((4 * g + tq) & 7)) * 16) + 8 * (tp & 1);
    pvh = lbase + HANDOFF + kh * 12288 + l * 16;
    dst = WS_KVP + (unsigned)((hp * 16 + panel) * 16 + (w & 6) * 2 + (g & 2) + (w & 1)) * 512 + (g & 1) * 256 + c15 * 16;
    dz = WS_ZP + (unsigned)((hp * 16 + (w & 6) * 2 + ((c15 >> 2) & 2) + (w & 1)) * 16 + panel) * 32 + (c15 & 7) * 4;
    pgw = lbase + gu_at(0) + kh * 4096 + (8 * g + tq) * 128 + (((2 * cw + (tp >> 1)) ^ ((((tq >> 1) & 1) << 2) | ((g & 1) << 1))) * 16) + 8 * (tp & 1);
    ogu = WS_GUP + (unsigned)gr * 512 + w * 64 + g * 16;
    oh1 = WS_KVP + (unsigned)((hr * 16 + (l >> 3)) * 16 + panel) * 512 + (l & 4) * 64 + cw * 64 + (l & 3) * 16;
    oh1z = WS_ZP + (unsigned)((hr * 16 + panel) * 16) * 32 + l * 4;
    asm volatile("" : "+v"(pa0), "+v"(pvh), "+v"(dst), "+v"(dz), "+v"(pgw), "+v"(ogu), "+v"(oh1), "+v"(oh1z));
  };
#define STUB(n) asm volatile(".Lkf_st" #n ":\n\ts_cbranch_execz .Lkf_rt" #n)
#define VISIT(n, wv) asm volatile("s_cmp_lg_u32 %0, " #wv "\n\ts_cbranch_scc1 .Lkf_rt" #n "\n\ts_mov_b64 exec, 0\n\ts_branch .Lkf_st" #n "\n.Lkf_rt" #n ":\n\ts_mov_b64 exec, -1" :: "s"(cw) : "scc")
#define IC(n) std::integral_constant<int, (n)>{}
#define CODE_PF(tagn, FROM, d) do { u64 pc_; unsigned len_; int off_; \
    asm volatile("s_getpc_b64 %0\n.Lkf_pf_h" #tagn ":\n\ts_mov_b32 %1, .Lkf_pf_end+1152-" FROM "\n\ts_mov_b32 %2, " FROM "-.Lkf_pf_h" #tagn : "=s"(pc_), "=s"(len_), "=s"(off_)); \
    unsigned o_ = (unsigned)(jj * 4 + cw) * 256u; o_ = o_ + 256u <= len_ ? o_ : len_ - 256u; \
    const u64 pa_ = (pc_ + (long long)off_ + o_) & ~63ull; \
    asm volatile("s_load_dword %0, %1, 0x0\n\ts_load_dword %0, %1, 0x40\n\ts_load_dword %0, %1, 0x80\n\ts_load_dword %0, %1, 0xc0" : "=&s"(d) : "s"(pa_) : "memory"); } while (0)
#define PF_DONE(d) asm volatile("s_waitcnt lgkmcnt(0)" : "+s"(d) :: "memory")
  if (w < 4) {
    unsigned oc2;
    {
      const unsigned r0 = (unsigned)(panel * 128 + hp * 8);
      oc2 = l < 33 ? WS_KVF + (unsigned)(hr * 16 + panel) * 640 + l * 16 : l == 33 ? WS_F1 + (unsigned)id * 16 : l == 34 ? WS_STAT + r0 * 8 : l == 35 ? WS_RI + (unsigned)(panel * 1024 + hp * 32)
          : l < 40 ? WS_F4 + (unsigned)(((panel * 4 + (l - 36)) * 16 + hp) * 2) * 32 : l < 44 ? WS_ZP + (unsigned)(hr * 16 + panel) * 512 + (l - 40) * 128 : l < 48 ? WS_GUP + (unsigned)gr * 512 + (l & 3) * 128 : l == 48 ? WS_RI + (unsigned)(panel * 1024 + 512 + hp * 16) : WS_END;
    }
    u32x4 tm, k0, k1; u32x2 tk;
    const unsigned tmofs = lbase + X_CSP + (l < 47 ? l : 46) * 16, kofs = lbase + X_CSP + 768, tgofs = lbase + X_AM + 48;
    unsigned opay;
    { const unsigned r0 = (unsigned)(panel * 128 + hp * 8);
      opay = l < 32 ? WS_CS + (unsigned)(hp * 8 + (panel >> 1)) * 768 + (panel & 1) * 512 + l * 16 : l < 40 ? WS_XA + r0 * 16 + (l - 32) * 16 : l < 44 ? WS_STAT + r0 * 8 + (l - 40) * 16 : l < 46 ? WS_RI + (unsigned)(panel * 1024 + hp * 32) + (l - 44) * 16 : WS_RI + (unsigned)(panel * 1024 + 512 + hp * 16); }
    const unsigned oflag = WS_F1 + (unsigned)id * 16;
    unsigned mz; asm volatile("s_cmp_eq_u32 %1, 0\n\ts_cselect_b32 %0, -1, 0" : "=s"(mz) : "s"(cw) : "scc");
    const u64 mk_all = ((u64)mz << 32) | mz, mk_pay = ((u64)(mz & 0x7FFFu) << 32) | (mz & ((panel & 1) ? 0xFFFFu : 0xFFFFFFFFu)), mk_one = mz & 1u;
    asm volatile("" : "+v"(oc2), "+v"(opay));
#define MST(op, data, off, mask) asm volatile("s_mov_b64 exec, %3\n\t" op " %0, %1, %2, 0 offen sc1\n\ts_mov_b64 exec, -1" :: "v"(data), "v"(off), "s"(rs), "s"(mask) : "memory")
    f32x4 csa[2] = {{0.f, 0.f, 0.f, 0.f}, {0.f, 0.f, 0.f, 0.f}};
    const bf16x8 ones = {0x3F80, 0x3F80, 0x3F80, 0x3F80, 0x3F80, 0x3F80, 0x3F80, 0x3F80};
    s16x4 cl[2][2], ch[2][2];
    const unsigned csw = lbase + X_CSP + (wm * 64 + wn * 32 + r31) * 4;
    f32x4 acc[4][6];
#pragma unroll
    for (int t = 0; t < 4; ++t)
#pragma unroll
      for (int a = 0; a < 6; ++a) acc[t][a] = f32x4{0.f, 0.f, 0.f, 0.f};
    const int g4 = l >> 4, c15 = l & 15;
    unsigned asw[2][2], wofs[2][2], cofs[2];
#pragma unroll
    for (int e = 0; e < 2; ++e) {
      asw[0][e] = lbase + (wm * 64 + c15) * 128 + (((4 * e + g4) ^ ((c15 >> 1) & 7)) * 16);
      wofs[0][e] = lbase + ABYTES + (8 * g4 + tq) * 128 + (((wn * 4 + 2 * e + (tp >> 1)) ^ ((((tq >> 1) & 1) << 2) | ((g4 & 1) << 1))) * 16) + 8 * (tp & 1);
      asw[1][e] = asw[0][e] + 2 * STAGE1; wofs[1][e] = wofs[0][e] + 2 * STAGE1;
      cofs[e] = wofs[0][e] + ((panel & 1) ? 2 : wm) * WSLAB;
    }
    asm volatile("" : "+v"(asw[0][0]), "+v"(asw[0][1]), "+v"(asw[1][0]), "+v"(asw[1][1]), "+v"(wofs[0][0]), "+v"(wofs[0][1]), "+v"(wofs[1][0]), "+v"(wofs[1][1]), "+v"(cofs[0]), "+v"(cofs[1]));
    bf16x8 af[2][4]; s16x4 wl[2][6], wh_[2][6];
    s16x4 ea[4], ew[6];
    const unsigned eaofs = lbase + X_XA + (wm * 64 + c15) * 16;
    const unsigned ewofs = lbase + X_XW + g4 * 1536 + (wn * 32 + c15) * 4;
    const unsigned riofs = lbase + X_ST + (wm * 64 + r31) * 4;
    auto rd = [&](auto ic, auto stc) {
      constexpr int i = decltype(ic)::value, S = decltype(stc)::value, s = S & 1, e = S & 1, buf = (S >> 1) % NST1;
      constexpr int b2 = buf == 2, bo = buf == 1 ? STAGE1 : 0;
      (void)&cl; (void)&ch; (void)&cofs; (void)&af; (void)&wl; (void)&wh_; (void)&asw; (void)&wofs;
      if constexpr (i == 0) DS_B128(af[s][0], asw[b2][e], bo);
      else if constexpr (i == 1) DS_TR(wl[s][0], wofs[b2][0], bo + e * 4096);
      else if constexpr (i == 2) DS_TR(wh_[s][0], wofs[b2][0], bo + e * 4096 + 512);
      else if constexpr (i < 6) DS_B128(af[s][i - 2], asw[b2][e], bo + (i - 2) * 2048);
      else if constexpr (i < 16) {
        constexpr int j = (i - 4) >> 1, off = bo + e * 4096 + (j >> 1) * WSLAB;
        if constexpr (i & 1) DS_TR(wh_[s][j], wofs[b2][j & 1], off + 512); else DS_TR(wl[s][j], wofs[b2][j & 1], off);
      } else {
        constexpr int c = (i - 16) >> 1;
        if constexpr (i & 1) DS_TR(ch[s][c], cofs[c], bo + e * 4096 + 512); else DS_TR(cl[s][c], cofs[c], bo + e * 4096);
      }
    };
    {
      SB0();
      asm volatile("s_waitcnt vmcnt(0) lgkmcnt(0)" : "+s"(pfd), "+v"(xr4[0]), "+v"(xr4[1]), "+v"(xr4[2]), "+v"(xr4[3]), "+v"(wr4[0]), "+v"(wr4[1]) :: "memory");
      SB0();
      float m, den, rinv, r;
      ln_rows(xr4, wr4, h, m, den, rinv, r);
      const int rw = tid >> 5;
      const unsigned rb = __float_as_uint(r) >> 16, ord = rb ^ ((rb & 0x8000u) ? 0xFFFFu : 0x8000u), nm = pk2(-m, -m);
      if (r31 == 0) { *(u32x4*)(smem + X_CSP + 512 + rw * 16) = u32x4{nm, nm, nm, nm};
        *(f32x2*)(smem + X_CSP + 640 + rw * 8) = f32x2{m, den}; *(float*)(smem + X_CSP + 704 + rw * 4) = rinv; *(u16*)(smem + X_CSP + 736 + rw * 2) = (u16)(__float_as_uint(den) >> 16);
        *(unsigned*)(smem + X_CSP + 768 + rw * 4) = (ord << 11) | (unsigned)(2047 - (id * 8 + rw)); }
      SB0();
    }
    __builtin_amdgcn_s_barrier();
    static_for<20>([&](auto ic) { rd(ic, IC(0)); });
    static_for<30>([&](auto stc) {
      constexpr int S = decltype(stc)::value, s = S & 1;
      constexpr bool bnd = (S & 1) == 1;
      static_for<24>([&](auto mc) {
        constexpr int m = decltype(mc)::value, j = m >> 2, t = (j & 1) ? 3 - (m & 3) : m & 3;
        (void)&tm; (void)&k0; (void)&k1; (void)&tk; (void)&ea; (void)&ew; (void)&oc2; (void)&opay; (void)&oflag; (void)&mk_all; (void)&mk_pay; (void)&mk_one; (void)&rs;
        (void)&tmofs; (void)&kofs; (void)&tgofs; (void)&csw; (void)&csa; (void)&eaofs; (void)&ewofs; (void)&riofs;
        if constexpr (m == 0) asm volatile("s_waitcnt lgkmcnt(%0)" :: "n"(bnd ? 0 : CSS(S) ? 14 : 10));
        else if constexpr (!bnd && m == 4) asm volatile("s_waitcnt lgkmcnt(%0)" :: "n"(CSS(S) ? 7 : 3));
        else if constexpr (!bnd && m == 12 && CSS(S)) asm volatile("s_waitcnt lgkmcnt(11)");
        SB0();
        const bf16x8 wf = cat8(wl[s][j], wh_[s][j]);
        if constexpr (j < 4) acc[t][j] = MFMA16(wf, af[s][t], acc[t][j]);
        else acc[t][j] = MFMA16(af[s][t], wf, acc[t][j]);
        SB0();
        if constexpr (S * 24 + m == 346) STUB(6);
        if constexpr (S * 24 + m == 646) STUB(7);
        if constexpr (m == 1) {
          if constexpr (S == 3) { const u32x4 sv = {SENT, SENT, SENT, SENT}; MST("buffer_store_dwordx4", sv, oc2, mk_all); }
          if constexpr (S == 11) { WAIT_V(0); asm volatile("" : "+v"(tm), "+v"(k0), "+v"(k1), "+v"(tk)); MST("buffer_store_dwordx4", tm, opay, mk_pay); }
          if constexpr (S == F1_S) { WAIT_V(0); MST("buffer_store_dwordx2", tk, oflag, mk_one); }
          if constexpr (S == 3) kvsetup();
          if constexpr (S == 5) { (void)&cw; VISIT(6, 1); VISIT(7, 2); VISIT(10, 3); }
          if constexpr (bnd) __builtin_amdgcn_s_barrier();
        }
        if constexpr (m >= 1 && m - 1 < (CSS(S + 1) ? 20 : 16)) rd(IC(m - 1), IC(S + 1));
        if constexpr (S == 8 && m == 18) {
          asm volatile("" : "+v"(csa[0]), "+v"(csa[1]));
          const float c = blk ? csa[1][0] : csa[0][0];
          const unsigned u = pk2(c, c - rbf(c));
          asm volatile("ds_write_b32 %0, %1" :: "v"(csw), "v"(u) : "memory");
        }
        if constexpr (S == 9 && m >= 18 && m < 22) {
          if constexpr (m == 18) DS_B128(tm, tmofs, 0);
          if constexpr (m == 19) DS_B128(k0, kofs, 0);
          if constexpr (m == 20) DS_B128(k1, kofs, 16);
          if constexpr (m == 21) asm volatile("ds_read_b64 %0, %1" : "=v"(tk) : "v"(tgofs));
        }
        if constexpr (S == 11 && m == 18) { k0 = __builtin_elementwise_max(k0, k1); asm volatile("" : "+v"(k0)); }
        if constexpr (S == 11 && m == 20) { tk[0] = (tk[0] & ~0x7FFFFFFu) | max(max(k0[0], k0[1]), max(k0[2], k0[3])); asm volatile("" : "+v"(tk)); }
#define DS_B64(dst, addr, off) asm volatile("ds_read_b64 %0, %1 offset:%2" : "=v"(dst) : "v"(addr), "n"(off))
#define DS_B32(dst, addr, off) asm volatile("ds_read_b32 %0, %1 offset:%2" : "=v"(dst) : "v"(addr), "n"(off))
#define DS_2(dst, addr, o0, o1) asm volatile("ds_read2_b32 %0, %1 offset0:%2 offset1:%3" : "=v"(dst) : "v"(addr), "n"(o0), "n"(o1))
        SB0();
      });
      if constexpr (CSS(S)) {
        SB0();
        csa[0] = MFMA16(ones, cat8(cl[s][0], ch[s][0]), csa[0]); csa[1] = MFMA16(ones, cat8(cl[s][1], ch[s][1]), csa[1]);
        SB0();
      }
    });
    static_for<16>([&](auto ic) {
      constexpr int i = decltype(ic)::value, j = 2 + (i >> 2), t = (j & 1) ? 3 - (i & 3) : i & 3;
      (void)&ea; (void)&ew; (void)&eaofs; (void)&ewofs;
      if constexpr (i == 0) asm volatile("s_waitcnt lgkmcnt(6)");
      else if constexpr (i == 4) asm volatile("s_waitcnt lgkmcnt(8)");
      SB0();
      const bf16x8 wf = cat8(wl[0][j], wh_[0][j]);
      if constexpr (j < 4) acc[t][j] = MFMA16(wf, af[0][t], acc[t][j]); else acc[t][j] = MFMA16(af[0][t], wf, acc[t][j]);
      SB0();
      if constexpr (i < 8) { rd(IC(2 * i), IC(31)); rd(IC(2 * i + 1), IC(31)); }
      else if constexpr (i < 12) DS_B64(ea[i - 8], eaofs, (i - 8) * 256);
      else if constexpr (i < 14) { const unsigned a_ = ewofs + 256; DS_2(ew[i - 10], a_, (i - 12) * 16, (i - 12) * 16 + 192); }
      SB0();
    });
    {
      __builtin_amdgcn_s_setprio(PRIO_T);
      asm volatile("s_waitcnt lgkmcnt(0)" : "+v"(af[1][0]), "+v"(af[1][1]), "+v"(af[1][2]), "+v"(af[1][3]), "+v"(wl[1][0]), "+v"(wh_[1][0]), "+v"(wl[1][1]), "+v"(wh_[1][1]),
                   "+v"(wl[1][2]), "+v"(wh_[1][2]), "+v"(wl[1][3]), "+v"(wh_[1][3]), "+v"(wl[1][4]), "+v"(wh_[1][4]), "+v"(wl[1][5]), "+v"(wh_[1][5]));
      asm volatile("" : "+v"(ea[0]), "+v"(ea[1]), "+v"(ea[2]), "+v"(ea[3]), "+v"(ew[2]), "+v"(ew[3]));
      SB0();
      auto mm = [&](int t, int j) { const bf16x8 wf = cat8(wl[1][j], wh_[1][j]); if (j < 4) acc[t][j] = MFMA16(wf, af[1][t], acc[t][j]); else acc[t][j] = MFMA16(af[1][t], wf, acc[t][j]); };
      auto ff = [&](int t, int j) { if (j < 4) acc[t][j] = MFMA16H(ew[j], ea[t], acc[t][j]); else acc[t][j] = MFMA16H(ea[t], ew[j], acc[t][j]); };
      auto dm = [&](int n) {
        SB0();
        const int t = n & 3, j = (n < 16 ? 4 : 0) + ((n >> 2) & 1);
        if (n & 8) ff(t, j); else mm(t, j);
        SB0();
      };
      const unsigned ew0 = ewofs, ew2 = ewofs + 512;
      mm(2, 2); SB0(); DS_B32(rt, riofs, 0); SB0();
      mm(3, 2); SB0();
      mm(2, 3); SB0(); DS_2(ew[4], ew2, 0, 192); DS_2(ew[5], ew2, 16, 208); SB0();
      mm(3, 3); SB0(); DS_2(ew[0], ew0, 0, 192); DS_2(ew[1], ew0, 16, 208); SB0();
      ff(2, 2); ff(3, 2); SB0(); wpload(); SB0();
      ff(2, 3); ff(3, 3); SB0(); SB0();
      mm(0, 2); mm(1, 2);
      SB0();
      SB0();
      char* ho = smem + HANDOFF + cw * 6144 + l * 16;
      *(u32x4*)(ho + 2048) = pack_u(acc[2][2], acc[3][2]);
      SB0(); mm(0, 3); mm(1, 3); SB0();
      *(u32x4*)(ho + 3072) = pack_u(acc[2][3], acc[3][3]);
      SB0(); ff(0, 2); ff(1, 2); ff(0, 3); ff(1, 3); SB0();
      RAW_BARRIER();
      asm volatile("" : "+v"(rt), "+v"(ew[0]), "+v"(ew[1]), "+v"(ew[4]), "+v"(ew[5]));
      dm(0); dm(1);
      fr[2] = __builtin_bit_cast(bf16x8, pack_u(acc[0][2], acc[1][2]));
      dm(2); dm(3);
      fr[3] = __builtin_bit_cast(bf16x8, pack_u(acc[0][3], acc[1][3]));
      dm(4);
      f32x2 ep[8], en[8];
      phi_fn(wp, fr[2], fr[3], ep, en, rt, rt, [&](auto kc) { constexpr int k = decltype(kc)::value; if constexpr (k == 0) { dm(5); dm(6); dm(7); } else if constexpr (7 + k < 16) dm(7 + k); });
    { const unsigned a_ = lbase + X_ST + 512 + (l >> 4) * 8;
      static_for<4>([&](auto tc) { constexpr int tb = decltype(tc)::value; (void)&dn; (void)&a_;
        asm volatile("ds_read2_b64 %0, %1 offset0:%2 offset1:%3" : "=v"(dn[tb]) : "v"(a_), "n"(tb * 8), "n"(tb * 8 + 4)); }); }
#pragma unroll
      for (int g = 0; g < 4; ++g) imgw(g, ep, en);
#pragma unroll
      for (int u = 0; u < 4; ++u) {
        const int t2 = u >> 1, db = u & 1;
        const f32x4 x0 = acc[2 * t2][4 + db], x1 = acc[2 * t2 + 1][4 + db];
        char* pv = t2 ? ho + 4096 : ho;
        *(u32x4*)(pv + db * 1024) = u32x4{pk2(x0[0], x0[1]), pk2(x0[2], x0[3]), pk2(x1[0], x1[1]), pk2(x1[2], x1[3])};
      }
#pragma unroll
      for (int t = 0; t < 4; ++t) { qa[t][0] = acc[t][0]; qa[t][1] = acc[t][1]; wr_[t] = __builtin_bit_cast(u32x4, af[1][t]); wr_[9 + t] = __builtin_bit_cast(u32x4, af[0][t]); }
      wr_[13] = __builtin_bit_cast(u32x4, cat8(wl[0][0], wh_[0][0])); wr_[14] = __builtin_bit_cast(u32x4, cat8(wl[0][1], wh_[0][1]));
      wr_[4] = __builtin_bit_cast(u32x4, cat8(wl[1][0], wh_[1][0])); wr_[5] = __builtin_bit_cast(u32x4, cat8(wl[1][1], wh_[1][1]));
      wr_[6] = __builtin_bit_cast(u32x4, cat8(ea[0], ea[1])); wr_[7] = __builtin_bit_cast(u32x4, cat8(ea[2], ea[3])); wr_[8] = __builtin_bit_cast(u32x4, cat8(ew[0], ew[1]));
    }
  } else {
    const int t = tid - 256;
    u64 epoch;
    asm volatile("s_load_dwordx2 %0, %1, 0x0" : "=&s"(epoch) : "s"(ws) : "memory");
    stage(0, IC(0));
    asm volatile("s_waitcnt lgkmcnt(0)" : "+s"(pfd), "+s"(epoch) :: "memory");
    const u64 enx = epoch + (((unsigned)epoch & 0xFEu) == 0xFEu ? 3u - ((unsigned)epoch & 1u) : 1u);
    const u64 tag = TAG_OF(enx);
    if (t == 0) { *(u32x2*)(smem + X_AM) = u32x2{0u, 0u};
      const unsigned e2 = ((unsigned)enx & 0x55u) | (((unsigned)enx & 0xAAu) << 15);
      *(u32x4*)(smem + X_AM + 32) = u32x4{e2, e2 >> 2, e2 >> 4, e2 >> 6} & 0x00010001u;
      *(u32x4*)(smem + X_AM + 48) = u32x4{(unsigned)tag, (unsigned)(tag >> 32), (unsigned)enx, (unsigned)(enx >> 32)}; }
    u32x2 fl, si; unsigned pfd2, tpace = 0, xs;
    unsigned ofl = WS_F1 + (unsigned)t * 16, orA = WS_CS + (unsigned)(hp * 6144 + cw * 1024 + l * 16);
    unsigned orB = (cw < 2 ? WS_CS + (unsigned)(hp * 6144 + 4096 + cw * 1024) : WS_XA + (unsigned)(panel * 2048 + (cw - 2) * 1024)) + l * 16;
    unsigned orC = ((cw & 1) ? WS_STAT + (unsigned)p3 * 1024 : WS_RI + (unsigned)panel * 1024) + l * 16;
    unsigned oxs = (unsigned)(gkq * 256 + cw * 64 + r31 * 2) * 2, akey = lbase + X_AM + 4, vol = (unsigned)l * 16;
    unsigned voG = (unsigned)((8 * cw + srow) * FF + ((sch ^ ((((srow >> 1) & 1) << 2) | ((cw & 1) << 1))) * 8)) * 2;
    const u16* gg_ = w_gate + (size_t)gkq * 256 * FF + ((gr + 1) % 56) * 64;
    const u16* gu_ = w_up + (size_t)gkq * 256 * FF + ((gr + 3) % 56) * 64;
    const u16* gwo = wo + (size_t)(((xcd >> 1) * 4 + (jj >> 3)) * 64 + 2 * cw * 8) * DM + ((xcd & 1) * 8 + (jj & 7)) * 64;
    const u16* gwp = wphi + hp * 2048 + cw * 512;
    const u16* gdw = w_down + (size_t)(((gr >> 3) & 31) * 128 + 32 * cw) * DM + ((gr + 1) & 7) * 128;
    unsigned odw = (unsigned)((l >> 4) * DM + (l & 15) * 8) * 2;
    const u16* gw2a = wo + (size_t)(((cw * 10) ^ (flip * 64)) * 8) * DM + nb * 64;
    asm volatile("" : "+s"(gw2a));
    asm volatile("" : "+s"(gg_), "+s"(gu_), "+s"(gwo), "+s"(gwp), "+s"(gdw));
    { const int f = (gr >> 3) * 128 + 32 * cw + 4 * (l & 7), fs = (f >> 6) - ((l & 8) ? 3 : 1);
      ogq = WS_GUP + (unsigned)((l >> 4) * 56 + (fs < 0 ? fs + 56 : fs)) * 512 + (f & 63) * 4 + (l & 8) * 32; }
    asm volatile("" : "+v"(voG), "+v"(odw), "+v"(ogq));
    asm volatile("s_waitcnt lgkmcnt(0)" : "+v"(ofl), "+v"(orA), "+v"(orB), "+v"(orC), "+v"(oxs), "+v"(akey), "+v"(vol) :: "memory");
    stage(1, IC(1));
    WAIT_V(10);
    __builtin_amdgcn_s_barrier();
    auto wopf = [&]() {
      GLDS_S(voO, gwo, lbase + X_WO + 2 * cw * 1024); GLDS_S(voO, gwo + 8 * DM, lbase + X_WO + (2 * cw + 1) * 1024);
      GLDS_S(vol, gwp, lbase + X_WP + cw * 1024);
    };
    auto dvl = [&](auto kc) {
      constexpr int k = decltype(kc)::value;
      static_for<DVN[k + 2]>([&](auto ic) {
        constexpr int i = dv_first(k) + decltype(ic)::value;
        u32x4& d_ = dv[i]; const unsigned o_ = odw; const u16* p_ = gdw + (size_t)(4 * i) * DM;
        asm volatile("global_load_dwordx4 %0, %1, %2 nt" : "=v"(d_) : "v"(o_), "s"(p_) : "memory");
      });
    };
    stage(2, IC(2));
    const unsigned lgu = lbase + cw * 1024;
    auto gul = [&](auto ksc) {
      constexpr int ks = decltype(ksc)::value;
      gl_nt<gu_at(ks)>(voG, gg_ + (size_t)ks * 32 * FF, lgu); gl_nt<gu_at(ks) + 4096>(voG, gu_ + (size_t)ks * 32 * FF, lgu);
    };
#define BLD64(dst, off) asm volatile("buffer_load_dwordx2 %0, %1, %2, 0 offen sc1" : "=v"(dst) : "v"(off), "s"(rs) : "memory")
    static_for<15>([&](auto ktc) {
      constexpr int kt = decltype(ktc)::value;
      constexpr auto exw = [](int k) { return DVN[k + 2]; };
      constexpr auto exo = [](int k) { return k == KP ? 1 : k == KC + 1 ? 3 : k == KC + 2 ? 2 : 0; };
      constexpr int exa = ((kt - 2 == WOK || kt - 1 == WOK) ? 3 : 0) + (kt - 2 > KC ? exo(kt - 2) : 0);
      if constexpr (exw(kt - 2) + exw(kt - 1) != 0) { if (has_gu) WAIT_V(10 + exa + exw(kt - 2) + exw(kt - 1) + exo(kt - 1)); else WAIT_V(10 + exa + exo(kt - 1)); }
      else if constexpr (kt < 14) WAIT_V(10 + exa + exo(kt - 1));
      else { if (has_gu) WAIT_V(10); else WAIT_V(0); }
      if constexpr (PACE_T[kt] != 0) { if (cw == 0) { while ((unsigned)__builtin_amdgcn_s_memrealtime() - tpace < (unsigned)(has_gu ? PACE_T[kt] : PACE_3[kt])) {} } }
      __builtin_amdgcn_s_barrier();
      if constexpr (PACE_T[kt + 1] != 0) { if (cw == 0) tpace = (unsigned)__builtin_amdgcn_s_memrealtime(); SB0(); }
#define UMAX_DPP(C) { const unsigned o = (unsigned)__builtin_amdgcn_update_dpp(0, (int)key, C, 0xf, 0xf, true); key = o > key ? o : key; asm volatile("" : "+v"(key)); }
      if constexpr (kt + NST1 < 16) { SB0(); stage(kt + NST1, IC(kt % NST1)); SB0(); }
      if constexpr (kt == WOK) wopf();
      if constexpr (kt == PF2_KT) CODE_PF(2, ".Lkf_pf_mid", pfd2);
      if constexpr (kt == PF2_KT + 2) PF_DONE(pfd2);
      if constexpr (kt == KP) BLD64(fl, ofl);
      if constexpr (kt == KC) {
        static_assert(DVN[KP + 2] == 0, "count");
        if constexpr (KC == KP + 1) asm volatile("s_waitcnt vmcnt(10)" : "+v"(fl) :: "memory"); else asm volatile("" : "+v"(fl) :: "memory");
#define FL_OK() __all((((fl[0] ^ (unsigned)tag) >> 27) | (fl[1] ^ (unsigned)(tag >> 32))) == 0u)
        if (__builtin_expect(!FL_OK(), 0)) {
          for (unsigned sp = 0;;) {
            asm volatile("buffer_load_dwordx2 %0, %1, %2, 0 offen sc1\n\ts_waitcnt vmcnt(0)" : "=v"(fl) : "v"(ofl), "s"(rs) : "memory");
            if (FL_OK()) break;
            SPIN_NEXT(sp);
          }
        }
        unsigned key = fl[0];
        UMAX_DPP(0x121) UMAX_DPP(0x122) UMAX_DPP(0x124) UMAX_DPP(0x128)
        asm volatile("s_mov_b64 exec, %2\n\tds_max_u32 %0, %1\n\ts_mov_b64 exec, -1\n\ts_waitcnt lgkmcnt(0)" :: "v"(akey), "v"(key), "s"(0x0001000100010001ull) : "memory");
      }
      if constexpr (kt == KC + 1) { GLDS_SC1(orA, ws, lbase + X_XW + cw * 1024); GLDS_SC1(orB, ws, lbase + X_XW + 4096 + cw * 1024); GLDS_SC1(orC, ws, lbase + X_ST + (cw & 1) * 1024); }
      if constexpr (kt == KC + 2) {
        const int bi = 2047 - (int)(__builtin_amdgcn_readfirstlane(*(const unsigned*)(smem + X_AM + 4)) & 2047u);
        asm volatile("global_load_dword %0, %1, %2" : "=v"(xs) : "v"(oxs), "s"(x + (size_t)bi * DM) : "memory");
        asm volatile("buffer_load_dwordx2 %0, off, %1, %2 sc1" : "=v"(si) : "s"(rs), "s"(WS_STAT + (unsigned)bi * 8) : "memory");
      }
      if constexpr (DVN[kt + 2] != 0) { if (has_gu) dvl(ktc); }
      if constexpr (kt == 0) kvsetup();
      if constexpr (kt == 13) { if (has_gu) { gul(IC(0)); gul(IC(1)); gul(IC(2)); gul(IC(3)); gul(IC(4)); } }
      if constexpr (kt == 14) asm volatile("" : "+v"(xs), "+v"(si) :: "memory");
      if constexpr (kt == 14) { if (has_gu) { static_for<1>([&](auto ic) { gul(IC(5 + decltype(ic)::value)); }); } }
    });
    auto wld = [&](auto ic) {
      constexpr int i = decltype(ic)::value;
      u32x4& d_ = wr_[i]; const unsigned o_ = voO; const u16* p_ = gw2a + (size_t)(i * 8) * DM;
      asm volatile("global_load_dwordx4 %0, %1, %2" : "=v"(d_) : "v"(o_), "s"(p_) : "memory");
    };
    wpload();
    rt = own(*(const float*)(smem + X_ST + (wt * 32 + r31) * 4));
    {
      const float m = __uint_as_float(si[0]), den = own(__uint_as_float(si[1])), rinv = rcp_nr(den);
      *(unsigned*)(smem + X_XS + cw * 128 + r31 * 4) = ln_pair(xs, m, den, rinv);
    }
    RAW_BARRIER();
    const char* ho = smem + HANDOFF + cw * 6144 + l * 16;
    fr[2] = *(const bf16x8*)(ho + 2048); fr[3] = *(const bf16x8*)(ho + 3072);
    f32x2 ep[8], en[8];
    phi_fn(wp, fr[2], fr[3], ep, en, rt, rt, [&](auto kc) {
      constexpr int k = decltype(kc)::value;
      if constexpr (k == 0) { SB0(); if (has_gu) gul(IC(6)); SB0(); }
      if constexpr (k == 5) { SB0(); if (has_gu) gul(IC(7)); SB0(); }
      static_for<NREG - WPOST>([&](auto ic) {
        constexpr int i = decltype(ic)::value;
        if constexpr (k == WH0 + (i * (17 - WH0)) / (NREG - WPOST)) { SB0(); wld(ic); SB0(); }
      });
    });
    { const unsigned a_ = lbase + X_ST + 512 + (l >> 4) * 8;
      static_for<4>([&](auto tc) { constexpr int tb = decltype(tc)::value; (void)&dn; (void)&a_;
        asm volatile("ds_read2_b64 %0, %1 offset0:%2 offset1:%3" : "=v"(dn[tb]) : "v"(a_), "n"(tb * 8), "n"(tb * 8 + 4)); }); }
#pragma unroll
    for (int g = 0; g < 4; ++g) imgw(g, ep, en);
    SB0();
    static_for<WPOST>([&](auto ic) { wld(IC(NREG - WPOST + decltype(ic)::value)); });
  }
  asm volatile(".Lkf_pf_mid:");
  f32x16 zero;
#pragma unroll
  for (int i = 0; i < 16; ++i) zero[i] = 0.f;
  u32x2 tg; unsigned t8, rkey; u32x4 t2;
  {
    __builtin_amdgcn_s_setprio(2);
    RAW_BARRIER();
    asm volatile("" : "+v"(dn[0]), "+v"(dn[1]), "+v"(dn[2]), "+v"(dn[3]));
    u32x4 am; unsigned rk;
    { const unsigned a_ = lbase + X_AM; DS_B128(am, a_, 48); DS_B128(t2, a_, 32); asm volatile("ds_read_b32 %0, %1 offset:4" : "=v"(rk) : "v"(a_)); }
    s16x4 al[4], ah[4]; bf16x8 v0[4], v1[4];
    DS_TR(al[0], pa0, 0); DS_TR(ah[0], pa0, 2048); DS_B128(v0[0], pvh, 0); DS_B128(v1[0], pvh, 1024);
    DS_TR(al[1], pa0, 4096); DS_TR(ah[1], pa0, 4096 + 2048); DS_B128(v0[1], pvh, 4096); DS_B128(v1[1], pvh, 4096 + 1024);
    DS_TR(al[2], pa0, 8192); DS_TR(ah[2], pa0, 8192 + 2048); DS_B128(v0[2], pvh, 6144); DS_B128(v1[2], pvh, 6144 + 1024);
    DS_TR(al[3], pa0, 12288); DS_TR(ah[3], pa0, 12288 + 2048); DS_B128(v0[3], pvh, 6144 + 4096); DS_B128(v1[3], pvh, 6144 + 4096 + 1024);
    f32x4 k0 = {0.f, 0.f, 0.f, 0.f}, k1 = k0, zz = k0;
    static_for<4>([&](auto tbc) {
      constexpr int tb = decltype(tbc)::value;
      asm volatile("s_waitcnt lgkmcnt(%4)" : "+v"(al[tb]), "+v"(ah[tb]), "+v"(v0[tb]), "+v"(v1[tb]) : "n"(12 - 4 * tb));
      const bf16x8 af = cat8(al[tb], ah[tb]);
      k0 = MFMA16(af, v0[tb], k0); k1 = MFMA16(af, v1[tb], k1); zz = MFMA16(dn[tb], af, zz);
      SB0();
    });
    asm volatile("" : "+v"(am), "+v"(rk), "+v"(t2));
    ST128((u32x4{pk2(k0[0], k0[1]), pk2(k0[2], k0[3]), pk2(k1[0], k1[1]), pk2(k1[2], k1[3])} & 0xFFFEFFFEu) | t2, dst);
    const unsigned zt = (__float_as_uint(zz[0]) & 0xFFFFFF00u) | (am[2] & 0xFFu);
    asm volatile("s_mov_b64 exec, 0xffff\n\tbuffer_store_dword %0, %1, %2, 0 offen sc1\n\ts_mov_b64 exec, -1" :: "v"(zt), "v"(dz), "s"(rs) : "memory");
    SB0();
    tg = u32x2{am[0], am[1]}; t8 = __builtin_amdgcn_readfirstlane(am[2]) & 0xFFu; rkey = __builtin_amdgcn_readfirstlane(rk);
  }
  const int idx = 2047 - (int)(rkey & 2047u);
  const int fb = kh ^ ((idx >> 4) & 1);
  const unsigned lxc = lbase + (cw < 2 ? 131072 : X_YS + 64 - 4096) + cw * 2048, lxv = w < 4 ? lxc : lbase + X_XW + cw * 2048;
  {
    bf16x8 xf[8];
    const unsigned px = lbase + X_XS + (l >> 4) * 16, hq = lbase + X_XW + cw * 2048 + l * 16;
    static_for<8>([&](auto kc) { constexpr int ks = decltype(kc)::value; (void)&xf; (void)&px; DS_B128(xf[ks], px, ks * 64); });
    if (w >= 4) {
      __builtin_amdgcn_s_setprio(0);
      WAIT_V(NREG + 2);
    } else {
      auto lo4 = [](u32x4 v) { return __builtin_bit_cast(s16x4, u32x2{v[0], v[1]}); };
      auto hi4 = [](u32x4 v) { return __builtin_bit_cast(s16x4, u32x2{v[2], v[3]}); };
#pragma unroll
      for (int tt = 0; tt < 4; ++tt) {
        const int t = tt ^ 2;
#pragma unroll
        for (int j = 0; j < 2; ++j) qa[t][j] = MFMA16(__builtin_bit_cast(bf16x8, wr_[13 + j]), __builtin_bit_cast(bf16x8, wr_[9 + t]), qa[t][j]);
#pragma unroll
        for (int j = 0; j < 2; ++j) qa[t][j] = MFMA16(__builtin_bit_cast(bf16x8, wr_[4 + j]), __builtin_bit_cast(bf16x8, wr_[t]), qa[t][j]);
        if (tt & 1) {
#pragma unroll
          for (int u = t - 1; u <= t; ++u)
#pragma unroll
            for (int j = 0; j < 2; ++j) qa[u][j] = MFMA16H(j ? hi4(wr_[8]) : lo4(wr_[8]), (u & 1) ? hi4(wr_[6 + (u >> 1)]) : lo4(wr_[6 + (u >> 1)]), qa[u][j]);
        }
      }
      const u32x4 q0 = pack_u(qa[2][0], qa[3][0]), q1 = pack_u(qa[2][1], qa[3][1]);
      asm volatile("ds_write_b128 %0, %1\n\tds_write_b128 %0, %2 offset:1024" :: "v"(hq), "v"(q0), "v"(q1) : "memory");
    }
    RAW_BARRIER();
    asm volatile("" : "+v"(xf[0]), "+v"(xf[1]), "+v"(xf[2]), "+v"(xf[3]), "+v"(xf[4]), "+v"(xf[5]), "+v"(xf[6]), "+v"(xf[7]));
    if (w >= 4) {
      { const u16* ga = wo + (size_t)(((cw * 10) ^ (flip * 64)) * 8) * DM + nb * 64; const u16* gb = wo + (size_t)(((104 + cw * 6) ^ (flip * 64)) * 8) * DM + nb * 64;
        static_for<16 - NREG>([&](auto ic) { constexpr int i = NREG + decltype(ic)::value;
          if constexpr (i < 10) gl_m0<i * 1024>(voO, ga + (size_t)(i * 8) * DM, lbase + cw * 10240); else gl_m0<(104 + i - 10) * 1024>(voO, gb + (size_t)((i - 10) * 8) * DM, lbase + cw * 6144); }); }
    }
    if (has_gu) {
      s16x4 gl[8], gh[8];
      static_for<8>([&](auto kc) { constexpr int ks = decltype(kc)::value; (void)&gl; (void)&gh; (void)&pgw;
        DS_TR(gl[ks], pgw, ks * 8192); DS_TR(gh[ks], pgw, ks * 8192 + 512); });
      f32x4 c0 = {0.f, 0.f, 0.f, 0.f};
      static_for<8>([&](auto kc) { constexpr int ks = decltype(kc)::value;
        asm volatile("s_waitcnt lgkmcnt(%2)" : "+v"(gl[ks]), "+v"(gh[ks]) : "n"(14 - 2 * ks));
        c0 = MFMA16(cat8(gl[ks], gh[ks]), xf[ks], c0);
        SB0(); });
      const u32x4 gt_ = f2u_tag(c0, t8);
      asm volatile("s_mov_b64 exec, %3\n\tbuffer_store_dwordx4 %0, %1, %2, 0 offen sc1\n\ts_mov_b64 exec, -1" :: "v"(gt_), "v"(ogu), "s"(rs), "s"(0x0001000100010001ull) : "memory");
    }
  }
  const int erow = cw * 32 + fb * 16 + (l & 15), ech = l >> 4;
  u32x4 xv[2];
  const bool sw = ech & 1;
  const bool has_idx = w < 4 && (idx >> 5) == p3 * 4 + w;
  unsigned xrt = 0;
  unsigned of4, op4, oo0, oo1, kba, kbz, odo, o2, oz2, wa_, wz_;
  auto later = [&]() {
    SB0();
    of4 = WS_F4 + (unsigned)(((panel * 4 + wt) * 16 + hp) * 2 + wn) * 32; op4 = WS_F4 + (unsigned)(((p3 * 4 + cw) * 16 + 8 * (kh ^ flip)) * 2 + (l & 15)) * 32;
    oo0 = WS_O + 512 + (unsigned)((((p3 * 4 + cw) * 2 + fb) * 32 + (kh ^ flip) * 16) * 128 + h * 64 + r31) * 16; oo1 = oo0 ^ 65536u;
    kba = lbase + X_KB + wn * 4224 + l * 16; kbz = lbase + X_KB + wn * 4224 + 4096 + h * 64;
    odo = WS_O + 512 + (unsigned)((((panel * 4 + wt) * 2 + blk) * 32 + hd) * 128 + h * 16 + (l & 15)) * 16;
    asm volatile("" : "+v"(kba), "+v"(kbz), "+v"(odo), "+v"(of4), "+v"(op4), "+v"(oo0), "+v"(oo1));
    SB0();
  };
  if (w < 4) {
    unsigned od; u64 mkp;
    {
      u32x4 pa[2]; unsigned z0 = t8, z1 = t8;
#define H1LD(p) asm volatile("buffer_load_dwordx4 %0, %4, %6, 0 offen sc1\n\tbuffer_load_dwordx4 %1, %4, %6, %7 offen sc1\n\t" \
                   "s_cmp_lg_u32 %8, 3\n\ts_cbranch_scc1 1f\n\tbuffer_load_dword %2, %5, %6, 0 offen sc1\n\tbuffer_load_dword %3, %5, %6, 0 offen offset:256 sc1\n1:" \
                   : "=&v"(p[0]), "=&v"(p[1]), "+v"(z0), "+v"(z1) : "v"(oh1), "v"(oh1z), "s"(rs), "s"(65536), "s"(cw) : "memory", "scc")
      __builtin_amdgcn_s_sleep(H1_DELAY);
      H1LD(pa);
      SB0();
      od = WS_KVF + (unsigned)(hr * 16 + panel) * 640 + ((l & 8) ? 512 + (l & 7) * 2 : cw * 128 + h * 64 + (l & 3) * 16 + (l & 4) * 2 + blk * 4);
      mkp = cw == 3 ? 0x00FF00FF00FF55FFull : 0x00FF00FF00FF00FFull;
      asm volatile("" : "+s"(mkp));
      fr[0] = __builtin_bit_cast(bf16x8, pack_u(qa[0][0], qa[1][0])); fr[1] = __builtin_bit_cast(bf16x8, pack_u(qa[0][1], qa[1][1]));
      asm volatile("" : "+v"(od), "+v"(fr[0]), "+v"(fr[1]));
      asm volatile("global_load_ushort %0, %1, %2" : "=v"(xrt) : "v"((unsigned)(idx * DM + nb * 64 + 4 * (l & 15) + ech) * 2), "s"(x) : "memory");
      later();
      o2 = WS_KVF + (unsigned)(hp * 16 + wn * 8 + wm * 4 + h) * 640 + ((r31 & 12) * 2 + (r31 >> 4) * 4 + (r31 & 3)) * 16;
      oz2 = WS_KVF + (unsigned)(hp * 16 + wn * 8 + wm * 4 + (l & 3)) * 640 + 512;
      wa_ = kba + wm * 2048; wz_ = lbase + X_KB + wn * 4224 + 4096 + (l & 1) * 64 + (wm * 2 + ((l >> 1) & 1)) * 16;
      asm volatile("" : "+v"(o2), "+v"(oz2), "+v"(wa_), "+v"(wz_));
      unsigned hb = 0x8000u, s_lo = 0x05040100u, s_hi = 0x07060100u;
      asm volatile("" : "+s"(s_lo), "+s"(s_hi));
      asm volatile("s_waitcnt vmcnt(1)" : "+v"(pa[0]), "+v"(pa[1]), "+v"(z0), "+v"(z1), "+v"(hb) :: "memory");
      u64 b0, b1, b2; u32x4 v0, v1;
      auto miss = [&]() __attribute__((always_inline)) {
        v0 = pa[0] ^ t2; v1 = pa[1] ^ t2;
        b0 = __ballot((or4(v0) & 0x00010001u) != 0u); b1 = __ballot((or4(v1) & 0x00010001u) != 0u);
        b2 = __ballot((((z0 ^ t8) | (z1 ^ t8)) & 0xFFu) != 0u);
        return (b0 | b1 | b2) != 0;
      };
      if (miss()) {
        unsigned sp = 0;
        do {
        b0 |= (b0 >> 4) & 0x0F0F0F0F0F0F0F0Full; b0 |= (b0 << 4) & 0xF0F0F0F0F0F0F0F0ull; b1 |= (b1 >> 4) & 0x0F0F0F0F0F0F0F0Full; b1 |= (b1 << 4) & 0xF0F0F0F0F0F0F0F0ull;
        asm volatile("s_wqm_b64 exec, %8\n\ts_cbranch_execz 1f\n\tbuffer_load_dwordx4 %0, %4, %6, 0 offen sc1\n1:\n\ts_wqm_b64 exec, %9\n\ts_cbranch_execz 2f\n\tbuffer_load_dwordx4 %1, %4, %6, %7 offen sc1\n2:\n\t"
                     "s_mov_b64 exec, -1\n\ts_cmp_eq_u64 %10, 0\n\ts_cbranch_scc1 3f\n\tbuffer_load_dword %2, %5, %6, 0 offen sc1\n\tbuffer_load_dword %3, %5, %6, 0 offen offset:256 sc1\n3:\n\ts_waitcnt vmcnt(0)"
                     : "+v"(pa[0]), "+v"(pa[1]), "+v"(z0), "+v"(z1) : "v"(oh1), "v"(oh1z), "s"(rs), "s"(65536), "s"(b0), "s"(b1), "s"(b2) : "memory", "scc");
        } while (miss() && ++sp < SPIN_CAP);
      }
    auto sw32 = [](float a, float c) { const auto r = __builtin_amdgcn_permlane32_swap(__float_as_uint(a), __float_as_uint(c), false, false); return __uint_as_float(r[0]) + __uint_as_float(r[1]); };
    auto sw16 = [](float a, float c) { const auto r = __builtin_amdgcn_permlane16_swap(__float_as_uint(a), __float_as_uint(c), false, false); return __uint_as_float(r[0]) + __uint_as_float(r[1]); };
    float sl[4], sh[4];
#pragma unroll
    for (int m = 0; m < 4; ++m) {
      sl[m] = own(__uint_as_float(__builtin_amdgcn_perm(v0[m], hb, s_lo)) + __uint_as_float(__builtin_amdgcn_perm(v1[m], hb, s_lo)));
      sh[m] = own(__uint_as_float(__builtin_amdgcn_perm(v0[m], hb, s_hi)) + __uint_as_float(__builtin_amdgcn_perm(v1[m], hb, s_hi))); }
    const float e0 = dpp_add<0x128, 0xf>(sw16(sw32(sl[0], sl[2]), sw32(sl[1], sl[3]))), e1 = dpp_add<0x128, 0xf>(sw16(sw32(sh[0], sh[2]), sw32(sh[1], sh[3])));
    const float zs = x32_add(x16_add(dpp_add<0x128, 0xf>(__uint_as_float(z0 ^ t8) + __uint_as_float(z1 ^ t8))));
    const float zn = __builtin_bit_cast(float, __builtin_amdgcn_update_dpp(0, __builtin_bit_cast(int, zs), 0xB1, 0xf, 0xf, true));
    const unsigned kvb = min((l & 8) ? pk2(zs, zn) : pk2(e0, e1), SENT - 1u);
    asm volatile("s_mov_b64 exec, %3\n\tbuffer_store_dword %0, %1, %2, 0 offen sc1\n\ts_mov_b64 exec, -1" :: "v"(kvb), "v"(od), "s"(rs), "s"(mkp) : "memory");
    }
  } else {
    const char* hq = smem + X_XW + cw * 2048 + l * 16;
    fr[0] = *(const bf16x8*)hq; fr[1] = *(const bf16x8*)(hq + 1024);
  }
  bf16x8 pf[4];
  {
    f32x2 ep[8], en[8];
    phi_fn(wp, fr[0], fr[1], ep, en, rt, 1.0f, [](auto) {});
#pragma unroll
    for (int s = 0; s < 2; ++s) {
      pf[s] = __builtin_bit_cast(bf16x8, u32x4{pk2(ep[4 * s][0], ep[4 * s][1]), pk2(ep[4 * s + 1][0], ep[4 * s + 1][1]), pk2(ep[4 * s + 2][0], ep[4 * s + 2][1]), pk2(ep[4 * s + 3][0], ep[4 * s + 3][1])});
      pf[2 + s] = __builtin_bit_cast(bf16x8, u32x4{pk2(en[4 * s][0], en[4 * s][1]), pk2(en[4 * s + 1][0], en[4 * s + 1][1]), pk2(en[4 * s + 2][0], en[4 * s + 2][1]), pk2(en[4 * s + 3][0], en[4 * s + 3][1])});
    }
    asm volatile("" : "+v"(pf[0]), "+v"(pf[1]), "+v"(pf[2]), "+v"(pf[3]));
  }
  SB0();
  if (b == 0 && tid == 64) ST64(*(const u32x2*)(smem + X_AM + 56), WS_EPOCH);
  u32x4 gq; unsigned ayr; u32x4 mk4;
#define GQLD(tail) asm volatile("buffer_load_dwordx4 %0, %1, %2, 0 offen sc1" tail : "=v"(gq) : "v"(ogq), "s"(rs) : "memory")
  if (w >= 4) {
    {
      WAIT_V(18 - NREG);
    { const unsigned o_ = (unsigned)((p3 * 128 + cw * 32 + (l >> 3)) * DM + nb * 64 + ((l & 7) ^ (l >> 3)) * 8) * 2; const u16* xa = x + fb * 16 * DM; const u16* xb = x + (fb ^ 1) * 16 * DM;
      gl_m0<0>(o_, xa, lxv); gl_m0<1024>(o_, xa + 8 * DM, lxv); gl_m0<0>(o_, xb, lxc); gl_m0<1024>(o_, xb + 8 * DM, lxc); }
      SB0();
      char* pa_ = smem + cw * 10 * 1024 + l * 16;
#pragma unroll
      for (int i = 0; i < NREG; ++i) *(u32x4*)(pa_ + i * 1024) = wr_[i];
      SB0();
    }
    asm volatile("" : "+v"(dv[0]), "+v"(dv[1]), "+v"(dv[2]), "+v"(dv[3]), "+v"(dv[4]), "+v"(dv[5]), "+v"(dv[6]), "+v"(dv[7]) :: "memory");
    later();
    ayr = lbase + X_YR + (cw * 128 + (l & 15) * 8 + blk * 4) * 4;
    { const unsigned hm = (l & 1) ? 0xFFFF0000u : 0x0000FFFFu; const int q = (l & 15) >> 1;
      mk4 = u32x4{q == 0 ? hm : 0u, q == 1 ? hm : 0u, q == 2 ? hm : 0u, q == 3 ? hm : 0u}; }
    asm volatile("" : "+v"(ayr), "+v"(mk4));
    SB0();
  }
  auto downp = [&]() __attribute__((always_inline)) {
            auto red4 = [](float s0, float s1, float s2, float s3) {
              const auto r01 = __builtin_amdgcn_permlane16_swap(__float_as_uint(s0), __float_as_uint(s1), false, false);
              const auto r23 = __builtin_amdgcn_permlane16_swap(__float_as_uint(s2), __float_as_uint(s3), false, false);
              const float t01 = __uint_as_float(r01[0]) + __uint_as_float(r01[1]), t23 = __uint_as_float(r23[0]) + __uint_as_float(r23[1]);
              const auto r = __builtin_amdgcn_permlane32_swap(__float_as_uint(t01), __float_as_uint(t23), false, false);
              return __uint_as_float(r[0]) + __uint_as_float(r[1]);
            };
            const float gg = rbf(red4(__uint_as_float(gq[0]), __uint_as_float(gq[1]), __uint_as_float(gq[2]), __uint_as_float(gq[3])));
            const float uu = __builtin_bit_cast(float, __builtin_amdgcn_update_dpp(0, __builtin_bit_cast(int, gg), 0x128, 0xf, 0xf, true));
            const float hv0 = rbf(silu_r(gg)) * uu;
            const int hh = (int)pk2(hv0, hv0);
            f32x4 ya = {0.f, 0.f, 0.f, 0.f};
            static_for<8>([&](auto ic) {
              constexpr int i = decltype(ic)::value;
              u32x4 sel;
#pragma unroll
              for (int e = 0; e < 4; ++e) sel[e] = (unsigned)__builtin_amdgcn_update_dpp(0, hh, 0x150 + i, 0xf, 0xf, true) & mk4[e];
              ya = MFMA16(__builtin_bit_cast(bf16x8, sel), __builtin_bit_cast(bf16x8, dv[i]), ya);
            });
            asm volatile("" : "+v"(ya));
            if (l < 32) *(__attribute__((address_space(3))) f32x4*)(uintptr_t)ayr = ya;
  };
  {
    u32x4 kf[4], zf[4];
    if (w < 4) {
      u32x4 a, c, z; u64 m0 = ~0ull, m1 = ~0ull, m2 = ~0ull;
      unsigned sp = 0;
      do {
        asm volatile("s_mov_b64 exec, %8\n\ts_cbranch_execz 1f\n\tbuffer_load_dwordx4 %0, %3, %5, 0 offen sc1\n1:\n\ts_mov_b64 exec, %9\n\ts_cbranch_execz 2f\n\tbuffer_load_dwordx4 %1, %3, %5, 0 offen offset:1280 sc1\n2:\n\t"
                     "s_mov_b64 exec, %10\n\ts_cbranch_execz 3f\n\tbuffer_load_dwordx4 %2, %4, %5, 0 offen sc1\n3:\n\ts_mov_b64 exec, -1\n\ts_waitcnt vmcnt(0)\n\t"
                     "ds_write_b128 %6, %0\n\tds_write_b128 %6, %1 offset:1024\n\tds_write_b128 %7, %2"
                     : "+v"(a), "+v"(c), "+v"(z) : "v"(o2), "v"(oz2), "s"(rs), "v"(wa_), "v"(wz_), "s"(m0), "s"(m1), "s"(m2) : "memory");
        m0 = __ballot(max(max(max(a[0], a[1]), a[2]), a[3]) == SENT); m1 = __ballot(max(max(max(c[0], c[1]), c[2]), c[3]) == SENT); m2 = __ballot(max(max(max(z[0], z[1]), z[2]), z[3]) == SENT);
      } while ((m0 | m1 | m2) != 0 && ++sp < SPIN_CAP);
      asm volatile("" : "+v"(xrt));
    }
    RAW_BARRIER();
    DS_B128(kf[0], kba, 0); DS_B128(zf[0], kbz, 0); DS_B128(kf[1], kba, 1024); DS_B128(zf[1], kbz, 16);
    DS_B128(kf[2], kba, 2048); DS_B128(zf[2], kbz, 32); DS_B128(kf[3], kba, 3072); DS_B128(zf[3], kbz, 48);
    if (w >= 4 && has_gu) GQLD("");
    __builtin_amdgcn_s_setprio(0);
    f32x16 num = zero;
    float d0 = 0.f, d1 = 0.f, d2 = 0.f, d3 = 0.f;
    static_for<4>([&](auto cc) {
      constexpr int c = decltype(cc)::value;
      asm volatile("s_waitcnt lgkmcnt(%0)" :: "n"(6 - 2 * c));
      SB0();
      num = MFMA(__builtin_bit_cast(bf16x8, kf[c]), pf[c], num);
      SB0();
      const u32x4 pc = __builtin_bit_cast(u32x4, pf[c]);
      d0 = dot2(zf[c][0], pc[0], d0); d1 = dot2(zf[c][1], pc[1], d1); d2 = dot2(zf[c][2], pc[2], d2); d3 = dot2(zf[c][3], pc[3], d3);
      SB0();
    });
    const float rinv = __builtin_amdgcn_rcpf(x32_add(own(d0 + d1) + own(d2 + d3)) + 1e-6f);
    const f32x2 rr = {rinv, rinv};
#pragma unroll
    for (int q = 0; q < 2; ++q) {
      u32x4 u;
#pragma unroll
      for (int e = 0; e < 4; ++e) {
        const f32x2 r = f32x2{num[8 * q + 2 * e], num[8 * q + 2 * e + 1]} * rr;
        u[e] = pk2(r[0], r[1]);
      }
      ST128(u, odo + q * 1024);
      SB0();
    }
  }
  const int p0 = 8 * w + 40;
  const u16* gw2 = wo + (size_t)((p0 ^ (flip * 64)) * 8) * DM + nb * 64; unsigned lw2 = lbase + p0 * 1024;
  asm volatile("" : "+s"(gw2), "+s"(lw2));
  bool glate = false;
  if (w >= 4 && has_gu) {
    asm volatile("s_waitcnt vmcnt(2)" : "+v"(gq) :: "memory");
    gq ^= t8;
    glate = !__all((or4(gq) & 0xFFu) == 0u);
    if (!glate) downp();
  }
  asm volatile("s_waitcnt vmcnt(0)\n\ts_mov_b64 exec, 1\n\tbuffer_store_dwordx2 %0, %1, %2, 0 offen sc1\n\ts_mov_b64 exec, -1" :: "v"(tg), "v"(of4), "s"(rs) : "memory");
  if (__builtin_expect(glate, 0)) {
    for (unsigned sp = 0;;) {
      GQLD("\n\ts_waitcnt vmcnt(0)");
      gq ^= t8;
      if (__all((or4(gq) & 0xFFu) == 0u)) break;
      SPIN_NEXT(sp);
    }
    downp();
  }
  RAW_BARRIER();
  float rxn = 0.f, rsg = 0.f;
  u32x4 yq[7];
  unsigned oy = WS_YP + (unsigned)(l >> 4) * 4096 + nb * 256 + (l & 15) * 16;
#define YLD(tail) asm volatile("buffer_load_dwordx4 %0, %7, %8, 0 offen sc1\n\tbuffer_load_dwordx4 %1, %7, %8, %9 offen sc1\n\tbuffer_load_dwordx4 %2, %7, %8, %10 offen sc1\n\t" \
    "buffer_load_dwordx4 %3, %7, %8, %11 offen sc1\n\tbuffer_load_dwordx4 %4, %7, %8, %12 offen sc1\n\tbuffer_load_dwordx4 %5, %7, %8, %13 offen sc1\n\t" \
    "buffer_load_dwordx4 %6, %7, %8, %14 offen sc1" tail \
    : "=&v"(yq[0]), "=&v"(yq[1]), "=&v"(yq[2]), "=&v"(yq[3]), "=&v"(yq[4]), "=&v"(yq[5]), "=&v"(yq[6]) \
    : "v"(oy), "s"(rs), "s"(16384), "s"(2 * 16384), "s"(3 * 16384), "s"(4 * 16384), "s"(5 * 16384), "s"(6 * 16384) : "memory")
  f32x2 xf[2][4], gf[2][4];
  __attribute__((address_space(3))) f32x4* pw = (__attribute__((address_space(3))) f32x4*)(smem + 131072 + w * 4096) + l;
  unsigned prd = lbase + 131072 + (w ^ 4) * 4096 + l * 16;
  u16* po = out + (size_t)(p3 * 128 + cw * 32 + fb * 16 + (l >> 3)) * DM + nb * 64 + (l & 7) * 8; u16* po2 = po + 8 * DM;
  unsigned tw0 = lbase + 131072 + (w ^ 4) * 4096 + (l & 15) * 128 + ((ech ^ (l & 7)) * 16) + (sw ? 8 : 0), tw1 = tw0 ^ 64, tw2 = tw0 ^ 8, tw3 = tw0 ^ 72, trd = lbase + 131072 + (w ^ 4) * 4096 + (l >> 3) * 128 + (((l & 7) ^ (l >> 3)) * 16);
    unsigned bw[4];
#pragma unroll
    for (int n = 0; n < 4; ++n) bw[n] = lbase + kh * 65536 + (16 * h + 4 * blk + tq) * 128 + (((4 * (n >> 1) + tp) ^ ((tq >> 1) | (blk << 2))) * 16) + 8 * ((n ^ tp) & 1);
  {
  if (w == 1 && has_gu && l < 32) {
    const f32x4* y = (const f32x4*)(smem + X_YR) + l;
    ST128(f2u_tag((y[0] + y[32]) + (y[64] + y[96]), t8), WS_YP + (unsigned)(gr >> 3) * 4096 + ((gr + 1) & 7) * 512 + l * 16);
  }
    SB0();
    gl_run<8>(voO, gw2, lw2);
    u32x2 f0;
    asm volatile("buffer_load_dwordx2 %0, %1, %2, 0 offen sc1" : "=v"(f0) : "v"(op4), "s"(rs) : "memory");
    SB0();
    {
      const f32x2 md = *(const f32x2*)(smem + X_MD + erow * 8);
      { const unsigned ax = lxv + (l & 15) * 128 + ((ech ^ (l & 7)) * 16), a0 = ax + (sw ? 8 : 0), a1 = ax + (sw ? 0 : 8);
        u32x2 q0, q1, q2, q3;
        asm volatile("ds_read_b64 %0, %4\n\tds_read_b64 %1, %5\n\tds_read_b64 %2, %6\n\tds_read_b64 %3, %7\n\ts_waitcnt lgkmcnt(0)" : "=&v"(q0), "=&v"(q1), "=&v"(q2), "=&v"(q3) : "v"(a0), "v"(a1), "v"(a0 ^ 64), "v"(a1 ^ 64) : "memory");
        xv[0] = u32x4{q0[0], q0[1], q1[0], q1[1]}; xv[1] = u32x4{q2[0], q2[1], q3[0], q3[1]}; }
      const float rinv = rcp_nr(own(md[1])), mr = -md[0] * rinv, L2E = 1.44269504088896340736f, rl = -L2E * rinv, ml = -L2E * mr;
#pragma unroll
      for (int i = 0; i < 2; ++i)
#pragma unroll
        for (int e = 0; e < 4; ++e) {
          xf[i][e] = f32x2{bflo(xv[i][e]), bfhi(xv[i][e])};
          const f32x2 xn = __builtin_elementwise_fma(xf[i][e], f32x2{rinv, rinv}, f32x2{mr, mr}), tn = __builtin_elementwise_fma(xf[i][e], f32x2{rl, rl}, f32x2{ml, ml});
          const f32x2 dd = f32x2{__builtin_amdgcn_exp2f(tn[0]), __builtin_amdgcn_exp2f(tn[1])} + f32x2{1.0f, 1.0f};
          gf[i][e] = xn * f32x2{__builtin_amdgcn_rcpf(dd[0]), __builtin_amdgcn_rcpf(dd[1])};
        }
#pragma unroll
      for (int i = 0; i < 2; ++i) asm volatile("" : "+v"(xf[i][0]), "+v"(xf[i][1]), "+v"(xf[i][2]), "+v"(xf[i][3]), "+v"(gf[i][0]), "+v"(gf[i][1]), "+v"(gf[i][2]), "+v"(gf[i][3]));
      asm volatile("" : "+v"(pw), "+v"(prd), "+v"(po2), "+v"(tw0), "+v"(tw1), "+v"(tw2), "+v"(tw3), "+v"(trd));
      asm volatile("" : "+v"(oy), "+v"(po), "+v"(bw[0]), "+v"(bw[1]), "+v"(bw[2]), "+v"(bw[3]));
    }
    if (has_idx) {
      const unsigned ord = (rkey >> 11) & 0xFFFFu;
      rsg = rbf(1.0f / (1.0f + __expf(-__uint_as_float((ord ^ ((ord & 0x8000u) ? 0x8000u : 0xFFFFu)) << 16))));
      const f32x2 md = *(const f32x2*)(smem + X_MD + (idx & 127) * 8);
      const float dn = own(md[1]), rinv = rcp_nr(dn);
      rxn = bflo(ln_pair(xrt, md[0], dn, rinv));
      asm volatile("" : "+v"(rxn), "+v"(rsg));
    }
    u64 ne;
    SB0();
    asm volatile("s_waitcnt vmcnt(0)\n\tv_cmp_ne_u64 %0, %1, %2" : "=s"(ne) : "v"(f0), "v"(tg) : "memory");
    for (unsigned sp = 0; ne != 0 && sp < SPIN_CAP; ++sp) {
      u32x2 f;
      asm volatile("buffer_load_dwordx2 %1, %2, %3, 0 offen sc1\n\ts_waitcnt vmcnt(0)\n\tv_cmp_ne_u64 %0, %1, %4" : "=s"(ne), "=&v"(f) : "v"(op4), "s"(rs), "v"(tg) : "memory");
    }
  }
  constexpr int OWIN = 4;
  u32x4 of[16][2];
  {
    SB0();
#pragma unroll
    for (int i = 0; i < OWIN; ++i) { of[i][0] = LD128(oo0 + i * 2048); of[i][1] = LD128(oo1 + i * 2048); }
    SB0();
    if (has_idx) YLD("");
    SB0();
  }
    auto owner = [&](auto nc) __attribute__((always_inline)) {
      asm volatile("s_waitcnt vmcnt(%7)" : "+v"(yq[0]), "+v"(yq[1]), "+v"(yq[2]), "+v"(yq[3]), "+v"(yq[4]), "+v"(yq[5]), "+v"(yq[6]) : "n"(decltype(nc)::value) : "memory");
      for (unsigned sp = 0;; ++sp) {
        unsigned bad = 0;
#pragma unroll
        for (int i = 0; i < 7; ++i) { yq[i] ^= t8; bad |= or4(yq[i]); }
        if (__builtin_expect(__all((bad & 0xFFu) == 0u) || sp > SPIN_CAP, 1)) break;
        YLD("\n\ts_waitcnt vmcnt(0)");
      }
#define YF(i) __builtin_bit_cast(f32x4, yq[i])
      const f32x4 y4 = ((YF(0) + YF(1)) + (YF(2) + YF(3))) + ((YF(4) + YF(5)) + YF(6));
      const auto r01 = __builtin_amdgcn_permlane16_swap(__float_as_uint(y4[0]), __float_as_uint(own(y4[1])), false, false);
      const auto r23 = __builtin_amdgcn_permlane16_swap(__float_as_uint(y4[2]), __float_as_uint(own(y4[3])), false, false);
      const float t01 = __uint_as_float(r01[0]) + __uint_as_float(r01[1]), t23 = __uint_as_float(r23[0]) + __uint_as_float(r23[1]);
      const auto r = __builtin_amdgcn_permlane32_swap(__float_as_uint(t01), __float_as_uint(t23), false, false);
      const float y = __uint_as_float(r[0]) + __uint_as_float(r[1]);
      const float g1 = rbf(silu_r(rbf(rxn + rbf(rbf(y) * rsg))));
      (void)&gf; (void)&oy; (void)&rs; const u64 mk_ = 0x0001000100010001ull << (idx & 15);
      const unsigned gb = lbase + (131072 + w * 4096), aw_ = gb + (l & 15) * 16 + ech * 4, a0 = gb + ech * 32 + (ech & 1) * 16, a1 = a0 ^ 16;
      asm volatile("ds_write_b32 %8, %9\n\ts_mov_b64 exec, %12\n\t"
                   "ds_read_b64 %0, %10\n\tds_read_b64 %1, %10 offset:8\n\tds_read_b64 %2, %11\n\tds_read_b64 %3, %11 offset:8\n\t"
                   "ds_read_b64 %4, %10 offset:128\n\tds_read_b64 %5, %10 offset:136\n\tds_read_b64 %6, %11 offset:128\n\tds_read_b64 %7, %11 offset:136\n\t"
                   "s_mov_b64 exec, -1\n\ts_waitcnt lgkmcnt(0)"
                   : "+v"(gf[0][0]), "+v"(gf[0][1]), "+v"(gf[0][2]), "+v"(gf[0][3]), "+v"(gf[1][0]), "+v"(gf[1][1]), "+v"(gf[1][2]), "+v"(gf[1][3])
                   : "v"(aw_), "v"(g1), "v"(a0), "v"(a1), "s"(mk_) : "memory");
    };
  f32x4 acc3[2][4];
#pragma unroll
  for (int t = 0; t < 2; ++t)
#pragma unroll
    for (int n = 0; n < 4; ++n) acc3[t][n] = f32x4{0.f, 0.f, 0.f, 0.f};
  {
    s16x4 wa[2][8];
    auto rd3 = [&](auto kc, auto nc) {
      constexpr int k = decltype(kc)::value, n = decltype(nc)::value;
      (void)&wa; (void)&bw;
      DS_TR(wa[k & 1][2 * n], bw[n], k * 4096); DS_TR(wa[k & 1][2 * n + 1], bw[n], k * 4096 + 1024);
    };
    RAW_BARRIER();
    static_for<4>([&](auto nc) { rd3(IC(0), nc); });
    static_for<15>([&](auto kc) {
      constexpr int k = decltype(kc)::value, sx = k & 1;
      static_for<4>([&](auto nc) {
        constexpr int n = decltype(nc)::value;
        asm volatile("s_waitcnt lgkmcnt(%2)" : "+v"(wa[sx][2 * n]), "+v"(wa[sx][2 * n + 1]) : "n"(6));
        SB0();
        const bf16x8 wf = cat8(wa[sx][2 * n], wa[sx][2 * n + 1]);
        acc3[0][n] = MFMA16(wf, __builtin_bit_cast(bf16x8, of[k][0]), acc3[0][n]);
        SB0();
        if constexpr (k * 4 + n == 16) STUB(10);
        if constexpr (n == 0 && (k & 3) == 0) __builtin_amdgcn_s_setprio(3 - (k >> 2));
        if constexpr (k + 1 < 16) rd3(IC(k + 1), nc);
        if constexpr (k + OWIN < 16 && n == 3) { of[k + OWIN][0] = LD128(oo0 + (k + OWIN) * 2048); }
        SB0();
        acc3[1][n] = MFMA16(wf, __builtin_bit_cast(bf16x8, of[k][1]), acc3[1][n]);
        SB0();
        if constexpr (k + OWIN < 16 && n == 3) { of[k + OWIN][1] = LD128(oo1 + (k + OWIN) * 2048); SB0(); }
        if constexpr (k == YP_ && n == 3) { constexpr int e_ = YP_ + OWIN < 16 ? YP_ : 15 - OWIN; if (has_idx) owner(IC(2 * (e_ + 1))); SB0(); }
      });
    });
    static_for<4>([&](auto nc) {
      constexpr int n = decltype(nc)::value;
      asm volatile("s_waitcnt lgkmcnt(%2)" : "+v"(wa[1][2 * n]), "+v"(wa[1][2 * n + 1]) : "n"(6 - 2 * n));
      SB0();
      acc3[1][n] = MFMA16(cat8(wa[1][2 * n], wa[1][2 * n + 1]), __builtin_bit_cast(bf16x8, of[15][1]), acc3[1][n]);
      SB0();
      if constexpr (n == 3) { pw[0] = acc3[1][0]; SB0(); }
    });
    static_for<4>([&](auto nc) {
      constexpr int n = decltype(nc)::value;
      acc3[0][n] = MFMA16(cat8(wa[1][2 * n], wa[1][2 * n + 1]), __builtin_bit_cast(bf16x8, of[15][0]), acc3[0][n]);
      SB0();
      if constexpr (n < 3) { pw[(n + 1) * 64] = acc3[1][n + 1]; SB0(); }
    });
  }
  {
    RAW_BARRIER();
    f32x4 pr[4];
    DS_B128(pr[0], prd, 0); DS_B128(pr[1], prd, 1024); DS_B128(pr[2], prd, 2048); DS_B128(pr[3], prd, 3072);
    SB0();
#pragma unroll
    for (int i = 0; i < 2; ++i)
#pragma unroll
      for (int e = 0; e < 4; ++e) {
        const f32x4 a = acc3[0][2 * i + (e >> 1)];
        xf[i][e] = __builtin_elementwise_fma(f32x2{a[2 * (e & 1)], a[2 * (e & 1) + 1]}, gf[i][e], xf[i][e]);
        asm volatile("" : "+v"(xf[i][e]));
      }
    SB0();
    u32x4 res[2];
#pragma unroll
    for (int i = 0; i < 2; ++i) {
      if (i == 0) asm volatile("s_waitcnt lgkmcnt(2)" : "+v"(pr[0]), "+v"(pr[1])); else asm volatile("s_waitcnt lgkmcnt(2)" : "+v"(pr[2]), "+v"(pr[3]));
      const f32x4 s0 = pr[2 * i], s1 = pr[2 * i + 1];
      const f32x2 o0 = __builtin_elementwise_fma(f32x2{s0[0], s0[1]}, gf[i][0], xf[i][0]), o1 = __builtin_elementwise_fma(f32x2{s0[2], s0[3]}, gf[i][1], xf[i][1]);
      const f32x2 o2 = __builtin_elementwise_fma(f32x2{s1[0], s1[1]}, gf[i][2], xf[i][2]), o3 = __builtin_elementwise_fma(f32x2{s1[2], s1[3]}, gf[i][3], xf[i][3]);
      res[i] = u32x4{pk2(o0[0], o0[1]), pk2(o1[0], o1[1]), pk2(o2[0], o2[1]), pk2(o3[0], o3[1])};
      asm volatile("ds_write_b64 %0, %2\n\tds_write_b64 %1, %3" :: "v"(i ? tw1 : tw0), "v"(i ? tw3 : tw2), "v"(u32x2{res[i][0], res[i][1]}), "v"(u32x2{res[i][2], res[i][3]}) : "memory");
    }
    asm volatile("ds_read_b128 %0, %2\n\tds_read_b128 %1, %2 offset:1024\n\ts_waitcnt lgkmcnt(1)\n\t"
                 "global_store_dwordx4 %3, %0, off sc1\n\ts_waitcnt lgkmcnt(0)\n\tglobal_store_dwordx4 %4, %1, off sc1\n\ts_nop 1"
                 : "=&v"(res[0]), "=&v"(res[1]) : "v"(trd), "v"(po), "v"(po2) : "memory");
  }
  asm volatile(".Lkf_pf_end:" ::: "memory");
}

extern "C" void kernel_launch(void* const* d_in, const int* in_sizes, int n_in,
                              void* d_out, int out_size, void* d_ws, size_t ws_size,
                              hipStream_t stream) {
  (void)hipFuncSetAttribute((const void*)kf_all, hipFuncAttributeMaxDynamicSharedMemorySize, LDS_TOTAL);
  kf_all<<<256, 512, LDS_TOTAL, stream>>>((const u16*)d_in[0], (const u16*)d_in[1], (const u16*)d_in[2], (const u16*)d_in[3],
                                          (const u16*)d_in[5], (const u16*)d_in[6], (const u16*)d_in[7], (const u16*)d_in[8],
                                          (char*)d_ws, (u16*)d_out);
}
```
